```python
import torch

def _softmax(x, axis=-1):
    return torch.softmax(x, dim=axis)
def _gelu(x):
    return torch.nn.functional.gelu(x, approximate="tanh")
def _silu(x):
    return torch.nn.functional.silu(x)
def _sigmoid(x):
    return torch.sigmoid(x)
def _layernorm(x, axis=-1, eps=1e-5):
    m = x.mean(dim=axis, keepdim=True)
    v = x.var(dim=axis, keepdim=True, unbiased=False)
    return (x - m) / torch.sqrt(v + eps)
def _scatter_add(tgt, idx, src, axis=0):
    return tgt.clone().index_add_(axis, idx, src)
def _segment_sum(src, seg, n):
    out = torch.zeros((n,) + src.shape[1:], dtype=src.dtype)
    return out.index_add_(0, seg, src)
def _pad(x, pw):
    flat = tuple(v for pair in reversed(pw) for v in pair)
    return torch.nn.functional.pad(x, flat)

kernel_name = 'concat_heads__sliding_local_dilated__sliding_block__prompt'

def setup_inputs():

    _w = (lambda a: (lambda t: t.to(torch.bfloat16) if t.is_floating_point() else t)(torch.as_tensor(a)))
    def _A_setup():
        B, S, D = 1, 2048, 640
        return {
            "x": _w((lambda *s: torch.randn(*s, dtype=torch.bfloat16) * 0.2)(B, S, D)),
            "wqkv": _w((lambda *s: torch.randn(*s, dtype=torch.bfloat16) * 0.2)(D, 3 * D)),
            "wo": _w((lambda *s: torch.randn(*s, dtype=torch.bfloat16) * 0.05)(D, D)),
        }
    _A_in = _A_setup()

    def _B_setup():
        B, S, D = 1, 2048, 640
        return {
            "x": _w((lambda *s: torch.randn(*s, dtype=torch.bfloat16) * 0.2)(B, S, D)),
            "wqkv": _w((lambda *s: torch.randn(*s, dtype=torch.bfloat16) * 0.2)(D, 3 * D)),
            "wo": _w((lambda *s: torch.randn(*s, dtype=torch.bfloat16) * 0.05)(D, D)),
        }
    _B_in = _B_setup()

    return {
        "A_x": _A_in["x"],
        "A_wqkv": _A_in["wqkv"],
        "A_wo": _A_in["wo"],
        "B_x": _B_in["x"],
        "B_wqkv": _B_in["wqkv"],
        "B_wo": _B_in["wo"],
    }

def reference(A_x, A_wqkv, A_wo, B_x, B_wqkv, B_wo):
    def _A_ref(x, wqkv, wo):
        B, S, D = x.shape
        H = 32; Dh = D // H
        W, dil = 8, 2
        qkv = (lambda x, *d: x.permute(*d))((x @ wqkv).reshape(B, S, 3, H, Dh), 2, 0, 3, 1, 4)
        q, k, v = qkv[0], qkv[1], qkv[2]
        scale = Dh ** -0.5
        pos = torch.arange(S)
        off = torch.arange(W) * dil
        idx = pos[:, None] - off[None, :]
        mask = idx >= 0
        idx_c = torch.where(mask, idx, 0).reshape(-1)
        kg = k[:, :, idx_c, :].reshape(B, H, S, W, Dh)
        vg = v[:, :, idx_c, :].reshape(B, H, S, W, Dh)
        logits = torch.einsum("bhsd,bhswd->bhsw", q, kg) * scale
        logits = torch.where(mask[None, None, :, :], logits, -1e9)
        attn = _softmax(logits, axis=-1)
        o = torch.einsum("bhsw,bhswd->bhsd", attn, vg)
        return (lambda x, *d: x.permute(*d))(o, 0, 2, 1, 3).reshape(B, S, D) @ wo

    def _B_ref(x, wqkv, wo):
        B, S, D = x.shape
        H = 32; Dh = D // H; W = 256
        qkv = (lambda x, *d: x.permute(*d))((x @ wqkv).reshape(B, S, 3, H, Dh), 2, 0, 3, 1, 4)
        q, k, v = qkv[0], qkv[1], qkv[2]
        scores = torch.einsum("bhqd,bhkd->bhqk", q, k) * (Dh ** -0.5)
        idx = torch.arange(S)
        mask = (idx[:, None] - idx[None, :] >= 0) & (idx[:, None] - idx[None, :] < W)
        scores = torch.where(mask[None, None], scores, -1e9)
        p = _softmax(scores, axis=-1)
        o = torch.einsum("bhqk,bhkd->bhqd", p, v)
        return (lambda x, *d: x.permute(*d))(o, 0, 2, 1, 3).reshape(B, S, D) @ wo

    a = _A_ref(A_x, A_wqkv, A_wo)
    b = _B_ref(B_x, B_wqkv, B_wo)
    return torch.concatenate([a, b], axis=-1)
```

```cpp
#include <hip/hip_runtime.h>
#include <hip/hip_bf16.h>
typedef __hip_bfloat16 bf16;
constexpr int S=2048, D=640, H=32, DH=20;

__global__ void gemm_naive(const bf16* A, const bf16* B, bf16* C, int M, int N, int K, int ldc, int coff){
  __shared__ float As[16][17], Bs[16][17];
  int tx=threadIdx.x&15, ty=threadIdx.x>>4;
  int row=blockIdx.y*16+ty, col=blockIdx.x*16+tx;
  float acc=0;
  for(int k0=0;k0<K;k0+=16){
    As[ty][tx]=__bfloat162float(A[row*K+k0+tx]);
    Bs[ty][tx]=__bfloat162float(B[(k0+ty)*N+col]);
    __syncthreads();
    for(int k=0;k<16;k++) acc+=As[ty][k]*Bs[k][tx];
    __syncthreads();
  }
  C[row*ldc+coff+col]=__float2bfloat16(acc);
}
__global__ void attn_naive(const bf16* qkv, bf16* o, int W, int dil){
  int idx=blockIdx.x*blockDim.x+threadIdx.x;
  int s=idx/H, h=idx%H;
  float q[DH];
  for(int d=0;d<DH;d++) q[d]=__bfloat162float(qkv[s*3*D+h*DH+d]);
  float m=-1e30f,l=0,acc[DH];
  for(int d=0;d<DH;d++) acc[d]=0;
  const float scale=0.22360679774997896f;
  for(int w=0;w<W;w++){
    int k=s-w*dil; if(k<0) break;
    float sc=0;
    for(int d=0;d<DH;d++) sc+=q[d]*__bfloat162float(qkv[k*3*D+D+h*DH+d]);
    sc*=scale;
    float mn=fmaxf(m,sc); float a=__expf(m-mn), p=__expf(sc-mn);
    l=l*a+p;
    for(int d=0;d<DH;d++) acc[d]=acc[d]*a+p*__bfloat162float(qkv[k*3*D+2*D+h*DH+d]);
    m=mn;
  }
  for(int d=0;d<DH;d++) o[s*D+h*DH+d]=__float2bfloat16(acc[d]/l);
}
extern "C" void kernel_launch(void* const* d_in, const int* in_sizes, int n_in,
                              void* d_out, int out_size, void* d_ws, size_t ws_size,
                              hipStream_t stream) {
  bf16* ws=(bf16*)d_ws;
  for(int br=0;br<2;br++){
    const bf16* x=(const bf16*)d_in[br*3+0];
    const bf16* wqkv=(const bf16*)d_in[br*3+1];
    const bf16* wo=(const bf16*)d_in[br*3+2];
    bf16* qkv=ws+(size_t)br*(S*3*D+S*D);
    bf16* o=qkv+S*3*D;
    gemm_naive<<<dim3(3*D/16,S/16),256,0,stream>>>(x,wqkv,qkv,S,3*D,D,3*D,0);
    attn_naive<<<S*H/256,256,0,stream>>>(qkv,o,br==0?8:256,br==0?2:1);
    gemm_naive<<<dim3(D/16,S/16),256,0,stream>>>(o,wo,(bf16*)d_out,S,D,D,2*D,br*D);
  }
}
```

```cpp
#include <hip/hip_runtime.h>
#include <hip/hip_bf16.h>
#include <stdint.h>

typedef short bf16x8 __attribute__((ext_vector_type(8)));
typedef short s16x4 __attribute__((ext_vector_type(4)));
typedef float f32x2 __attribute__((ext_vector_type(2)));
typedef float f32x4 __attribute__((ext_vector_type(4)));
typedef float f32x16 __attribute__((ext_vector_type(16)));
typedef unsigned u32x4 __attribute__((ext_vector_type(4)));
typedef unsigned u32x2 __attribute__((ext_vector_type(2)));
typedef __bf16 bfx2 __attribute__((ext_vector_type(2)));
typedef unsigned long long u64;
#define LAS __attribute__((address_space(3)))
#define GAS __attribute__((address_space(1)))
#define DI __device__ __forceinline__
#define RLX_AGENT __ATOMIC_RELAXED, __HIP_MEMORY_SCOPE_AGENT
#define RLX_WG __ATOMIC_RELAXED, __HIP_MEMORY_SCOPE_WORKGROUP

constexpr int LDS_BYTES = 163840;
constexpr int VOFF = 20480, VP = 168;
constexpr int KVB = VOFF + 128 * VP;
constexpr int KV0 = LDS_BYTES - 3 * KVB;
constexpr int OWN = KV0 + 2 * KVB;
constexpr int C_F = 0, C_P = 256, C_E = 272, C_CNT = 288;
constexpr int XS0 = 512, WS0 = XS0 + 3 * 16384, QST = 512, ONE = 31744, WOB = 512, RED = WOB + 102400;
static_assert(WS0 + 7 * 10240 <= OWN, "ring overlaps the own image");
static_assert(ONE + 5120 + 8 <= KV0 && RED + 8 * 6144 <= LDS_BYTES, "LDS map");
constexpr unsigned WS_FKV = 4096, WS_FO = WS_FKV + 256 * 128, WS_KV = 131072;
constexpr unsigned WS_O = WS_KV + 256 * KVB;
constexpr unsigned WS_END = WS_O + 2 * 64 * 40 * 1024;
constexpr unsigned WS_DBG = WS_END;
constexpr unsigned SPIN_CAP = 400000, LSPIN_CAP = 4000000;
constexpr float QSCALE = 0.22360679774997896f * 1.4426950408889634f;
#define WIN 5
#define ADELAY 5

#define MFMA32(a, b, c) __builtin_amdgcn_mfma_f32_32x32x16_bf16((a), (b), (c), 0, 0, 0)
#define MFMA32K8(a, b, c) __builtin_amdgcn_mfma_f32_32x32x8bf16_1k((a), (b), (c), 0, 0, 0)
#define MFMA32I(acc, a, b) asm volatile("v_mfma_f32_32x32x16_bf16 %0, %1, %2, %0" : "+v"(acc) : "v"(a), "v"(b))
#define MFMA32Z(acc, a, b) asm volatile("v_mfma_f32_32x32x16_bf16 %0, %1, %2, 0" : "=&v"(acc) : "v"(a), "v"(b))
#define MFMA_DRAIN() asm volatile("s_nop 7\n\ts_nop 4" ::: "memory")
#define WAITV(N) asm volatile("s_waitcnt vmcnt(" #N ")" ::: "memory")
#define WAITL() asm volatile("s_waitcnt lgkmcnt(0)" ::: "memory")
#define BAR_L() asm volatile("s_waitcnt lgkmcnt(0)\n\ts_barrier" ::: "memory")
#define SB() __builtin_amdgcn_sched_barrier(0)
#define FENCE() asm volatile("" ::: "memory")

#define STAMP(i) do {} while (0)

template <int V> struct IC { static constexpr int value = V; };
template <int I, int N, class F> DI void static_for(F&& f) { if constexpr (I < N) { f(IC<I>{}); static_for<I + 1, N>(f); } }
#define SFOR(var, N) static_for<0, N>([&](auto ic_##var) __attribute__((always_inline)) { constexpr int var = decltype(ic_##var)::value;
#define SEND });

DI void dma16(unsigned voff, const char* sbase, unsigned ldsaddr) {
    asm volatile("s_mov_b32 m0, %2\n\ts_nop 0\n\tglobal_load_lds_dwordx4 %0, %1" :: "v"(voff), "s"(sbase), "s"(ldsaddr) : "memory");
}
DI unsigned pk2(float lo, float hi) { f32x2 v = {lo, hi}; return __builtin_bit_cast(unsigned, __builtin_convertvector(v, bfx2)); }
typedef short v4i16_t __attribute__((ext_vector_type(4)));
DI bf16x8 trfrag(LAS char* p, int second) {
    s16x4 a = __builtin_bit_cast(s16x4, __builtin_amdgcn_ds_read_tr16_b64_v4i16((LAS v4i16_t*)p));
    s16x4 b = __builtin_bit_cast(s16x4, __builtin_amdgcn_ds_read_tr16_b64_v4i16((LAS v4i16_t*)(p + second)));
    return __builtin_shufflevector(a, b, 0, 1, 2, 3, 4, 5, 6, 7);
}
DI float halfmax(float m) { auto rr = __builtin_amdgcn_permlane32_swap(__float_as_uint(m), __float_as_uint(m), false, false);
    return __builtin_fmaxf(__uint_as_float(rr[0]), __uint_as_float(rr[1])); }

DI void poll_flags(GAS u64* word, bool active, u64 tag) {
#pragma unroll 1
    for (unsigned spins = 0; spins < SPIN_CAP; ++spins) {
        u64 v = tag;
        if (active) v = __hip_atomic_load(word, RLX_AGENT);
        if (__all(v == tag)) break;
        __builtin_amdgcn_s_sleep(1);
    }
    __builtin_amdgcn_fence(__ATOMIC_ACQUIRE, "wavefront");
}
DI int min4(LAS char* p) {
    const u64 a = __hip_atomic_load((LAS u64*)p, RLX_WG), b = __hip_atomic_load((LAS u64*)(p + 8), RLX_WG);
    const unsigned m = min(min((unsigned)a, (unsigned)(a >> 32)), min((unsigned)b, (unsigned)(b >> 32)));
    return __builtin_amdgcn_readfirstlane((int)m);
}
DI int wait_min4(LAS char* p, int need) {
    int v = min4(p);
#pragma unroll 1
    for (unsigned spins = 0; v < need && spins < LSPIN_CAP; ++spins) { __builtin_amdgcn_s_sleep(1); v = min4(p); }
    FENCE();
    return v;
}
DI void lds_set(LAS char* p, unsigned v) { FENCE(); __hip_atomic_store((LAS unsigned*)p, v, RLX_WG); FENCE(); }

__global__ __launch_bounds__(512, 2) void fused_attn_kernel(const char* __restrict__ xA, const char* __restrict__ wA, const char* __restrict__ oA,
                                                           const char* __restrict__ xB, const char* __restrict__ wB, const char* __restrict__ oB,
                                                           char* __restrict__ out, char* __restrict__ ws) {
    __shared__ __attribute__((aligned(16))) char smem_[LDS_BYTES];
    LAS char* smem = (LAS char*)smem_;
    const unsigned lds0 = (unsigned)(uintptr_t)smem;
    STAMP(0);
    const int tid = threadIdx.x, lane = tid & 63;
    const int wid = __builtin_amdgcn_readfirstlane(tid >> 6);
    const int bx = blockIdx.x, xcd = bx & 7, idx = bx >> 3;
    const int isA = idx >> 4, rt = (xcd >> 1) * 4 + ((idx >> 2) & 3), hg = (xcd & 1) * 4 + (idx & 3);
    const int br = isA ? 0 : 1;
    const int k0 = (rt * 2 + ((hg & 3) * 5 >> 1) + (hg & 1)) % 10;
    const char* xb = (isA ? xA : xB) + (size_t)rt * (128 * 1280);
    const char* wb = (isA ? wA : wB) + hg * 160;
    const int me = (br * 16 + rt) * 8 + hg;
    const int r32 = lane & 31, hl = lane >> 5;
    auto kt_of = [&](int s) { int k = s + k0; return k >= 10 ? k - 10 : k; };
    if (ADELAY && isA) { for (int i = 0; i < ADELAY; ++i) __builtin_amdgcn_s_sleep(16); }

    const unsigned xo = (unsigned)((lane >> 3) * 1280 + (((lane & 7) ^ (lane >> 4)) << 4));
    auto w_off = [&](int q) { const int c = q * 64 + lane, row = c / 20, j = c % 20; return (unsigned)(row * 3840 + (j < 10 ? 1280 + j * 16 : 2400 + j * 16)); };
    { const char* xs = xb + k0 * 128 + wid * (16 * 1280);
      dma16(xo, xs, lds0 + XS0 + wid * 2048);
      dma16(xo ^ 64, xs + 8 * 1280, lds0 + XS0 + wid * 2048 + 1024);
      const char* wsrc = wb + (size_t)k0 * (64 * 3840);
      const unsigned wv = w_off(wid < 5 ? wid : wid - 5);
      if (wid < 5) { dma16(wv, wsrc, lds0 + WS0 + wid * 1024); dma16(wv, wsrc + 16 * 3840, lds0 + WS0 + (5 + wid) * 1024);
          if (wid >= 3) { dma16(wv, wsrc + 32 * 3840, lds0 + WS0 + (10 + wid) * 1024); dma16(wv, wsrc + 48 * 3840, lds0 + WS0 + (15 + wid) * 1024); } }
      else { dma16(wv, wsrc + 32 * 3840, lds0 + WS0 + (5 + wid) * 1024); dma16(wv, wsrc + 48 * 3840, lds0 + WS0 + (10 + wid) * 1024); } }
    if (tid < 30) *(LAS u32x2*)(smem + C_F + tid * 8) = (u32x2){tid == 1 ? 1u : 0u, (tid & 1) || tid >= 20 ? 3u : 0u};
    else if (tid >= 64 && tid < 80) *(LAS unsigned*)(smem + C_P + (tid - 64) * 4) = 0u;
    u64 epoch;
    asm volatile("s_load_dwordx2 %0, %1, 0x0\n\ts_waitcnt lgkmcnt(0)" : "=s"(epoch) : "s"(ws) : "memory");
    const u64 tag = epoch * 6364136223846793005ull + 1442695040888963407ull;
    const int nnb = isA ? (rt > 0 ? 1 : 0) : (rt > 1 ? 2 : rt);
    auto kq_base = [&](int fg) { const int hd = fg / 5, dg = fg % 5; return dg < 4 ? hd * 5120 + (dg >> 1) * 2048 + (dg & 1) * 8 : hd * 5120 + 4096; };

    if (wid < 4) {
        asm volatile("s_waitcnt vmcnt(0) lgkmcnt(0)\n\ts_barrier" ::: "memory");
        STAMP(1);
        bf16x8 xr[28], xt[2], wf[WIN + 1];
        f32x16 acc[5];
        const int tok = wid * 32 + r32;
        const int xa = XS0 + tok * 128 + ((hl ^ ((tok >> 1) & 7)) << 4);
        int wa0 = WS0 + (8 * hl + ((lane & 15) >> 2)) * 320 + (lane & 16) * 2 + (lane & 3) * 8, wa1 = wa0 + 61440;
        asm volatile("" : "+v"(wa0), "+v"(wa1));
        auto rdx = [&](int i) { return *(LAS bf16x8*)(smem + ((i >> 2) % 3) * 16384 + (xa ^ ((i & 3) * 32))); };
        auto rdw = [&](int f) { const int i = f / 5, t = f % 5, sl = (i >> 1) % 7;
            return trfrag(smem + (sl < 6 ? wa0 + sl * 10240 : wa1) + (i & 1) * 5120 + t * 64, 1280); };
        u64 fl = 0;
        auto rdflag = [&](int u) { fl = __hip_atomic_load((LAS u64*)(smem + C_F + u * 8), RLX_WG); };
        auto check = [&](int u) {
            if (!__builtin_amdgcn_readfirstlane((unsigned)fl & (((unsigned)(fl >> 32) + 1u) >> 2))) {
#pragma unroll 1
                for (unsigned spins = 0; spins < LSPIN_CAP; ++spins) { rdflag(u); if (__builtin_amdgcn_readfirstlane((unsigned)fl & (((unsigned)(fl >> 32) + 1u) >> 2))) break; }
            }
            FENCE(); };
        xr[0] = rdx(0);
#pragma unroll
        for (int f = 0; f < WIN; ++f) wf[f] = rdw(f);
        SB();
        SFOR(f, 200)
            constexpr int i = f / 5, t = f % 5;
            if constexpr (f % 10 == 0) rdflag((i >> 1) + 1);
            if constexpr (f % 10 == 10 - WIN) { lds_set(smem + C_P + wid * 4, i >> 1); if constexpr (f < 190) check((i >> 1) + 1); }
            if constexpr (f + WIN < 200) wf[(f + WIN) % (WIN + 1)] = rdw(f + WIN);
            if constexpr (t == 1 && i < 39) { if constexpr (i + 1 < 28) xr[i + 1] = rdx(i + 1); else xt[(i + 1) & 1] = rdx(i + 1); }
            SB();
            if constexpr (i == 0) MFMA32Z(acc[t], wf[f % (WIN + 1)], xr[0]);
            else if constexpr (i < 28) MFMA32I(acc[t], wf[f % (WIN + 1)], xr[i]);
            else MFMA32I(acc[t], wf[f % (WIN + 1)], xt[i & 1]);
            SB();
        SEND
        lds_set(smem + C_P + wid * 4, 20);
        MFMA_DRAIN();
        STAMP(2);
#pragma unroll
        for (int t = 0; t < 5; ++t)
#pragma unroll
            for (int b = 0; b < 4; ++b) {
                const int g0 = 8 * t + 2 * b, g1 = g0 + 1;
                const int o0 = g0 < 20 ? kq_base(g0) + tok * (g0 % 5 < 4 ? 16 : 8) : VOFF + (g0 - 20) * 8 + tok * VP;
                const int o1 = g1 < 20 ? kq_base(g1) + tok * (g1 % 5 < 4 ? 16 : 8) : VOFF + (g1 - 20) * 8 + tok * VP;
                *(LAS u32x2*)(smem + OWN + (hl ? o1 : o0)) = (u32x2){pk2(acc[t][4 * b], acc[t][4 * b + 1]), pk2(acc[t][4 * b + 2], acc[t][4 * b + 3])};
            }
        if (hl == 0) *(LAS u32x2*)(smem + OWN + VOFF + tok * VP + 160) = (u32x2){0x3F803F80u, 0x3F803F80u};
        WAITL();
        lds_set(smem + C_E + wid * 4, 1);
        STAMP(3);
        int l2 = tid; asm volatile("" : "+v"(l2)); l2 &= 63;
        int qa0 = (8 * (l2 >> 5) + 2 * ((l2 & 15) >> 2)) * 160 + (l2 & 16) * 2 + (l2 & 3) * 8, qa1 = qa0 + WS0 + 10240;
        asm volatile("" : "+v"(qa0), "+v"(qa1));
        auto rdq = [&](int f) { const int i = f / 3, t = f % 3, j = i >> 2, sl = (20 + j) % 7;
            return trfrag(smem + (j >= 7 ? qa0 + XS0 + (j - 7) * 10240 : sl == 0 ? qa0 + WS0 : qa1 + (sl - 1) * 10240) + (i & 3) * 2560 + t * 64, 160); };
        check(20);
        xt[0] = rdx(28);
#pragma unroll
        for (int f = 0; f < WIN; ++f) wf[f] = rdq(f);
        SB();
        SFOR(f, 120)
            constexpr int i = f / 3, t = f % 3, j = i >> 2;
            if constexpr (f % 12 == 0 && j < 9) rdflag(21 + j);
            if constexpr (f % 12 == 12 - WIN) { lds_set(smem + C_P + wid * 4, 20 + j); if constexpr (j < 9) check(21 + j); }
            if constexpr (f + WIN < 120) wf[(f + WIN) % (WIN + 1)] = rdq(f + WIN);
            if constexpr (t == 0 && i < 11) xt[(i + 1) & 1] = rdx(28 + i + 1);
            SB();
            if constexpr (i == 0) MFMA32Z(acc[t], wf[f % (WIN + 1)], xt[0]);
            else if constexpr (i < 12) MFMA32I(acc[t], wf[f % (WIN + 1)], xt[i & 1]);
            else MFMA32I(acc[t], wf[f % (WIN + 1)], xr[i - 12]);
            SB();
        SEND
        MFMA_DRAIN();
        STAMP(4);
        wait_min4(smem + C_P, 29);
#pragma unroll
        for (int t = 0; t < 3; ++t)
#pragma unroll
            for (int b = 0; b < 4; ++b)
                if (t < 2 || b < 2) {
                    const int g0 = 8 * t + 2 * b, g1 = g0 + 1;
                    const int o0 = kq_base(g0) + tok * (g0 % 5 < 4 ? 16 : 8), o1 = kq_base(g1) + tok * (g1 % 5 < 4 ? 16 : 8);
                    *(LAS u32x2*)(smem + QST + (hl ? o1 : o0)) =
                        (u32x2){pk2(acc[t][4 * b] * QSCALE, acc[t][4 * b + 1] * QSCALE), pk2(acc[t][4 * b + 2] * QSCALE, acc[t][4 * b + 3] * QSCALE)};
                }
        STAMP(5);
    } else {
        const int hw = wid - 4;
        int known = 0;
        auto lds_or = [&](int off, unsigned v) { FENCE(); __hip_atomic_fetch_or((LAS unsigned*)(smem + off), v, RLX_WG); FENCE(); };
        if (hw == 0 || hw == 3) {
            const int half = hw ? 1 : 0;
#pragma unroll 1
            for (int j = 1; j < 10; ++j) {
                if (2 * j - 4 > known) { WAITV(0); lds_or(C_F + (j - 1) * 16 + 4, 1u << half); known = wait_min4(smem + C_P, 2 * j - 4); }
                const char* xs = xb + kt_of(j) * 128 + half * (64 * 1280); const unsigned dst = lds0 + XS0 + (j % 3) * 16384 + half * 8192;
#pragma unroll
                for (int i = 0; i < 8; ++i) dma16(i & 1 ? xo ^ 64 : xo, xs + i * (8 * 1280), dst + i * 1024);
                WAITV(8);
                if (j == 1) BAR_L(); else lds_or(C_F + (j - 1) * 16 + 4, 1u << half);
            }
            WAITV(0); lds_or(C_F + 9 * 16 + 4, 1u << half);
        } else {
            unsigned wo[5], qo[5];
#pragma unroll
            for (int q = 0; q < 5; ++q) { wo[q] = w_off(q);
                const int c = q * 64 + lane, rho = c / 10; qo[q] = (unsigned)(((rho & ~7) + 4 * (rho & 1) + ((rho >> 1) & 3)) * 3840 + (c % 10) * 16); }
#pragma unroll 1
            for (int u = hw + 1; u < 30; u += 2) {
                const int need = u < 27 ? u - 6 : 23;
                if (need > known) { WAITV(0); lds_set(smem + C_F + (u - 2) * 8, 1); known = wait_min4(smem + C_P, need); }
                const unsigned dst = lds0 + (u < 27 ? WS0 + (u % 7) * 10240 : XS0 + (u - 27) * 10240);
                const char* src = u < 20 ? wb + (size_t)(kt_of(u >> 1) * 64 + (u & 1) * 32) * 3840 : wb + (size_t)kt_of(u < 23 ? u - 13 : u - 23) * (64 * 3840);
                if (u < 20) {
#pragma unroll
                    for (int i = 0; i < 5; ++i) dma16(wo[i], src, dst + i * 1024);
                } else {
#pragma unroll
                    for (int i = 0; i < 5; ++i) dma16(qo[i], src, dst + i * 1024);
                }
                WAITV(5);
                if (u == hw + 1) BAR_L(); else lds_set(smem + C_F + (u - 2) * 8, 1);
                if (u < 20) {
#pragma unroll
                    for (int i = 0; i < 5; ++i) dma16(wo[i], src + 16 * 3840, dst + (5 + i) * 1024);
                } else {
#pragma unroll
                    for (int i = 0; i < 5; ++i) dma16(qo[i], src + 32 * 3840, dst + (5 + i) * 1024);
                }
            }
            WAITV(0); lds_set(smem + C_F + (27 + hw) * 8, 1);
        }
        STAMP(1);
        auto a_chunk = [&](int a) { int o;
            if (a < 320) { const int hd = a / 80, rem = a % 80; o = hd * 5120 + (rem < 32 ? (96 + rem) * 16 : rem < 64 ? 2048 + (64 + rem) * 16 : 4096 + 768 + (rem - 64) * 16); }
            else o = VOFF + 96 * VP + (a - 320) * 16;
            return o; };
        if ((hw == 0 || hw == 3) && rt < 15) {
            const __amdgpu_buffer_rsrc_t kvr = __builtin_amdgcn_make_buffer_rsrc(ws + WS_KV + me * KVB, 0, KVB, 0x00020000);
            const int half = hw ? 1 : 0;
            wait_min4(smem + C_E, 1);
            STAMP(2);
            if (!isA) { u32x4 v[21];
#pragma unroll
                for (int j = 0; j < 21; ++j) if (j < 20 || half) v[j] = *(LAS u32x4*)(smem + OWN + half * 20480 + j * 1024 + lane * 16);
#pragma unroll
                for (int j = 0; j < 21; ++j) if (j < 20 || half) __builtin_amdgcn_raw_buffer_store_b128(v[j], kvr, half * 20480 + j * 1024 + lane * 16, 0, 16);
            } else {
#pragma unroll
                for (int j = 0; j < 6; ++j) { const int a = (2 * j + half) * 64 + lane;
                    if (a < 656) { const int o = a_chunk(a); __builtin_amdgcn_raw_buffer_store_b128(*(LAS u32x4*)(smem + OWN + o), kvr, o, 0, 16); } }
            }
            STAMP(3);
            WAITV(0);
            unsigned old = 0;
            if (lane == 0) old = __hip_atomic_fetch_add((LAS unsigned*)(smem + C_CNT), 1u, RLX_WG);
            if (lane == 0 && old == 1) __hip_atomic_store((GAS u64*)(ws + WS_FKV + me * 128), tag, RLX_AGENT);
            STAMP(4);
        }
        poll_flags((GAS u64*)(ws + WS_FKV + (me - 8 * (lane + 1)) * 128), lane < nnb, tag);
        STAMP(5);
        if (!isA) {
            const int n = hw >> 1, half = hw & 1;
            if (n < nnb) {
                const __amdgpu_buffer_rsrc_t nbr = __builtin_amdgcn_make_buffer_rsrc(ws + WS_KV + (me - 8 * (n + 1)) * KVB, 0, KVB, 0x00020000);
                u32x4 v[21];
#pragma unroll
                for (int j = 0; j < 21; ++j) if (j < 20 || half) v[j] = __builtin_amdgcn_raw_buffer_load_b128(nbr, half * 20480 + j * 1024 + lane * 16, 0, 16);
                wait_min4(smem + C_P, 27);
#pragma unroll
                for (int j = 0; j < 21; ++j) if (j < 20 || half) *(LAS u32x4*)(smem + KV0 + (1 - n) * KVB + half * 20480 + j * 1024 + lane * 16) = v[j];
            }
        } else if (nnb) {
            const __amdgpu_buffer_rsrc_t nbr = __builtin_amdgcn_make_buffer_rsrc(ws + WS_KV + (me - 8) * KVB, 0, KVB, 0x00020000);
            u32x4 v[3];
#pragma unroll
            for (int j = 0; j < 3; ++j) { const int a = (4 * j + hw) * 64 + lane; if (a < 656) v[j] = __builtin_amdgcn_raw_buffer_load_b128(nbr, a_chunk(a), 0, 16); }
            wait_min4(smem + C_P, 27);
#pragma unroll
            for (int j = 0; j < 3; ++j) { const int a = (4 * j + hw) * 64 + lane; if (a < 656) *(LAS u32x4*)(smem + KV0 + KVB + a_chunk(a)) = v[j]; }
        }
        if (hw == 3) { wait_min4(smem + C_P, 27); if (lane < 2) *(LAS u32x2*)(smem + ONE + lane * 5120) = (u32x2){0x3F803F80u, 0x3F803F80u}; }
        STAMP(6);
    }
    const int qi = wid & 3, hp = wid >> 2;
    const int ud = qi + 8, uf = isA ? qi + 7 : qi;
    const bool far = isA ? (4 * rt + qi >= 1) : (4 * rt + qi >= 8);
    const int ulo = isA ? ud : (far ? qi + 1 : 8 - 4 * rt);
    f32x16 cd, cf; unsigned md[8];
    { const unsigned low = (2u << r32) - 1u;
      unsigned vd = low, vf = ~low;
      if (isA) { const u64 w = 0x5555ull << (r32 + 18);
          vd = (unsigned)(w >> 32); vf = (unsigned)w; }
      const unsigned id = ~vd >> (4 * hl), jf = ~vf >> (4 * hl);
#pragma unroll
      for (int r = 0; r < 16; r += 2) {
          const int c0 = (r & 3) + 8 * (r >> 2);
          const unsigned a0 = (unsigned)__builtin_amdgcn_sbfe((int)id, c0, 1), a1 = (unsigned)__builtin_amdgcn_sbfe((int)id, c0 + 1, 1);
          cd[r] = __uint_as_float(a0 & 0xFF800000u); cd[r + 1] = __uint_as_float(a1 & 0xFF800000u);
          cf[r] = __uint_as_float((unsigned)__builtin_amdgcn_sbfe((int)jf, c0, 1) & 0xFF800000u);
          cf[r + 1] = __uint_as_float((unsigned)__builtin_amdgcn_sbfe((int)jf, c0 + 1, 1) & 0xFF800000u);
          md[r >> 1] = __builtin_amdgcn_perm(a1, a0, 0x07060100u);
      } }
    unsigned oo[5];
#pragma unroll
    for (int q = 0; q < 5; ++q) { const int c = q * 64 + lane, rho = c / 10; oo[q] = (unsigned)(((rho & ~7) + 4 * (rho & 1) + ((rho >> 1) & 3)) * 1280 + (c % 10) * 16); }
    BAR_L();
    STAMP(7);
    u32x2 ob[2][3];
    {
        bf16x8 q1[2]; u32x2 q2[2];
#pragma unroll
        for (int e = 0; e < 2; ++e) {
            q1[e] = *(LAS bf16x8*)(smem + QST + (2 * hp + e) * 5120 + hl * 2048 + (qi * 32 + r32) * 16);
            q2[e] = *(LAS u32x2*)(smem + QST + (2 * hp + e) * 5120 + 4096 + (qi * 32 + r32) * 8);
            if (hl) q2[e] = (u32x2){0u, 0u};
        }
        const int kb1 = KV0 + hp * 10240 + hl * 2048 + r32 * 16;
        const int kb2 = hl ? ONE : KV0 + hp * 10240 + 4096 + r32 * 8, nh = hl ^ 1;
        const int vrow = KV0 + VOFF + (4 * hl + ((lane & 15) >> 2)) * VP;
        const bool ones = (lane & 16) && (lane & 3);
        const int vb0 = vrow + (ones ? 160 : hp * 80 + (lane & 16) * 2 + (lane & 3) * 8), vb1 = vb0 + (ones ? 0 : 40);
        struct KF { bf16x8 a; s16x4 b; };
        struct VF { bf16x8 a, b; };
        auto rdk = [&](int u, int e) { const int so = (u >> 2) * KVB, tw = u & 3; KF k;
            k.a = *(LAS bf16x8*)(smem + kb1 + so + tw * 512 + e * 5120); k.b = *(LAS s16x4*)(smem + kb2 + (so + tw * 256) * nh + e * 5120); return k; };
        auto rdv = [&](int u, int e) { LAS char* p = smem + (e ? vb1 : vb0) + (u >> 2) * KVB + (u & 3) * (32 * VP); VF v;
            v.a = trfrag(p, 8 * VP); v.b = trfrag(p + 16 * VP, 8 * VP); return v; };
        auto qk = [&](const KF& k, int e, const f32x16& c) { return MFMA32K8(k.b, __builtin_bit_cast(s16x4, q2[e]), MFMA32(k.a, q1[e], c)); };
        const f32x16 zero = {};
        auto merged = [&](int e) {
            f32x16 sm = qk(rdk(ud, e), e, cd);
            if (far) { const f32x16 sf = qk(rdk(uf, e), e, cf);
#pragma unroll
                for (int r = 0; r < 16; ++r) sm[r] = __builtin_fmaxf(sm[r], sf[r]); }
            return sm; };
        auto max16 = [&](const f32x16& s, float m) {
#pragma unroll
            for (int r = 0; r < 16; ++r) m = __builtin_fmaxf(m, s[r]);
            return m; };
        auto set_ref = [&](float m, int e) { const unsigned nb = pk2(-m, 0.f); if (hl) q2[e][0] = nb; return __uint_as_float(nb << 16); };
        auto boundary_pv = [&](const f32x16& sm, float nm, int e) {
            u32x4 pd[2], pf[2];
#pragma unroll
            for (int r = 0; r < 16; r += 2) {
                const unsigned p = pk2(__builtin_amdgcn_exp2f(sm[r] + nm), __builtin_amdgcn_exp2f(sm[r + 1] + nm));
                pd[r >> 3][(r >> 1) & 3] = p & ~md[r >> 1]; pf[r >> 3][(r >> 1) & 3] = p & md[r >> 1]; }
            VF v = rdv(ud, e);
            f32x16 o = MFMA32(v.b, __builtin_bit_cast(bf16x8, pd[1]), MFMA32(v.a, __builtin_bit_cast(bf16x8, pd[0]), zero));
            if (far) { v = rdv(uf, e); o = MFMA32(v.b, __builtin_bit_cast(bf16x8, pf[1]), MFMA32(v.a, __builtin_bit_cast(bf16x8, pf[0]), o)); }
            return o; };
        auto exp8 = [&](const f32x16& s, int b) { u32x4 p;
#pragma unroll
            for (int r = 0; r < 4; ++r) p[r] = pk2(__builtin_amdgcn_exp2f(s[b + 2 * r]), __builtin_amdgcn_exp2f(s[b + 2 * r + 1]));
            return __builtin_bit_cast(bf16x8, p); };
        f32x16 o0, o1;
        { const f32x16 s0 = merged(0), s1 = merged(1);
          const float n0 = set_ref(halfmax(max16(s0, s0[0])), 0), n1 = set_ref(halfmax(max16(s1, s1[0])), 1);
          f32x16 sa, sb; KF kx; VF vx;
          if (ulo < ud) { sa = qk(rdk(ulo, 0), 0, zero); kx = rdk(ulo, 1); vx = rdv(ulo, 0); }
          o0 = boundary_pv(s0, n0, 0); o1 = boundary_pv(s1, n1, 1);
#pragma unroll 1
          for (int u = ulo; u < ud; ++u) {
              SB();
              sb = qk(kx, 1, zero);
              kx = rdk(u + 1, 0);
              VF vy = rdv(u, 1);
              SB();
              { const bf16x8 p = exp8(sa, 0); o0 = MFMA32(vx.a, p, o0); }
              SB();
              { const bf16x8 p = exp8(sa, 8); o0 = MFMA32(vx.b, p, o0); }
              SB();
              sa = qk(kx, 0, zero);
              kx = rdk(u + 1, 1);
              vx = rdv(u + 1, 0);
              SB();
              { const bf16x8 p = exp8(sb, 0); o1 = MFMA32(vy.a, p, o1); }
              SB();
              { const bf16x8 p = exp8(sb, 8); o1 = MFMA32(vy.b, p, o1); }
              SB();
          } }
        if (!__all(o0[12] < 1.152921504606847e18f && o1[12] < 1.152921504606847e18f)) {
#pragma unroll
            for (int e = 0; e < 2; ++e) {
                if (hl) q2[e][0] = 0u;
                const f32x16 s = merged(e);
                float m = max16(s, s[0]);
#pragma unroll 1
                for (int u = ulo; u < ud; ++u) m = max16(qk(rdk(u, e), e, zero), m);
                const float n = set_ref(halfmax(m), e);
                f32x16 o = boundary_pv(s, n, e);
#pragma unroll 1
                for (int u = ulo; u < ud; ++u) { const f32x16 t = qk(rdk(u, e), e, zero); const VF v = rdv(u, e);
                    o = MFMA32(v.a, exp8(t, 0), o); o = MFMA32(v.b, exp8(t, 8), o); }
                if (e) o1 = o; else o0 = o;
            }
        }
        const float rl0 = __builtin_amdgcn_rcpf(o0[12]), rl1 = __builtin_amdgcn_rcpf(o1[12]);
#pragma unroll
        for (int j = 0; j < 3; ++j) {
            ob[0][j] = (u32x2){pk2(o0[4 * j] * rl0, o0[4 * j + 1] * rl0), pk2(o0[4 * j + 2] * rl0, o0[4 * j + 3] * rl0)};
            ob[1][j] = (u32x2){pk2(o1[4 * j] * rl1, o1[4 * j + 1] * rl1), pk2(o1[4 * j + 2] * rl1, o1[4 * j + 3] * rl1)}; }
    }
    STAMP(8);
    const __amdgpu_buffer_rsrc_t orr = __builtin_amdgcn_make_buffer_rsrc(ws + WS_O + (br * 64 + rt * 4) * 40960, 0, 4 * 40960, 0x00020000);
    { const unsigned obase = (unsigned)(qi * 40960 + (hg * 10 + hp * 5) * 512 + r32 * 16);
      { auto rx = __builtin_amdgcn_permlane32_swap(ob[0][0][0], ob[0][1][0], false, false);
        auto ry = __builtin_amdgcn_permlane32_swap(ob[0][0][1], ob[0][1][1], false, false);
        __builtin_amdgcn_raw_buffer_store_b128((u32x4){rx[0], ry[0], rx[1], ry[1]}, orr, obase + hl * 512, 0, 16); }
      if (hl == 0) __builtin_amdgcn_raw_buffer_store_b128((u32x4){ob[0][2][0], ob[0][2][1], ob[1][0][0], ob[1][0][1]}, orr, obase + 1024, 0, 16);
      { auto fx = __builtin_amdgcn_permlane32_swap(ob[1][0][0], ob[1][1][0], false, false);
        auto fy = __builtin_amdgcn_permlane32_swap(ob[1][0][1], ob[1][1][1], false, false);
        auto sx = __builtin_amdgcn_permlane32_swap(ob[1][1][0], ob[1][2][0], false, false);
        auto sy = __builtin_amdgcn_permlane32_swap(ob[1][1][1], ob[1][2][1], false, false);
        __builtin_amdgcn_raw_buffer_store_b128((u32x4){fx[1], fy[1], sx[0], sy[0]}, orr, obase + 1536 + hl * 512, 0, 16); } }
    BAR_L();
    { const char* wsrc = (isA ? oA : oB) + hg * 160 + wid * (80 * 1280); const unsigned dst = lds0 + WOB + wid * 12800;
#pragma unroll
      for (int i = 0; i < 4; ++i) dma16(oo[i], wsrc, dst + i * 1024);
      WAITV(4);
      unsigned old = 0;
      if (lane == 0) old = __hip_atomic_fetch_add((LAS unsigned*)(smem + C_CNT + 4), 1u, RLX_WG);
      if (lane == 0 && old == 7) __hip_atomic_store((GAS u64*)(ws + WS_FO + me * 128), tag, RLX_AGENT);
      STAMP(9);
#pragma unroll
      for (int i = 4; i < 12; ++i) dma16(oo[i % 5], wsrc + (i / 5) * (32 * 1280), dst + i * 1024);
      if (lane < 32) dma16(oo[2], wsrc + 64 * 1280, dst + 12 * 1024); }
    poll_flags((GAS u64*)(ws + WS_FO + ((me & ~7) + (lane & 7)) * 128), lane < 8, tag);
    STAMP(10);
    bf16x8 of[20];
#pragma unroll
    for (int s = 0; s < 20; ++s)
        of[s] = __builtin_bit_cast(bf16x8, __builtin_amdgcn_raw_buffer_load_b128(orr, qi * 40960 + (hp * 20 + s) * 1024 + lane * 16, 0, 16));
    asm volatile("s_waitcnt vmcnt(20)\n\ts_barrier" ::: "memory");
    STAMP(11);
    f32x16 oc[3];
    { bf16x8 af[5];
      const int aa = WOB + hp * 51200 + (8 * hl + 2 * ((lane & 15) >> 2)) * 160 + (lane & 16) * 2 + (lane & 3) * 8;
      auto rda = [&](int f) { return trfrag(smem + aa + (f / 3) * 2560 + (f % 3) * 64, 160); };
#pragma unroll
      for (int f = 0; f < 4; ++f) af[f] = rda(f);
      SB();
#pragma unroll
      for (int f = 0; f < 60; ++f) {
          if (f + 4 < 60) af[(f + 4) % 5] = rda(f + 4);
          SB();
          if (f < 3) MFMA32Z(oc[f], af[f % 5], of[0]); else MFMA32I(oc[f % 3], af[f % 5], of[f / 3]);
          SB();
      } }
    MFMA_DRAIN();
    STAMP(12);
    { LAS char* mine = smem + RED + wid * 6144 + lane * 16; LAS char* other = smem + RED + (wid ^ 4) * 6144 + lane * 16;
      if (hp) {
#pragma unroll
          for (int b = 0; b < 4; ++b) *(LAS f32x4*)(mine + b * 1024) = (f32x4){oc[0][4 * b], oc[0][4 * b + 1], oc[0][4 * b + 2], oc[0][4 * b + 3]};
#pragma unroll
          for (int b = 0; b < 2; ++b) *(LAS f32x4*)(mine + (4 + b) * 1024) = (f32x4){oc[2][4 * b], oc[2][4 * b + 1], oc[2][4 * b + 2], oc[2][4 * b + 3]};
      } else {
#pragma unroll
          for (int b = 0; b < 4; ++b) *(LAS f32x4*)(mine + b * 1024) = (f32x4){oc[1][4 * b], oc[1][4 * b + 1], oc[1][4 * b + 2], oc[1][4 * b + 3]};
      }
      BAR_L();
      char* orow = out + (size_t)(rt * 128 + qi * 32 + r32) * 2560 + br * 1280 + hg * 160 + hl * 16;
      auto fin = [&](const f32x16& c, int b, int slot) { const f32x4 x = *(LAS f32x4*)(other + slot * 1024);
          return (u32x2){pk2(c[4 * b] + x[0], c[4 * b + 1] + x[1]), pk2(c[4 * b + 2] + x[2], c[4 * b + 3] + x[3])}; };
      auto st2 = [&](u32x2 a, u32x2 b, int col) {
          auto rx = __builtin_amdgcn_permlane32_swap(a[0], b[0], false, false); auto ry = __builtin_amdgcn_permlane32_swap(a[1], b[1], false, false);
          *(u32x4*)(orow + col * 2) = (u32x4){rx[0], ry[0], rx[1], ry[1]}; };
      if (hp) { st2(fin(oc[1], 0, 0), fin(oc[1], 1, 1), 32); st2(fin(oc[1], 2, 2), fin(oc[1], 3, 3), 48); }
      else { st2(fin(oc[0], 0, 0), fin(oc[0], 1, 1), 0); st2(fin(oc[0], 2, 2), fin(oc[0], 3, 3), 16); st2(fin(oc[2], 0, 4), fin(oc[2], 1, 5), 64); } }
    if (bx == 0) {
        if (wid < 4) poll_flags((GAS u64*)(ws + WS_FO + tid * 128), true, tag);
        __syncthreads();
        if (tid == 0) __hip_atomic_store((GAS u64*)ws, tag, RLX_AGENT);
    }
}

extern "C" void kernel_launch(void* const* d_in, const int* in_sizes, int n_in, void* d_out, int out_size, void* d_ws, size_t ws_size,
                              hipStream_t stream) {
    fused_attn_kernel<<<256, 512, 0, stream>>>((const char*)d_in[0], (const char*)d_in[1], (const char*)d_in[2], (const char*)d_in[3],
                                               (const char*)d_in[4], (const char*)d_in[5], (char*)d_out, (char*)d_ws);
}
```

```cpp
#include <hip/hip_runtime.h>
#include <hip/hip_bf16.h>
#include <stdint.h>
#include <stdio.h>
#define STAMPS 1

typedef short bf16x8 __attribute__((ext_vector_type(8)));
typedef short s16x4 __attribute__((ext_vector_type(4)));
typedef float f32x2 __attribute__((ext_vector_type(2)));
typedef float f32x4 __attribute__((ext_vector_type(4)));
typedef float f32x16 __attribute__((ext_vector_type(16)));
typedef unsigned u32x4 __attribute__((ext_vector_type(4)));
typedef unsigned u32x2 __attribute__((ext_vector_type(2)));
typedef __bf16 bfx2 __attribute__((ext_vector_type(2)));
typedef unsigned long long u64;
#define LAS __attribute__((address_space(3)))
#define GAS __attribute__((address_space(1)))
#define DI __device__ __forceinline__
#define RLX_AGENT __ATOMIC_RELAXED, __HIP_MEMORY_SCOPE_AGENT
#define RLX_WG __ATOMIC_RELAXED, __HIP_MEMORY_SCOPE_WORKGROUP

constexpr int LDS_BYTES = 163840;
constexpr int VOFF = 20480, VP = 168;
constexpr int KVB = VOFF + 128 * VP;
constexpr int KV0 = LDS_BYTES - 3 * KVB;
constexpr int OWN = KV0 + 2 * KVB;
constexpr int C_F = 0, C_P = 256, C_E = 272, C_CNT = 288;
constexpr int XS0 = 512, WS0 = XS0 + 3 * 16384, QST = 512, ONE = 31744, WOB = 512, RED = WOB + 102400;
static_assert(WS0 + 7 * 10240 <= OWN, "ring overlaps the own image");
static_assert(ONE + 5120 + 8 <= KV0 && RED + 8 * 6144 <= LDS_BYTES, "LDS map");
constexpr unsigned WS_FKV = 4096, WS_FO = WS_FKV + 256 * 128, WS_KV = 131072;
constexpr unsigned WS_O = WS_KV + 256 * KVB;
constexpr unsigned WS_END = WS_O + 2 * 64 * 40 * 1024;
constexpr unsigned WS_DBG = WS_END;
constexpr unsigned SPIN_CAP = 400000, LSPIN_CAP = 4000000;
constexpr float QSCALE = 0.22360679774997896f * 1.4426950408889634f;
#define WIN 5
#define ADELAY 0

#define MFMA32(a, b, c) __builtin_amdgcn_mfma_f32_32x32x16_bf16((a), (b), (c), 0, 0, 0)
#define MFMA32K8(a, b, c) __builtin_amdgcn_mfma_f32_32x32x8bf16_1k((a), (b), (c), 0, 0, 0)
#define MFMA32I(acc, a, b) asm volatile("v_mfma_f32_32x32x16_bf16 %0, %1, %2, %0" : "+v"(acc) : "v"(a), "v"(b))
#define MFMA32Z(acc, a, b) asm volatile("v_mfma_f32_32x32x16_bf16 %0, %1, %2, 0" : "=&v"(acc) : "v"(a), "v"(b))
#define MFMA_DRAIN() asm volatile("s_nop 7\n\ts_nop 4" ::: "memory")
#define WAITV(N) asm volatile("s_waitcnt vmcnt(" #N ")" ::: "memory")
#define WAITL() asm volatile("s_waitcnt lgkmcnt(0)" ::: "memory")
#define BAR_L() asm volatile("s_waitcnt lgkmcnt(0)\n\ts_barrier" ::: "memory")
#define SB() __builtin_amdgcn_sched_barrier(0)
#define FENCE() asm volatile("" ::: "memory")

#define STAMP(i) do { SB(); stamps[i] = __builtin_amdgcn_s_memrealtime(); SB(); } while (0)

template <int V> struct IC { static constexpr int value = V; };
template <int I, int N, class F> DI void static_for(F&& f) { if constexpr (I < N) { f(IC<I>{}); static_for<I + 1, N>(f); } }
#define SFOR(var, N) static_for<0, N>([&](auto ic_##var) __attribute__((always_inline)) { constexpr int var = decltype(ic_##var)::value;
#define SEND });

DI void dma16(unsigned voff, const char* sbase, unsigned ldsaddr) {
    asm volatile("s_mov_b32 m0, %2\n\ts_nop 0\n\tglobal_load_lds_dwordx4 %0, %1" :: "v"(voff), "s"(sbase), "s"(ldsaddr) : "memory");
}
DI unsigned pk2(float lo, float hi) { f32x2 v = {lo, hi}; return __builtin_bit_cast(unsigned, __builtin_convertvector(v, bfx2)); }
typedef short v4i16_t __attribute__((ext_vector_type(4)));
DI bf16x8 trfrag(LAS char* p, int second) {
    s16x4 a = __builtin_bit_cast(s16x4, __builtin_amdgcn_ds_read_tr16_b64_v4i16((LAS v4i16_t*)p));
    s16x4 b = __builtin_bit_cast(s16x4, __builtin_amdgcn_ds_read_tr16_b64_v4i16((LAS v4i16_t*)(p + second)));
    return __builtin_shufflevector(a, b, 0, 1, 2, 3, 4, 5, 6, 7);
}
DI float halfmax(float m) { auto rr = __builtin_amdgcn_permlane32_swap(__float_as_uint(m), __float_as_uint(m), false, false);
    return __builtin_fmaxf(__uint_as_float(rr[0]), __uint_as_float(rr[1])); }

DI void poll_flags(GAS u64* word, bool active, u64 tag) {
#pragma unroll 1
    for (unsigned spins = 0; spins < SPIN_CAP; ++spins) {
        u64 v = tag;
        if (active) v = __hip_atomic_load(word, RLX_AGENT);
        if (__all(v == tag)) break;
        __builtin_amdgcn_s_sleep(1);
    }
    __builtin_amdgcn_fence(__ATOMIC_ACQUIRE, "wavefront");
}
DI int min4(LAS char* p) {
    const u64 a = __hip_atomic_load((LAS u64*)p, RLX_WG), b = __hip_atomic_load((LAS u64*)(p + 8), RLX_WG);
    const unsigned m = min(min((unsigned)a, (unsigned)(a >> 32)), min((unsigned)b, (unsigned)(b >> 32)));
    return __builtin_amdgcn_readfirstlane((int)m);
}
DI int wait_min4(LAS char* p, int need) {
    int v = min4(p);
#pragma unroll 1
    for (unsigned spins = 0; v < need && spins < LSPIN_CAP; ++spins) { __builtin_amdgcn_s_sleep(1); v = min4(p); }
    FENCE();
    return v;
}
DI void lds_set(LAS char* p, unsigned v) { FENCE(); __hip_atomic_store((LAS unsigned*)p, v, RLX_WG); FENCE(); }

__global__ __launch_bounds__(512, 2) void fused_attn_kernel(const char* __restrict__ xA, const char* __restrict__ wA, const char* __restrict__ oA,
                                                           const char* __restrict__ xB, const char* __restrict__ wB, const char* __restrict__ oB,
                                                           char* __restrict__ out, char* __restrict__ ws) {
    __shared__ __attribute__((aligned(16))) char smem_[LDS_BYTES];
    LAS char* smem = (LAS char*)smem_;
    const unsigned lds0 = (unsigned)(uintptr_t)smem;
    u64 stamps[14]; const u64 clk0 = __builtin_amdgcn_s_memtime();
    for (int i = 0; i < 14; ++i) stamps[i] = 0;
    STAMP(0);
    const int tid = threadIdx.x, lane = tid & 63;
    const int wid = __builtin_amdgcn_readfirstlane(tid >> 6);
    const int bx = blockIdx.x, xcd = bx & 7, idx = bx >> 3;
    const int isA = idx >> 4, rt = (xcd >> 1) * 4 + ((idx >> 2) & 3), hg = (xcd & 1) * 4 + (idx & 3);
    const int br = isA ? 0 : 1;
    const int k0 = (rt * 2 + ((hg & 3) * 5 >> 1) + (hg & 1)) % 10;
    const char* xb = (isA ? xA : xB) + (size_t)rt * (128 * 1280);
    const char* wb = (isA ? wA : wB) + hg * 160;
    const int me = (br * 16 + rt) * 8 + hg;
    const int r32 = lane & 31, hl = lane >> 5;
    auto kt_of = [&](int s) { int k = s + k0; return k >= 10 ? k - 10 : k; };
    if (ADELAY && isA) { for (int i = 0; i < ADELAY; ++i) __builtin_amdgcn_s_sleep(16); }

    const unsigned xo = (unsigned)((lane >> 3) * 1280 + (((lane & 7) ^ (lane >> 4)) << 4));
    auto w_off = [&](int q) { const int c = q * 64 + lane, row = c / 20, j = c % 20; return (unsigned)(row * 3840 + (j < 10 ? 1280 + j * 16 : 2400 + j * 16)); };
    { const char* xs = xb + k0 * 128 + wid * (16 * 1280);
      dma16(xo, xs, lds0 + XS0 + wid * 2048);
      dma16(xo ^ 64, xs + 8 * 1280, lds0 + XS0 + wid * 2048 + 1024);
      const char* wsrc = wb + (size_t)k0 * (64 * 3840);
      const unsigned wv = w_off(wid < 5 ? wid : wid - 5);
      if (wid < 5) { dma16(wv, wsrc, lds0 + WS0 + wid * 1024); dma16(wv, wsrc + 16 * 3840, lds0 + WS0 + (5 + wid) * 1024);
          if (wid >= 3) { dma16(wv, wsrc + 32 * 3840, lds0 + WS0 + (10 + wid) * 1024); dma16(wv, wsrc + 48 * 3840, lds0 + WS0 + (15 + wid) * 1024); } }
      else { dma16(wv, wsrc + 32 * 3840, lds0 + WS0 + (5 + wid) * 1024); dma16(wv, wsrc + 48 * 3840, lds0 + WS0 + (10 + wid) * 1024); } }
    if (tid < 30) *(LAS u32x2*)(smem + C_F + tid * 8) = (u32x2){tid == 1 ? 1u : 0u, (tid & 1) || tid >= 20 ? 3u : 0u};
    else if (tid >= 64 && tid < 80) *(LAS unsigned*)(smem + C_P + (tid - 64) * 4) = 0u;
    u64 epoch;
    asm volatile("s_load_dwordx2 %0, %1, 0x0\n\ts_waitcnt lgkmcnt(0)" : "=s"(epoch) : "s"(ws) : "memory");
    const u64 tag = epoch * 6364136223846793005ull + 1442695040888963407ull;
    const int nnb = isA ? (rt > 0 ? 1 : 0) : (rt > 1 ? 2 : rt);
    auto kq_base = [&](int fg) { const int hd = fg / 5, dg = fg % 5; return dg < 4 ? hd * 5120 + (dg >> 1) * 2048 + (dg & 1) * 8 : hd * 5120 + 4096; };

    if (wid < 4) {
        asm volatile("s_waitcnt vmcnt(0) lgkmcnt(0)\n\ts_barrier" ::: "memory");
        STAMP(1);
        bf16x8 xr[28], xt[2], wf[WIN + 1];
        f32x16 acc[5];
        const int tok = wid * 32 + r32;
        const int xa = XS0 + tok * 128 + ((hl ^ ((tok >> 1) & 7)) << 4);
        int wa0 = WS0 + (8 * hl + ((lane & 15) >> 2)) * 320 + (lane & 16) * 2 + (lane & 3) * 8, wa1 = wa0 + 61440;
        asm volatile("" : "+v"(wa0), "+v"(wa1));
        auto rdx = [&](int i) { return *(LAS bf16x8*)(smem + ((i >> 2) % 3) * 16384 + (xa ^ ((i & 3) * 32))); };
        auto rdw = [&](int f) { const int i = f / 5, t = f % 5, sl = (i >> 1) % 7;
            return trfrag(smem + (sl < 6 ? wa0 + sl * 10240 : wa1) + (i & 1) * 5120 + t * 64, 1280); };
        u64 fl = 0;
        auto rdflag = [&](int u) { fl = __hip_atomic_load((LAS u64*)(smem + C_F + u * 8), RLX_WG); };
        auto check = [&](int u) {
            if (!__builtin_amdgcn_readfirstlane((unsigned)fl & (((unsigned)(fl >> 32) + 1u) >> 2))) {
#pragma unroll 1
                for (unsigned spins = 0; spins < LSPIN_CAP; ++spins) { rdflag(u); if (__builtin_amdgcn_readfirstlane((unsigned)fl & (((unsigned)(fl >> 32) + 1u) >> 2))) break; }
            }
            FENCE(); };
        xr[0] = rdx(0);
#pragma unroll
        for (int f = 0; f < WIN; ++f) wf[f] = rdw(f);
        SB();
        SFOR(f, 200)
            constexpr int i = f / 5, t = f % 5;
            if constexpr (f % 10 == 0) rdflag((i >> 1) + 1);
            if constexpr (f % 10 == 10 - WIN) { lds_set(smem + C_P + wid * 4, i >> 1); if constexpr (f < 190) check((i >> 1) + 1); }
            if constexpr (f + WIN < 200) wf[(f + WIN) % (WIN + 1)] = rdw(f + WIN);
            if constexpr (t == 1 && i < 39) { if constexpr (i + 1 < 28) xr[i + 1] = rdx(i + 1); else xt[(i + 1) & 1] = rdx(i + 1); }
            SB();
            if constexpr (i == 0) MFMA32Z(acc[t], wf[f % (WIN + 1)], xr[0]);
            else if constexpr (i < 28) MFMA32I(acc[t], wf[f % (WIN + 1)], xr[i]);
            else MFMA32I(acc[t], wf[f % (WIN + 1)], xt[i & 1]);
            SB();
        SEND
        lds_set(smem + C_P + wid * 4, 20);
        MFMA_DRAIN();
        STAMP(2);
#pragma unroll
        for (int t = 0; t < 5; ++t)
#pragma unroll
            for (int b = 0; b < 4; ++b) {
                const int g0 = 8 * t + 2 * b, g1 = g0 + 1;
                const int o0 = g0 < 20 ? kq_base(g0) + tok * (g0 % 5 < 4 ? 16 : 8) : VOFF + (g0 - 20) * 8 + tok * VP;
                const int o1 = g1 < 20 ? kq_base(g1) + tok * (g1 % 5 < 4 ? 16 : 8) : VOFF + (g1 - 20) * 8 + tok * VP;
                *(LAS u32x2*)(smem + OWN + (hl ? o1 : o0)) = (u32x2){pk2(acc[t][4 * b], acc[t][4 * b + 1]), pk2(acc[t][4 * b + 2], acc[t][4 * b + 3])};
            }
        if (hl == 0) *(LAS u32x2*)(smem + OWN + VOFF + tok * VP + 160) = (u32x2){0x3F803F80u, 0x3F803F80u};
        WAITL();
        lds_set(smem + C_E + wid * 4, 1);
        STAMP(3);
        int l2 = tid; asm volatile("" : "+v"(l2)); l2 &= 63;
        int qa0 = (8 * (l2 >> 5) + 2 * ((l2 & 15) >> 2)) * 160 + (l2 & 16) * 2 + (l2 & 3) * 8, qa1 = qa0 + WS0 + 10240;
        asm volatile("" : "+v"(qa0), "+v"(qa1));
        auto rdq = [&](int f) { const int i = f / 3, t = f % 3, j = i >> 2, sl = (20 + j) % 7;
            return trfrag(smem + (j >= 7 ? qa0 + XS0 + (j - 7) * 10240 : sl == 0 ? qa0 + WS0 : qa1 + (sl - 1) * 10240) + (i & 3) * 2560 + t * 64, 160); };
        check(20);
        xt[0] = rdx(28);
#pragma unroll
        for (int f = 0; f < WIN; ++f) wf[f] = rdq(f);
        SB();
        SFOR(f, 120)
            constexpr int i = f / 3, t = f % 3, j = i >> 2;
            if constexpr (f % 12 == 0 && j < 9) rdflag(21 + j);
            if constexpr (f % 12 == 12 - WIN) { lds_set(smem + C_P + wid * 4, 20 + j); if constexpr (j < 9) check(21 + j); }
            if constexpr (f + WIN < 120) wf[(f + WIN) % (WIN + 1)] = rdq(f + WIN);
            if constexpr (t == 0 && i < 11) xt[(i + 1) & 1] = rdx(28 + i + 1);
            SB();
            if constexpr (i == 0) MFMA32Z(acc[t], wf[f % (WIN + 1)], xt[0]);
            else if constexpr (i < 12) MFMA32I(acc[t], wf[f % (WIN + 1)], xt[i & 1]);
            else MFMA32I(acc[t], wf[f % (WIN + 1)], xr[i - 12]);
            SB();
        SEND
        MFMA_DRAIN();
        STAMP(4);
        wait_min4(smem + C_P, 29);
#pragma unroll
        for (int t = 0; t < 3; ++t)
#pragma unroll
            for (int b = 0; b < 4; ++b)
                if (t < 2 || b < 2) {
                    const int g0 = 8 * t + 2 * b, g1 = g0 + 1;
                    const int o0 = kq_base(g0) + tok * (g0 % 5 < 4 ? 16 : 8), o1 = kq_base(g1) + tok * (g1 % 5 < 4 ? 16 : 8);
                    *(LAS u32x2*)(smem + QST + (hl ? o1 : o0)) =
                        (u32x2){pk2(acc[t][4 * b] * QSCALE, acc[t][4 * b + 1] * QSCALE), pk2(acc[t][4 * b + 2] * QSCALE, acc[t][4 * b + 3] * QSCALE)};
                }
        STAMP(5);
    } else {
        const int hw = wid - 4;
        int known = 0;
        auto lds_or = [&](int off, unsigned v) { FENCE(); __hip_atomic_fetch_or((LAS unsigned*)(smem + off), v, RLX_WG); FENCE(); };
        if (hw == 0 || hw == 3) {
            const int half = hw ? 1 : 0;
#pragma unroll 1
            for (int j = 1; j < 10; ++j) {
                if (2 * j - 4 > known) { WAITV(0); lds_or(C_F + (j - 1) * 16 + 4, 1u << half); known = wait_min4(smem + C_P, 2 * j - 4); }
                const char* xs = xb + kt_of(j) * 128 + half * (64 * 1280); const unsigned dst = lds0 + XS0 + (j % 3) * 16384 + half * 8192;
#pragma unroll
                for (int i = 0; i < 8; ++i) dma16(i & 1 ? xo ^ 64 : xo, xs + i * (8 * 1280), dst + i * 1024);
                WAITV(8);
                if (j == 1) BAR_L(); else lds_or(C_F + (j - 1) * 16 + 4, 1u << half);
            }
            WAITV(0); lds_or(C_F + 9 * 16 + 4, 1u << half);
        } else {
            unsigned wo[5], qo[5];
#pragma unroll
            for (int q = 0; q < 5; ++q) { wo[q] = w_off(q);
                const int c = q * 64 + lane, rho = c / 10; qo[q] = (unsigned)(((rho & ~7) + 4 * (rho & 1) + ((rho >> 1) & 3)) * 3840 + (c % 10) * 16); }
#pragma unroll 1
            for (int u = hw + 1; u < 30; u += 2) {
                const int need = u < 27 ? u - 6 : 23;
                if (need > known) { WAITV(0); lds_set(smem + C_F + (u - 2) * 8, 1); known = wait_min4(smem + C_P, need); }
                const unsigned dst = lds0 + (u < 27 ? WS0 + (u % 7) * 10240 : XS0 + (u - 27) * 10240);
                const char* src = u < 20 ? wb + (size_t)(kt_of(u >> 1) * 64 + (u & 1) * 32) * 3840 : wb + (size_t)kt_of(u < 23 ? u - 13 : u - 23) * (64 * 3840);
                if (u < 20) {
#pragma unroll
                    for (int i = 0; i < 5; ++i) dma16(wo[i], src, dst + i * 1024);
                } else {
#pragma unroll
                    for (int i = 0; i < 5; ++i) dma16(qo[i], src, dst + i * 1024);
                }
                WAITV(5);
                if (u == hw + 1) BAR_L(); else lds_set(smem + C_F + (u - 2) * 8, 1);
                if (u < 20) {
#pragma unroll
                    for (int i = 0; i < 5; ++i) dma16(wo[i], src + 16 * 3840, dst + (5 + i) * 1024);
                } else {
#pragma unroll
                    for (int i = 0; i < 5; ++i) dma16(qo[i], src + 32 * 3840, dst + (5 + i) * 1024);
                }
            }
            WAITV(0); lds_set(smem + C_F + (27 + hw) * 8, 1);
        }
        STAMP(1);
        auto a_chunk = [&](int a) { int o;
            if (a < 320) { const int hd = a / 80, rem = a % 80; o = hd * 5120 + (rem < 32 ? (96 + rem) * 16 : rem < 64 ? 2048 + (64 + rem) * 16 : 4096 + 768 + (rem - 64) * 16); }
            else o = VOFF + 96 * VP + (a - 320) * 16;
            return o; };
        if ((hw == 0 || hw == 3) && rt < 15) {
            const __amdgpu_buffer_rsrc_t kvr = __builtin_amdgcn_make_buffer_rsrc(ws + WS_KV + me * KVB, 0, KVB, 0x00020000);
            const int half = hw ? 1 : 0;
            wait_min4(smem + C_E, 1);
            STAMP(2);
            if (!isA) { u32x4 v[21];
#pragma unroll
                for (int j = 0; j < 21; ++j) if (j < 20 || half) v[j] = *(LAS u32x4*)(smem + OWN + half * 20480 + j * 1024 + lane * 16);
#pragma unroll
                for (int j = 0; j < 21; ++j) if (j < 20 || half) __builtin_amdgcn_raw_buffer_store_b128(v[j], kvr, half * 20480 + j * 1024 + lane * 16, 0, 16);
            } else {
#pragma unroll
                for (int j = 0; j < 6; ++j) { const int a = (2 * j + half) * 64 + lane;
                    if (a < 656) { const int o = a_chunk(a); __builtin_amdgcn_raw_buffer_store_b128(*(LAS u32x4*)(smem + OWN + o), kvr, o, 0, 16); } }
            }
            STAMP(3);
            WAITV(0);
            unsigned old = 0;
            if (lane == 0) old = __hip_atomic_fetch_add((LAS unsigned*)(smem + C_CNT), 1u, RLX_WG);
            if (lane == 0 && old == 1) __hip_atomic_store((GAS u64*)(ws + WS_FKV + me * 128), tag, RLX_AGENT);
            STAMP(4);
        }
        poll_flags((GAS u64*)(ws + WS_FKV + (me - 8 * (lane + 1)) * 128), lane < nnb, tag);
        STAMP(5);
        if (!isA) {
            const int n = hw >> 1, half = hw & 1;
            if (n < nnb) {
                const __amdgpu_buffer_rsrc_t nbr = __builtin_amdgcn_make_buffer_rsrc(ws + WS_KV + (me - 8 * (n + 1)) * KVB, 0, KVB, 0x00020000);
                u32x4 v[21];
#pragma unroll
                for (int j = 0; j < 21; ++j) if (j < 20 || half) v[j] = __builtin_amdgcn_raw_buffer_load_b128(nbr, half * 20480 + j * 1024 + lane * 16, 0, 16);
                wait_min4(smem + C_P, 27);
#pragma unroll
                for (int j = 0; j < 21; ++j) if (j < 20 || half) *(LAS u32x4*)(smem + KV0 + (1 - n) * KVB + half * 20480 + j * 1024 + lane * 16) = v[j];
            }
        } else if (nnb) {
            const __amdgpu_buffer_rsrc_t nbr = __builtin_amdgcn_make_buffer_rsrc(ws + WS_KV + (me - 8) * KVB, 0, KVB, 0x00020000);
            u32x4 v[3];
#pragma unroll
            for (int j = 0; j < 3; ++j) { const int a = (4 * j + hw) * 64 + lane; if (a < 656) v[j] = __builtin_amdgcn_raw_buffer_load_b128(nbr, a_chunk(a), 0, 16); }
            wait_min4(smem + C_P, 27);
#pragma unroll
            for (int j = 0; j < 3; ++j) { const int a = (4 * j + hw) * 64 + lane; if (a < 656) *(LAS u32x4*)(smem + KV0 + KVB + a_chunk(a)) = v[j]; }
        }
        if (hw == 3) { wait_min4(smem + C_P, 27); if (lane < 2) *(LAS u32x2*)(smem + ONE + lane * 5120) = (u32x2){0x3F803F80u, 0x3F803F80u}; }
        STAMP(6);
    }
    const int qi = wid & 3, hp = wid >> 2;
    const int ud = qi + 8, uf = isA ? qi + 7 : qi;
    const bool far = isA ? (4 * rt + qi >= 1) : (4 * rt + qi >= 8);
    const int ulo = isA ? ud : (far ? qi + 1 : 8 - 4 * rt);
    f32x16 cd, cf; unsigned md[8];
    { const unsigned low = (2u << r32) - 1u;
      unsigned vd = low, vf = ~low;
      if (isA) { const u64 w = 0x5555ull << (r32 + 18);
          vd = (unsigned)(w >> 32); vf = (unsigned)w; }
      const unsigned id = ~vd >> (4 * hl), jf = ~vf >> (4 * hl);
#pragma unroll
      for (int r = 0; r < 16; r += 2) {
          const int c0 = (r & 3) + 8 * (r >> 2);
          const unsigned a0 = (unsigned)__builtin_amdgcn_sbfe((int)id, c0, 1), a1 = (unsigned)__builtin_amdgcn_sbfe((int)id, c0 + 1, 1);
          cd[r] = __uint_as_float(a0 & 0xFF800000u); cd[r + 1] = __uint_as_float(a1 & 0xFF800000u);
          cf[r] = __uint_as_float((unsigned)__builtin_amdgcn_sbfe((int)jf, c0, 1) & 0xFF800000u);
          cf[r + 1] = __uint_as_float((unsigned)__builtin_amdgcn_sbfe((int)jf, c0 + 1, 1) & 0xFF800000u);
          md[r >> 1] = __builtin_amdgcn_perm(a1, a0, 0x07060100u);
      } }
    unsigned oo[5];
#pragma unroll
    for (int q = 0; q < 5; ++q) { const int c = q * 64 + lane, rho = c / 10; oo[q] = (unsigned)(((rho & ~7) + 4 * (rho & 1) + ((rho >> 1) & 3)) * 1280 + (c % 10) * 16); }
    BAR_L();
    STAMP(7);
    u32x2 ob[2][3];
    {
        bf16x8 q1[2]; u32x2 q2[2];
#pragma unroll
        for (int e = 0; e < 2; ++e) {
            q1[e] = *(LAS bf16x8*)(smem + QST + (2 * hp + e) * 5120 + hl * 2048 + (qi * 32 + r32) * 16);
            q2[e] = *(LAS u32x2*)(smem + QST + (2 * hp + e) * 5120 + 4096 + (qi * 32 + r32) * 8);
            if (hl) q2[e] = (u32x2){0u, 0u};
        }
        const int kb1 = KV0 + hp * 10240 + hl * 2048 + r32 * 16;
        const int kb2 = hl ? ONE : KV0 + hp * 10240 + 4096 + r32 * 8, nh = hl ^ 1;
        const int vrow = KV0 + VOFF + (4 * hl + ((lane & 15) >> 2)) * VP;
        const bool ones = (lane & 16) && (lane & 3);
        const int vb0 = vrow + (ones ? 160 : hp * 80 + (lane & 16) * 2 + (lane & 3) * 8), vb1 = vb0 + (ones ? 0 : 40);
        struct KF { bf16x8 a; s16x4 b; };
        struct VF { bf16x8 a, b; };
        auto rdk = [&](int u, int e) { const int so = (u >> 2) * KVB, tw = u & 3; KF k;
            k.a = *(LAS bf16x8*)(smem + kb1 + so + tw * 512 + e * 5120); k.b = *(LAS s16x4*)(smem + kb2 + (so + tw * 256) * nh + e * 5120); return k; };
        auto rdv = [&](int u, int e) { LAS char* p = smem + (e ? vb1 : vb0) + (u >> 2) * KVB + (u & 3) * (32 * VP); VF v;
            v.a = trfrag(p, 8 * VP); v.b = trfrag(p + 16 * VP, 8 * VP); return v; };
        auto qk = [&](const KF& k, int e, const f32x16& c) { return MFMA32K8(k.b, __builtin_bit_cast(s16x4, q2[e]), MFMA32(k.a, q1[e], c)); };
        const f32x16 zero = {};
        auto merged = [&](int e) {
            f32x16 sm = qk(rdk(ud, e), e, cd);
            if (far) { const f32x16 sf = qk(rdk(uf, e), e, cf);
#pragma unroll
                for (int r = 0; r < 16; ++r) sm[r] = __builtin_fmaxf(sm[r], sf[r]); }
            return sm; };
        auto max16 = [&](const f32x16& s, float m) {
#pragma unroll
            for (int r = 0; r < 16; ++r) m = __builtin_fmaxf(m, s[r]);
            return m; };
        auto set_ref = [&](float m, int e) { const unsigned nb = pk2(-m, 0.f); if (hl) q2[e][0] = nb; return __uint_as_float(nb << 16); };
        auto boundary_pv = [&](const f32x16& sm, float nm, int e) {
            u32x4 pd[2], pf[2];
#pragma unroll
            for (int r = 0; r < 16; r += 2) {
                const unsigned p = pk2(__builtin_amdgcn_exp2f(sm[r] + nm), __builtin_amdgcn_exp2f(sm[r + 1] + nm));
                pd[r >> 3][(r >> 1) & 3] = p & ~md[r >> 1]; pf[r >> 3][(r >> 1) & 3] = p & md[r >> 1]; }
            VF v = rdv(ud, e);
            f32x16 o = MFMA32(v.b, __builtin_bit_cast(bf16x8, pd[1]), MFMA32(v.a, __builtin_bit_cast(bf16x8, pd[0]), zero));
            if (far) { v = rdv(uf, e); o = MFMA32(v.b, __builtin_bit_cast(bf16x8, pf[1]), MFMA32(v.a, __builtin_bit_cast(bf16x8, pf[0]), o)); }
            return o; };
        auto exp8 = [&](const f32x16& s, int b) { u32x4 p;
#pragma unroll
            for (int r = 0; r < 4; ++r) p[r] = pk2(__builtin_amdgcn_exp2f(s[b + 2 * r]), __builtin_amdgcn_exp2f(s[b + 2 * r + 1]));
            return __builtin_bit_cast(bf16x8, p); };
        f32x16 o0, o1;
        { const f32x16 s0 = merged(0), s1 = merged(1);
          const float n0 = set_ref(halfmax(max16(s0, s0[0])), 0), n1 = set_ref(halfmax(max16(s1, s1[0])), 1);
          f32x16 sa, sb; KF kx; VF vx;
          if (ulo < ud) { sa = qk(rdk(ulo, 0), 0, zero); kx = rdk(ulo, 1); vx = rdv(ulo, 0); }
          o0 = boundary_pv(s0, n0, 0); o1 = boundary_pv(s1, n1, 1);
#pragma unroll 1
          for (int u = ulo; u < ud; ++u) {
              SB();
              sb = qk(kx, 1, zero);
              kx = rdk(u + 1, 0);
              VF vy = rdv(u, 1);
              SB();
              { const bf16x8 p = exp8(sa, 0); o0 = MFMA32(vx.a, p, o0); }
              SB();
              { const bf16x8 p = exp8(sa, 8); o0 = MFMA32(vx.b, p, o0); }
              SB();
              sa = qk(kx, 0, zero);
              kx = rdk(u + 1, 1);
              vx = rdv(u + 1, 0);
              SB();
              { const bf16x8 p = exp8(sb, 0); o1 = MFMA32(vy.a, p, o1); }
              SB();
              { const bf16x8 p = exp8(sb, 8); o1 = MFMA32(vy.b, p, o1); }
              SB();
          } }
        if (!__all(o0[12] < 1.152921504606847e18f && o1[12] < 1.152921504606847e18f)) {
#pragma unroll
            for (int e = 0; e < 2; ++e) {
                if (hl) q2[e][0] = 0u;
                const f32x16 s = merged(e);
                float m = max16(s, s[0]);
#pragma unroll 1
                for (int u = ulo; u < ud; ++u) m = max16(qk(rdk(u, e), e, zero), m);
                const float n = set_ref(halfmax(m), e);
                f32x16 o = boundary_pv(s, n, e);
#pragma unroll 1
                for (int u = ulo; u < ud; ++u) { const f32x16 t = qk(rdk(u, e), e, zero); const VF v = rdv(u, e);
                    o = MFMA32(v.a, exp8(t, 0), o); o = MFMA32(v.b, exp8(t, 8), o); }
                if (e) o1 = o; else o0 = o;
            }
        }
        const float rl0 = __builtin_amdgcn_rcpf(o0[12]), rl1 = __builtin_amdgcn_rcpf(o1[12]);
#pragma unroll
        for (int j = 0; j < 3; ++j) {
            ob[0][j] = (u32x2){pk2(o0[4 * j] * rl0, o0[4 * j + 1] * rl0), pk2(o0[4 * j + 2] * rl0, o0[4 * j + 3] * rl0)};
            ob[1][j] = (u32x2){pk2(o1[4 * j] * rl1, o1[4 * j + 1] * rl1), pk2(o1[4 * j + 2] * rl1, o1[4 * j + 3] * rl1)}; }
    }
    STAMP(8);
    const __amdgpu_buffer_rsrc_t orr = __builtin_amdgcn_make_buffer_rsrc(ws + WS_O + (br * 64 + rt * 4) * 40960, 0, 4 * 40960, 0x00020000);
    { const unsigned obase = (unsigned)(qi * 40960 + (hg * 10 + hp * 5) * 512 + r32 * 16);
      { auto rx = __builtin_amdgcn_permlane32_swap(ob[0][0][0], ob[0][1][0], false, false);
        auto ry = __builtin_amdgcn_permlane32_swap(ob[0][0][1], ob[0][1][1], false, false);
        __builtin_amdgcn_raw_buffer_store_b128((u32x4){rx[0], ry[0], rx[1], ry[1]}, orr, obase + hl * 512, 0, 16); }
      if (hl == 0) __builtin_amdgcn_raw_buffer_store_b128((u32x4){ob[0][2][0], ob[0][2][1], ob[1][0][0], ob[1][0][1]}, orr, obase + 1024, 0, 16);
      { auto fx = __builtin_amdgcn_permlane32_swap(ob[1][0][0], ob[1][1][0], false, false);
        auto fy = __builtin_amdgcn_permlane32_swap(ob[1][0][1], ob[1][1][1], false, false);
        auto sx = __builtin_amdgcn_permlane32_swap(ob[1][1][0], ob[1][2][0], false, false);
        auto sy = __builtin_amdgcn_permlane32_swap(ob[1][1][1], ob[1][2][1], false, false);
        __builtin_amdgcn_raw_buffer_store_b128((u32x4){fx[1], fy[1], sx[0], sy[0]}, orr, obase + 1536 + hl * 512, 0, 16); } }
    BAR_L();
    { const char* wsrc = (isA ? oA : oB) + hg * 160 + wid * (80 * 1280); const unsigned dst = lds0 + WOB + wid * 12800;
#pragma unroll
      for (int i = 0; i < 4; ++i) dma16(oo[i], wsrc, dst + i * 1024);
      WAITV(4);
      unsigned old = 0;
      if (lane == 0) old = __hip_atomic_fetch_add((LAS unsigned*)(smem + C_CNT + 4), 1u, RLX_WG);
      if (lane == 0 && old == 7) __hip_atomic_store((GAS u64*)(ws + WS_FO + me * 128), tag, RLX_AGENT);
      STAMP(9);
#pragma unroll
      for (int i = 4; i < 12; ++i) dma16(oo[i % 5], wsrc + (i / 5) * (32 * 1280), dst + i * 1024);
      if (lane < 32) dma16(oo[2], wsrc + 64 * 1280, dst + 12 * 1024); }
    poll_flags((GAS u64*)(ws + WS_FO + ((me & ~7) + (lane & 7)) * 128), lane < 8, tag);
    STAMP(10);
    bf16x8 of[20];
#pragma unroll
    for (int s = 0; s < 20; ++s)
        of[s] = __builtin_bit_cast(bf16x8, __builtin_amdgcn_raw_buffer_load_b128(orr, qi * 40960 + (hp * 20 + s) * 1024 + lane * 16, 0, 16));
    asm volatile("s_waitcnt vmcnt(20)\n\ts_barrier" ::: "memory");
    STAMP(11);
    f32x16 oc[3];
    { bf16x8 af[5];
      const int aa = WOB + hp * 51200 + (8 * hl + 2 * ((lane & 15) >> 2)) * 160 + (lane & 16) * 2 + (lane & 3) * 8;
      auto rda = [&](int f) { return trfrag(smem + aa + (f / 3) * 2560 + (f % 3) * 64, 160); };
#pragma unroll
      for (int f = 0; f < 4; ++f) af[f] = rda(f);
      SB();
#pragma unroll
      for (int f = 0; f < 60; ++f) {
          if (f + 4 < 60) af[(f + 4) % 5] = rda(f + 4);
          SB();
          if (f < 3) MFMA32Z(oc[f], af[f % 5], of[0]); else MFMA32I(oc[f % 3], af[f % 5], of[f / 3]);
          SB();
      } }
    MFMA_DRAIN();
    STAMP(12);
    { LAS char* mine = smem + RED + wid * 6144 + lane * 16; LAS char* other = smem + RED + (wid ^ 4) * 6144 + lane * 16;
      if (hp) {
#pragma unroll
          for (int b = 0; b < 4; ++b) *(LAS f32x4*)(mine + b * 1024) = (f32x4){oc[0][4 * b], oc[0][4 * b + 1], oc[0][4 * b + 2], oc[0][4 * b + 3]};
#pragma unroll
          for (int b = 0; b < 2; ++b) *(LAS f32x4*)(mine + (4 + b) * 1024) = (f32x4){oc[2][4 * b], oc[2][4 * b + 1], oc[2][4 * b + 2], oc[2][4 * b + 3]};
      } else {
#pragma unroll
          for (int b = 0; b < 4; ++b) *(LAS f32x4*)(mine + b * 1024) = (f32x4){oc[1][4 * b], oc[1][4 * b + 1], oc[1][4 * b + 2], oc[1][4 * b + 3]};
      }
      BAR_L();
      char* orow = out + (size_t)(rt * 128 + qi * 32 + r32) * 2560 + br * 1280 + hg * 160 + hl * 16;
      auto fin = [&](const f32x16& c, int b, int slot) { const f32x4 x = *(LAS f32x4*)(other + slot * 1024);
          return (u32x2){pk2(c[4 * b] + x[0], c[4 * b + 1] + x[1]), pk2(c[4 * b + 2] + x[2], c[4 * b + 3] + x[3])}; };
      auto st2 = [&](u32x2 a, u32x2 b, int col) {
          auto rx = __builtin_amdgcn_permlane32_swap(a[0], b[0], false, false); auto ry = __builtin_amdgcn_permlane32_swap(a[1], b[1], false, false);
          *(u32x4*)(orow + col * 2) = (u32x4){rx[0], ry[0], rx[1], ry[1]}; };
      if (hp) { st2(fin(oc[1], 0, 0), fin(oc[1], 1, 1), 32); st2(fin(oc[1], 2, 2), fin(oc[1], 3, 3), 48); }
      else { st2(fin(oc[0], 0, 0), fin(oc[0], 1, 1), 0); st2(fin(oc[0], 2, 2), fin(oc[0], 3, 3), 16); st2(fin(oc[2], 0, 4), fin(oc[2], 1, 5), 64); } }
    if (bx == 0) {
        if (wid < 4) poll_flags((GAS u64*)(ws + WS_FO + tid * 128), true, tag);
        __syncthreads();
        if (tid == 0) __hip_atomic_store((GAS u64*)ws, tag, RLX_AGENT);
    }
    asm volatile("s_waitcnt vmcnt(0)" ::: "memory");
    STAMP(13);
    if (lane == 0 && (wid == 0 || wid == 5) && (bx == 40 || bx == 41 || bx == 200 || bx == 99))
        printf("blk %d w %d: %d %d %d %d %d %d %d %d %d %d %d %d %d\n", bx, wid, (int)(stamps[1] - stamps[0]), (int)(stamps[2] - stamps[0]), (int)(stamps[3] - stamps[0]), (int)(stamps[4] - stamps[0]),
           (int)(stamps[5] - stamps[0]), (int)(stamps[6] - stamps[0]), (int)(stamps[7] - stamps[0]), (int)(stamps[8] - stamps[0]), (int)(stamps[9] - stamps[0]), (int)(stamps[10] - stamps[0]),
           (int)(stamps[11] - stamps[0]), (int)(stamps[12] - stamps[0]), (int)(stamps[13] - stamps[0]));
}

extern "C" void kernel_launch(void* const* d_in, const int* in_sizes, int n_in, void* d_out, int out_size, void* d_ws, size_t ws_size,
                              hipStream_t stream) {
    fused_attn_kernel<<<256, 512, 0, stream>>>((const char*)d_in[0], (const char*)d_in[1], (const char*)d_in[2], (const char*)d_in[3],
                                               (const char*)d_in[4], (const char*)d_in[5], (char*)d_out, (char*)d_ws);
}
```

```cpp
#define ADELAY 6
#include <hip/hip_runtime.h>
#include <hip/hip_bf16.h>
#include <stdint.h>

typedef short bf16x8 __attribute__((ext_vector_type(8)));
typedef short s16x4 __attribute__((ext_vector_type(4)));
typedef float f32x2 __attribute__((ext_vector_type(2)));
typedef float f32x4 __attribute__((ext_vector_type(4)));
typedef float f32x16 __attribute__((ext_vector_type(16)));
typedef unsigned u32x4 __attribute__((ext_vector_type(4)));
typedef unsigned u32x2 __attribute__((ext_vector_type(2)));
typedef __bf16 bfx2 __attribute__((ext_vector_type(2)));
typedef unsigned long long u64;
#define LAS __attribute__((address_space(3)))
#define GAS __attribute__((address_space(1)))
#define DI __device__ __forceinline__
#define RLX_AGENT __ATOMIC_RELAXED, __HIP_MEMORY_SCOPE_AGENT
#define RLX_WG __ATOMIC_RELAXED, __HIP_MEMORY_SCOPE_WORKGROUP

constexpr int LDS_BYTES = 163840;
constexpr int VOFF = 20480, VP = 168;
constexpr int KVB = VOFF + 128 * VP;
constexpr int KV0 = LDS_BYTES - 3 * KVB;
constexpr int OWN = KV0 + 2 * KVB;
constexpr int C_F = 0, C_P = 256, C_E = 272, C_CNT = 288, C_TAG = 320;
constexpr int XS0 = 512, WS0 = XS0 + 3 * 16384, QST = 512, ONE = 31744, WOB = 512, RED = WOB + 102400;
static_assert(WS0 + 7 * 10240 <= OWN, "ring overlaps the own image");
static_assert(ONE + 5120 + 8 <= KV0 && RED + 8 * 6144 <= LDS_BYTES, "LDS map");
constexpr unsigned WS_FKV = 4096, WS_FO = WS_FKV + 256 * 128, WS_KV = 131072;
constexpr unsigned KVG = KVB + 128, OG = 4 * 40960 + 128;
constexpr unsigned WS_O = WS_KV + 256 * KVG;
constexpr unsigned WS_END = WS_O + 32 * OG;
constexpr unsigned WS_DBG = WS_END;
constexpr unsigned SPIN_CAP = 400000, LSPIN_CAP = 4000000;
constexpr float QSCALE = 0.22360679774997896f * 1.4426950408889634f;
#define OPF 6
#define WIN 5

#define MFMA32(a, b, c) __builtin_amdgcn_mfma_f32_32x32x16_bf16((a), (b), (c), 0, 0, 0)
#define MFMA32K8(a, b, c) __builtin_amdgcn_mfma_f32_32x32x8bf16_1k((a), (b), (c), 0, 0, 0)
#define MFMA32I(acc, a, b) asm volatile("v_mfma_f32_32x32x16_bf16 %0, %1, %2, %0" : "+v"(acc) : "v"(a), "v"(b))
#define MFMA32Z(acc, a, b) asm volatile("v_mfma_f32_32x32x16_bf16 %0, %1, %2, 0" : "=&v"(acc) : "v"(a), "v"(b))
#define MFMA_DRAIN() asm volatile("s_nop 7\n\ts_nop 4" ::: "memory")
#define WAITV(N) asm volatile("s_waitcnt vmcnt(" #N ")" ::: "memory")
#define WAITL() asm volatile("s_waitcnt lgkmcnt(0)" ::: "memory")
#define BAR_L() asm volatile("s_waitcnt lgkmcnt(0)\n\ts_barrier" ::: "memory")
#define SB() __builtin_amdgcn_sched_barrier(0)
#define FENCE() asm volatile("" ::: "memory")

#define STAMP(i) do {} while (0)

template <int V> struct IC { static constexpr int value = V; };
template <int I, int N, class F> DI void static_for(F&& f) { if constexpr (I < N) { f(IC<I>{}); static_for<I + 1, N>(f); } }
#define SFOR(var, N) static_for<0, N>([&](auto ic_##var) __attribute__((always_inline)) { constexpr int var = decltype(ic_##var)::value;
#define SEND });

DI void dma16(unsigned voff, const char* sbase, unsigned ldsaddr) {
    asm volatile("s_mov_b32 m0, %2\n\ts_nop 0\n\tglobal_load_lds_dwordx4 %0, %1" :: "v"(voff), "s"(sbase), "s"(ldsaddr) : "memory");
}
DI void dma4(const char* p, unsigned ldsaddr) {
    asm volatile("s_mov_b32 m0, %1\n\ts_nop 0\n\tglobal_load_lds_dword %0, off" :: "v"(p), "s"(ldsaddr) : "memory");
}
DI unsigned pk2(float lo, float hi) { f32x2 v = {lo, hi}; return __builtin_bit_cast(unsigned, __builtin_convertvector(v, bfx2)); }
typedef short v4i16_t __attribute__((ext_vector_type(4)));
DI bf16x8 trfrag(LAS char* p, int second) {
    s16x4 a = __builtin_bit_cast(s16x4, __builtin_amdgcn_ds_read_tr16_b64_v4i16((LAS v4i16_t*)p));
    s16x4 b = __builtin_bit_cast(s16x4, __builtin_amdgcn_ds_read_tr16_b64_v4i16((LAS v4i16_t*)(p + second)));
    return __builtin_shufflevector(a, b, 0, 1, 2, 3, 4, 5, 6, 7);
}
DI float halfmax(float m) { auto rr = __builtin_amdgcn_permlane32_swap(__float_as_uint(m), __float_as_uint(m), false, false);
    return __builtin_elementwise_maximum(__uint_as_float(rr[0]), __uint_as_float(rr[1])); }

DI void poll_flags(GAS u64* word, bool active, u64 tag) {
#pragma unroll 1
    for (unsigned spins = 0; spins < SPIN_CAP; ++spins) {
        u64 v = tag;
        if (active) v = __hip_atomic_load(word, RLX_AGENT);
        if (__all(v == tag)) break;
        __builtin_amdgcn_s_sleep(1);
    }
    __builtin_amdgcn_fence(__ATOMIC_ACQUIRE, "wavefront");
}
DI int min4(LAS char* p) {
    const u64 a = __hip_atomic_load((LAS u64*)p, RLX_WG), b = __hip_atomic_load((LAS u64*)(p + 8), RLX_WG);
    const unsigned m = min(min((unsigned)a, (unsigned)(a >> 32)), min((unsigned)b, (unsigned)(b >> 32)));
    return __builtin_amdgcn_readfirstlane((int)m);
}
DI int wait_min4(LAS char* p, int need) {
    int v = min4(p);
#pragma unroll 1
    for (unsigned spins = 0; v < need && spins < LSPIN_CAP; ++spins) { __builtin_amdgcn_s_sleep(1); v = min4(p); }
    FENCE();
    return v;
}
DI void lds_set(LAS char* p, unsigned v) { FENCE(); __hip_atomic_store((LAS unsigned*)p, v, RLX_WG); FENCE(); }

__global__ __launch_bounds__(512, 2) void fused_attn_kernel(const char* __restrict__ xA, const char* __restrict__ wA, const char* __restrict__ oA,
                                                           const char* __restrict__ xB, const char* __restrict__ wB, const char* __restrict__ oB,
                                                           char* __restrict__ out, char* __restrict__ ws) {
    __shared__ __attribute__((aligned(16))) char smem_[LDS_BYTES];
    LAS char* smem = (LAS char*)smem_;
    const unsigned lds0 = (unsigned)(uintptr_t)smem;
    STAMP(0);
    const int tid = threadIdx.x, lane = tid & 63;
    const int wid = __builtin_amdgcn_readfirstlane(tid >> 6);
    const int bx = blockIdx.x, xcd = bx & 7, idx = bx >> 3;
    const int isA = idx >> 4, rt = (xcd >> 1) * 4 + ((idx >> 2) & 3), hg = (xcd & 1) * 4 + (idx & 3);
    const int br = isA ? 0 : 1;
    const int k0 = (rt * 2 + ((hg & 3) * 5 >> 1) + (hg & 1)) % 10;
    const char* xb = (isA ? xA : xB) + (size_t)rt * (128 * 1280);
    const char* wb = (isA ? wA : wB) + hg * 160;
    const int me = (br * 16 + rt) * 8 + hg;
    const int r32 = lane & 31, hl = lane >> 5;
    auto kt_of = [&](int s) { int k = s + k0; return k >= 10 ? k - 10 : k; };
    if (ADELAY && isA) { for (int i = 0; i < ADELAY; ++i) __builtin_amdgcn_s_sleep(16); }

    if (wid == 4 && lane < 2) dma4(ws + lane * 4, lds0 + C_TAG);
    if (wid == 7) { const char* p = ws + 2048;
        if (lane < 6) { const int im = me - 8 * (lane >> 1); if (im >= 0) p = ws + WS_KV + im * KVG + ((lane & 1) ? KVB : -128); }
        else if (lane < 8) p = ws + WS_O + (br * 16 + rt) * OG + ((lane & 1) ? (int)OG - 128 : -128);
        else if (lane < 10) p = (isA ? oA : oB) + (lane & 1) * (639 * 1280);
        else if (lane < 12) p = out + (size_t)(rt * 128 + (lane & 1) * 127) * 2560;
        dma4(p, lds0 + OWN); }
    const unsigned xo = (unsigned)((lane >> 3) * 1280 + (((lane & 7) ^ (lane >> 4)) << 4));
    auto w_off = [&](int q) { const int c = q * 64 + lane, row = c / 20, j = c % 20; return (unsigned)(row * 3840 + (j < 10 ? 1280 + j * 16 : 2400 + j * 16)); };
    { const char* xs = xb + k0 * 128 + wid * (16 * 1280);
      dma16(xo, xs, lds0 + XS0 + wid * 2048);
      dma16(xo ^ 64, xs + 8 * 1280, lds0 + XS0 + wid * 2048 + 1024);
      const char* wsrc = wb + (size_t)k0 * (64 * 3840);
      const unsigned wv = w_off(wid < 5 ? wid : wid - 5);
      if (wid < 5) { dma16(wv, wsrc, lds0 + WS0 + wid * 1024); dma16(wv, wsrc + 16 * 3840, lds0 + WS0 + (5 + wid) * 1024);
          if (wid >= 3) { dma16(wv, wsrc + 32 * 3840, lds0 + WS0 + (10 + wid) * 1024); dma16(wv, wsrc + 48 * 3840, lds0 + WS0 + (15 + wid) * 1024); } }
      else { dma16(wv, wsrc + 32 * 3840, lds0 + WS0 + (5 + wid) * 1024); dma16(wv, wsrc + 48 * 3840, lds0 + WS0 + (10 + wid) * 1024); } }
    if (tid < 80) *(LAS unsigned*)(smem + tid * 4) = 0u;
    const int nnb = isA ? (rt > 0 ? 1 : 0) : (rt > 1 ? 2 : rt);
    auto kq_base = [&](int fg) { const int hd = fg / 5, dg = fg % 5; return dg < 4 ? hd * 5120 + (dg >> 1) * 2048 + (dg & 1) * 8 : hd * 5120 + 4096; };

    auto rdtag = [&]() { const u64 t = *(LAS u64*)(smem + C_TAG);
        const u64 e = ((u64)(unsigned)__builtin_amdgcn_readfirstlane((int)(t >> 32)) << 32) | (unsigned)__builtin_amdgcn_readfirstlane((int)t);
        return e * 6364136223846793005ull + 1442695040888963407ull; };
    const __amdgpu_buffer_rsrc_t wor = __builtin_amdgcn_make_buffer_rsrc((void*)(isA ? oA : oB), 0, 640 * 1280, 0x00020000);

    if (wid < 4) {
        asm volatile("s_waitcnt vmcnt(0) lgkmcnt(0)\n\ts_barrier" ::: "memory");
        STAMP(1);
        bf16x8 xr[28], xt[2], wf[WIN + 1];
        f32x16 acc[5];
        const int tok = wid * 32 + r32;
        const int xa = XS0 + tok * 128 + ((hl ^ ((tok >> 1) & 7)) << 4);
        int wa0 = WS0 + (8 * hl + ((lane & 15) >> 2)) * 320 + (lane & 16) * 2 + (lane & 3) * 8, wa1 = wa0 + 61440;
        asm volatile("" : "+v"(wa0), "+v"(wa1));
        auto rdx = [&](int i) { return *(LAS bf16x8*)(smem + ((i >> 2) % 3) * 16384 + (xa ^ ((i & 3) * 32))); };
        auto rdw = [&](int f) { const int i = f / 5, t = f % 5, sl = (i >> 1) % 7;
            return trfrag(smem + (sl < 6 ? wa0 + sl * 10240 : wa1) + (i & 1) * 5120 + t * 64, 1280); };
        unsigned fl = 0;
        auto rdflag = [&](int u) { fl = __hip_atomic_load((LAS unsigned*)(smem + C_F + u * 4), RLX_WG); };
        auto check = [&](int u) {
            if (__builtin_amdgcn_readfirstlane(fl) != 15) {
#pragma unroll 1
                for (unsigned spins = 0; spins < LSPIN_CAP; ++spins) { rdflag(u); if (__builtin_amdgcn_readfirstlane(fl) == 15) break; __builtin_amdgcn_s_sleep(1); }
            }
            FENCE(); };
#pragma unroll
        for (int i = 0; i < 4; ++i) xr[i] = rdx(i);
#pragma unroll
        for (int f = 0; f < WIN; ++f) wf[f] = rdw(f);
        SB();
        SFOR(f, 200)
            constexpr int i = f / 5, t = f % 5;
            if constexpr (f % 20 == 10 - WIN) lds_set(smem + C_P + wid * 4, (i >> 1) + 1);
            if constexpr (f % 20 == 10) rdflag(f / 20 + 1);
            if constexpr (f % 20 == 20 - WIN && f < 190) check(f / 20 + 1);
            if constexpr (f + WIN < 200) wf[(f + WIN) % (WIN + 1)] = rdw(f + WIN);
            if constexpr (f % 20 >= 16 && f < 120) xr[(f / 20 + 1) * 4 + f % 20 - 16] = rdx((f / 20 + 1) * 4 + f % 20 - 16);
            if constexpr (t == 1 && i >= 27 && i < 39) xt[(i + 1) & 1] = rdx(i + 1);
            SB();
            if constexpr (i == 0) MFMA32Z(acc[t], wf[f % (WIN + 1)], xr[0]);
            else if constexpr (i < 28) MFMA32I(acc[t], wf[f % (WIN + 1)], xr[i]);
            else MFMA32I(acc[t], wf[f % (WIN + 1)], xt[i & 1]);
            SB();
        SEND
        MFMA_DRAIN();
        WAITV(0);
        STAMP(2);
#pragma unroll
        for (int t = 0; t < 5; ++t)
#pragma unroll
            for (int b = 0; b < 4; ++b) {
                const int g0 = 8 * t + 2 * b, g1 = g0 + 1;
                const int o0 = g0 < 20 ? kq_base(g0) + tok * (g0 % 5 < 4 ? 16 : 8) : VOFF + (g0 - 20) * 8 + tok * VP;
                const int o1 = g1 < 20 ? kq_base(g1) + tok * (g1 % 5 < 4 ? 16 : 8) : VOFF + (g1 - 20) * 8 + tok * VP;
                *(LAS u32x2*)(smem + OWN + (hl ? o1 : o0)) = (u32x2){pk2(acc[t][4 * b], acc[t][4 * b + 1]), pk2(acc[t][4 * b + 2], acc[t][4 * b + 3])};
            }
        if (hl == 0) *(LAS u32x2*)(smem + OWN + VOFF + tok * VP + 160) = (u32x2){0x3F803F80u, 0x3F803F80u};
        WAITL();
        lds_set(smem + C_E + wid * 4, 1);
        STAMP(3);
        int l2 = tid; asm volatile("" : "+v"(l2)); l2 &= 63;
        int qa0 = (8 * (l2 >> 5) + 2 * ((l2 & 15) >> 2)) * 160 + (l2 & 16) * 2 + (l2 & 3) * 8, qa1 = qa0 + WS0 + 10240;
        asm volatile("" : "+v"(qa0), "+v"(qa1));
        auto rdq = [&](int f) { const int i = f / 3, t = f % 3, j = i >> 2, sl = (20 + j) % 7;
            return trfrag(smem + (j >= 7 ? qa0 + XS0 + (j - 7) * 10240 : sl == 0 ? qa0 + WS0 : qa1 + (sl - 1) * 10240) + (i & 3) * 2560 + t * 64, 160); };
        check(10);
        xt[0] = rdx(28);
#pragma unroll
        for (int f = 0; f < WIN; ++f) wf[f] = rdq(f);
        SB();
        SFOR(f, 120)
            constexpr int i = f / 3, t = f % 3, j = i >> 2;
            if constexpr (f % 24 == 12 && j < 9) rdflag(11 + j / 2);
            if constexpr (f % 24 == 12 - WIN && (j == 2 || j >= 6)) lds_set(smem + C_P + wid * 4, 21 + j);
            if constexpr (f % 24 == 24 - WIN && j < 9) check(11 + j / 2);
            if constexpr (f + WIN < 120) wf[(f + WIN) % (WIN + 1)] = rdq(f + WIN);
            if constexpr (t == 0 && i < 11) xt[(i + 1) & 1] = rdx(28 + i + 1);
            SB();
            if constexpr (i == 0) MFMA32Z(acc[t], wf[f % (WIN + 1)], xt[0]);
            else if constexpr (i < 12) MFMA32I(acc[t], wf[f % (WIN + 1)], xt[i & 1]);
            else MFMA32I(acc[t], wf[f % (WIN + 1)], xr[i - 12]);
            SB();
        SEND
        MFMA_DRAIN();
        STAMP(4);
        wait_min4(smem + C_P, 29);
#pragma unroll
        for (int t = 0; t < 3; ++t)
#pragma unroll
            for (int b = 0; b < 4; ++b)
                if (t < 2 || b < 2) {
                    const int g0 = 8 * t + 2 * b, g1 = g0 + 1;
                    const int o0 = kq_base(g0) + tok * (g0 % 5 < 4 ? 16 : 8), o1 = kq_base(g1) + tok * (g1 % 5 < 4 ? 16 : 8);
                    *(LAS u32x2*)(smem + QST + (hl ? o1 : o0)) =
                        (u32x2){pk2(acc[t][4 * b] * QSCALE, acc[t][4 * b + 1] * QSCALE), pk2(acc[t][4 * b + 2] * QSCALE, acc[t][4 * b + 3] * QSCALE)};
                }
        STAMP(5);
    } else {
        const int hw = wid - 4;
        int known = 0;
        auto lds_or = [&](int off, unsigned v) { FENCE(); if (lane == 0) __hip_atomic_fetch_or((LAS unsigned*)(smem + off), v, RLX_WG); FENCE(); };
        unsigned wo[5], qo[5];
#pragma unroll
        for (int q = 0; q < 5; ++q) { wo[q] = w_off(q) + (hw & 1) * (16 * 3840);
            const int c = q * 64 + lane, rho = c / 10; qo[q] = (unsigned)(((hw & 1) * 32 + (rho & ~7) + 4 * (rho & 1) + ((rho >> 1) & 3)) * 3840 + (c % 10) * 16); }
        auto done = [&](int n) { lds_or(C_F + n * 4, 1u << hw); };
        auto round_x = [&](int n) { const char* xs = xb + kt_of(n) * 128 + hw * (32 * 1280); const unsigned dst = lds0 + XS0 + (n % 3) * 16384 + hw * 4096;
#pragma unroll
            for (int i = 0; i < 4; ++i) dma16(i & 1 ? xo ^ 64 : xo, xs + i * (8 * 1280), dst + i * 1024); };
        auto round_w = [&](int n) {
            const int u = 2 * n + (hw >> 1);
            const unsigned dst = lds0 + (u < 27 ? WS0 + (u % 7) * 10240 : XS0 + (u - 27) * 10240) + (hw & 1) * 5120;
            if (u < 20) { const char* src = wb + (size_t)(kt_of(u >> 1) * 64 + (u & 1) * 32) * 3840;
#pragma unroll
                for (int i = 0; i < 5; ++i) dma16(wo[i], src, dst + i * 1024);
            } else { const char* src = wb + (size_t)kt_of(u < 23 ? u - 13 : u - 23) * (64 * 3840);
#pragma unroll
                for (int i = 0; i < 5; ++i) dma16(qo[i], src, dst + i * 1024);
            } };
        round_x(1); round_w(1);
        WAITV(9);
        BAR_L();
        round_x(2); round_w(2);
#pragma unroll 1
        for (int n = 3; n <= 12; ++n) {
            const int need = 2 * n - 5;
            if (need > known) known = min4(smem + C_P);
            if (need > known) { if (n <= 10) WAITV(9); else WAITV(5);
                done(n - 2); WAITV(0); done(n - 1); known = wait_min4(smem + C_P, need); }
            if (n < 10) { round_x(n); WAITV(13); } else if (n == 10) WAITV(9); else WAITV(5);
            done(n - 2);
            round_w(n);
        }
        WAITV(5); done(11); WAITV(0); done(12);
        STAMP(1);
        const u64 tag = rdtag();
        auto a_chunk = [&](int a) { int o;
            if (a < 320) { const int hd = a / 80, rem = a % 80; o = hd * 5120 + (rem < 32 ? (96 + rem) * 16 : rem < 64 ? 2048 + (64 + rem) * 16 : 4096 + 768 + (rem - 64) * 16); }
            else o = VOFF + 96 * VP + (a - 320) * 16;
            return o; };
        if (rt < 15) {
            const __amdgpu_buffer_rsrc_t kvr = __builtin_amdgcn_make_buffer_rsrc(ws + WS_KV + me * KVG, 0, KVB, 0x00020000);
            wait_min4(smem + C_E, 1);
            STAMP(2);
            if (!isA) { u32x4 v[11];
#pragma unroll
                for (int j = 0; j < 11; ++j) if (j < 10 || hw == 3) v[j] = *(LAS u32x4*)(smem + OWN + hw * 10240 + j * 1024 + lane * 16);
#pragma unroll
                for (int j = 0; j < 11; ++j) if (j < 10 || hw == 3) __builtin_amdgcn_raw_buffer_store_b128(v[j], kvr, hw * 10240 + j * 1024 + lane * 16, 0, 16);
            } else {
#pragma unroll
                for (int j = 0; j < 3; ++j) { const int a = (4 * j + hw) * 64 + lane;
                    if (a < 656) { const int o = a_chunk(a); __builtin_amdgcn_raw_buffer_store_b128(*(LAS u32x4*)(smem + OWN + o), kvr, o, 0, 16); } }
            }
            STAMP(3);
            WAITV(0);
            unsigned old = 0;
            if (lane == 0) old = __hip_atomic_fetch_add((LAS unsigned*)(smem + C_CNT), 1u, RLX_WG);
            if (lane == 0 && old == 3) __hip_atomic_store((GAS u64*)(ws + WS_FKV + me * 128), tag, RLX_AGENT);
            STAMP(4);
        }
        wait_min4(smem + C_P, 23);
        round_w(13); round_w(14);
        WAITV(5); done(13); WAITV(0); done(14);
        poll_flags((GAS u64*)(ws + WS_FKV + (me - 8 * (lane + 1)) * 128), lane < nnb, tag);
        STAMP(5);
        if (!isA) {
            const int n = hw >> 1, half = hw & 1;
            if (n < nnb) {
                const __amdgpu_buffer_rsrc_t nbr = __builtin_amdgcn_make_buffer_rsrc(ws + WS_KV + (me - 8 * (n + 1)) * KVG, 0, KVB, 0x00020000);
                u32x4 v[21];
#pragma unroll
                for (int j = 0; j < 21; ++j) if (j < 20 || half) v[j] = __builtin_amdgcn_raw_buffer_load_b128(nbr, half * 20480 + j * 1024 + lane * 16, 0, 16);
                wait_min4(smem + C_P, 27);
#pragma unroll
                for (int j = 0; j < 21; ++j) if (j < 20 || half) *(LAS u32x4*)(smem + KV0 + (1 - n) * KVB + half * 20480 + j * 1024 + lane * 16) = v[j];
            }
        } else if (nnb) {
            const __amdgpu_buffer_rsrc_t nbr = __builtin_amdgcn_make_buffer_rsrc(ws + WS_KV + (me - 8) * KVG, 0, KVB, 0x00020000);
            u32x4 v[3];
#pragma unroll
            for (int j = 0; j < 3; ++j) { const int a = (4 * j + hw) * 64 + lane; if (a < 656) v[j] = __builtin_amdgcn_raw_buffer_load_b128(nbr, a_chunk(a), 0, 16); }
            wait_min4(smem + C_P, 27);
#pragma unroll
            for (int j = 0; j < 3; ++j) { const int a = (4 * j + hw) * 64 + lane; if (a < 656) *(LAS u32x4*)(smem + KV0 + KVB + a_chunk(a)) = v[j]; }
        }
        if (hw == 3) { wait_min4(smem + C_P, 27); if (lane < 2) *(LAS u32x2*)(smem + ONE + lane * 5120) = (u32x2){0x3F803F80u, 0x3F803F80u}; }
        STAMP(6);
    }
    const int qi = wid & 3, hp = wid >> 2;
    const int ud = qi + 8;
    const bool far = isA ? (4 * rt + qi >= 1) : (4 * rt + qi >= 8);
    const int u0 = isA ? (far ? ud - 1 : ud) : (far ? qi : 8 - 4 * rt);
    f32x16 cd, cf;
    auto masks = [&](int q) {
      const unsigned low = (2u << q) - 1u;
      unsigned vd = low, vf = ~low;
      if (isA) { const u64 w = 0x5555ull << (q + 18);
          vd = (unsigned)(w >> 32); vf = (unsigned)w; }
      if (!far) vf = ~0u;
      const unsigned id = ~vd >> (4 * hl), jf = ~vf >> (4 * hl);
#pragma unroll
      for (int r = 0; r < 16; ++r) {
          const int c0 = (r & 3) + 8 * (r >> 2);
          cd[r] = __uint_as_float((unsigned)__builtin_amdgcn_sbfe((int)id, c0, 1) & 0xFF800000u);
          cf[r] = __uint_as_float((unsigned)__builtin_amdgcn_sbfe((int)jf, c0, 1) & 0xFF800000u);
      } };
    masks(r32);
    unsigned oo[5]; u32x4 wreg[13];
#pragma unroll
    for (int q = 0; q < 5; ++q) { const int c = q * 64 + lane, rho = c / 10;
        oo[q] = (unsigned)((wid * 80 + (rho & ~7) + 4 * (rho & 1) + ((rho >> 1) & 3)) * 1280 + hg * 160 + (c % 10) * 16); }
    auto ldwo = [&](int i) { wreg[i] = __builtin_amdgcn_raw_buffer_load_b128(wor, oo[i % 5] + (i / 5) * (32 * 1280), 0, 0); };
    BAR_L();
    STAMP(7);
    u32x2 ob[2][3];
    {
        bf16x8 q1[2]; u32x2 q2[2];
#pragma unroll
        for (int e = 0; e < 2; ++e) {
            q1[e] = *(LAS bf16x8*)(smem + QST + (2 * hp + e) * 5120 + hl * 2048 + (qi * 32 + r32) * 16);
            q2[e] = *(LAS u32x2*)(smem + QST + (2 * hp + e) * 5120 + 4096 + (qi * 32 + r32) * 8);
            if (hl) q2[e] = (u32x2){0u, 0u};
        }
        const int kb1 = KV0 + hp * 10240 + hl * 2048 + r32 * 16;
        const int kb2 = hl ? ONE : KV0 + hp * 10240 + 4096 + r32 * 8, nh = hl ^ 1;
        const int vrow = KV0 + VOFF + (4 * hl + ((lane & 15) >> 2)) * VP;
        const bool ones = (lane & 16) && (lane & 3);
        const int vb0 = vrow + (ones ? 160 : hp * 80 + (lane & 16) * 2 + (lane & 3) * 8), vb1 = vb0 + (ones ? 0 : 40);
        struct KF { bf16x8 a; s16x4 b; };
        struct VF { bf16x8 a, b; };
        auto rdk = [&](int u, int e) { const int so = (u >> 2) * KVB, tw = u & 3; KF k;
            k.a = *(LAS bf16x8*)(smem + kb1 + so + tw * 512 + e * 5120); k.b = *(LAS s16x4*)(smem + __umul24(so + tw * 256, nh) + kb2 + e * 5120); return k; };
        auto rdv = [&](int u, int e) { LAS char* p = smem + (e ? vb1 : vb0) + (u >> 2) * KVB + (u & 3) * (32 * VP); VF v;
            v.a = trfrag(p, 8 * VP); v.b = trfrag(p + 16 * VP, 8 * VP); return v; };
        auto qk = [&](const KF& k, int e, const f32x16& c) { return MFMA32K8(k.b, __builtin_bit_cast(s16x4, q2[e]), MFMA32(k.a, q1[e], c)); };
        const f32x16 zero = {};
        auto max16 = [&](const f32x16& s, float m) {
#pragma unroll
            for (int r = 0; r < 16; ++r) m = __builtin_elementwise_maximum(m, s[r]);
            return m; };
        auto set_ref = [&](float m, int e) { const unsigned nb = pk2(-m, 0.f); if (hl) q2[e][0] = nb; };
        auto ex4 = [&](f32x16& s, int b) {
#pragma unroll
            for (int r = 0; r < 4; ++r) s[b + r] = __builtin_amdgcn_exp2f(s[b + r]); };
        auto pk8 = [&](const f32x16& s, int b) { return __builtin_bit_cast(bf16x8, (u32x4){pk2(s[b], s[b + 1]), pk2(s[b + 2], s[b + 3]), pk2(s[b + 4], s[b + 5]), pk2(s[b + 6], s[b + 7])}); };
        auto phase = [&](auto nx, f32x16& cur, const VF& v, f32x16& o, f32x16& nxt, const KF& k, int e) __attribute__((always_inline)) {
            constexpr int NX = decltype(nx)::value;
            SB(); ex4(cur, 0); SB();
            if constexpr (NX) nxt = MFMA32(k.a, q1[e], NX == 1 ? zero : NX == 2 ? cf : cd);
            SB(); ex4(cur, 4); { const bf16x8 p = pk8(cur, 0); SB(); o = MFMA32(v.a, p, o); }
            SB(); ex4(cur, 8); SB();
            if constexpr (NX) nxt = MFMA32K8(k.b, __builtin_bit_cast(s16x4, q2[e]), nxt);
            SB(); ex4(cur, 12); { const bf16x8 p = pk8(cur, 8); SB(); o = MFMA32(v.b, p, o); }
            SB(); };
        f32x16 o0, o1, sa, sb; KF kx; VF vx;
        auto step = [&](auto m0, auto m1, int u) __attribute__((always_inline)) {
            constexpr int M1 = decltype(m1)::value;
            KF ky; if constexpr (M1) ky = rdk(u + 1, 0);
            const VF vy = rdv(u, 1);
            phase(m0, sa, vx, o0, sb, kx, 1);
            if constexpr (M1) { kx = rdk(u + 1, 1); vx = rdv(u + 1, 0); }
            phase(m1, sb, vy, o1, sa, ky, 0); };
        auto run = [&](auto fast) __attribute__((always_inline)) {
            o0 = zero; o1 = zero;
            kx = rdk(u0, 1); vx = rdv(u0, 0);
            if constexpr (decltype(fast)::value) {
                sa = qk(rdk(u0, 0), 0, cf);
                SFOR(it, 9)
                    if constexpr (it < 7) { ldwo(2 * it); if constexpr (it < 6) ldwo(2 * it + 1); }
                    step(IC<it == 0 ? 2 : it == 8 ? 3 : 1>{}, IC<it == 8 ? 0 : it == 7 ? 3 : 1>{}, u0 + it);
                SEND
            } else {
                if (u0 < ud) {
                    sa = qk(rdk(u0, 0), 0, cf);
                    if (u0 + 1 < ud) {
                        step(IC<2>{}, IC<1>{}, u0);
#pragma unroll 1
                        for (int u = u0 + 1; u + 1 < ud; ++u) step(IC<1>{}, IC<1>{}, u);
                        step(IC<1>{}, IC<3>{}, ud - 1);
                    } else step(IC<2>{}, IC<3>{}, u0);
                } else sa = qk(rdk(ud, 0), 0, cd);
                step(IC<3>{}, IC<0>{}, ud);
            } };
        { const f32x16 s0 = qk(rdk(ud, 0), 0, cd), s1 = qk(rdk(ud, 1), 1, cd);
          set_ref(halfmax(max16(s0, s0[0])), 0); set_ref(halfmax(max16(s1, s1[0])), 1); }
        if (u0 + 8 == ud) run(IC<1>{});
        else {
#pragma unroll
            for (int i = 0; i < 13; ++i) ldwo(i);
            run(IC<0>{});
        }
        if (!__all(o0[12] < 1.152921504606847e18f && o1[12] < 1.152921504606847e18f)) {
            { int q = r32; asm volatile("" : "+v"(q)); masks(q); }
#pragma unroll
            for (int e = 0; e < 2; ++e) {
                if (hl) q2[e][0] = 0u;
                const f32x16 s = qk(rdk(ud, e), e, cd);
                float m = max16(s, s[0]);
                if (u0 < ud) m = max16(qk(rdk(u0, e), e, cf), m);
#pragma unroll 1
                for (int u = u0 + 1; u < ud; ++u) m = max16(qk(rdk(u, e), e, zero), m);
                set_ref(halfmax(m), e);
            }
#pragma unroll
            for (int e = 0; e < 2; ++e) {
                f32x16 o = zero;
#pragma unroll 1
                for (int u = u0; u <= ud; ++u) { f32x16 t = u == ud ? qk(rdk(u, e), e, cd) : u == u0 ? qk(rdk(u, e), e, cf) : qk(rdk(u, e), e, zero);
                    const VF v = rdv(u, e); ex4(t, 0); ex4(t, 4); ex4(t, 8); ex4(t, 12);
                    o = MFMA32(v.a, pk8(t, 0), o); o = MFMA32(v.b, pk8(t, 8), o); }
                if (e) o1 = o; else o0 = o;
            }
        }
        const float rl0 = __builtin_amdgcn_rcpf(o0[12]), rl1 = __builtin_amdgcn_rcpf(o1[12]);
#pragma unroll
        for (int j = 0; j < 3; ++j) {
            ob[0][j] = (u32x2){pk2(o0[4 * j] * rl0, o0[4 * j + 1] * rl0), pk2(o0[4 * j + 2] * rl0, o0[4 * j + 3] * rl0)};
            ob[1][j] = (u32x2){pk2(o1[4 * j] * rl1, o1[4 * j + 1] * rl1), pk2(o1[4 * j + 2] * rl1, o1[4 * j + 3] * rl1)}; }
    }
    STAMP(8);
    const __amdgpu_buffer_rsrc_t orr = __builtin_amdgcn_make_buffer_rsrc(ws + WS_O + (br * 16 + rt) * OG, 0, 4 * 40960, 0x00020000);
    { const unsigned obase = (unsigned)(qi * 40960 + (hg * 10 + hp * 5) * 512 + r32 * 16);
      { auto rx = __builtin_amdgcn_permlane32_swap(ob[0][0][0], ob[0][1][0], false, false);
        auto ry = __builtin_amdgcn_permlane32_swap(ob[0][0][1], ob[0][1][1], false, false);
        __builtin_amdgcn_raw_buffer_store_b128((u32x4){rx[0], ry[0], rx[1], ry[1]}, orr, obase + hl * 512, 0, 16); }
      if (hl == 0) __builtin_amdgcn_raw_buffer_store_b128((u32x4){ob[0][2][0], ob[0][2][1], ob[1][0][0], ob[1][0][1]}, orr, obase + 1024, 0, 16);
      { auto fx = __builtin_amdgcn_permlane32_swap(ob[1][0][0], ob[1][1][0], false, false);
        auto fy = __builtin_amdgcn_permlane32_swap(ob[1][0][1], ob[1][1][1], false, false);
        auto sx = __builtin_amdgcn_permlane32_swap(ob[1][1][0], ob[1][2][0], false, false);
        auto sy = __builtin_amdgcn_permlane32_swap(ob[1][1][1], ob[1][2][1], false, false);
        __builtin_amdgcn_raw_buffer_store_b128((u32x4){fx[1], fy[1], sx[0], sy[0]}, orr, obase + 1536 + hl * 512, 0, 16); } }
    const u64 tag = rdtag();
    WAITV(0);
    { unsigned old = 0;
      if (lane == 0) old = __hip_atomic_fetch_add((LAS unsigned*)(smem + C_CNT + 4), 1u, RLX_WG);
      if (lane == 0 && old == 7) __hip_atomic_store((GAS u64*)(ws + WS_FO + me * 128), tag, RLX_AGENT); }
    STAMP(9);
    BAR_L();
#pragma unroll
    for (int i = 0; i < 13; ++i) if (i < 12 || lane < 32) *(LAS u32x4*)(smem + WOB + wid * 12800 + i * 1024 + lane * 16) = wreg[i];
    BAR_L();
    STAMP(10);
    poll_flags((GAS u64*)(ws + WS_FO + ((me & ~7) + (lane & 7)) * 128), lane < 8, tag);
    STAMP(11);
    f32x16 oc[3];
    { bf16x8 af[5], of[20];
      const int aa = WOB + hp * 51200 + (8 * hl + 2 * ((lane & 15) >> 2)) * 160 + (lane & 16) * 2 + (lane & 3) * 8;
      auto rda = [&](int f) { return trfrag(smem + aa + (f / 3) * 2560 + (f % 3) * 64, 160); };
      auto ldo = [&](int k) { of[k] = __builtin_bit_cast(bf16x8, __builtin_amdgcn_raw_buffer_load_b128(orr, qi * 40960 + (hp * 20 + k) * 1024 + lane * 16, 0, 16)); };
#pragma unroll
      for (int k = 0; k < OPF; ++k) ldo(k);
#pragma unroll
      for (int f = 0; f < 4; ++f) af[f] = rda(f);
      SB();
      SFOR(f, 60)
          if constexpr (f % 3 == 0 && f / 3 + OPF < 20) ldo(f / 3 + OPF);
          if constexpr (f + 4 < 60) af[(f + 4) % 5] = rda(f + 4);
          SB();
          if constexpr (f < 3) MFMA32Z(oc[f], af[f % 5], of[0]); else MFMA32I(oc[f % 3], af[f % 5], of[f / 3]);
          SB();
      SEND }
    MFMA_DRAIN();
    STAMP(12);
    { LAS char* mine = smem + RED + wid * 6144 + lane * 16; LAS char* other = smem + RED + (wid ^ 4) * 6144 + lane * 16;
      if (hp) {
#pragma unroll
          for (int b = 0; b < 4; ++b) *(LAS f32x4*)(mine + b * 1024) = (f32x4){oc[0][4 * b], oc[0][4 * b + 1], oc[0][4 * b + 2], oc[0][4 * b + 3]};
#pragma unroll
          for (int b = 0; b < 2; ++b) *(LAS f32x4*)(mine + (4 + b) * 1024) = (f32x4){oc[2][4 * b], oc[2][4 * b + 1], oc[2][4 * b + 2], oc[2][4 * b + 3]};
      } else {
#pragma unroll
          for (int b = 0; b < 4; ++b) *(LAS f32x4*)(mine + b * 1024) = (f32x4){oc[1][4 * b], oc[1][4 * b + 1], oc[1][4 * b + 2], oc[1][4 * b + 3]};
      }
      BAR_L();
      char* orow = out + (size_t)(rt * 128 + qi * 32 + r32) * 2560 + br * 1280 + hg * 160 + hl * 16;
      auto fin = [&](const f32x16& c, int b, int slot) { const f32x4 x = *(LAS f32x4*)(other + slot * 1024);
          return (u32x2){pk2(c[4 * b] + x[0], c[4 * b + 1] + x[1]), pk2(c[4 * b + 2] + x[2], c[4 * b + 3] + x[3])}; };
      auto st2 = [&](u32x2 a, u32x2 b, int col) {
          auto rx = __builtin_amdgcn_permlane32_swap(a[0], b[0], false, false); auto ry = __builtin_amdgcn_permlane32_swap(a[1], b[1], false, false);
          *(u32x4*)(orow + col * 2) = (u32x4){rx[0], ry[0], rx[1], ry[1]}; };
      if (hp) { st2(fin(oc[1], 0, 0), fin(oc[1], 1, 1), 32); st2(fin(oc[1], 2, 2), fin(oc[1], 3, 3), 48); }
      else { st2(fin(oc[0], 0, 0), fin(oc[0], 1, 1), 0); st2(fin(oc[0], 2, 2), fin(oc[0], 3, 3), 16); st2(fin(oc[2], 0, 4), fin(oc[2], 1, 5), 64); } }
    if (bx == 0) {
        if (wid < 4) poll_flags((GAS u64*)(ws + WS_FO + tid * 128), true, tag);
        __syncthreads();
        if (tid == 0) __hip_atomic_store((GAS u64*)ws, tag, RLX_AGENT);
    }
}

extern "C" void kernel_launch(void* const* d_in, const int* in_sizes, int n_in, void* d_out, int out_size, void* d_ws, size_t ws_size,
                              hipStream_t stream) {
    fused_attn_kernel<<<256, 512, 0, stream>>>((const char*)d_in[0], (const char*)d_in[1], (const char*)d_in[2], (const char*)d_in[3],
                                               (const char*)d_in[4], (const char*)d_in[5], (char*)d_out, (char*)d_ws);
}
```

```cpp
#define ADELAY 3
#define OPF 12
#include <hip/hip_runtime.h>
#include <hip/hip_bf16.h>
#include <stdint.h>

typedef short bf16x8 __attribute__((ext_vector_type(8)));
typedef short s16x4 __attribute__((ext_vector_type(4)));
typedef float f32x2 __attribute__((ext_vector_type(2)));
typedef float f32x4 __attribute__((ext_vector_type(4)));
typedef float f32x16 __attribute__((ext_vector_type(16)));
typedef unsigned u32x4 __attribute__((ext_vector_type(4)));
typedef unsigned u32x2 __attribute__((ext_vector_type(2)));
typedef __bf16 bfx2 __attribute__((ext_vector_type(2)));
typedef unsigned long long u64;
#define LAS __attribute__((address_space(3)))
#define GAS __attribute__((address_space(1)))
#define DI __device__ __forceinline__
#define RLX_AGENT __ATOMIC_RELAXED, __HIP_MEMORY_SCOPE_AGENT
#define RLX_WG __ATOMIC_RELAXED, __HIP_MEMORY_SCOPE_WORKGROUP

constexpr int LDS_BYTES = 163840;
constexpr int VOFF = 20480, VP = 168;
constexpr int KVB = VOFF + 128 * VP;
constexpr int KV0 = LDS_BYTES - 3 * KVB;
constexpr int OWN = KV0 + 2 * KVB;
constexpr int C_F = 0, C_P = 128, C_E = 144, C_QF = 160, C_RD = 176, C_CNT = 192, C_TAG = 320;
constexpr int N_PUB = C_CNT, N_NB = C_CNT + 4, N_G0 = C_CNT + 8, N_G1 = C_CNT + 12, N_WO = C_CNT + 16, N_OS = C_CNT + 24;
constexpr int XS0 = 512, WS0 = XS0 + 3 * 16384, QST = 512, ONE = 336;
static_assert(WS0 + 7 * 10240 <= OWN, "ring overlaps the own image");
constexpr unsigned WS_FKV = 4096, WS_FO = WS_FKV + 256 * 128, WS_KV = 131072;
constexpr unsigned KVG = KVB + 128, OG = 4 * 40960 + 128;
constexpr unsigned WS_O = WS_KV + 256 * KVG;
constexpr unsigned WS_END = WS_O + 32 * OG;
constexpr unsigned WS_DBG = WS_END;
constexpr unsigned SPIN_CAP = 400000, LSPIN_CAP = 4000000;
constexpr float QSCALE = 0.22360679774997896f * 1.4426950408889634f;
#define ABL4N 2
#define WIN 5

#define MFMA32(a, b, c) __builtin_amdgcn_mfma_f32_32x32x16_bf16((a), (b), (c), 0, 0, 0)
#define MFMA32K8(a, b, c) __builtin_amdgcn_mfma_f32_32x32x8bf16_1k((a), (b), (c), 0, 0, 0)
#define MFMA32I(acc, a, b) asm volatile("v_mfma_f32_32x32x16_bf16 %0, %1, %2, %0" : "+v"(acc) : "v"(a), "v"(b))
#define MFMA32Z(acc, a, b) asm volatile("v_mfma_f32_32x32x16_bf16 %0, %1, %2, 0" : "=&v"(acc) : "v"(a), "v"(b))
#define MFMA_DRAIN() asm volatile("s_nop 7\n\ts_nop 4" ::: "memory")
#define WAITV(N) asm volatile("s_waitcnt vmcnt(" #N ")" ::: "memory")
#define WAITL() asm volatile("s_waitcnt lgkmcnt(0)" ::: "memory")
#define BAR_L() asm volatile("s_waitcnt lgkmcnt(0)\n\ts_barrier" ::: "memory")
#define SB() __builtin_amdgcn_sched_barrier(0)
#define FENCE() asm volatile("" ::: "memory")

#define STAMP(i) do {} while (0)

template <int V> struct IC { static constexpr int value = V; };
template <int I, int N, class F> DI void static_for(F&& f) { if constexpr (I < N) { f(IC<I>{}); static_for<I + 1, N>(f); } }
#define SFOR(var, N) static_for<0, N>([&](auto ic_##var) __attribute__((always_inline)) { constexpr int var = decltype(ic_##var)::value;
#define SEND });

DI void dma16(unsigned voff, const char* sbase, unsigned ldsaddr) {
    asm volatile("s_mov_b32 m0, %2\n\ts_nop 0\n\tglobal_load_lds_dwordx4 %0, %1" :: "v"(voff), "s"(sbase), "s"(ldsaddr) : "memory");
}
DI void dma16x4(unsigned v0, unsigned v1, unsigned v2, unsigned v3, const char* sbase, unsigned ldsaddr) {
    asm volatile("s_mov_b32 m0, %5\n\ts_nop 0\n\tglobal_load_lds_dwordx4 %0, %4\n\tglobal_load_lds_dwordx4 %1, %4 offset:1024\n\t"
                 "global_load_lds_dwordx4 %2, %4 offset:2048\n\tglobal_load_lds_dwordx4 %3, %4 offset:3072"
                 :: "v"(v0), "v"(v1), "v"(v2), "v"(v3), "s"(sbase), "s"(ldsaddr) : "memory");
}
DI void dma16x5(const unsigned (&v)[5], const char* sbase, unsigned ldsaddr) {
    asm volatile("s_mov_b32 m0, %6\n\ts_nop 0\n\tglobal_load_lds_dwordx4 %0, %5 offset:-2048\n\tglobal_load_lds_dwordx4 %1, %5 offset:-1024\n\t"
                 "global_load_lds_dwordx4 %2, %5\n\tglobal_load_lds_dwordx4 %3, %5 offset:1024\n\tglobal_load_lds_dwordx4 %4, %5 offset:2048"
                 :: "v"(v[0]), "v"(v[1]), "v"(v[2]), "v"(v[3]), "v"(v[4]), "s"(sbase), "s"(ldsaddr) : "memory");
}
DI void dma4(const char* p, unsigned ldsaddr) {
    asm volatile("s_mov_b32 m0, %1\n\ts_nop 0\n\tglobal_load_lds_dword %0, off" :: "v"(p), "s"(ldsaddr) : "memory");
}
DI unsigned pk2(float lo, float hi) { f32x2 v = {lo, hi}; return __builtin_bit_cast(unsigned, __builtin_convertvector(v, bfx2)); }
typedef short v4i16_t __attribute__((ext_vector_type(4)));
DI bf16x8 trfrag(LAS char* p, int second) {
    s16x4 a = __builtin_bit_cast(s16x4, __builtin_amdgcn_ds_read_tr16_b64_v4i16((LAS v4i16_t*)p));
    s16x4 b = __builtin_bit_cast(s16x4, __builtin_amdgcn_ds_read_tr16_b64_v4i16((LAS v4i16_t*)(p + second)));
    return __builtin_shufflevector(a, b, 0, 1, 2, 3, 4, 5, 6, 7);
}
DI float halfmax(float m) { auto rr = __builtin_amdgcn_permlane32_swap(__float_as_uint(m), __float_as_uint(m), false, false);
    return __builtin_elementwise_maximum(__uint_as_float(rr[0]), __uint_as_float(rr[1])); }

DI void poll_flags(GAS u64* word, bool active, u64 tag) {
#pragma unroll 1
    for (unsigned spins = 0; spins < SPIN_CAP; ++spins) {
        u64 v = tag;
        if (active) v = __hip_atomic_load(word, RLX_AGENT);
        if (__all(v == tag)) break;
        __builtin_amdgcn_s_sleep(1);
    }
    __builtin_amdgcn_fence(__ATOMIC_ACQUIRE, "wavefront");
}
DI int min4(LAS char* p) {
    const u64 a = __hip_atomic_load((LAS u64*)p, RLX_WG), b = __hip_atomic_load((LAS u64*)(p + 8), RLX_WG);
    const unsigned m = min(min((unsigned)a, (unsigned)(a >> 32)), min((unsigned)b, (unsigned)(b >> 32)));
    return __builtin_amdgcn_readfirstlane((int)m);
}
DI int wait_min4(LAS char* p, int need) {
    int v = min4(p);
#pragma unroll 1
    for (unsigned spins = 0; v < need && spins < LSPIN_CAP; ++spins) { __builtin_amdgcn_s_sleep(1); v = min4(p); }
    FENCE();
    return v;
}
DI void wait_word(LAS char* p, unsigned need) {
#pragma unroll 1
    for (unsigned spins = 0; spins < LSPIN_CAP; ++spins) {
        if ((unsigned)__builtin_amdgcn_readfirstlane((int)__hip_atomic_load((LAS unsigned*)p, RLX_WG)) >= need) break;
        __builtin_amdgcn_s_sleep(1); }
    FENCE();
}
DI void lds_inc0(LAS char* p, int lane) { FENCE(); if (lane == 0) (void)__hip_atomic_fetch_add((LAS unsigned*)p, 1u, RLX_WG); FENCE(); }
DI void wait_pair(LAS char* p, unsigned need) {
#pragma unroll 1
    for (unsigned spins = 0; spins < LSPIN_CAP; ++spins) { const u64 v = __hip_atomic_load((LAS u64*)p, RLX_WG);
        if ((unsigned)__builtin_amdgcn_readfirstlane((int)min((unsigned)v, (unsigned)(v >> 32))) >= need) break;
        __builtin_amdgcn_s_sleep(1); }
    FENCE();
}
DI unsigned lds_inc(LAS char* p, int lane) { FENCE(); unsigned old = 0; if (lane == 0) old = __hip_atomic_fetch_add((LAS unsigned*)p, 1u, RLX_WG); FENCE(); return old; }
DI void lds_set(LAS char* p, unsigned v) { FENCE(); __hip_atomic_store((LAS unsigned*)p, v, RLX_WG); FENCE(); }

__global__ __launch_bounds__(512, 2) void fused_attn_kernel(const char* __restrict__ xA, const char* __restrict__ wA, const char* __restrict__ oA,
                                                           const char* __restrict__ xB, const char* __restrict__ wB, const char* __restrict__ oB,
                                                           char* __restrict__ out, char* __restrict__ ws) {
    __shared__ __attribute__((aligned(16))) char smem_[LDS_BYTES];
    LAS char* smem = (LAS char*)smem_;
    const unsigned lds0 = (unsigned)(uintptr_t)smem;
    STAMP(0);
    const int tid = threadIdx.x, lane = tid & 63;
    const int wid = __builtin_amdgcn_readfirstlane(tid >> 6);
    const int bx = blockIdx.x, xcd = bx & 7, idx = bx >> 3;
    const int isA = idx >> 4, rt = (xcd >> 1) * 4 + ((idx >> 2) & 3), hg = (xcd & 1) * 4 + (idx & 3);
    const int br = isA ? 0 : 1;
    const int k0 = isA ? (((rt & 3) * 5 + 1) / 2 + ((hg & 3) * 5 + 1) / 2) % 10 : 0;
    const char* xb = (isA ? xA : xB) + (size_t)rt * (128 * 1280);
    const char* wb = (isA ? wA : wB) + hg * 160;
    const int me = (br * 16 + rt) * 8 + hg;
    const int r32 = lane & 31, hl = lane >> 5;
    auto kt_of = [&](int s) { int k = s + k0; return k >= 10 ? k - 10 : k; };
    if (isA) {
        const int j16 = idx & 15;
        const char* wl = wB + (hg >> 2) * 640 + j16 * (4 * 3840);
        const char* xl = xB + (size_t)(rt * 128 + (hg & 3) * 32 + r32) * 1280 + hl * 128;
        auto pfx = [&](int i) { return xl + i * 256; };
        auto pfkv = [&](int i) { int n = i * 64 + lane; n = n < 400 ? n : 399; const int sl = n / 40, r = n % 40, c = r % 10;
            return wl + (sl * 64 + r / 10) * 3840 + 1280 + (c < 5 ? c * 128 : 640 + c * 128); };
        auto pfq = [&](int i) { int n = i * 64 + lane; n = n < 200 ? n : 199; int sl = n / 20 + 7; sl = sl >= 10 ? sl - 10 : sl; const int r = n % 20;
            return wl + (sl * 64 + r / 5) * 3840 + (r % 5) * 128; };
        dma4(wid & 1 ? pfx(wid >> 1) : pfkv(wid >> 1), lds0 + OWN + wid * 256);
        dma4(wid < 3 ? pfkv(4 + wid) : wid == 3 ? pfx(4) : pfq(wid - 4), lds0 + OWN + wid * 256);
        for (int i = 0; i < ADELAY; ++i) __builtin_amdgcn_s_sleep(16);
    }

    if (wid == 4 && lane < 2) dma4(ws + lane * 4, lds0 + C_TAG);
    if (wid == 7) { const char* p = ws + 2048;
        if (lane < 6) { const int im = me - 8 * (lane >> 1); if (im >= 0) p = ws + WS_KV + im * KVG + ((lane & 1) ? KVB : -128); }
        else if (lane < 8) p = ws + WS_O + (br * 16 + rt) * OG + ((lane & 1) ? (int)OG - 128 : -128);
        else if (lane < 10) p = (isA ? oA : oB) + (lane & 1) * (639 * 1280);
        else if (lane < 12) p = out + (size_t)(rt * 128 + (lane & 1) * 127) * 2560;
        dma4(p, lds0 + OWN); }
    const unsigned xo = (unsigned)((lane >> 3) * 1280 + (((lane & 7) ^ (lane >> 4)) << 4));
    auto w_off = [&](int q) { const int c = q * 64 + lane, row = c / 20, j = c % 20; return (unsigned)(row * 3840 + (j < 10 ? 1280 + j * 16 : 2400 + j * 16)); };
    { const char* xs = xb + k0 * 128 + wid * (16 * 1280);
      dma16(xo, xs, lds0 + XS0 + wid * 2048);
      dma16(xo ^ 64, xs + 8 * 1280, lds0 + XS0 + wid * 2048 + 1024);
      const char* wsrc = wb + (size_t)k0 * (64 * 3840);
      const unsigned wv = w_off(wid < 5 ? wid : wid - 5);
      if (wid < 5) { dma16(wv, wsrc, lds0 + WS0 + wid * 1024); dma16(wv, wsrc + 16 * 3840, lds0 + WS0 + (5 + wid) * 1024);
          if (wid >= 3) { dma16(wv, wsrc + 32 * 3840, lds0 + WS0 + (10 + wid) * 1024); dma16(wv, wsrc + 48 * 3840, lds0 + WS0 + (15 + wid) * 1024); } }
      else { dma16(wv, wsrc + 32 * 3840, lds0 + WS0 + (5 + wid) * 1024); dma16(wv, wsrc + 48 * 3840, lds0 + WS0 + (10 + wid) * 1024); } }
    if (tid < 80) *(LAS unsigned*)(smem + tid * 4) = 0u;
    else if (tid < 82) *(LAS unsigned*)(smem + ONE + (tid - 80) * 4) = 0x3F803F80u;
    const int nnb = isA ? (rt > 0 ? 1 : 0) : (rt > 1 ? 2 : rt);
    auto kq_base = [&](int fg) { const int hd = fg / 5, dg = fg % 5; return dg < 4 ? hd * 5120 + (dg >> 1) * 2048 + (dg & 1) * 8 : hd * 5120 + 4096; };

    auto rdtag = [&]() { const u64 t = *(LAS u64*)(smem + C_TAG);
        const u64 e = ((u64)(unsigned)__builtin_amdgcn_readfirstlane((int)(t >> 32)) << 32) | (unsigned)__builtin_amdgcn_readfirstlane((int)t);
        return e * 6364136223846793005ull + 1442695040888963407ull; };
    const __amdgpu_buffer_rsrc_t wor = __builtin_amdgcn_make_buffer_rsrc((void*)(isA ? oA : oB), 0, 640 * 1280, 0x00020000);

    if (wid < 4) {
        asm volatile("s_waitcnt vmcnt(0) lgkmcnt(0)\n\ts_barrier" ::: "memory");
        STAMP(1);
        bf16x8 xr[28], xt[2], wf[WIN + 1];
        f32x16 acc[5];
        const int tok = wid * 32 + r32;
        const int xa = XS0 + tok * 128 + ((hl ^ ((tok >> 1) & 7)) << 4);
        int wa0 = WS0 + (8 * hl + ((lane & 15) >> 2)) * 320 + (lane & 16) * 2 + (lane & 3) * 8, wa1 = wa0 + 61440;
        asm volatile("" : "+v"(wa0), "+v"(wa1));
        auto rdx = [&](int i) { return *(LAS bf16x8*)(smem + ((i >> 2) % 3) * 16384 + (xa ^ ((i & 3) * 32))); };
        auto rdw = [&](int f) { const int i = f / 5, t = f % 5, sl = (i >> 1) % 7;
            return trfrag(smem + (sl < 6 ? wa0 + sl * 10240 : wa1) + (i & 1) * 5120 + t * 64, 1280); };
        unsigned fl = 0;
        auto rdflag = [&](int u) { fl = __hip_atomic_load((LAS unsigned*)(smem + C_F + u * 4), RLX_WG); };
        auto check = [&](int u) {
            if (__builtin_amdgcn_readfirstlane(fl) != 15) {
#pragma unroll 1
                for (unsigned spins = 0; spins < LSPIN_CAP; ++spins) { rdflag(u); if (__builtin_amdgcn_readfirstlane(fl) == 15) break; __builtin_amdgcn_s_sleep(1); }
            }
            FENCE(); };
#pragma unroll
        for (int i = 0; i < 4; ++i) xr[i] = rdx(i);
#pragma unroll
        for (int f = 0; f < WIN; ++f) wf[f] = rdw(f);
        SB();
        SFOR(f, 200)
            constexpr int i = f / 5, t = f % 5;
            if constexpr (f % 20 == 10 - WIN) lds_set(smem + C_P + wid * 4, (i >> 1) + 1);
            if constexpr (f % 20 == 10) rdflag(f / 20 + 1);
            if constexpr (f % 20 == 20 - WIN && f < 190) check(f / 20 + 1);
            if constexpr (f + WIN < 200) wf[(f + WIN) % (WIN + 1)] = rdw(f + WIN);
            if constexpr (f % 20 >= 16 && f < 120) xr[(f / 20 + 1) * 4 + f % 20 - 16] = rdx((f / 20 + 1) * 4 + f % 20 - 16);
            if constexpr (t == 1 && i >= 27 && i < 39) xt[(i + 1) & 1] = rdx(i + 1);
            SB();
            if constexpr (i == 0) MFMA32Z(acc[t], wf[f % (WIN + 1)], xr[0]);
            else if constexpr (i < 28) MFMA32I(acc[t], wf[f % (WIN + 1)], xr[i]);
            else MFMA32I(acc[t], wf[f % (WIN + 1)], xt[i & 1]);
            SB();
        SEND
        MFMA_DRAIN();
        WAITV(0);
        STAMP(2);
#pragma unroll
        for (int t = 0; t < 5; ++t)
#pragma unroll
            for (int b = 0; b < 4; ++b) {
                const int g0 = 8 * t + 2 * b, g1 = g0 + 1;
                const int o0 = g0 < 20 ? kq_base(g0) + tok * (g0 % 5 < 4 ? 16 : 8) : VOFF + (g0 - 20) * 8 + tok * VP;
                const int o1 = g1 < 20 ? kq_base(g1) + tok * (g1 % 5 < 4 ? 16 : 8) : VOFF + (g1 - 20) * 8 + tok * VP;
                *(LAS u32x2*)(smem + OWN + (hl ? o1 : o0)) = (u32x2){pk2(acc[t][4 * b], acc[t][4 * b + 1]), pk2(acc[t][4 * b + 2], acc[t][4 * b + 3])};
            }
        if (hl == 0) *(LAS u32x2*)(smem + OWN + VOFF + tok * VP + 160) = (u32x2){0x3F803F80u, 0x3F803F80u};
        WAITL();
        lds_set(smem + C_E + wid * 4, 1);
        STAMP(3);
        int l2 = tid; asm volatile("" : "+v"(l2)); l2 &= 63;
        int qa0 = (8 * (l2 >> 5) + 2 * ((l2 & 15) >> 2)) * 160 + (l2 & 16) * 2 + (l2 & 3) * 8, qa1 = qa0 + WS0 + 10240;
        asm volatile("" : "+v"(qa0), "+v"(qa1));
        auto rdq = [&](int f) { const int i = f / 3, t = f % 3, j = i >> 2, sl = (20 + j) % 7;
            return trfrag(smem + (j >= 7 ? qa0 + XS0 + (j - 7) * 10240 : sl == 0 ? qa0 + WS0 : qa1 + (sl - 1) * 10240) + (i & 3) * 2560 + t * 64, 160); };
        check(10);
        xt[0] = rdx(28);
#pragma unroll
        for (int f = 0; f < WIN; ++f) wf[f] = rdq(f);
        SB();
        SFOR(f, 120)
            constexpr int i = f / 3, t = f % 3, j = i >> 2;
            if constexpr (f % 24 == 12 && j < 9) rdflag(11 + j / 2);
            if constexpr (f % 24 == 12 - WIN && (j == 2 || j >= 6)) lds_set(smem + C_P + wid * 4, 21 + j);
            if constexpr (f % 24 == 24 - WIN && j < 9) check(11 + j / 2);
            if constexpr (f + WIN < 120) wf[(f + WIN) % (WIN + 1)] = rdq(f + WIN);
            if constexpr (t == 0 && i < 11) xt[(i + 1) & 1] = rdx(28 + i + 1);
            SB();
            if constexpr (i == 0) MFMA32Z(acc[t], wf[f % (WIN + 1)], xt[0]);
            else if constexpr (i < 12) MFMA32I(acc[t], wf[f % (WIN + 1)], xt[i & 1]);
            else MFMA32I(acc[t], wf[f % (WIN + 1)], xr[i - 12]);
            SB();
        SEND
        MFMA_DRAIN();
        STAMP(4);
        wait_min4(smem + C_P, 29);
#pragma unroll
        for (int t = 0; t < 3; ++t)
#pragma unroll
            for (int b = 0; b < 4; ++b)
                if (t < 2 || b < 2) {
                    const int g0 = 8 * t + 2 * b, g1 = g0 + 1;
                    const int o0 = kq_base(g0) + tok * (g0 % 5 < 4 ? 16 : 8), o1 = kq_base(g1) + tok * (g1 % 5 < 4 ? 16 : 8);
                    *(LAS u32x2*)(smem + QST + (hl ? o1 : o0)) =
                        (u32x2){pk2(acc[t][4 * b] * QSCALE, acc[t][4 * b + 1] * QSCALE), pk2(acc[t][4 * b + 2] * QSCALE, acc[t][4 * b + 3] * QSCALE)};
                }
        lds_set(smem + C_QF + wid * 4, 1);
        __builtin_amdgcn_s_setprio(2);
        STAMP(5);
    } else {
        const int hw = wid - 4;
        int known = 0;
        __builtin_amdgcn_s_setprio(3);
        auto lds_or = [&](int off, unsigned v) { FENCE(); if (lane == 0) __hip_atomic_fetch_or((LAS unsigned*)(smem + off), v, RLX_WG); FENCE(); };
        unsigned wo[5], qo[5];
#pragma unroll
        for (int q = 0; q < 5; ++q) { wo[q] = w_off(q) + (hw & 1) * (16 * 3840) + (4 - q) * 1024;
            const int c = q * 64 + lane, rho = c / 10; qo[q] = (unsigned)(((hw & 1) * 32 + (rho & ~7) + 4 * (rho & 1) + ((rho >> 1) & 3)) * 3840 + (c % 10) * 16 + (4 - q) * 1024); }
        auto done = [&](int n) { lds_or(C_F + n * 4, 1u << hw); };
        const unsigned xo1 = (xo ^ 64) + 9216, xo2 = xo + 2 * 9216, xo3 = (xo ^ 64) + 3 * 9216;
        auto round_x = [&](int n) { dma16x4(xo, xo1, xo2, xo3, xb + kt_of(n) * 128 + hw * (32 * 1280), lds0 + XS0 + (n % 3) * 16384 + hw * 4096); };
        auto round_w = [&](int n) {
            const int u = 2 * n + (hw >> 1);
            const unsigned dst = lds0 + (u < 27 ? WS0 + (u % 7) * 10240 : XS0 + (u - 27) * 10240) + (hw & 1) * 5120 + 2048;
            if (u < 20) dma16x5(wo, wb + (size_t)(kt_of(u >> 1) * 64 + (u & 1) * 32) * 3840 - 2048, dst);
            else dma16x5(qo, wb + (size_t)kt_of(u < 23 ? u - 13 : u - 23) * (64 * 3840) - 2048, dst); };
        round_x(1); round_w(1);
        WAITV(9);
        BAR_L();
#pragma unroll 1
        for (int n = ABL4N; n <= 12; ++n) {
            const int need = 2 * n - 5;
            if (need > known) known = min4(smem + C_P);
            if (need > known) { WAITV(0); done(n - 1); known = wait_min4(smem + C_P, need); }
            if (n < 10) round_x(n);
            round_w(n);
            if (n < 10) WAITV(9); else WAITV(5);
            done(n - 1);
        }
        WAITV(0); done(12);
        STAMP(1);
        const u64 tag = rdtag();
        auto a_chunk = [&](int a) { int o;
            if (a < 320) { const int hd = a / 80, rem = a % 80; o = hd * 5120 + (rem < 32 ? (96 + rem) * 16 : rem < 64 ? 2048 + (64 + rem) * 16 : 4096 + 768 + (rem - 64) * 16); }
            else o = VOFF + 96 * VP + (a - 320) * 16;
            return o; };
        if (rt < 15) {
            const __amdgpu_buffer_rsrc_t kvr = __builtin_amdgcn_make_buffer_rsrc(ws + WS_KV + me * KVG, 0, KVB, 0x00020000);
            wait_min4(smem + C_E, 1);
            STAMP(2);
            if (!isA) { u32x4 v[11];
#pragma unroll
                for (int j = 0; j < 11; ++j) if (j < 10 || hw == 3) v[j] = *(LAS u32x4*)(smem + OWN + hw * 10240 + j * 1024 + lane * 16);
#pragma unroll
                for (int j = 0; j < 11; ++j) if (j < 10 || hw == 3) __builtin_amdgcn_raw_buffer_store_b128(v[j], kvr, hw * 10240 + j * 1024 + lane * 16, 0, 16);
            } else {
#pragma unroll
                for (int j = 0; j < 3; ++j) { const int a = (4 * j + hw) * 64 + lane;
                    if (a < 656) { const int o = a_chunk(a); __builtin_amdgcn_raw_buffer_store_b128(*(LAS u32x4*)(smem + OWN + o), kvr, o, 0, 16); } }
            }
            STAMP(3);
        }
        wait_min4(smem + C_P, 23);
        round_w(13); round_w(14);
        WAITV(10);
        if (rt < 15) {
            unsigned old = 0;
            if (lane == 0) old = __hip_atomic_fetch_add((LAS unsigned*)(smem + N_PUB), 1u, RLX_WG);
            if (lane == 0 && old == 3) __hip_atomic_store((GAS u64*)(ws + WS_FKV + me * 128), tag, RLX_AGENT);
            STAMP(4);
        }
        asm volatile("s_waitcnt vmcnt(6)" ::: "memory"); WAITV(5); done(13); WAITV(0); done(14);
        poll_flags((GAS u64*)(ws + WS_FKV + (me - 8 * (lane + 1)) * 128), lane < nnb, tag);
        STAMP(5);
        if (!isA) {
            const int n = hw >> 1, half = hw & 1;
            if (n < nnb) {
                const __amdgpu_buffer_rsrc_t nbr = __builtin_amdgcn_make_buffer_rsrc(ws + WS_KV + (me - 8 * (n + 1)) * KVG, 0, KVB, 0x00020000);
                u32x4 v[21];
#pragma unroll
                for (int j = 0; j < 21; ++j) if (j < 20 || half) v[j] = __builtin_amdgcn_raw_buffer_load_b128(nbr, half * 20480 + j * 1024 + lane * 16, 0, 16);
                wait_min4(smem + C_P, 27);
#pragma unroll
                for (int j = 0; j < 21; ++j) if (j < 20 || half) *(LAS u32x4*)(smem + KV0 + (1 - n) * KVB + half * 20480 + j * 1024 + lane * 16) = v[j];
            }
        } else if (nnb) {
            const __amdgpu_buffer_rsrc_t nbr = __builtin_amdgcn_make_buffer_rsrc(ws + WS_KV + (me - 8) * KVG, 0, KVB, 0x00020000);
            u32x4 v[3];
#pragma unroll
            for (int j = 0; j < 3; ++j) { const int a = (4 * j + hw) * 64 + lane; if (a < 656) v[j] = __builtin_amdgcn_raw_buffer_load_b128(nbr, a_chunk(a), 0, 16); }
            wait_min4(smem + C_P, 27);
#pragma unroll
            for (int j = 0; j < 3; ++j) { const int a = (4 * j + hw) * 64 + lane; if (a < 656) *(LAS u32x4*)(smem + KV0 + KVB + a_chunk(a)) = v[j]; }
        }
        lds_inc0(smem + N_NB, lane);
        __builtin_amdgcn_s_setprio(1);
        STAMP(6);
    }
    const int qi = wid & 3, hp = wid >> 2;
    const int ud = qi + 8;
    const bool far = isA ? (4 * rt + qi >= 1) : (4 * rt + qi >= 8);
    const int u0 = isA ? (far ? ud - 1 : ud) : (far ? qi : 8 - 4 * rt);
    f32x16 cd, cf;
    auto masks = [&](int q) {
      const unsigned low = (2u << q) - 1u;
      unsigned vd = low, vf = ~low;
      if (isA) { const u64 w = 0x5555ull << (q + 18);
          vd = (unsigned)(w >> 32); vf = (unsigned)w; }
      if (!far) vf = ~0u;
      const unsigned id = ~vd >> (4 * hl), jf = ~vf >> (4 * hl);
#pragma unroll
      for (int r = 0; r < 16; ++r) {
          const int c0 = (r & 3) + 8 * (r >> 2);
          cd[r] = __uint_as_float((unsigned)__builtin_amdgcn_sbfe((int)id, c0, 1) & 0xFF800000u);
          cf[r] = __uint_as_float((unsigned)__builtin_amdgcn_sbfe((int)jf, c0, 1) & 0xFF800000u);
      } };
    masks(r32);
    unsigned oo[5]; u32x4 wreg[13];
#pragma unroll
    for (int q = 0; q < 5; ++q) { const int c = q * 64 + lane, rho = c / 10;
        oo[q] = (unsigned)((qi * 160 + hp * 40 + (rho & ~7) + 4 * (rho & 1) + ((rho >> 1) & 3)) * 1280 + hg * 160 + (c % 10) * 16); }
    auto ldwo = [&](int i) { wreg[i] = __builtin_amdgcn_raw_buffer_load_b128(wor, oo[i % 5] + (i / 5) * (32 * 1280) + (i > 6 || (i == 6 && lane >= 16) ? 40 * 1280 : 0), 0, 0); };
    const u64 tag = rdtag();
    const __amdgpu_buffer_rsrc_t orr = __builtin_amdgcn_make_buffer_rsrc(ws + WS_O + (br * 16 + rt) * OG, 0, 4 * 40960, 0x00020000);
    if (hp) { wait_word(smem + C_QF + qi * 4, 1); wait_word(smem + N_NB, 4); }
    STAMP(7);
    u32x2 ob[2][3];
    {
        bf16x8 q1[2]; u32x2 q2[2];
#pragma unroll
        for (int e = 0; e < 2; ++e) {
            q1[e] = *(LAS bf16x8*)(smem + QST + (2 * hp + e) * 5120 + hl * 2048 + (qi * 32 + r32) * 16);
            q2[e] = *(LAS u32x2*)(smem + QST + (2 * hp + e) * 5120 + 4096 + (qi * 32 + r32) * 8);
            if (hl) q2[e] = (u32x2){0u, 0u};
        }
        if (hp) lds_inc0(smem + N_G0, lane);
        const int kb1 = KV0 + hp * 10240 + hl * 2048 + r32 * 16;
        const int kb2 = hl ? ONE : KV0 + hp * 10240 + 4096 + r32 * 8, nh = hl ^ 1;
        const int vrow = KV0 + VOFF + (4 * hl + ((lane & 15) >> 2)) * VP;
        const bool ones = (lane & 16) && (lane & 3);
        const int vb0 = vrow + (ones ? 160 : hp * 80 + (lane & 16) * 2 + (lane & 3) * 8), vb1 = vb0 + (ones ? 0 : 40);
        struct KF { bf16x8 a; s16x4 b; };
        struct VF { bf16x8 a, b; };
        auto rdk = [&](int u, int e) { const int so = (u >> 2) * KVB, tw = u & 3; KF k;
            k.a = *(LAS bf16x8*)(smem + kb1 + so + tw * 512 + e * 5120); k.b = *(LAS s16x4*)(smem + __umul24(so + tw * 256 + e * 5120, nh) + kb2); return k; };
        auto rdv = [&](int u, int e) { LAS char* p = smem + (e ? vb1 : vb0) + (u >> 2) * KVB + (u & 3) * (32 * VP); VF v;
            v.a = trfrag(p, 8 * VP); v.b = trfrag(p + 16 * VP, 8 * VP); return v; };
        auto qk = [&](const KF& k, int e, const f32x16& c) { return MFMA32K8(k.b, __builtin_bit_cast(s16x4, q2[e]), MFMA32(k.a, q1[e], c)); };
        const f32x16 zero = {};
        auto max16 = [&](const f32x16& s, float m) {
#pragma unroll
            for (int r = 0; r < 16; ++r) m = __builtin_elementwise_maximum(m, s[r]);
            return m; };
        auto set_ref = [&](float m, int e) { const unsigned nb = pk2(-m, 0.f); if (hl) q2[e][0] = nb; };
        auto ex4 = [&](f32x16& s, int b) {
#pragma unroll
            for (int r = 0; r < 4; ++r) s[b + r] = __builtin_amdgcn_exp2f(s[b + r]); };
        auto pk8 = [&](const f32x16& s, int b) { return __builtin_bit_cast(bf16x8, (u32x4){pk2(s[b], s[b + 1]), pk2(s[b + 2], s[b + 3]), pk2(s[b + 4], s[b + 5]), pk2(s[b + 6], s[b + 7])}); };
        auto phase = [&](auto nx, f32x16& cur, const VF& v, f32x16& o, f32x16& nxt, const KF& k, int e) __attribute__((always_inline)) {
            constexpr int NX = decltype(nx)::value;
            SB(); ex4(cur, 0); SB();
            if constexpr (NX) nxt = MFMA32(k.a, q1[e], NX == 1 ? zero : NX == 2 ? cf : cd);
            SB(); ex4(cur, 4); { const bf16x8 p = pk8(cur, 0); SB(); o = MFMA32(v.a, p, o); }
            SB(); ex4(cur, 8); SB();
            if constexpr (NX) nxt = MFMA32K8(k.b, __builtin_bit_cast(s16x4, q2[e]), nxt);
            SB(); ex4(cur, 12); { const bf16x8 p = pk8(cur, 8); SB(); o = MFMA32(v.b, p, o); }
            SB(); };
        f32x16 o0, o1, sa, sb; KF kx; VF vx;
        auto step = [&](auto m0, auto m1, int u) __attribute__((always_inline)) {
            constexpr int M1 = decltype(m1)::value;
            KF ky; if constexpr (M1) ky = rdk(u - 1, 0);
            const VF vy = rdv(u, 1);
            phase(m0, sa, vx, o0, sb, kx, 1);
            if constexpr (M1) { kx = rdk(u - 1, 1); vx = rdv(u - 1, 0); }
            phase(m1, sb, vy, o1, sa, ky, 0); };
        auto nbwait = [&](int u) { if (u == 8 && !hp) wait_word(smem + N_NB, 4); };
        { const KF k0f = rdk(ud, 0); kx = rdk(ud, 1); vx = rdv(ud, 0);
          const f32x16 s0 = qk(k0f, 0, cd), s1 = qk(kx, 1, cd);
          set_ref(halfmax(max16(s0, s0[0])), 0); set_ref(halfmax(max16(s1, s1[0])), 1);
          sa = qk(k0f, 0, cd); }
        o0 = zero; o1 = zero;
        if (u0 + 8 == ud) {
            SFOR(it, 9)
                if constexpr (it < 7) { ldwo(2 * it); if constexpr (it < 6) ldwo(2 * it + 1); }
                if constexpr (it < 4) nbwait(ud - it);
                step(IC<it == 0 ? 3 : it == 8 ? 2 : 1>{}, IC<it == 8 ? 0 : it == 7 ? 2 : 1>{}, ud - it);
            SEND
        } else {
            if (u0 < ud) {
                nbwait(ud);
                if (u0 + 1 < ud) {
                    step(IC<3>{}, IC<1>{}, ud);
#pragma unroll 1
                    for (int u = ud - 1; u > u0 + 1; --u) { nbwait(u); step(IC<1>{}, IC<1>{}, u); }
                    nbwait(u0 + 1);
                    step(IC<1>{}, IC<2>{}, u0 + 1);
                } else step(IC<3>{}, IC<2>{}, ud);
                step(IC<2>{}, IC<0>{}, u0);
            } else step(IC<3>{}, IC<0>{}, ud);
#pragma unroll
            for (int i = 0; i < 13; ++i) ldwo(i);
        }
        if (!__all(o0[12] < 1.152921504606847e18f && o1[12] < 1.152921504606847e18f)) {
            { int q = r32; asm volatile("" : "+v"(q)); masks(q); }
#pragma unroll
            for (int e = 0; e < 2; ++e) {
                if (hl) q2[e][0] = 0u;
                const f32x16 s = qk(rdk(ud, e), e, cd);
                float m = max16(s, s[0]);
                if (u0 < ud) m = max16(qk(rdk(u0, e), e, cf), m);
#pragma unroll 1
                for (int u = u0 + 1; u < ud; ++u) m = max16(qk(rdk(u, e), e, zero), m);
                set_ref(halfmax(m), e);
            }
#pragma unroll
            for (int e = 0; e < 2; ++e) {
                f32x16 o = zero;
#pragma unroll 1
                for (int u = u0; u <= ud; ++u) { f32x16 t = u == ud ? qk(rdk(u, e), e, cd) : u == u0 ? qk(rdk(u, e), e, cf) : qk(rdk(u, e), e, zero);
                    const VF v = rdv(u, e); ex4(t, 0); ex4(t, 4); ex4(t, 8); ex4(t, 12);
                    o = MFMA32(v.a, pk8(t, 0), o); o = MFMA32(v.b, pk8(t, 8), o); }
                if (e) o1 = o; else o0 = o;
            }
        }
        const float rl0 = __builtin_amdgcn_rcpf(o0[12]), rl1 = __builtin_amdgcn_rcpf(o1[12]);
#pragma unroll
        for (int j = 0; j < 3; ++j) {
            ob[0][j] = (u32x2){pk2(o0[4 * j] * rl0, o0[4 * j + 1] * rl0), pk2(o0[4 * j + 2] * rl0, o0[4 * j + 3] * rl0)};
            ob[1][j] = (u32x2){pk2(o1[4 * j] * rl1, o1[4 * j + 1] * rl1), pk2(o1[4 * j + 2] * rl1, o1[4 * j + 3] * rl1)}; }
    }
    STAMP(8);
    { const unsigned obase = (unsigned)(qi * 40960 + hp * 20480 + hg * 2560 + r32 * 16);
      { auto rx = __builtin_amdgcn_permlane32_swap(ob[0][0][0], ob[0][1][0], false, false);
        auto ry = __builtin_amdgcn_permlane32_swap(ob[0][0][1], ob[0][1][1], false, false);
        __builtin_amdgcn_raw_buffer_store_b128((u32x4){rx[0], ry[0], rx[1], ry[1]}, orr, obase + hl * 512, 0, 16); }
      if (hl == 0) __builtin_amdgcn_raw_buffer_store_b128((u32x4){ob[0][2][0], ob[0][2][1], ob[1][0][0], ob[1][0][1]}, orr, obase + 1024, 0, 16);
      { auto fx = __builtin_amdgcn_permlane32_swap(ob[1][0][0], ob[1][1][0], false, false);
        auto fy = __builtin_amdgcn_permlane32_swap(ob[1][0][1], ob[1][1][1], false, false);
        auto sx = __builtin_amdgcn_permlane32_swap(ob[1][1][0], ob[1][2][0], false, false);
        auto sy = __builtin_amdgcn_permlane32_swap(ob[1][1][1], ob[1][2][1], false, false);
        __builtin_amdgcn_raw_buffer_store_b128((u32x4){fx[1], fy[1], sx[0], sy[0]}, orr, obase + 1536 + hl * 512, 0, 16); } }
    if (lane == 0) { (void)__hip_atomic_fetch_add((LAS unsigned*)(smem + N_G1), 1u, RLX_WG);
        if (!hp) (void)__hip_atomic_fetch_add((LAS unsigned*)(smem + N_G0), 1u, RLX_WG); }
    FENCE();
    STAMP(6);
    auto signal_o = [&]() { WAITV(0);
        if (lds_inc(smem + N_OS + hp * 4, lane) == 3 && lane == 0) __hip_atomic_store((GAS u64*)(ws + WS_FO + (me * 2 + hp) * 128), tag, RLX_AGENT); };
    if (!hp) signal_o();
    wait_word(smem + (hp ? N_G1 : N_G0), 8);
    const int p1 = hp ? 30720 : 35840, p2 = hp ? 65536 : 43520;
    const int wb0 = hp ? KV0 + 10240 : 512, wb1 = hp ? KV0 + KVB + 10240 - 30720 : KV0 - 35840, wb2 = KV0 + KVB - 43520;
#pragma unroll
    for (int i = 0; i < 13; ++i) { const int L = qi * 12800 + i * 1024;
        if (i < 12 || lane < 32) *(LAS u32x4*)(smem + (L + (L < p1 ? wb0 : L < p2 ? wb1 : wb2)) + lane * 16) = wreg[i]; }
    lds_inc0(smem + N_WO + hp * 4, lane);
    STAMP(9);
    if (hp) signal_o();
    STAMP(10);
    poll_flags((GAS u64*)(ws + WS_FO + (((me & ~7) + (lane & 7)) * 2 + hp) * 128), lane < 8, tag);
    STAMP(11);
    f32x16 oc[3]; f32x4 ps[10];
    LAS char* red = smem + OWN + qi * 10240 + lane * 16;
    { bf16x8 af[5], of[20];
      const int la = (8 * hl + 2 * ((lane & 15) >> 2)) * 160 + (lane & 16) * 2 + (lane & 3) * 8;
      const int aa0 = wb0 + la, aa1 = (hp ? wb1 : wb0) + la, aa2 = wb1 + la, aa3 = (hp ? wb1 : wb2) + la;
      auto rda = [&](int f) { return trfrag(smem + (f < 36 ? aa0 : f < 42 ? aa1 : f < 51 ? aa2 : aa3) + (f / 3) * 2560 + (f % 3) * 64, 160); };
      auto ldo = [&](int k) { of[k] = __builtin_bit_cast(bf16x8, __builtin_amdgcn_raw_buffer_load_b128(orr, qi * 40960 + (hp * 20 + k) * 1024 + lane * 16, 0, 16)); };
#pragma unroll
      for (int k = 0; k < OPF; ++k) ldo(k);
      wait_word(smem + N_WO + hp * 4, 4);
      if (!hp) { __builtin_amdgcn_s_setprio(0); wait_word(smem + N_G1, 8); }
#pragma unroll
      for (int f = 0; f < 4; ++f) af[f] = rda(f);
      SB();
      SFOR(f, 60)
          if constexpr (f % 3 == 0 && f / 3 + OPF < 20) ldo(f / 3 + OPF);
          if constexpr (f + 4 < 60) af[(f + 4) % 5] = rda(f + 4);
          if constexpr (f == 57) { if (hp) { wait_word(smem + C_RD + qi * 4, 1);
#pragma unroll
              for (int b = 0; b < 10; ++b) ps[b] = *(LAS f32x4*)(red + b * 1024); } }
          SB();
          if constexpr (f < 3) MFMA32Z(oc[f], af[f % 5], of[0]); else MFMA32I(oc[f % 3], af[f % 5], of[f / 3]);
          SB();
      SEND }
    MFMA_DRAIN();
    STAMP(12);
    { if (!hp) {
          wait_word(smem + N_G1, 8);
#pragma unroll
          for (int b = 0; b < 10; ++b) *(LAS f32x4*)(red + b * 1024) = (f32x4){oc[b >> 2][4 * (b & 3)], oc[b >> 2][4 * (b & 3) + 1], oc[b >> 2][4 * (b & 3) + 2], oc[b >> 2][4 * (b & 3) + 3]};
          lds_set(smem + C_RD + qi * 4, 1);
      } else {
          char* orow = out + (size_t)(rt * 128 + qi * 32 + r32) * 2560 + br * 1280 + hg * 160 + hl * 16;
          auto fin = [&](int b) { const f32x4 x = ps[b]; const f32x16& c = oc[b >> 2]; const int r = 4 * (b & 3);
              return (u32x2){pk2(c[r] + x[0], c[r + 1] + x[1]), pk2(c[r + 2] + x[2], c[r + 3] + x[3])}; };
#pragma unroll
          for (int b = 0; b < 10; b += 2) { const u32x2 a = fin(b), c = fin(b + 1);
              auto rx = __builtin_amdgcn_permlane32_swap(a[0], c[0], false, false); auto ry = __builtin_amdgcn_permlane32_swap(a[1], c[1], false, false);
              *(u32x4*)(orow + b * 16) = (u32x4){rx[0], ry[0], rx[1], ry[1]}; }
      } }
    if (bx == 0 && wid == 0) {
#pragma unroll 1
        for (int i = 0; i < 4; ++i) poll_flags((GAS u64*)(ws + WS_FO + (i * 64 + lane) * 256), true, tag);
        if (lane == 0) __hip_atomic_store((GAS u64*)ws, tag, RLX_AGENT);
    }
}

extern "C" void kernel_launch(void* const* d_in, const int* in_sizes, int n_in, void* d_out, int out_size, void* d_ws, size_t ws_size,
                              hipStream_t stream) {
    fused_attn_kernel<<<256, 512, 0, stream>>>((const char*)d_in[0], (const char*)d_in[1], (const char*)d_in[2], (const char*)d_in[3],
                                               (const char*)d_in[4], (const char*)d_in[5], (char*)d_out, (char*)d_ws);
}
```

```cpp
#define ADELAY 3
#define DIAG 1
#include <hip/hip_runtime.h>
#include <hip/hip_bf16.h>
#include <stdint.h>

typedef short bf16x8 __attribute__((ext_vector_type(8)));
typedef short s16x4 __attribute__((ext_vector_type(4)));
typedef float f32x2 __attribute__((ext_vector_type(2)));
typedef float f32x4 __attribute__((ext_vector_type(4)));
typedef float f32x16 __attribute__((ext_vector_type(16)));
typedef unsigned u32x4 __attribute__((ext_vector_type(4)));
typedef unsigned u32x2 __attribute__((ext_vector_type(2)));
typedef __bf16 bfx2 __attribute__((ext_vector_type(2)));
typedef unsigned long long u64;
#define LAS __attribute__((address_space(3)))
#define GAS __attribute__((address_space(1)))
#define DI __device__ __forceinline__
#define RLX_AGENT __ATOMIC_RELAXED, __HIP_MEMORY_SCOPE_AGENT
#define RLX_WG __ATOMIC_RELAXED, __HIP_MEMORY_SCOPE_WORKGROUP

constexpr int LDS_BYTES = 163840;
constexpr int VOFF = 20480, VP = 168;
constexpr int KVB = VOFF + 128 * VP;
constexpr int KV0 = LDS_BYTES - 3 * KVB;
constexpr int OWN = KV0 + 2 * KVB;
constexpr int C_F = 0, C_P = 256, C_E = 272, C_CNT = 288, C_TAG = 320;
constexpr int XS0 = 512, WS0 = XS0 + 3 * 16384, QST = 512, ONE = 31744, WOB = 512, RED = WOB + 102400;
static_assert(WS0 + 7 * 10240 <= OWN, "ring overlaps the own image");
static_assert(ONE + 5120 + 8 <= KV0 && RED + 8 * 6144 <= LDS_BYTES, "LDS map");
constexpr unsigned WS_FKV = 4096, WS_FO = WS_FKV + 256 * 128, WS_KV = 131072;
constexpr unsigned KVG = KVB + 128, OG = 4 * 40960 + 128;
constexpr unsigned WS_O = WS_KV + 256 * KVG;
constexpr unsigned WS_END = WS_O + 32 * OG;
constexpr unsigned WS_DBG = WS_END;
constexpr unsigned SPIN_CAP = 400000, LSPIN_CAP = 4000000;
constexpr float QSCALE = 0.22360679774997896f * 1.4426950408889634f;
#define OPF 6
#define GSLEEP 1
#define ABL4N 2
#define RING (WIN + 2)
#define WIN 5

#define MFMA32(a, b, c) __builtin_amdgcn_mfma_f32_32x32x16_bf16((a), (b), (c), 0, 0, 0)
#define MFMA32K8(a, b, c) __builtin_amdgcn_mfma_f32_32x32x8bf16_1k((a), (b), (c), 0, 0, 0)
#define MFMA32I(acc, a, b) asm volatile("v_mfma_f32_32x32x16_bf16 %0, %1, %2, %0" : "+v"(acc) : "v"(a), "v"(b))
#define MFMA32Z(acc, a, b) asm volatile("v_mfma_f32_32x32x16_bf16 %0, %1, %2, 0" : "=&v"(acc) : "v"(a), "v"(b))
#define MFMA_DRAIN() asm volatile("s_nop 7\n\ts_nop 4" ::: "memory")
#define WAITV(N) asm volatile("s_waitcnt vmcnt(" #N ")" ::: "memory")
#define WAITL() asm volatile("s_waitcnt lgkmcnt(0)" ::: "memory")
#define BAR_L() asm volatile("s_waitcnt lgkmcnt(0)\n\ts_barrier" ::: "memory")
#define SB() __builtin_amdgcn_sched_barrier(0)
#define FENCE() asm volatile("" ::: "memory")

#define STAMP(i) do { if ((i) == 0 || (i) == 1 || (i) == 2 || (i) == 7 || (i) == 8) { SB(); dst_[i] = (unsigned)__builtin_amdgcn_s_memrealtime(); SB(); } } while (0)

template <int V> struct IC { static constexpr int value = V; };
template <int I, int N, class F> DI void static_for(F&& f) { if constexpr (I < N) { f(IC<I>{}); static_for<I + 1, N>(f); } }
#define SFOR(var, N) static_for<0, N>([&](auto ic_##var) __attribute__((always_inline)) { constexpr int var = decltype(ic_##var)::value;
#define SEND });

DI void dma16(unsigned voff, const char* sbase, unsigned ldsaddr) {
    asm volatile("s_mov_b32 m0, %2\n\ts_nop 0\n\tglobal_load_lds_dwordx4 %0, %1" :: "v"(voff), "s"(sbase), "s"(ldsaddr) : "memory");
}
DI void dma16x4(unsigned v0, unsigned v1, unsigned v2, unsigned v3, const char* sbase, unsigned ldsaddr) {
    asm volatile("s_mov_b32 m0, %5\n\ts_nop 0\n\tglobal_load_lds_dwordx4 %0, %4\n\tglobal_load_lds_dwordx4 %1, %4 offset:1024\n\t"
                 "global_load_lds_dwordx4 %2, %4 offset:2048\n\tglobal_load_lds_dwordx4 %3, %4 offset:3072"
                 :: "v"(v0), "v"(v1), "v"(v2), "v"(v3), "s"(sbase), "s"(ldsaddr) : "memory");
}
DI void dma16x5(const unsigned (&v)[5], const char* sbase, unsigned ldsaddr) {
    asm volatile("s_mov_b32 m0, %6\n\ts_nop 0\n\tglobal_load_lds_dwordx4 %0, %5 offset:-2048\n\tglobal_load_lds_dwordx4 %1, %5 offset:-1024\n\t"
                 "global_load_lds_dwordx4 %2, %5\n\tglobal_load_lds_dwordx4 %3, %5 offset:1024\n\tglobal_load_lds_dwordx4 %4, %5 offset:2048"
                 :: "v"(v[0]), "v"(v[1]), "v"(v[2]), "v"(v[3]), "v"(v[4]), "s"(sbase), "s"(ldsaddr) : "memory");
}
DI void dma4(const char* p, unsigned ldsaddr) {
    asm volatile("s_mov_b32 m0, %1\n\ts_nop 0\n\tglobal_load_lds_dword %0, off" :: "v"(p), "s"(ldsaddr) : "memory");
}
DI unsigned pk2(float lo, float hi) { f32x2 v = {lo, hi}; return __builtin_bit_cast(unsigned, __builtin_convertvector(v, bfx2)); }
typedef short v4i16_t __attribute__((ext_vector_type(4)));
DI bf16x8 trfrag(LAS char* p, int second) {
    s16x4 a = __builtin_bit_cast(s16x4, __builtin_amdgcn_ds_read_tr16_b64_v4i16((LAS v4i16_t*)p));
    s16x4 b = __builtin_bit_cast(s16x4, __builtin_amdgcn_ds_read_tr16_b64_v4i16((LAS v4i16_t*)(p + second)));
    return __builtin_shufflevector(a, b, 0, 1, 2, 3, 4, 5, 6, 7);
}
DI float halfmax(float m) { auto rr = __builtin_amdgcn_permlane32_swap(__float_as_uint(m), __float_as_uint(m), false, false);
    return __builtin_elementwise_maximum(__uint_as_float(rr[0]), __uint_as_float(rr[1])); }

DI void poll_flags(GAS u64* word, bool active, u64 tag) {
#pragma unroll 1
    for (unsigned spins = 0; spins < SPIN_CAP; ++spins) {
        u64 v = tag;
        if (active) v = __hip_atomic_load(word, RLX_AGENT);
        if (__all(v == tag)) break;
        __builtin_amdgcn_s_sleep(1);
    }
    __builtin_amdgcn_fence(__ATOMIC_ACQUIRE, "wavefront");
}
DI int min4(LAS char* p) {
    const u64 a = __hip_atomic_load((LAS u64*)p, RLX_WG), b = __hip_atomic_load((LAS u64*)(p + 8), RLX_WG);
    const unsigned m = min(min((unsigned)a, (unsigned)(a >> 32)), min((unsigned)b, (unsigned)(b >> 32)));
    return __builtin_amdgcn_readfirstlane((int)m);
}
DI int wait_min4(LAS char* p, int need) {
    int v = min4(p);
#pragma unroll 1
    for (unsigned spins = 0; v < need && spins < LSPIN_CAP; ++spins) { __builtin_amdgcn_s_sleep(GSLEEP); v = min4(p); }
    FENCE();
    return v;
}
DI void lds_set(LAS char* p, unsigned v) { FENCE(); __hip_atomic_store((LAS unsigned*)p, v, RLX_WG); FENCE(); }

__global__ __launch_bounds__(512, 2) void fused_attn_kernel(const char* __restrict__ xA, const char* __restrict__ wA, const char* __restrict__ oA,
                                                           const char* __restrict__ xB, const char* __restrict__ wB, const char* __restrict__ oB,
                                                           char* __restrict__ out, char* __restrict__ ws) {
    __shared__ __attribute__((aligned(16))) char smem_[LDS_BYTES];
    LAS char* smem = (LAS char*)smem_;
    const unsigned lds0 = (unsigned)(uintptr_t)smem;
    unsigned dst_[9];
    STAMP(0);
    const int tid = threadIdx.x, lane = tid & 63;
    const int wid = __builtin_amdgcn_readfirstlane(tid >> 6);
    const int bx = blockIdx.x, xcd = bx & 7, idx = bx >> 3;
    const int isA = idx >> 4, rt = (xcd >> 1) * 4 + ((idx >> 2) & 3), hg = (xcd & 1) * 4 + (idx & 3);
    const int br = isA ? 0 : 1;
    const int k0 = isA ? (((rt & 3) * 5 + 1) / 2 + ((hg & 3) * 5 + 1) / 2) % 10 : 0;
    const char* xb = (isA ? xA : xB) + (size_t)rt * (128 * 1280);
    const char* wb = (isA ? wA : wB) + hg * 160;
    const int me = (br * 16 + rt) * 8 + hg;
    const int r32 = lane & 31, hl = lane >> 5;
    auto kt_of = [&](int s) { int k = s + k0; return k >= 10 ? k - 10 : k; };
    if (isA) {
        const int j16 = idx & 15;
        const char* wl = wB + (hg >> 2) * 640 + j16 * (4 * 3840);
        const char* xl = xB + (size_t)(rt * 128 + (hg & 3) * 32 + r32) * 1280 + hl * 128;
        auto pfx = [&](int i) { return xl + i * 256; };
        auto pfkv = [&](int i) { int n = i * 64 + lane; n = n < 400 ? n : 399; const int sl = n / 40, r = n % 40, c = r % 10;
            return wl + (sl * 64 + r / 10) * 3840 + 1280 + (c < 5 ? c * 128 : 640 + c * 128); };
        auto pfq = [&](int i) { int n = i * 64 + lane; n = n < 200 ? n : 199; int sl = n / 20 + 7; sl = sl >= 10 ? sl - 10 : sl; const int r = n % 20;
            return wl + (sl * 64 + r / 5) * 3840 + (r % 5) * 128; };
        dma4(wid & 1 ? pfx(wid >> 1) : pfkv(wid >> 1), lds0 + OWN + wid * 256);
        dma4(wid < 3 ? pfkv(4 + wid) : wid == 3 ? pfx(4) : pfq(wid - 4), lds0 + OWN + wid * 256);
        for (int i = 0; i < ADELAY; ++i) __builtin_amdgcn_s_sleep(16);
    }

    if (wid == 4 && lane < 2) dma4(ws + lane * 4, lds0 + C_TAG);
    if (wid == 7) { const char* p = ws + 2048;
        if (lane < 6) { const int im = me - 8 * (lane >> 1); if (im >= 0) p = ws + WS_KV + im * KVG + ((lane & 1) ? KVB : -128); }
        else if (lane < 8) p = ws + WS_O + (br * 16 + rt) * OG + ((lane & 1) ? (int)OG - 128 : -128);
        else if (lane < 10) p = (isA ? oA : oB) + (lane & 1) * (639 * 1280);
        else if (lane < 12) p = out + (size_t)(rt * 128 + (lane & 1) * 127) * 2560;
        dma4(p, lds0 + OWN); }
    const unsigned xo = (unsigned)((lane >> 3) * 1280 + (((lane & 7) ^ (lane >> 4)) << 4));
    auto w_off = [&](int q) { const int c = q * 64 + lane, row = c / 20, j = c % 20; return (unsigned)(row * 3840 + (j < 10 ? 1280 + j * 16 : 2400 + j * 16)); };
    { const char* xs = xb + k0 * 128 + wid * (16 * 1280);
      dma16(xo, xs, lds0 + XS0 + wid * 2048);
      dma16(xo ^ 64, xs + 8 * 1280, lds0 + XS0 + wid * 2048 + 1024);
      const char* wsrc = wb + (size_t)k0 * (64 * 3840);
      const unsigned wv = w_off(wid < 5 ? wid : wid - 5);
      if (wid < 5) { dma16(wv, wsrc, lds0 + WS0 + wid * 1024); dma16(wv, wsrc + 16 * 3840, lds0 + WS0 + (5 + wid) * 1024);
          if (wid >= 3) { dma16(wv, wsrc + 32 * 3840, lds0 + WS0 + (10 + wid) * 1024); dma16(wv, wsrc + 48 * 3840, lds0 + WS0 + (15 + wid) * 1024); } }
      else { dma16(wv, wsrc + 32 * 3840, lds0 + WS0 + (5 + wid) * 1024); dma16(wv, wsrc + 48 * 3840, lds0 + WS0 + (10 + wid) * 1024); } }
    if (tid < 80) *(LAS unsigned*)(smem + tid * 4) = 0u;
    const int nnb = isA ? (rt > 0 ? 1 : 0) : (rt > 1 ? 2 : rt);
    auto kq_base = [&](int fg) { const int hd = fg / 5, dg = fg % 5; return dg < 4 ? hd * 5120 + (dg >> 1) * 2048 + (dg & 1) * 8 : hd * 5120 + 4096; };

    auto rdtag = [&]() { const u64 t = *(LAS u64*)(smem + C_TAG);
        const u64 e = ((u64)(unsigned)__builtin_amdgcn_readfirstlane((int)(t >> 32)) << 32) | (unsigned)__builtin_amdgcn_readfirstlane((int)t);
        return e * 6364136223846793005ull + 1442695040888963407ull; };
    const __amdgpu_buffer_rsrc_t wor = __builtin_amdgcn_make_buffer_rsrc((void*)(isA ? oA : oB), 0, 640 * 1280, 0x00020000);

    if (wid < 4) {
        asm volatile("s_waitcnt vmcnt(0) lgkmcnt(0)\n\ts_barrier" ::: "memory");
        STAMP(1);
        bf16x8 xr[28], xt[2], wf[RING];
        f32x16 acc[5];
        const int tok = wid * 32 + r32;
        const int xa = XS0 + tok * 128 + ((hl ^ ((tok >> 1) & 7)) << 4);
        int wa0 = WS0 + (8 * hl + ((lane & 15) >> 2)) * 320 + (lane & 16) * 2 + (lane & 3) * 8, wa1 = wa0 + 61440;
        asm volatile("" : "+v"(wa0), "+v"(wa1));
        auto rdx = [&](int i) { return *(LAS bf16x8*)(smem + ((i >> 2) % 3) * 16384 + (xa ^ ((i & 3) * 32))); };
        auto rdw = [&](int f) { const int i = f / 5, t = f % 5, sl = (i >> 1) % 7;
            return trfrag(smem + (sl < 6 ? wa0 + sl * 10240 : wa1) + (i & 1) * 5120 + t * 64, 1280); };
        unsigned fl = 0;
        auto rdflag = [&](int u) { fl = __hip_atomic_load((LAS unsigned*)(smem + C_F + u * 4), RLX_WG); };
        auto check = [&](int u) {
            if (__builtin_amdgcn_readfirstlane(fl) != 15) {
#pragma unroll 1
                for (unsigned spins = 0; spins < LSPIN_CAP; ++spins) { rdflag(u); if (__builtin_amdgcn_readfirstlane(fl) == 15) break; __builtin_amdgcn_s_sleep(1); }
            }
            FENCE(); };
#pragma unroll
        for (int i = 0; i < 4; ++i) xr[i] = rdx(i);
#pragma unroll
        for (int f = 0; f < WIN; ++f) wf[f] = rdw(f);
        SB();
        SFOR(f, 200)
            constexpr int i = f / 5, t = f % 5;
            if constexpr (f % 20 == 10 - WIN) lds_set(smem + C_P + wid * 4, (i >> 1) + 1);
            if constexpr (f % 20 == 10) rdflag(f / 20 + 1);
            if constexpr (f % 20 == 20 - WIN && f < 190) check(f / 20 + 1);
            if constexpr (f + WIN < 200) wf[(f + WIN) % RING] = rdw(f + WIN);
            if constexpr (f % 20 >= 16 && f < 120) xr[(f / 20 + 1) * 4 + f % 20 - 16] = rdx((f / 20 + 1) * 4 + f % 20 - 16);
            if constexpr (t == 1 && i >= 27 && i < 39) xt[(i + 1) & 1] = rdx(i + 1);
            SB();
            if constexpr (i == 0) MFMA32Z(acc[t], wf[f % RING], xr[0]);
            else if constexpr (i < 28) MFMA32I(acc[t], wf[f % RING], xr[i]);
            else MFMA32I(acc[t], wf[f % RING], xt[i & 1]);
            SB();
        SEND
        MFMA_DRAIN();
        WAITV(0);
        STAMP(2);
#pragma unroll
        for (int t = 0; t < 5; ++t)
#pragma unroll
            for (int b = 0; b < 4; ++b) {
                const int g0 = 8 * t + 2 * b, g1 = g0 + 1;
                const int o0 = g0 < 20 ? kq_base(g0) + tok * (g0 % 5 < 4 ? 16 : 8) : VOFF + (g0 - 20) * 8 + tok * VP;
                const int o1 = g1 < 20 ? kq_base(g1) + tok * (g1 % 5 < 4 ? 16 : 8) : VOFF + (g1 - 20) * 8 + tok * VP;
                *(LAS u32x2*)(smem + OWN + (hl ? o1 : o0)) = (u32x2){pk2(acc[t][4 * b], acc[t][4 * b + 1]), pk2(acc[t][4 * b + 2], acc[t][4 * b + 3])};
            }
        if (hl == 0) *(LAS u32x2*)(smem + OWN + VOFF + tok * VP + 160) = (u32x2){0x3F803F80u, 0x3F803F80u};
        WAITL();
        lds_set(smem + C_E + wid * 4, 1);
        STAMP(3);
        int l2 = tid; asm volatile("" : "+v"(l2)); l2 &= 63;
        int qa0 = (8 * (l2 >> 5) + 2 * ((l2 & 15) >> 2)) * 160 + (l2 & 16) * 2 + (l2 & 3) * 8, qa1 = qa0 + WS0 + 10240;
        asm volatile("" : "+v"(qa0), "+v"(qa1));
        auto rdq = [&](int f) { const int i = f / 3, t = f % 3, j = i >> 2, sl = (20 + j) % 7;
            return trfrag(smem + (j >= 7 ? qa0 + XS0 + (j - 7) * 10240 : sl == 0 ? qa0 + WS0 : qa1 + (sl - 1) * 10240) + (i & 3) * 2560 + t * 64, 160); };
        check(10);
        xt[0] = rdx(28);
#pragma unroll
        for (int f = 0; f < WIN; ++f) wf[f] = rdq(f);
        SB();
        SFOR(f, 120)
            constexpr int i = f / 3, t = f % 3, j = i >> 2;
            if constexpr (f % 24 == 12 && j < 9) rdflag(11 + j / 2);
            if constexpr (f % 24 == 12 - WIN && (j == 2 || j >= 6)) lds_set(smem + C_P + wid * 4, 21 + j);
            if constexpr (f % 24 == 24 - WIN && j < 9) check(11 + j / 2);
            if constexpr (f + WIN < 120) wf[(f + WIN) % RING] = rdq(f + WIN);
            if constexpr (t == 0 && i < 11) xt[(i + 1) & 1] = rdx(28 + i + 1);
            SB();
            if constexpr (i == 0) MFMA32Z(acc[t], wf[f % RING], xt[0]);
            else if constexpr (i < 12) MFMA32I(acc[t], wf[f % RING], xt[i & 1]);
            else MFMA32I(acc[t], wf[f % RING], xr[i - 12]);
            SB();
        SEND
        MFMA_DRAIN();
        STAMP(4);
        wait_min4(smem + C_P, 29);
#pragma unroll
        for (int t = 0; t < 3; ++t)
#pragma unroll
            for (int b = 0; b < 4; ++b)
                if (t < 2 || b < 2) {
                    const int g0 = 8 * t + 2 * b, g1 = g0 + 1;
                    const int o0 = kq_base(g0) + tok * (g0 % 5 < 4 ? 16 : 8), o1 = kq_base(g1) + tok * (g1 % 5 < 4 ? 16 : 8);
                    *(LAS u32x2*)(smem + QST + (hl ? o1 : o0)) =
                        (u32x2){pk2(acc[t][4 * b] * QSCALE, acc[t][4 * b + 1] * QSCALE), pk2(acc[t][4 * b + 2] * QSCALE, acc[t][4 * b + 3] * QSCALE)};
                }
        STAMP(5);
    } else {
        const int hw = wid - 4;
        int known = 0;
        __builtin_amdgcn_s_setprio(3);
        auto lds_or = [&](int off, unsigned v) { FENCE(); if (lane == 0) __hip_atomic_fetch_or((LAS unsigned*)(smem + off), v, RLX_WG); FENCE(); };
        unsigned wo[5], qo[5];
#pragma unroll
        for (int q = 0; q < 5; ++q) { wo[q] = w_off(q) + (hw & 1) * (16 * 3840) + (4 - q) * 1024;
            const int c = q * 64 + lane, rho = c / 10; qo[q] = (unsigned)(((hw & 1) * 32 + (rho & ~7) + 4 * (rho & 1) + ((rho >> 1) & 3)) * 3840 + (c % 10) * 16 + (4 - q) * 1024); }
        auto done = [&](int n) { lds_or(C_F + n * 4, 1u << hw); };
        const unsigned xo1 = (xo ^ 64) + 9216, xo2 = xo + 2 * 9216, xo3 = (xo ^ 64) + 3 * 9216;
        auto round_x = [&](int n) { dma16x4(xo, xo1, xo2, xo3, xb + kt_of(n) * 128 + hw * (32 * 1280), lds0 + XS0 + (n % 3) * 16384 + hw * 4096); };
        auto round_w = [&](int n) {
            const int u = 2 * n + (hw >> 1);
            const unsigned dst = lds0 + (u < 27 ? WS0 + (u % 7) * 10240 : XS0 + (u - 27) * 10240) + (hw & 1) * 5120 + 2048;
            if (u < 20) dma16x5(wo, wb + (size_t)(kt_of(u >> 1) * 64 + (u & 1) * 32) * 3840 - 2048, dst);
            else dma16x5(qo, wb + (size_t)kt_of(u < 23 ? u - 13 : u - 23) * (64 * 3840) - 2048, dst); };
        round_x(1); round_w(1);
        WAITV(9);
        BAR_L();
#pragma unroll 1
        for (int n = ABL4N; n <= 12; ++n) {
            const int need = 2 * n - 5;
            if (need > known) known = min4(smem + C_P);
            if (need > known) { WAITV(0); done(n - 1); known = wait_min4(smem + C_P, need); }
            if (n < 10) round_x(n);
            round_w(n);
            if (n < 10) WAITV(9); else WAITV(5);
            done(n - 1);
        }
        WAITV(0); done(12);
        STAMP(1);
        const u64 tag = rdtag();
        auto a_chunk = [&](int a) { int o;
            if (a < 320) { const int hd = a / 80, rem = a % 80; o = hd * 5120 + (rem < 32 ? (96 + rem) * 16 : rem < 64 ? 2048 + (64 + rem) * 16 : 4096 + 768 + (rem - 64) * 16); }
            else o = VOFF + 96 * VP + (a - 320) * 16;
            return o; };
        if (rt < 15) {
            const __amdgpu_buffer_rsrc_t kvr = __builtin_amdgcn_make_buffer_rsrc(ws + WS_KV + me * KVG, 0, KVB, 0x00020000);
            wait_min4(smem + C_E, 1);
            STAMP(2);
            if (!isA) { u32x4 v[11];
#pragma unroll
                for (int j = 0; j < 11; ++j) if (j < 10 || hw == 3) v[j] = *(LAS u32x4*)(smem + OWN + hw * 10240 + j * 1024 + lane * 16);
#pragma unroll
                for (int j = 0; j < 11; ++j) if (j < 10 || hw == 3) __builtin_amdgcn_raw_buffer_store_b128(v[j], kvr, hw * 10240 + j * 1024 + lane * 16, 0, 16);
            } else {
#pragma unroll
                for (int j = 0; j < 3; ++j) { const int a = (4 * j + hw) * 64 + lane;
                    if (a < 656) { const int o = a_chunk(a); __builtin_amdgcn_raw_buffer_store_b128(*(LAS u32x4*)(smem + OWN + o), kvr, o, 0, 16); } }
            }
            STAMP(3);
        }
        wait_min4(smem + C_P, 23);
        round_w(13); round_w(14);
        WAITV(10);
        if (rt < 15) {
            unsigned old = 0;
            if (lane == 0) old = __hip_atomic_fetch_add((LAS unsigned*)(smem + C_CNT), 1u, RLX_WG);
            if (lane == 0 && old == 3) __hip_atomic_store((GAS u64*)(ws + WS_FKV + me * 128), tag, RLX_AGENT);
            STAMP(4);
        }
        asm volatile("s_waitcnt vmcnt(6)" ::: "memory"); WAITV(5); done(13); WAITV(0); done(14);
        poll_flags((GAS u64*)(ws + WS_FKV + (me - 8 * (lane + 1)) * 128), lane < nnb, tag);
        STAMP(5);
        if (!isA) {
            const int n = hw >> 1, half = hw & 1;
            if (n < nnb) {
                const __amdgpu_buffer_rsrc_t nbr = __builtin_amdgcn_make_buffer_rsrc(ws + WS_KV + (me - 8 * (n + 1)) * KVG, 0, KVB, 0x00020000);
                u32x4 v[21];
#pragma unroll
                for (int j = 0; j < 21; ++j) if (j < 20 || half) v[j] = __builtin_amdgcn_raw_buffer_load_b128(nbr, half * 20480 + j * 1024 + lane * 16, 0, 16);
                wait_min4(smem + C_P, 27);
#pragma unroll
                for (int j = 0; j < 21; ++j) if (j < 20 || half) *(LAS u32x4*)(smem + KV0 + (1 - n) * KVB + half * 20480 + j * 1024 + lane * 16) = v[j];
            }
        } else if (nnb) {
            const __amdgpu_buffer_rsrc_t nbr = __builtin_amdgcn_make_buffer_rsrc(ws + WS_KV + (me - 8) * KVG, 0, KVB, 0x00020000);
            u32x4 v[3];
#pragma unroll
            for (int j = 0; j < 3; ++j) { const int a = (4 * j + hw) * 64 + lane; if (a < 656) v[j] = __builtin_amdgcn_raw_buffer_load_b128(nbr, a_chunk(a), 0, 16); }
            wait_min4(smem + C_P, 27);
#pragma unroll
            for (int j = 0; j < 3; ++j) { const int a = (4 * j + hw) * 64 + lane; if (a < 656) *(LAS u32x4*)(smem + KV0 + KVB + a_chunk(a)) = v[j]; }
        }
        if (hw == 3) { wait_min4(smem + C_P, 27); if (lane < 2) *(LAS u32x2*)(smem + ONE + lane * 5120) = (u32x2){0x3F803F80u, 0x3F803F80u}; }
        __builtin_amdgcn_s_setprio(0);
        STAMP(6);
    }
    const int qi = wid & 3, hp = wid >> 2;
    const int ud = qi + 8;
    const bool far = isA ? (4 * rt + qi >= 1) : (4 * rt + qi >= 8);
    const int u0 = isA ? (far ? ud - 1 : ud) : (far ? qi : 8 - 4 * rt);
    f32x16 cd, cf;
    auto masks = [&](int q) {
      const unsigned low = (2u << q) - 1u;
      unsigned vd = low, vf = ~low;
      if (isA) { const u64 w = 0x5555ull << (q + 18);
          vd = (unsigned)(w >> 32); vf = (unsigned)w; }
      if (!far) vf = ~0u;
      const unsigned id = ~vd >> (4 * hl), jf = ~vf >> (4 * hl);
#pragma unroll
      for (int r = 0; r < 16; ++r) {
          const int c0 = (r & 3) + 8 * (r >> 2);
          cd[r] = __uint_as_float((unsigned)__builtin_amdgcn_sbfe((int)id, c0, 1) & 0xFF800000u);
          cf[r] = __uint_as_float((unsigned)__builtin_amdgcn_sbfe((int)jf, c0, 1) & 0xFF800000u);
      } };
    masks(r32);
    unsigned oo[5]; u32x4 wreg[13];
#pragma unroll
    for (int q = 0; q < 5; ++q) { const int c = q * 64 + lane, rho = c / 10;
        oo[q] = (unsigned)((wid * 80 + (rho & ~7) + 4 * (rho & 1) + ((rho >> 1) & 3)) * 1280 + hg * 160 + (c % 10) * 16); }
    auto ldwo = [&](int i) { wreg[i] = __builtin_amdgcn_raw_buffer_load_b128(wor, oo[i % 5] + (i / 5) * (32 * 1280), 0, 0); };
    BAR_L();
    STAMP(7);
    u32x2 ob[2][3];
    {
        bf16x8 q1[2]; u32x2 q2[2];
#pragma unroll
        for (int e = 0; e < 2; ++e) {
            q1[e] = *(LAS bf16x8*)(smem + QST + (2 * hp + e) * 5120 + hl * 2048 + (qi * 32 + r32) * 16);
            q2[e] = *(LAS u32x2*)(smem + QST + (2 * hp + e) * 5120 + 4096 + (qi * 32 + r32) * 8);
            if (hl) q2[e] = (u32x2){0u, 0u};
        }
        const int kb1 = KV0 + hp * 10240 + hl * 2048 + r32 * 16;
        const int kb2 = hl ? ONE : KV0 + hp * 10240 + 4096 + r32 * 8, nh = hl ^ 1;
        const int vrow = KV0 + VOFF + (4 * hl + ((lane & 15) >> 2)) * VP;
        const bool ones = (lane & 16) && (lane & 3);
        const int vb0 = vrow + (ones ? 160 : hp * 80 + (lane & 16) * 2 + (lane & 3) * 8), vb1 = vb0 + (ones ? 0 : 40);
        struct KF { bf16x8 a; s16x4 b; };
        struct VF { bf16x8 a, b; };
        auto rdk = [&](int u, int e) { const int so = (u >> 2) * KVB, tw = u & 3; KF k;
            k.a = *(LAS bf16x8*)(smem + kb1 + so + tw * 512 + e * 5120); k.b = *(LAS s16x4*)(smem + __umul24(so + tw * 256, nh) + kb2 + e * 5120); return k; };
        auto rdv = [&](int u, int e) { LAS char* p = smem + (e ? vb1 : vb0) + (u >> 2) * KVB + (u & 3) * (32 * VP); VF v;
            v.a = trfrag(p, 8 * VP); v.b = trfrag(p + 16 * VP, 8 * VP); return v; };
        auto qk = [&](const KF& k, int e, const f32x16& c) { return MFMA32K8(k.b, __builtin_bit_cast(s16x4, q2[e]), MFMA32(k.a, q1[e], c)); };
        const f32x16 zero = {};
        auto max16 = [&](const f32x16& s, float m) {
#pragma unroll
            for (int r = 0; r < 16; ++r) m = __builtin_elementwise_maximum(m, s[r]);
            return m; };
        auto set_ref = [&](float m, int e) { const unsigned nb = pk2(-m, 0.f); if (hl) q2[e][0] = nb; };
        auto ex4 = [&](f32x16& s, int b) {
#pragma unroll
            for (int r = 0; r < 4; ++r) s[b + r] = __builtin_amdgcn_exp2f(s[b + r]); };
        auto pk8 = [&](const f32x16& s, int b) { return __builtin_bit_cast(bf16x8, (u32x4){pk2(s[b], s[b + 1]), pk2(s[b + 2], s[b + 3]), pk2(s[b + 4], s[b + 5]), pk2(s[b + 6], s[b + 7])}); };
        auto phase = [&](auto nx, f32x16& cur, const VF& v, f32x16& o, f32x16& nxt, const KF& k, int e) __attribute__((always_inline)) {
            constexpr int NX = decltype(nx)::value;
            SB(); ex4(cur, 0); SB();
            if constexpr (NX) nxt = MFMA32(k.a, q1[e], NX == 1 ? zero : NX == 2 ? cf : cd);
            SB(); ex4(cur, 4); { const bf16x8 p = pk8(cur, 0); SB(); o = MFMA32(v.a, p, o); }
            SB(); ex4(cur, 8); SB();
            if constexpr (NX) nxt = MFMA32K8(k.b, __builtin_bit_cast(s16x4, q2[e]), nxt);
            SB(); ex4(cur, 12); { const bf16x8 p = pk8(cur, 8); SB(); o = MFMA32(v.b, p, o); }
            SB(); };
        f32x16 o0, o1, sa, sb; KF kx; VF vx;
        auto step = [&](auto m0, auto m1, int u) __attribute__((always_inline)) {
            constexpr int M1 = decltype(m1)::value;
            KF ky; if constexpr (M1) ky = rdk(u + 1, 0);
            const VF vy = rdv(u, 1);
            phase(m0, sa, vx, o0, sb, kx, 1);
            if constexpr (M1) { kx = rdk(u + 1, 1); vx = rdv(u + 1, 0); }
            phase(m1, sb, vy, o1, sa, ky, 0); };
        auto run = [&](auto fast) __attribute__((always_inline)) {
            o0 = zero; o1 = zero;
            kx = rdk(u0, 1); vx = rdv(u0, 0);
            if constexpr (decltype(fast)::value) {
                sa = qk(rdk(u0, 0), 0, cf);
                SFOR(it, 9)
                    if constexpr (it < 7) { ldwo(2 * it); if constexpr (it < 6) ldwo(2 * it + 1); }
                    step(IC<it == 0 ? 2 : it == 8 ? 3 : 1>{}, IC<it == 8 ? 0 : it == 7 ? 3 : 1>{}, u0 + it);
                SEND
            } else {
                if (u0 < ud) {
                    sa = qk(rdk(u0, 0), 0, cf);
                    if (u0 + 1 < ud) {
                        step(IC<2>{}, IC<1>{}, u0);
#pragma unroll 1
                        for (int u = u0 + 1; u + 1 < ud; ++u) step(IC<1>{}, IC<1>{}, u);
                        step(IC<1>{}, IC<3>{}, ud - 1);
                    } else step(IC<2>{}, IC<3>{}, u0);
                } else sa = qk(rdk(ud, 0), 0, cd);
                step(IC<3>{}, IC<0>{}, ud);
            } };
        { const f32x16 s0 = qk(rdk(ud, 0), 0, cd), s1 = qk(rdk(ud, 1), 1, cd);
          set_ref(halfmax(max16(s0, s0[0])), 0); set_ref(halfmax(max16(s1, s1[0])), 1); }
        if (u0 + 8 == ud) run(IC<1>{});
        else {
#pragma unroll
            for (int i = 0; i < 13; ++i) ldwo(i);
            run(IC<0>{});
        }
        if (!__all(o0[12] < 1.152921504606847e18f && o1[12] < 1.152921504606847e18f)) {
            { int q = r32; asm volatile("" : "+v"(q)); masks(q); }
#pragma unroll
            for (int e = 0; e < 2; ++e) {
                if (hl) q2[e][0] = 0u;
                const f32x16 s = qk(rdk(ud, e), e, cd);
                float m = max16(s, s[0]);
                if (u0 < ud) m = max16(qk(rdk(u0, e), e, cf), m);
#pragma unroll 1
                for (int u = u0 + 1; u < ud; ++u) m = max16(qk(rdk(u, e), e, zero), m);
                set_ref(halfmax(m), e);
            }
#pragma unroll
            for (int e = 0; e < 2; ++e) {
                f32x16 o = zero;
#pragma unroll 1
                for (int u = u0; u <= ud; ++u) { f32x16 t = u == ud ? qk(rdk(u, e), e, cd) : u == u0 ? qk(rdk(u, e), e, cf) : qk(rdk(u, e), e, zero);
                    const VF v = rdv(u, e); ex4(t, 0); ex4(t, 4); ex4(t, 8); ex4(t, 12);
                    o = MFMA32(v.a, pk8(t, 0), o); o = MFMA32(v.b, pk8(t, 8), o); }
                if (e) o1 = o; else o0 = o;
            }
        }
        const float rl0 = __builtin_amdgcn_rcpf(o0[12]), rl1 = __builtin_amdgcn_rcpf(o1[12]);
#pragma unroll
        for (int j = 0; j < 3; ++j) {
            ob[0][j] = (u32x2){pk2(o0[4 * j] * rl0, o0[4 * j + 1] * rl0), pk2(o0[4 * j + 2] * rl0, o0[4 * j + 3] * rl0)};
            ob[1][j] = (u32x2){pk2(o1[4 * j] * rl1, o1[4 * j + 1] * rl1), pk2(o1[4 * j + 2] * rl1, o1[4 * j + 3] * rl1)}; }
    }
    STAMP(8);
    const __amdgpu_buffer_rsrc_t orr = __builtin_amdgcn_make_buffer_rsrc(ws + WS_O + (br * 16 + rt) * OG, 0, 4 * 40960, 0x00020000);
    { const unsigned obase = (unsigned)(qi * 40960 + (hg * 10 + hp * 5) * 512 + r32 * 16);
      { auto rx = __builtin_amdgcn_permlane32_swap(ob[0][0][0], ob[0][1][0], false, false);
        auto ry = __builtin_amdgcn_permlane32_swap(ob[0][0][1], ob[0][1][1], false, false);
        __builtin_amdgcn_raw_buffer_store_b128((u32x4){rx[0], ry[0], rx[1], ry[1]}, orr, obase + hl * 512, 0, 16); }
      if (hl == 0) __builtin_amdgcn_raw_buffer_store_b128((u32x4){ob[0][2][0], ob[0][2][1], ob[1][0][0], ob[1][0][1]}, orr, obase + 1024, 0, 16);
      { auto fx = __builtin_amdgcn_permlane32_swap(ob[1][0][0], ob[1][1][0], false, false);
        auto fy = __builtin_amdgcn_permlane32_swap(ob[1][0][1], ob[1][1][1], false, false);
        auto sx = __builtin_amdgcn_permlane32_swap(ob[1][1][0], ob[1][2][0], false, false);
        auto sy = __builtin_amdgcn_permlane32_swap(ob[1][1][1], ob[1][2][1], false, false);
        __builtin_amdgcn_raw_buffer_store_b128((u32x4){fx[1], fy[1], sx[0], sy[0]}, orr, obase + 1536 + hl * 512, 0, 16); } }
    const u64 tag = rdtag();
    if (wid == 0 && lane == 0) { GAS unsigned* d = (GAS unsigned*)(ws + WS_DBG + bx * 128);
        __hip_atomic_store(d + 0, dst_[0], RLX_AGENT); __hip_atomic_store(d + 1, dst_[1], RLX_AGENT); __hip_atomic_store(d + 2, dst_[2], RLX_AGENT);
        __hip_atomic_store(d + 3, dst_[7], RLX_AGENT); __hip_atomic_store(d + 4, dst_[8], RLX_AGENT); }
    WAITV(0);
    { unsigned old = 0;
      if (lane == 0) old = __hip_atomic_fetch_add((LAS unsigned*)(smem + C_CNT + 4), 1u, RLX_WG);
      if (lane == 0 && old == 7) __hip_atomic_store((GAS u64*)(ws + WS_FO + me * 128), tag, RLX_AGENT); }
    STAMP(9);
    BAR_L();
#pragma unroll
    for (int i = 0; i < 13; ++i) if (i < 12 || lane < 32) *(LAS u32x4*)(smem + WOB + wid * 12800 + i * 1024 + lane * 16) = wreg[i];
    BAR_L();
    STAMP(10);
    poll_flags((GAS u64*)(ws + WS_FO + ((me & ~7) + (lane & 7)) * 128), lane < 8, tag);
    STAMP(11);
    f32x16 oc[3];
    { bf16x8 af[6], of[20];
      const int aa = WOB + hp * 51200 + (8 * hl + 2 * ((lane & 15) >> 2)) * 160 + (lane & 16) * 2 + (lane & 3) * 8;
      auto rda = [&](int f) { return trfrag(smem + aa + (f / 3) * 2560 + (f % 3) * 64, 160); };
      auto ldo = [&](int k) { of[k] = __builtin_bit_cast(bf16x8, __builtin_amdgcn_raw_buffer_load_b128(orr, qi * 40960 + (hp * 20 + k) * 1024 + lane * 16, 0, 16)); };
#pragma unroll
      for (int k = 0; k < OPF; ++k) ldo(k);
#pragma unroll
      for (int f = 0; f < 4; ++f) af[f] = rda(f);
      SB();
      SFOR(f, 60)
          if constexpr (f % 3 == 0 && f / 3 + OPF < 20) ldo(f / 3 + OPF);
          if constexpr (f + 4 < 60) af[(f + 4) % 6] = rda(f + 4);
          SB();
          if constexpr (f < 3) MFMA32Z(oc[f], af[f % 6], of[0]); else MFMA32I(oc[f % 3], af[f % 6], of[f / 3]);
          SB();
      SEND }
    MFMA_DRAIN();
    STAMP(12);
    { LAS char* mine = smem + RED + wid * 6144 + lane * 16; LAS char* other = smem + RED + (wid ^ 4) * 6144 + lane * 16;
      if (hp) {
#pragma unroll
          for (int b = 0; b < 4; ++b) *(LAS f32x4*)(mine + b * 1024) = (f32x4){oc[0][4 * b], oc[0][4 * b + 1], oc[0][4 * b + 2], oc[0][4 * b + 3]};
#pragma unroll
          for (int b = 0; b < 2; ++b) *(LAS f32x4*)(mine + (4 + b) * 1024) = (f32x4){oc[2][4 * b], oc[2][4 * b + 1], oc[2][4 * b + 2], oc[2][4 * b + 3]};
      } else {
#pragma unroll
          for (int b = 0; b < 4; ++b) *(LAS f32x4*)(mine + b * 1024) = (f32x4){oc[1][4 * b], oc[1][4 * b + 1], oc[1][4 * b + 2], oc[1][4 * b + 3]};
      }
      BAR_L();
      char* orow = out + (size_t)(rt * 128 + qi * 32 + r32) * 2560 + br * 1280 + hg * 160 + hl * 16;
      auto fin = [&](const f32x16& c, int b, int slot) { const f32x4 x = *(LAS f32x4*)(other + slot * 1024);
          return (u32x2){pk2(c[4 * b] + x[0], c[4 * b + 1] + x[1]), pk2(c[4 * b + 2] + x[2], c[4 * b + 3] + x[3])}; };
      auto st2 = [&](u32x2 a, u32x2 b, int col) {
          auto rx = __builtin_amdgcn_permlane32_swap(a[0], b[0], false, false); auto ry = __builtin_amdgcn_permlane32_swap(a[1], b[1], false, false);
          *(u32x4*)(orow + col * 2) = (u32x4){rx[0], ry[0], rx[1], ry[1]}; };
      if (hp) { st2(fin(oc[1], 0, 0), fin(oc[1], 1, 1), 32); st2(fin(oc[1], 2, 2), fin(oc[1], 3, 3), 48); }
      else { st2(fin(oc[0], 0, 0), fin(oc[0], 1, 1), 0); st2(fin(oc[0], 2, 2), fin(oc[0], 3, 3), 16); st2(fin(oc[2], 0, 4), fin(oc[2], 1, 5), 64); } }
    if (bx == 0) {
        if (wid < 4) poll_flags((GAS u64*)(ws + WS_FO + tid * 128), true, tag);
        __syncthreads();
        if (tid == 0) __hip_atomic_store((GAS u64*)ws, tag, RLX_AGENT);
        if (wid == 0) {
            const unsigned now = (unsigned)__builtin_amdgcn_s_memrealtime();
            int mn0 = 0x7fffffff, mx0 = -0x7fffffff, a1 = 0, a2 = 0, a3 = 0, a4 = 0, mx8 = -0x7fffffff, b1 = 0;
            for (int i = 0; i < 4; ++i) { const int b = i * 64 + lane; GAS unsigned* d = (GAS unsigned*)(ws + WS_DBG + b * 128);
                const unsigned t0 = __hip_atomic_load(d, RLX_AGENT), t1 = __hip_atomic_load(d + 1, RLX_AGENT), t2 = __hip_atomic_load(d + 2, RLX_AGENT), t7 = __hip_atomic_load(d + 3, RLX_AGENT), t8 = __hip_atomic_load(d + 4, RLX_AGENT);
                const int r0 = (int)(t0 - now); mn0 = min(mn0, r0); mx0 = max(mx0, r0); mx8 = max(mx8, (int)(t8 - now));
                if (i < 2) { a1 += t1 - t0; a2 += t2 - t1; a3 += t7 - t2; a4 += t8 - t7; } else b1 += t2 - t0; }
            for (int m = 32; m >= 1; m >>= 1) { mn0 = min(mn0, __shfl_xor(mn0, m)); mx0 = max(mx0, __shfl_xor(mx0, m)); mx8 = max(mx8, __shfl_xor(mx8, m));
                a1 += __shfl_xor(a1, m); a2 += __shfl_xor(a2, m); a3 += __shfl_xor(a3, m); a4 += __shfl_xor(a4, m); b1 += __shfl_xor(b1, m); }
            int M = DIAG == 0 ? 500 : DIAG == 1 ? mx0 - mn0 : DIAG == 2 ? a1 / 128 : DIAG == 3 ? a2 / 128 : DIAG == 4 ? a3 / 128 : DIAG == 5 ? -mn0 : DIAG == 6 ? a4 / 128 : b1 / 128;
            M = min(max(M, 0), 5000);
            const unsigned until = now + 1000 + 2 * M;
            while ((int)((unsigned)__builtin_amdgcn_s_memrealtime() - until) < 0) __builtin_amdgcn_s_sleep(2);
        }
    }
}

extern "C" void kernel_launch(void* const* d_in, const int* in_sizes, int n_in, void* d_out, int out_size, void* d_ws, size_t ws_size,
                              hipStream_t stream) {
    fused_attn_kernel<<<256, 512, 0, stream>>>((const char*)d_in[0], (const char*)d_in[1], (const char*)d_in[2], (const char*)d_in[3],
                                               (const char*)d_in[4], (const char*)d_in[5], (char*)d_out, (char*)d_ws);
}
```

```cpp
#define ADELAY 3
#include <hip/hip_runtime.h>
#include <hip/hip_bf16.h>
#include <stdint.h>

typedef short bf16x8 __attribute__((ext_vector_type(8)));
typedef short s16x4 __attribute__((ext_vector_type(4)));
typedef float f32x2 __attribute__((ext_vector_type(2)));
typedef float f32x4 __attribute__((ext_vector_type(4)));
typedef float f32x16 __attribute__((ext_vector_type(16)));
typedef unsigned u32x4 __attribute__((ext_vector_type(4)));
typedef unsigned u32x2 __attribute__((ext_vector_type(2)));
typedef __bf16 bfx2 __attribute__((ext_vector_type(2)));
typedef unsigned long long u64;
#define LAS __attribute__((address_space(3)))
#define GAS __attribute__((address_space(1)))
#define DI __device__ __forceinline__
#define RLX_AGENT __ATOMIC_RELAXED, __HIP_MEMORY_SCOPE_AGENT
#define RLX_WG __ATOMIC_RELAXED, __HIP_MEMORY_SCOPE_WORKGROUP

constexpr int LDS_BYTES = 163840;
constexpr int VOFF = 20480, VP = 168;
constexpr int KVB = VOFF + 128 * VP;
constexpr int KV0 = LDS_BYTES - 3 * KVB;
constexpr int OWN = KV0 + 2 * KVB;
constexpr int C_F = 0, C_P = 256, C_E = 272, C_CNT = 288, C_TAG = 320;
constexpr int XS0 = 512, WS0 = XS0 + 3 * 16384, QST = 512, ONE = 31744, WOB = 512, RED = WOB + 102400;
static_assert(WS0 + 7 * 10240 <= OWN, "ring overlaps the own image");
static_assert(ONE + 5120 + 8 <= KV0 && RED + 8 * 6144 <= LDS_BYTES, "LDS map");
constexpr unsigned WS_FKV = 4096, WS_FO = WS_FKV + 256 * 128, WS_KV = 131072;
constexpr unsigned KVG = KVB + 128, OG = 4 * 40960 + 128;
constexpr unsigned WS_O = WS_KV + 256 * KVG;
constexpr unsigned WS_END = WS_O + 32 * OG;
constexpr unsigned WS_DBG = WS_END;
constexpr unsigned SPIN_CAP = 400000, LSPIN_CAP = 4000000;
constexpr float QSCALE = 0.22360679774997896f * 1.4426950408889634f;
#define OPF 6
#define GSLEEP 1
#define ABL4N 2
#define RING (WIN + 2)
#define WIN 5

#define MFMA32(a, b, c) __builtin_amdgcn_mfma_f32_32x32x16_bf16((a), (b), (c), 0, 0, 0)
#define MFMA32K8(a, b, c) __builtin_amdgcn_mfma_f32_32x32x8bf16_1k((a), (b), (c), 0, 0, 0)
#define MFMA32I(acc, a, b) asm volatile("v_mfma_f32_32x32x16_bf16 %0, %1, %2, %0" : "+v"(acc) : "v"(a), "v"(b))
#define MFMA32Z(acc, a, b) asm volatile("v_mfma_f32_32x32x16_bf16 %0, %1, %2, 0" : "=&v"(acc) : "v"(a), "v"(b))
#define MFMA_DRAIN() asm volatile("s_nop 7\n\ts_nop 4" ::: "memory")
#define WAITV(N) asm volatile("s_waitcnt vmcnt(" #N ")" ::: "memory")
#define WAITL() asm volatile("s_waitcnt lgkmcnt(0)" ::: "memory")
#define BAR_L() asm volatile("s_waitcnt lgkmcnt(0)\n\ts_barrier" ::: "memory")
#define SB() __builtin_amdgcn_sched_barrier(0)
#define FENCE() asm volatile("" ::: "memory")

#define STAMP(i) do {} while (0)

template <int V> struct IC { static constexpr int value = V; };
template <int I, int N, class F> DI void static_for(F&& f) { if constexpr (I < N) { f(IC<I>{}); static_for<I + 1, N>(f); } }
#define SFOR(var, N) static_for<0, N>([&](auto ic_##var) __attribute__((always_inline)) { constexpr int var = decltype(ic_##var)::value;
#define SEND });

DI void dma16(unsigned voff, const char* sbase, unsigned ldsaddr) {
    asm volatile("s_mov_b32 m0, %2\n\ts_nop 0\n\tglobal_load_lds_dwordx4 %0, %1" :: "v"(voff), "s"(sbase), "s"(ldsaddr) : "memory");
}
DI void dma16x4(unsigned v0, unsigned v1, unsigned v2, unsigned v3, const char* sbase, unsigned ldsaddr) {
    asm volatile("s_mov_b32 m0, %5\n\ts_nop 0\n\tglobal_load_lds_dwordx4 %0, %4\n\tglobal_load_lds_dwordx4 %1, %4 offset:1024\n\t"
                 "global_load_lds_dwordx4 %2, %4 offset:2048\n\tglobal_load_lds_dwordx4 %3, %4 offset:3072"
                 :: "v"(v0), "v"(v1), "v"(v2), "v"(v3), "s"(sbase), "s"(ldsaddr) : "memory");
}
DI void dma16x5(const unsigned (&v)[5], const char* sbase, unsigned ldsaddr) {
    asm volatile("s_mov_b32 m0, %6\n\ts_nop 0\n\tglobal_load_lds_dwordx4 %0, %5 offset:-2048\n\tglobal_load_lds_dwordx4 %1, %5 offset:-1024\n\t"
                 "global_load_lds_dwordx4 %2, %5\n\tglobal_load_lds_dwordx4 %3, %5 offset:1024\n\tglobal_load_lds_dwordx4 %4, %5 offset:2048"
                 :: "v"(v[0]), "v"(v[1]), "v"(v[2]), "v"(v[3]), "v"(v[4]), "s"(sbase), "s"(ldsaddr) : "memory");
}
DI void dma4(const char* p, unsigned ldsaddr) {
    asm volatile("s_mov_b32 m0, %1\n\ts_nop 0\n\tglobal_load_lds_dword %0, off" :: "v"(p), "s"(ldsaddr) : "memory");
}
DI unsigned pk2(float lo, float hi) { f32x2 v = {lo, hi}; return __builtin_bit_cast(unsigned, __builtin_convertvector(v, bfx2)); }
typedef short v4i16_t __attribute__((ext_vector_type(4)));
DI bf16x8 trfrag(LAS char* p, int second) {
    s16x4 a = __builtin_bit_cast(s16x4, __builtin_amdgcn_ds_read_tr16_b64_v4i16((LAS v4i16_t*)p));
    s16x4 b = __builtin_bit_cast(s16x4, __builtin_amdgcn_ds_read_tr16_b64_v4i16((LAS v4i16_t*)(p + second)));
    return __builtin_shufflevector(a, b, 0, 1, 2, 3, 4, 5, 6, 7);
}
DI float halfmax(float m) { auto rr = __builtin_amdgcn_permlane32_swap(__float_as_uint(m), __float_as_uint(m), false, false);
    return __builtin_elementwise_maximum(__uint_as_float(rr[0]), __uint_as_float(rr[1])); }

DI void poll_flags(GAS u64* word, bool active, u64 tag) {
#pragma unroll 1
    for (unsigned spins = 0; spins < SPIN_CAP; ++spins) {
        u64 v = tag;
        if (active) v = __hip_atomic_load(word, RLX_AGENT);
        if (__all(v == tag)) break;
        __builtin_amdgcn_s_sleep(1);
    }
    __builtin_amdgcn_fence(__ATOMIC_ACQUIRE, "wavefront");
}
DI int min4(LAS char* p) {
    const u64 a = __hip_atomic_load((LAS u64*)p, RLX_WG), b = __hip_atomic_load((LAS u64*)(p + 8), RLX_WG);
    const unsigned m = min(min((unsigned)a, (unsigned)(a >> 32)), min((unsigned)b, (unsigned)(b >> 32)));
    return __builtin_amdgcn_readfirstlane((int)m);
}
DI int wait_min4(LAS char* p, int need) {
    int v = min4(p);
#pragma unroll 1
    for (unsigned spins = 0; v < need && spins < LSPIN_CAP; ++spins) { __builtin_amdgcn_s_sleep(GSLEEP); v = min4(p); }
    FENCE();
    return v;
}
DI void lds_set(LAS char* p, unsigned v) { FENCE(); __hip_atomic_store((LAS unsigned*)p, v, RLX_WG); FENCE(); }

__global__ __launch_bounds__(512, 2) void fused_attn_kernel(const char* __restrict__ xA, const char* __restrict__ wA, const char* __restrict__ oA,
                                                           const char* __restrict__ xB, const char* __restrict__ wB, const char* __restrict__ oB,
                                                           char* __restrict__ out, char* __restrict__ ws) {
    __shared__ __attribute__((aligned(16))) char smem_[LDS_BYTES];
    LAS char* smem = (LAS char*)smem_;
    const unsigned lds0 = (unsigned)(uintptr_t)smem;
    STAMP(0);
    const int tid = threadIdx.x, lane = tid & 63;
    const int wid = __builtin_amdgcn_readfirstlane(tid >> 6);
    const int bx = blockIdx.x, xcd = bx & 7, idx = bx >> 3;
    const int isA = idx >> 4, rt = (xcd >> 1) * 4 + ((idx >> 2) & 3), hg = (xcd & 1) * 4 + (idx & 3);
    const int br = isA ? 0 : 1;
    const int k0 = isA ? (((rt & 3) * 5 + 1) / 2 + ((hg & 3) * 5 + 1) / 2) % 10 : 0;
    const char* xb = (isA ? xA : xB) + (size_t)rt * (128 * 1280);
    const char* wb = (isA ? wA : wB) + hg * 160;
    const int me = (br * 16 + rt) * 8 + hg;
    const int r32 = lane & 31, hl = lane >> 5;
    auto kt_of = [&](int s) { int k = s + k0; return k >= 10 ? k - 10 : k; };
    if (isA) {
        const int j16 = idx & 15;
        const char* wl = wB + (hg >> 2) * 640 + j16 * (4 * 3840);
        const char* xl = xB + (size_t)(rt * 128 + (hg & 3) * 32 + r32) * 1280 + hl * 128;
        auto pfx = [&](int i) { return xl + i * 256; };
        auto pfkv = [&](int i) { int n = i * 64 + lane; n = n < 400 ? n : 399; const int sl = n / 40, r = n % 40, c = r % 10;
            return wl + (sl * 64 + r / 10) * 3840 + 1280 + (c < 5 ? c * 128 : 640 + c * 128); };
        auto pfq = [&](int i) { int n = i * 64 + lane; n = n < 200 ? n : 199; int sl = n / 20 + 7; sl = sl >= 10 ? sl - 10 : sl; const int r = n % 20;
            return wl + (sl * 64 + r / 5) * 3840 + (r % 5) * 128; };
        dma4(wid & 1 ? pfx(wid >> 1) : pfkv(wid >> 1), lds0 + OWN + wid * 256);
        dma4(wid < 3 ? pfkv(4 + wid) : wid == 3 ? pfx(4) : pfq(wid - 4), lds0 + OWN + wid * 256);
        for (int i = 0; i < ADELAY; ++i) __builtin_amdgcn_s_sleep(16);
    }

    if (wid == 4 && lane < 2) dma4(ws + lane * 4, lds0 + C_TAG);
    if (wid == 7) { const char* p = ws + 2048;
        if (lane < 6) { const int im = me - 8 * (lane >> 1); if (im >= 0) p = ws + WS_KV + im * KVG + ((lane & 1) ? KVB : -128); }
        else if (lane < 8) p = ws + WS_O + (br * 16 + rt) * OG + ((lane & 1) ? (int)OG - 128 : -128);
        else if (lane < 10) p = (isA ? oA : oB) + (lane & 1) * (639 * 1280);
        else if (lane < 12) p = out + (size_t)(rt * 128 + (lane & 1) * 127) * 2560;
        dma4(p, lds0 + OWN); }
    const unsigned xo = (unsigned)((lane >> 3) * 1280 + (((lane & 7) ^ (lane >> 4)) << 4));
    auto w_off = [&](int q) { const int c = q * 64 + lane, row = c / 20, j = c % 20; return (unsigned)(row * 3840 + (j < 10 ? 1280 + j * 16 : 2400 + j * 16)); };
    { const char* xs = xb + k0 * 128 + wid * (16 * 1280);
      dma16(xo, xs, lds0 + XS0 + wid * 2048);
      dma16(xo ^ 64, xs + 8 * 1280, lds0 + XS0 + wid * 2048 + 1024);
      const char* wsrc = wb + (size_t)k0 * (64 * 3840);
      const unsigned wv = w_off(wid < 5 ? wid : wid - 5);
      if (wid < 5) { dma16(wv, wsrc, lds0 + WS0 + wid * 1024); dma16(wv, wsrc + 16 * 3840, lds0 + WS0 + (5 + wid) * 1024);
          if (wid >= 3) { dma16(wv, wsrc + 32 * 3840, lds0 + WS0 + (10 + wid) * 1024); dma16(wv, wsrc + 48 * 3840, lds0 + WS0 + (15 + wid) * 1024); } }
      else { dma16(wv, wsrc + 32 * 3840, lds0 + WS0 + (5 + wid) * 1024); dma16(wv, wsrc + 48 * 3840, lds0 + WS0 + (10 + wid) * 1024); } }
    if (tid < 80) *(LAS unsigned*)(smem + tid * 4) = 0u;
    const int nnb = isA ? (rt > 0 ? 1 : 0) : (rt > 1 ? 2 : rt);
    auto kq_base = [&](int fg) { const int hd = fg / 5, dg = fg % 5; return dg < 4 ? hd * 5120 + (dg >> 1) * 2048 + (dg & 1) * 8 : hd * 5120 + 4096; };

    auto rdtag = [&]() { const u64 t = *(LAS u64*)(smem + C_TAG);
        const u64 e = ((u64)(unsigned)__builtin_amdgcn_readfirstlane((int)(t >> 32)) << 32) | (unsigned)__builtin_amdgcn_readfirstlane((int)t);
        return e * 6364136223846793005ull + 1442695040888963407ull; };
    const __amdgpu_buffer_rsrc_t wor = __builtin_amdgcn_make_buffer_rsrc((void*)(isA ? oA : oB), 0, 640 * 1280, 0x00020000);

    if (wid < 4) {
        asm volatile("s_waitcnt vmcnt(0) lgkmcnt(0)\n\ts_barrier" ::: "memory");
        STAMP(1);
        bf16x8 xr[28], xt[2], wf[RING];
        f32x16 acc[5];
        const int tok = wid * 32 + r32;
        const int xa = XS0 + tok * 128 + ((hl ^ ((tok >> 1) & 7)) << 4);
        int wa0 = WS0 + (8 * hl + ((lane & 15) >> 2)) * 320 + (lane & 16) * 2 + (lane & 3) * 8, wa1 = wa0 + 61440;
        asm volatile("" : "+v"(wa0), "+v"(wa1));
        auto rdx = [&](int i) { return *(LAS bf16x8*)(smem + ((i >> 2) % 3) * 16384 + (xa ^ ((i & 3) * 32))); };
        auto rdw = [&](int f) { const int i = f / 5, t = f % 5, sl = (i >> 1) % 7;
            return trfrag(smem + (sl < 6 ? wa0 + sl * 10240 : wa1) + (i & 1) * 5120 + t * 64, 1280); };
        unsigned fl = 0;
        auto rdflag = [&](int u) { fl = __hip_atomic_load((LAS unsigned*)(smem + C_F + u * 4), RLX_WG); };
        auto check = [&](int u) {
            if (__builtin_amdgcn_readfirstlane(fl) != 15) {
#pragma unroll 1
                for (unsigned spins = 0; spins < LSPIN_CAP; ++spins) { rdflag(u); if (__builtin_amdgcn_readfirstlane(fl) == 15) break; __builtin_amdgcn_s_sleep(1); }
            }
            FENCE(); };
#pragma unroll
        for (int i = 0; i < 4; ++i) xr[i] = rdx(i);
#pragma unroll
        for (int f = 0; f < WIN; ++f) wf[f] = rdw(f);
        SB();
        SFOR(f, 200)
            constexpr int i = f / 5, t = f % 5;
            if constexpr (f % 20 == 10 - WIN) lds_set(smem + C_P + wid * 4, (i >> 1) + 1);
            if constexpr (f % 20 == 10) rdflag(f / 20 + 1);
            if constexpr (f % 20 == 20 - WIN && f < 190) check(f / 20 + 1);
            if constexpr (f + WIN < 200) wf[(f + WIN) % RING] = rdw(f + WIN);
            if constexpr (f % 20 >= 16 && f < 120) xr[(f / 20 + 1) * 4 + f % 20 - 16] = rdx((f / 20 + 1) * 4 + f % 20 - 16);
            if constexpr (t == 1 && i >= 27 && i < 39) xt[(i + 1) & 1] = rdx(i + 1);
            SB();
            if constexpr (i == 0) MFMA32Z(acc[t], wf[f % RING], xr[0]);
            else if constexpr (i < 28) MFMA32I(acc[t], wf[f % RING], xr[i]);
            else MFMA32I(acc[t], wf[f % RING], xt[i & 1]);
            SB();
        SEND
        MFMA_DRAIN();
        WAITV(0);
        STAMP(2);
#pragma unroll
        for (int t = 0; t < 5; ++t)
#pragma unroll
            for (int b = 0; b < 4; ++b) {
                const int g0 = 8 * t + 2 * b, g1 = g0 + 1;
                const int o0 = g0 < 20 ? kq_base(g0) + tok * (g0 % 5 < 4 ? 16 : 8) : VOFF + (g0 - 20) * 8 + tok * VP;
                const int o1 = g1 < 20 ? kq_base(g1) + tok * (g1 % 5 < 4 ? 16 : 8) : VOFF + (g1 - 20) * 8 + tok * VP;
                *(LAS u32x2*)(smem + OWN + (hl ? o1 : o0)) = (u32x2){pk2(acc[t][4 * b], acc[t][4 * b + 1]), pk2(acc[t][4 * b + 2], acc[t][4 * b + 3])};
            }
        if (hl == 0) *(LAS u32x2*)(smem + OWN + VOFF + tok * VP + 160) = (u32x2){0x3F803F80u, 0x3F803F80u};
        WAITL();
        lds_set(smem + C_E + wid * 4, 1);
        STAMP(3);
        int l2 = tid; asm volatile("" : "+v"(l2)); l2 &= 63;
        int qa0 = (8 * (l2 >> 5) + 2 * ((l2 & 15) >> 2)) * 160 + (l2 & 16) * 2 + (l2 & 3) * 8, qa1 = qa0 + WS0 + 10240;
        asm volatile("" : "+v"(qa0), "+v"(qa1));
        auto rdq = [&](int f) { const int i = f / 3, t = f % 3, j = i >> 2, sl = (20 + j) % 7;
            return trfrag(smem + (j >= 7 ? qa0 + XS0 + (j - 7) * 10240 : sl == 0 ? qa0 + WS0 : qa1 + (sl - 1) * 10240) + (i & 3) * 2560 + t * 64, 160); };
        check(10);
        xt[0] = rdx(28);
#pragma unroll
        for (int f = 0; f < WIN; ++f) wf[f] = rdq(f);
        SB();
        SFOR(f, 120)
            constexpr int i = f / 3, t = f % 3, j = i >> 2;
            if constexpr (f % 24 == 12 && j < 9) rdflag(11 + j / 2);
            if constexpr (f % 24 == 12 - WIN && (j == 2 || j >= 6)) lds_set(smem + C_P + wid * 4, 21 + j);
            if constexpr (f % 24 == 24 - WIN && j < 9) check(11 + j / 2);
            if constexpr (f + WIN < 120) wf[(f + WIN) % RING] = rdq(f + WIN);
            if constexpr (t == 0 && i < 11) xt[(i + 1) & 1] = rdx(28 + i + 1);
            SB();
            if constexpr (i == 0) MFMA32Z(acc[t], wf[f % RING], xt[0]);
            else if constexpr (i < 12) MFMA32I(acc[t], wf[f % RING], xt[i & 1]);
            else MFMA32I(acc[t], wf[f % RING], xr[i - 12]);
            SB();
        SEND
        MFMA_DRAIN();
        STAMP(4);
        wait_min4(smem + C_P, 29);
#pragma unroll
        for (int t = 0; t < 3; ++t)
#pragma unroll
            for (int b = 0; b < 4; ++b)
                if (t < 2 || b < 2) {
                    const int g0 = 8 * t + 2 * b, g1 = g0 + 1;
                    const int o0 = kq_base(g0) + tok * (g0 % 5 < 4 ? 16 : 8), o1 = kq_base(g1) + tok * (g1 % 5 < 4 ? 16 : 8);
                    *(LAS u32x2*)(smem + QST + (hl ? o1 : o0)) =
                        (u32x2){pk2(acc[t][4 * b] * QSCALE, acc[t][4 * b + 1] * QSCALE), pk2(acc[t][4 * b + 2] * QSCALE, acc[t][4 * b + 3] * QSCALE)};
                }
        STAMP(5);
    } else {
        const int hw = wid - 4;
        int known = 0;
        __builtin_amdgcn_s_setprio(3);
        auto lds_or = [&](int off, unsigned v) { FENCE(); if (lane == 0) __hip_atomic_fetch_or((LAS unsigned*)(smem + off), v, RLX_WG); FENCE(); };
        unsigned wo[5], qo[5];
#pragma unroll
        for (int q = 0; q < 5; ++q) { wo[q] = w_off(q) + (hw & 1) * (16 * 3840) + (4 - q) * 1024;
            const int c = q * 64 + lane, rho = c / 10; qo[q] = (unsigned)(((hw & 1) * 32 + (rho & ~7) + 4 * (rho & 1) + ((rho >> 1) & 3)) * 3840 + (c % 10) * 16 + (4 - q) * 1024); }
        auto done = [&](int n) { lds_or(C_F + n * 4, 1u << hw); };
        const unsigned xo1 = (xo ^ 64) + 9216, xo2 = xo + 2 * 9216, xo3 = (xo ^ 64) + 3 * 9216;
        auto round_x = [&](int n) { dma16x4(xo, xo1, xo2, xo3, xb + kt_of(n) * 128 + hw * (32 * 1280), lds0 + XS0 + (n % 3) * 16384 + hw * 4096); };
        auto round_w = [&](int n) {
            const int u = 2 * n + (hw >> 1);
            const unsigned dst = lds0 + (u < 27 ? WS0 + (u % 7) * 10240 : XS0 + (u - 27) * 10240) + (hw & 1) * 5120 + 2048;
            if (u < 20) dma16x5(wo, wb + (size_t)(kt_of(u >> 1) * 64 + (u & 1) * 32) * 3840 - 2048, dst);
            else dma16x5(qo, wb + (size_t)kt_of(u < 23 ? u - 13 : u - 23) * (64 * 3840) - 2048, dst); };
        round_x(1); round_w(1);
        WAITV(9);
        BAR_L();
#pragma unroll 1
        for (int n = ABL4N; n <= 12; ++n) {
            const int need = 2 * n - 5;
            if (need > known) known = min4(smem + C_P);
            if (need > known) { WAITV(0); done(n - 1); known = wait_min4(smem + C_P, need); }
            if (n < 10) round_x(n);
            round_w(n);
            if (n < 10) WAITV(9); else WAITV(5);
            done(n - 1);
        }
        WAITV(0); done(12);
        STAMP(1);
        const u64 tag = rdtag();
        auto a_chunk = [&](int a) { int o;
            if (a < 320) { const int hd = a / 80, rem = a % 80; o = hd * 5120 + (rem < 32 ? (96 + rem) * 16 : rem < 64 ? 2048 + (64 + rem) * 16 : 4096 + 768 + (rem - 64) * 16); }
            else o = VOFF + 96 * VP + (a - 320) * 16;
            return o; };
        if (rt < 15) {
            const __amdgpu_buffer_rsrc_t kvr = __builtin_amdgcn_make_buffer_rsrc(ws + WS_KV + me * KVG, 0, KVB, 0x00020000);
            wait_min4(smem + C_E, 1);
            STAMP(2);
            if (!isA) { u32x4 v[11];
#pragma unroll
                for (int j = 0; j < 11; ++j) if (j < 10 || hw == 3) v[j] = *(LAS u32x4*)(smem + OWN + hw * 10240 + j * 1024 + lane * 16);
#pragma unroll
                for (int j = 0; j < 11; ++j) if (j < 10 || hw == 3) __builtin_amdgcn_raw_buffer_store_b128(v[j], kvr, hw * 10240 + j * 1024 + lane * 16, 0, 16);
            } else {
#pragma unroll
                for (int j = 0; j < 3; ++j) { const int a = (4 * j + hw) * 64 + lane;
                    if (a < 656) { const int o = a_chunk(a); __builtin_amdgcn_raw_buffer_store_b128(*(LAS u32x4*)(smem + OWN + o), kvr, o, 0, 16); } }
            }
            STAMP(3);
        }
        wait_min4(smem + C_P, 23);
        round_w(13); round_w(14);
        WAITV(10);
        if (rt < 15) {
            unsigned old = 0;
            if (lane == 0) old = __hip_atomic_fetch_add((LAS unsigned*)(smem + C_CNT), 1u, RLX_WG);
            if (lane == 0 && old == 3) __hip_atomic_store((GAS u64*)(ws + WS_FKV + me * 128), tag, RLX_AGENT);
            STAMP(4);
        }
        asm volatile("s_waitcnt vmcnt(6)" ::: "memory"); WAITV(5); done(13); WAITV(0); done(14);
        poll_flags((GAS u64*)(ws + WS_FKV + (me - 8 * (lane + 1)) * 128), lane < nnb, tag);
        STAMP(5);
        if (!isA) {
            const int n = hw >> 1, half = hw & 1;
            if (n < nnb) {
                const __amdgpu_buffer_rsrc_t nbr = __builtin_amdgcn_make_buffer_rsrc(ws + WS_KV + (me - 8 * (n + 1)) * KVG, 0, KVB, 0x00020000);
                u32x4 v[21];
#pragma unroll
                for (int j = 0; j < 21; ++j) if (j < 20 || half) v[j] = __builtin_amdgcn_raw_buffer_load_b128(nbr, half * 20480 + j * 1024 + lane * 16, 0, 16);
                wait_min4(smem + C_P, 27);
#pragma unroll
                for (int j = 0; j < 21; ++j) if (j < 20 || half) *(LAS u32x4*)(smem + KV0 + (1 - n) * KVB + half * 20480 + j * 1024 + lane * 16) = v[j];
            }
        } else if (nnb) {
            const __amdgpu_buffer_rsrc_t nbr = __builtin_amdgcn_make_buffer_rsrc(ws + WS_KV + (me - 8) * KVG, 0, KVB, 0x00020000);
            u32x4 v[3];
#pragma unroll
            for (int j = 0; j < 3; ++j) { const int a = (4 * j + hw) * 64 + lane; if (a < 656) v[j] = __builtin_amdgcn_raw_buffer_load_b128(nbr, a_chunk(a), 0, 16); }
            wait_min4(smem + C_P, 27);
#pragma unroll
            for (int j = 0; j < 3; ++j) { const int a = (4 * j + hw) * 64 + lane; if (a < 656) *(LAS u32x4*)(smem + KV0 + KVB + a_chunk(a)) = v[j]; }
        }
        if (hw == 3) { wait_min4(smem + C_P, 27); if (lane < 2) *(LAS u32x2*)(smem + ONE + lane * 5120) = (u32x2){0x3F803F80u, 0x3F803F80u}; }
        __builtin_amdgcn_s_setprio(0);
        STAMP(6);
    }
    const int qi = wid & 3, hp = wid >> 2;
    const int ud = qi + 8;
    const bool far = isA ? (4 * rt + qi >= 1) : (4 * rt + qi >= 8);
    const int u0 = isA ? (far ? ud - 1 : ud) : (far ? qi : 8 - 4 * rt);
    f32x16 cd, cf;
    auto masks = [&](int q) {
      const unsigned low = (2u << q) - 1u;
      unsigned vd = low, vf = ~low;
      if (isA) { const u64 w = 0x5555ull << (q + 18);
          vd = (unsigned)(w >> 32); vf = (unsigned)w; }
      if (!far) vf = ~0u;
      const unsigned id = ~vd >> (4 * hl), jf = ~vf >> (4 * hl);
#pragma unroll
      for (int r = 0; r < 16; ++r) {
          const int c0 = (r & 3) + 8 * (r >> 2);
          cd[r] = __uint_as_float((unsigned)__builtin_amdgcn_sbfe((int)id, c0, 1) & 0xFF800000u);
          cf[r] = __uint_as_float((unsigned)__builtin_amdgcn_sbfe((int)jf, c0, 1) & 0xFF800000u);
      } };
    masks(r32);
    unsigned oo[5]; u32x4 wreg[13];
#pragma unroll
    for (int q = 0; q < 5; ++q) { const int c = q * 64 + lane, rho = c / 10;
        oo[q] = (unsigned)((wid * 80 + (rho & ~7) + 4 * (rho & 1) + ((rho >> 1) & 3)) * 1280 + hg * 160 + (c % 10) * 16); }
    auto ldwo = [&](int i) { wreg[i] = __builtin_amdgcn_raw_buffer_load_b128(wor, oo[i % 5] + (i / 5) * (32 * 1280), 0, 0); };
    BAR_L();
    STAMP(7);
    u32x2 ob[2][3];
    {
        bf16x8 q1[2]; u32x2 q2[2];
#pragma unroll
        for (int e = 0; e < 2; ++e) {
            q1[e] = *(LAS bf16x8*)(smem + QST + (2 * hp + e) * 5120 + hl * 2048 + (qi * 32 + r32) * 16);
            q2[e] = *(LAS u32x2*)(smem + QST + (2 * hp + e) * 5120 + 4096 + (qi * 32 + r32) * 8);
            if (hl) q2[e] = (u32x2){0u, 0u};
        }
        const int kb1 = KV0 + hp * 10240 + hl * 2048 + r32 * 16;
        const int kb2 = hl ? ONE : KV0 + hp * 10240 + 4096 + r32 * 8, nh = hl ^ 1;
        const int vrow = KV0 + VOFF + (4 * hl + ((lane & 15) >> 2)) * VP;
        const bool ones = (lane & 16) && (lane & 3);
        const int vb0 = vrow + (ones ? 160 : hp * 80 + (lane & 16) * 2 + (lane & 3) * 8), vb1 = vb0 + (ones ? 0 : 40);
        struct KF { bf16x8 a; s16x4 b; };
        struct VF { bf16x8 a, b; };
        auto rdk = [&](int u, int e) { const int so = (u >> 2) * KVB, tw = u & 3; KF k;
            k.a = *(LAS bf16x8*)(smem + kb1 + so + tw * 512 + e * 5120); k.b = *(LAS s16x4*)(smem + __umul24(so + tw * 256, nh) + kb2 + e * 5120); return k; };
        auto rdv = [&](int u, int e) { LAS char* p = smem + (e ? vb1 : vb0) + (u >> 2) * KVB + (u & 3) * (32 * VP); VF v;
            v.a = trfrag(p, 8 * VP); v.b = trfrag(p + 16 * VP, 8 * VP); return v; };
        auto qk = [&](const KF& k, int e, const f32x16& c) { return MFMA32K8(k.b, __builtin_bit_cast(s16x4, q2[e]), MFMA32(k.a, q1[e], c)); };
        const f32x16 zero = {};
        auto max16 = [&](const f32x16& s, float m) {
#pragma unroll
            for (int r = 0; r < 16; ++r) m = __builtin_elementwise_maximum(m, s[r]);
            return m; };
        auto set_ref = [&](float m, int e) { const unsigned nb = pk2(-m, 0.f); if (hl) q2[e][0] = nb; };
        auto ex4 = [&](f32x16& s, int b) {
#pragma unroll
            for (int r = 0; r < 4; ++r) s[b + r] = __builtin_amdgcn_exp2f(s[b + r]); };
        auto pk8 = [&](const f32x16& s, int b) { return __builtin_bit_cast(bf16x8, (u32x4){pk2(s[b], s[b + 1]), pk2(s[b + 2], s[b + 3]), pk2(s[b + 4], s[b + 5]), pk2(s[b + 6], s[b + 7])}); };
        auto phase = [&](auto nx, f32x16& cur, const VF& v, f32x16& o, f32x16& nxt, const KF& k, int e) __attribute__((always_inline)) {
            constexpr int NX = decltype(nx)::value;
            SB(); ex4(cur, 0); SB();
            if constexpr (NX) nxt = MFMA32(k.a, q1[e], NX == 1 ? zero : NX == 2 ? cf : cd);
            SB(); ex4(cur, 4); { const bf16x8 p = pk8(cur, 0); SB(); o = MFMA32(v.a, p, o); }
            SB(); ex4(cur, 8); SB();
            if constexpr (NX) nxt = MFMA32K8(k.b, __builtin_bit_cast(s16x4, q2[e]), nxt);
            SB(); ex4(cur, 12); { const bf16x8 p = pk8(cur, 8); SB(); o = MFMA32(v.b, p, o); }
            SB(); };
        f32x16 o0, o1, sa, sb; KF kx; VF vx;
        auto step = [&](auto m0, auto m1, int u) __attribute__((always_inline)) {
            constexpr int M1 = decltype(m1)::value;
            KF ky; if constexpr (M1) ky = rdk(u + 1, 0);
            const VF vy = rdv(u, 1);
            phase(m0, sa, vx, o0, sb, kx, 1);
            if constexpr (M1) { kx = rdk(u + 1, 1); vx = rdv(u + 1, 0); }
            phase(m1, sb, vy, o1, sa, ky, 0); };
        auto run = [&](auto fast) __attribute__((always_inline)) {
            o0 = zero; o1 = zero;
            kx = rdk(u0, 1); vx = rdv(u0, 0);
            if constexpr (decltype(fast)::value) {
                sa = qk(rdk(u0, 0), 0, cf);
                SFOR(it, 9)
                    if constexpr (it < 7) { ldwo(2 * it); if constexpr (it < 6) ldwo(2 * it + 1); }
                    step(IC<it == 0 ? 2 : it == 8 ? 3 : 1>{}, IC<it == 8 ? 0 : it == 7 ? 3 : 1>{}, u0 + it);
                SEND
            } else {
                if (u0 < ud) {
                    sa = qk(rdk(u0, 0), 0, cf);
                    if (u0 + 1 < ud) {
                        step(IC<2>{}, IC<1>{}, u0);
#pragma unroll 1
                        for (int u = u0 + 1; u + 1 < ud; ++u) step(IC<1>{}, IC<1>{}, u);
                        step(IC<1>{}, IC<3>{}, ud - 1);
                    } else step(IC<2>{}, IC<3>{}, u0);
                } else sa = qk(rdk(ud, 0), 0, cd);
                step(IC<3>{}, IC<0>{}, ud);
            } };
        { const f32x16 s0 = qk(rdk(ud, 0), 0, cd), s1 = qk(rdk(ud, 1), 1, cd);
          set_ref(halfmax(max16(s0, s0[0])), 0); set_ref(halfmax(max16(s1, s1[0])), 1); }
        if (u0 + 8 == ud) run(IC<1>{});
        else {
#pragma unroll
            for (int i = 0; i < 13; ++i) ldwo(i);
            run(IC<0>{});
        }
        if (!__all(o0[12] < 1.152921504606847e18f && o1[12] < 1.152921504606847e18f)) {
            { int q = r32; asm volatile("" : "+v"(q)); masks(q); }
#pragma unroll
            for (int e = 0; e < 2; ++e) {
                if (hl) q2[e][0] = 0u;
                const f32x16 s = qk(rdk(ud, e), e, cd);
                float m = max16(s, s[0]);
                if (u0 < ud) m = max16(qk(rdk(u0, e), e, cf), m);
#pragma unroll 1
                for (int u = u0 + 1; u < ud; ++u) m = max16(qk(rdk(u, e), e, zero), m);
                set_ref(halfmax(m), e);
            }
#pragma unroll
            for (int e = 0; e < 2; ++e) {
                f32x16 o = zero;
#pragma unroll 1
                for (int u = u0; u <= ud; ++u) { f32x16 t = u == ud ? qk(rdk(u, e), e, cd) : u == u0 ? qk(rdk(u, e), e, cf) : qk(rdk(u, e), e, zero);
                    const VF v = rdv(u, e); ex4(t, 0); ex4(t, 4); ex4(t, 8); ex4(t, 12);
                    o = MFMA32(v.a, pk8(t, 0), o); o = MFMA32(v.b, pk8(t, 8), o); }
                if (e) o1 = o; else o0 = o;
            }
        }
        const float rl0 = __builtin_amdgcn_rcpf(o0[12]), rl1 = __builtin_amdgcn_rcpf(o1[12]);
#pragma unroll
        for (int j = 0; j < 3; ++j) {
            ob[0][j] = (u32x2){pk2(o0[4 * j] * rl0, o0[4 * j + 1] * rl0), pk2(o0[4 * j + 2] * rl0, o0[4 * j + 3] * rl0)};
            ob[1][j] = (u32x2){pk2(o1[4 * j] * rl1, o1[4 * j + 1] * rl1), pk2(o1[4 * j + 2] * rl1, o1[4 * j + 3] * rl1)}; }
    }
    STAMP(8);
    const __amdgpu_buffer_rsrc_t orr = __builtin_amdgcn_make_buffer_rsrc(ws + WS_O + (br * 16 + rt) * OG, 0, 4 * 40960, 0x00020000);
    { const unsigned obase = (unsigned)(qi * 40960 + (hg * 10 + hp * 5) * 512 + r32 * 16);
      { auto rx = __builtin_amdgcn_permlane32_swap(ob[0][0][0], ob[0][1][0], false, false);
        auto ry = __builtin_amdgcn_permlane32_swap(ob[0][0][1], ob[0][1][1], false, false);
        __builtin_amdgcn_raw_buffer_store_b128((u32x4){rx[0], ry[0], rx[1], ry[1]}, orr, obase + hl * 512, 0, 16); }
      if (hl == 0) __builtin_amdgcn_raw_buffer_store_b128((u32x4){ob[0][2][0], ob[0][2][1], ob[1][0][0], ob[1][0][1]}, orr, obase + 1024, 0, 16);
      { auto fx = __builtin_amdgcn_permlane32_swap(ob[1][0][0], ob[1][1][0], false, false);
        auto fy = __builtin_amdgcn_permlane32_swap(ob[1][0][1], ob[1][1][1], false, false);
        auto sx = __builtin_amdgcn_permlane32_swap(ob[1][1][0], ob[1][2][0], false, false);
        auto sy = __builtin_amdgcn_permlane32_swap(ob[1][1][1], ob[1][2][1], false, false);
        __builtin_amdgcn_raw_buffer_store_b128((u32x4){fx[1], fy[1], sx[0], sy[0]}, orr, obase + 1536 + hl * 512, 0, 16); } }
    const u64 tag = rdtag();
    WAITV(0);
    { unsigned old = 0;
      if (lane == 0) old = __hip_atomic_fetch_add((LAS unsigned*)(smem + C_CNT + 4), 1u, RLX_WG);
      if (lane == 0 && old == 7) __hip_atomic_store((GAS u64*)(ws + WS_FO + me * 128), tag, RLX_AGENT); }
    STAMP(9);
    BAR_L();
#pragma unroll
    for (int i = 0; i < 13; ++i) if (i < 12 || lane < 32) *(LAS u32x4*)(smem + WOB + wid * 12800 + i * 1024 + lane * 16) = wreg[i];
    BAR_L();
    STAMP(10);
    poll_flags((GAS u64*)(ws + WS_FO + ((me & ~7) + (lane & 7)) * 128), lane < 8, tag);
    STAMP(11);
    f32x16 oc[3];
    { bf16x8 af[6], of[20];
      const int aa = WOB + hp * 51200 + (8 * hl + 2 * ((lane & 15) >> 2)) * 160 + (lane & 16) * 2 + (lane & 3) * 8;
      auto rda = [&](int f) { return trfrag(smem + aa + (f / 3) * 2560 + (f % 3) * 64, 160); };
      auto ldo = [&](int k) { of[k] = __builtin_bit_cast(bf16x8, __builtin_amdgcn_raw_buffer_load_b128(orr, qi * 40960 + (hp * 20 + k) * 1024 + lane * 16, 0, 16)); };
#pragma unroll
      for (int k = 0; k < OPF; ++k) ldo(k);
#pragma unroll
      for (int f = 0; f < 4; ++f) af[f] = rda(f);
      SB();
      SFOR(f, 60)
          if constexpr (f % 3 == 0 && f / 3 + OPF < 20) ldo(f / 3 + OPF);
          if constexpr (f + 4 < 60) af[(f + 4) % 6] = rda(f + 4);
          SB();
          if constexpr (f < 3) MFMA32Z(oc[f], af[f % 6], of[0]); else MFMA32I(oc[f % 3], af[f % 6], of[f / 3]);
          SB();
      SEND }
    MFMA_DRAIN();
    STAMP(12);
    { LAS char* mine = smem + RED + wid * 6144 + lane * 16; LAS char* other = smem + RED + (wid ^ 4) * 6144 + lane * 16;
      if (hp) {
#pragma unroll
          for (int b = 0; b < 4; ++b) *(LAS f32x4*)(mine + b * 1024) = (f32x4){oc[0][4 * b], oc[0][4 * b + 1], oc[0][4 * b + 2], oc[0][4 * b + 3]};
#pragma unroll
          for (int b = 0; b < 2; ++b) *(LAS f32x4*)(mine + (4 + b) * 1024) = (f32x4){oc[2][4 * b], oc[2][4 * b + 1], oc[2][4 * b + 2], oc[2][4 * b + 3]};
      } else {
#pragma unroll
          for (int b = 0; b < 4; ++b) *(LAS f32x4*)(mine + b * 1024) = (f32x4){oc[1][4 * b], oc[1][4 * b + 1], oc[1][4 * b + 2], oc[1][4 * b + 3]};
      }
      BAR_L();
      const __amdgpu_buffer_rsrc_t outr = __builtin_amdgcn_make_buffer_rsrc(out + (size_t)rt * (128 * 2560), 0, 128 * 2560, 0x00020000);
      const int orow = (qi * 32 + r32) * 2560 + br * 1280 + hg * 160 + hl * 16;
      auto fin = [&](const f32x16& c, int b, int slot) { const f32x4 x = *(LAS f32x4*)(other + slot * 1024);
          return (u32x2){pk2(c[4 * b] + x[0], c[4 * b + 1] + x[1]), pk2(c[4 * b + 2] + x[2], c[4 * b + 3] + x[3])}; };
      auto st2 = [&](u32x2 a, u32x2 b, int col) {
          auto rx = __builtin_amdgcn_permlane32_swap(a[0], b[0], false, false); auto ry = __builtin_amdgcn_permlane32_swap(a[1], b[1], false, false);
          __builtin_amdgcn_raw_buffer_store_b128((u32x4){rx[0], ry[0], rx[1], ry[1]}, outr, orow + col * 2, 0, 0); };
      if (hp) { st2(fin(oc[1], 0, 0), fin(oc[1], 1, 1), 32); st2(fin(oc[1], 2, 2), fin(oc[1], 3, 3), 48); }
      else { st2(fin(oc[0], 0, 0), fin(oc[0], 1, 1), 0); st2(fin(oc[0], 2, 2), fin(oc[0], 3, 3), 16); st2(fin(oc[2], 0, 4), fin(oc[2], 1, 5), 64); } }
    if (bx == 0) {
        if (wid < 4) poll_flags((GAS u64*)(ws + WS_FO + tid * 128), true, tag);
        __syncthreads();
        if (tid == 0) __hip_atomic_store((GAS u64*)ws, tag, RLX_AGENT);
    }
}

extern "C" void kernel_launch(void* const* d_in, const int* in_sizes, int n_in, void* d_out, int out_size, void* d_ws, size_t ws_size,
                              hipStream_t stream) {
    fused_attn_kernel<<<256, 512, 0, stream>>>((const char*)d_in[0], (const char*)d_in[1], (const char*)d_in[2], (const char*)d_in[3],
                                               (const char*)d_in[4], (const char*)d_in[5], (char*)d_out, (char*)d_ws);
}
```

```cpp
#define ADELAY 3
#define DIAG 0
#include <hip/hip_runtime.h>
#include <hip/hip_bf16.h>
#include <stdint.h>

typedef short bf16x8 __attribute__((ext_vector_type(8)));
typedef short s16x4 __attribute__((ext_vector_type(4)));
typedef float f32x2 __attribute__((ext_vector_type(2)));
typedef float f32x4 __attribute__((ext_vector_type(4)));
typedef float f32x16 __attribute__((ext_vector_type(16)));
typedef unsigned u32x4 __attribute__((ext_vector_type(4)));
typedef unsigned u32x2 __attribute__((ext_vector_type(2)));
typedef __bf16 bfx2 __attribute__((ext_vector_type(2)));
typedef unsigned long long u64;
#define LAS __attribute__((address_space(3)))
#define GAS __attribute__((address_space(1)))
#define DI __device__ __forceinline__
#define RLX_AGENT __ATOMIC_RELAXED, __HIP_MEMORY_SCOPE_AGENT
#define RLX_WG __ATOMIC_RELAXED, __HIP_MEMORY_SCOPE_WORKGROUP

constexpr int LDS_BYTES = 163840;
constexpr int VOFF = 20480, VP = 168;
constexpr int KVB = VOFF + 128 * VP;
constexpr int KV0 = LDS_BYTES - 3 * KVB;
constexpr int OWN = KV0 + 2 * KVB;
constexpr int C_F = 0, C_P = 256, C_E = 272, C_CNT = 288, C_TAG = 320;
constexpr int XS0 = 512, WS0 = XS0 + 3 * 16384, QST = 512, ONE = 31744, WOB = 512, RED = WOB + 102400;
static_assert(WS0 + 7 * 10240 <= OWN, "ring overlaps the own image");
static_assert(ONE + 5120 + 8 <= KV0 && RED + 8 * 6144 <= LDS_BYTES, "LDS map");
constexpr unsigned WS_FKV = 4096, WS_FO = WS_FKV + 256 * 128, WS_KV = 131072;
constexpr unsigned KVG = KVB + 128, OG = 4 * 40960 + 128;
constexpr unsigned WS_O = WS_KV + 256 * KVG;
constexpr unsigned WS_END = WS_O + 32 * OG;
constexpr unsigned WS_DBG = WS_END;
constexpr unsigned SPIN_CAP = 400000, LSPIN_CAP = 4000000;
constexpr float QSCALE = 0.22360679774997896f * 1.4426950408889634f;
#define OPF 6
#define GSLEEP 1
#define ABL4N 2
#define RING (WIN + 2)
#define WIN 5

#define MFMA32(a, b, c) __builtin_amdgcn_mfma_f32_32x32x16_bf16((a), (b), (c), 0, 0, 0)
#define MFMA32K8(a, b, c) __builtin_amdgcn_mfma_f32_32x32x8bf16_1k((a), (b), (c), 0, 0, 0)
#define MFMA32I(acc, a, b) asm volatile("v_mfma_f32_32x32x16_bf16 %0, %1, %2, %0" : "+v"(acc) : "v"(a), "v"(b))
#define MFMA32Z(acc, a, b) asm volatile("v_mfma_f32_32x32x16_bf16 %0, %1, %2, 0" : "=&v"(acc) : "v"(a), "v"(b))
#define MFMA_DRAIN() asm volatile("s_nop 7\n\ts_nop 4" ::: "memory")
#define WAITV(N) asm volatile("s_waitcnt vmcnt(" #N ")" ::: "memory")
#define WAITL() asm volatile("s_waitcnt lgkmcnt(0)" ::: "memory")
#define BAR_L() asm volatile("s_waitcnt lgkmcnt(0)\n\ts_barrier" ::: "memory")
#define SB() __builtin_amdgcn_sched_barrier(0)
#define FENCE() asm volatile("" ::: "memory")

#define STAMP(i) do { if ((i) == 0 || (i) == 1 || (i) == 2 || (i) == 7 || (i) == 8) { SB(); dst_[i] = (unsigned)__builtin_amdgcn_s_memrealtime(); SB(); } } while (0)

template <int V> struct IC { static constexpr int value = V; };
template <int I, int N, class F> DI void static_for(F&& f) { if constexpr (I < N) { f(IC<I>{}); static_for<I + 1, N>(f); } }
#define SFOR(var, N) static_for<0, N>([&](auto ic_##var) __attribute__((always_inline)) { constexpr int var = decltype(ic_##var)::value;
#define SEND });

DI void dma16(unsigned voff, const char* sbase, unsigned ldsaddr) {
    asm volatile("s_mov_b32 m0, %2\n\ts_nop 0\n\tglobal_load_lds_dwordx4 %0, %1" :: "v"(voff), "s"(sbase), "s"(ldsaddr) : "memory");
}
DI void dma16x4(unsigned v0, unsigned v1, unsigned v2, unsigned v3, const char* sbase, unsigned ldsaddr) {
    asm volatile("s_mov_b32 m0, %5\n\ts_nop 0\n\tglobal_load_lds_dwordx4 %0, %4\n\tglobal_load_lds_dwordx4 %1, %4 offset:1024\n\t"
                 "global_load_lds_dwordx4 %2, %4 offset:2048\n\tglobal_load_lds_dwordx4 %3, %4 offset:3072"
                 :: "v"(v0), "v"(v1), "v"(v2), "v"(v3), "s"(sbase), "s"(ldsaddr) : "memory");
}
DI void dma16x5(const unsigned (&v)[5], const char* sbase, unsigned ldsaddr) {
    asm volatile("s_mov_b32 m0, %6\n\ts_nop 0\n\tglobal_load_lds_dwordx4 %0, %5 offset:-2048\n\tglobal_load_lds_dwordx4 %1, %5 offset:-1024\n\t"
                 "global_load_lds_dwordx4 %2, %5\n\tglobal_load_lds_dwordx4 %3, %5 offset:1024\n\tglobal_load_lds_dwordx4 %4, %5 offset:2048"
                 :: "v"(v[0]), "v"(v[1]), "v"(v[2]), "v"(v[3]), "v"(v[4]), "s"(sbase), "s"(ldsaddr) : "memory");
}
DI void dma4(const char* p, unsigned ldsaddr) {
    asm volatile("s_mov_b32 m0, %1\n\ts_nop 0\n\tglobal_load_lds_dword %0, off" :: "v"(p), "s"(ldsaddr) : "memory");
}
DI unsigned pk2(float lo, float hi) { f32x2 v = {lo, hi}; return __builtin_bit_cast(unsigned, __builtin_convertvector(v, bfx2)); }
typedef short v4i16_t __attribute__((ext_vector_type(4)));
DI bf16x8 trfrag(LAS char* p, int second) {
    s16x4 a = __builtin_bit_cast(s16x4, __builtin_amdgcn_ds_read_tr16_b64_v4i16((LAS v4i16_t*)p));
    s16x4 b = __builtin_bit_cast(s16x4, __builtin_amdgcn_ds_read_tr16_b64_v4i16((LAS v4i16_t*)(p + second)));
    return __builtin_shufflevector(a, b, 0, 1, 2, 3, 4, 5, 6, 7);
}
DI float halfmax(float m) { auto rr = __builtin_amdgcn_permlane32_swap(__float_as_uint(m), __float_as_uint(m), false, false);
    return __builtin_elementwise_maximum(__uint_as_float(rr[0]), __uint_as_float(rr[1])); }

DI void poll_flags(GAS u64* word, bool active, u64 tag) {
#pragma unroll 1
    for (unsigned spins = 0; spins < SPIN_CAP; ++spins) {
        u64 v = tag;
        if (active) v = __hip_atomic_load(word, RLX_AGENT);
        if (__all(v == tag)) break;
        __builtin_amdgcn_s_sleep(1);
    }
    __builtin_amdgcn_fence(__ATOMIC_ACQUIRE, "wavefront");
}
DI int min4(LAS char* p) {
    const u64 a = __hip_atomic_load((LAS u64*)p, RLX_WG), b = __hip_atomic_load((LAS u64*)(p + 8), RLX_WG);
    const unsigned m = min(min((unsigned)a, (unsigned)(a >> 32)), min((unsigned)b, (unsigned)(b >> 32)));
    return __builtin_amdgcn_readfirstlane((int)m);
}
DI int wait_min4(LAS char* p, int need) {
    int v = min4(p);
#pragma unroll 1
    for (unsigned spins = 0; v < need && spins < LSPIN_CAP; ++spins) { __builtin_amdgcn_s_sleep(GSLEEP); v = min4(p); }
    FENCE();
    return v;
}
DI void lds_set(LAS char* p, unsigned v) { FENCE(); __hip_atomic_store((LAS unsigned*)p, v, RLX_WG); FENCE(); }

__global__ __launch_bounds__(512, 2) void fused_attn_kernel(const char* __restrict__ xA, const char* __restrict__ wA, const char* __restrict__ oA,
                                                           const char* __restrict__ xB, const char* __restrict__ wB, const char* __restrict__ oB,
                                                           char* __restrict__ out, char* __restrict__ ws) {
    __shared__ __attribute__((aligned(16))) char smem_[LDS_BYTES];
    LAS char* smem = (LAS char*)smem_;
    const unsigned lds0 = (unsigned)(uintptr_t)smem;
    unsigned dst_[9];
    STAMP(0);
    const int tid = threadIdx.x, lane = tid & 63;
    const int wid = __builtin_amdgcn_readfirstlane(tid >> 6);
    const int bx = blockIdx.x, xcd = bx & 7, idx = bx >> 3;
    const int isA = idx >> 4, rt = (xcd >> 1) * 4 + ((idx >> 2) & 3), hg = (xcd & 1) * 4 + (idx & 3);
    const int br = isA ? 0 : 1;
    const int k0 = isA ? (((rt & 3) * 5 + 1) / 2 + ((hg & 3) * 5 + 1) / 2) % 10 : 0;
    const char* xb = (isA ? xA : xB) + (size_t)rt * (128 * 1280);
    const char* wb = (isA ? wA : wB) + hg * 160;
    const int me = (br * 16 + rt) * 8 + hg;
    const int r32 = lane & 31, hl = lane >> 5;
    auto kt_of = [&](int s) { int k = s + k0; return k >= 10 ? k - 10 : k; };
    if (isA) {
        const int j16 = idx & 15;
        const char* wl = wB + (hg >> 2) * 640 + j16 * (4 * 3840);
        const char* xl = xB + (size_t)(rt * 128 + (hg & 3) * 32 + r32) * 1280 + hl * 128;
        auto pfx = [&](int i) { return xl + i * 256; };
        auto pfkv = [&](int i) { int n = i * 64 + lane; n = n < 400 ? n : 399; const int sl = n / 40, r = n % 40, c = r % 10;
            return wl + (sl * 64 + r / 10) * 3840 + 1280 + (c < 5 ? c * 128 : 640 + c * 128); };
        auto pfq = [&](int i) { int n = i * 64 + lane; n = n < 200 ? n : 199; int sl = n / 20 + 7; sl = sl >= 10 ? sl - 10 : sl; const int r = n % 20;
            return wl + (sl * 64 + r / 5) * 3840 + (r % 5) * 128; };
        dma4(wid & 1 ? pfx(wid >> 1) : pfkv(wid >> 1), lds0 + OWN + wid * 256);
        dma4(wid < 3 ? pfkv(4 + wid) : wid == 3 ? pfx(4) : pfq(wid - 4), lds0 + OWN + wid * 256);
        for (int i = 0; i < ADELAY; ++i) __builtin_amdgcn_s_sleep(16);
    }

    if (wid == 4 && lane < 2) dma4(ws + lane * 4, lds0 + C_TAG);
    if (wid == 7) { const char* p = ws + 2048;
        if (lane < 6) { const int im = me - 8 * (lane >> 1); if (im >= 0) p = ws + WS_KV + im * KVG + ((lane & 1) ? KVB : -128); }
        else if (lane < 8) p = ws + WS_O + (br * 16 + rt) * OG + ((lane & 1) ? (int)OG - 128 : -128);
        else if (lane < 10) p = (isA ? oA : oB) + (lane & 1) * (639 * 1280);
        else if (lane < 12) p = out + (size_t)(rt * 128 + (lane & 1) * 127) * 2560;
        dma4(p, lds0 + OWN); }
    const unsigned xo = (unsigned)((lane >> 3) * 1280 + (((lane & 7) ^ (lane >> 4)) << 4));
    auto w_off = [&](int q) { const int c = q * 64 + lane, row = c / 20, j = c % 20; return (unsigned)(row * 3840 + (j < 10 ? 1280 + j * 16 : 2400 + j * 16)); };
    { const char* xs = xb + k0 * 128 + wid * (16 * 1280);
      dma16(xo, xs, lds0 + XS0 + wid * 2048);
      dma16(xo ^ 64, xs + 8 * 1280, lds0 + XS0 + wid * 2048 + 1024);
      const char* wsrc = wb + (size_t)k0 * (64 * 3840);
      const unsigned wv = w_off(wid < 5 ? wid : wid - 5);
      if (wid < 5) { dma16(wv, wsrc, lds0 + WS0 + wid * 1024); dma16(wv, wsrc + 16 * 3840, lds0 + WS0 + (5 + wid) * 1024);
          if (wid >= 3) { dma16(wv, wsrc + 32 * 3840, lds0 + WS0 + (10 + wid) * 1024); dma16(wv, wsrc + 48 * 3840, lds0 + WS0 + (15 + wid) * 1024); } }
      else { dma16(wv, wsrc + 32 * 3840, lds0 + WS0 + (5 + wid) * 1024); dma16(wv, wsrc + 48 * 3840, lds0 + WS0 + (10 + wid) * 1024); } }
    if (tid < 80) *(LAS unsigned*)(smem + tid * 4) = 0u;
    const int nnb = isA ? (rt > 0 ? 1 : 0) : (rt > 1 ? 2 : rt);
    auto kq_base = [&](int fg) { const int hd = fg / 5, dg = fg % 5; return dg < 4 ? hd * 5120 + (dg >> 1) * 2048 + (dg & 1) * 8 : hd * 5120 + 4096; };

    auto rdtag = [&]() { const u64 t = *(LAS u64*)(smem + C_TAG);
        const u64 e = ((u64)(unsigned)__builtin_amdgcn_readfirstlane((int)(t >> 32)) << 32) | (unsigned)__builtin_amdgcn_readfirstlane((int)t);
        return e * 6364136223846793005ull + 1442695040888963407ull; };
    const __amdgpu_buffer_rsrc_t wor = __builtin_amdgcn_make_buffer_rsrc((void*)(isA ? oA : oB), 0, 640 * 1280, 0x00020000);

    if (wid < 4) {
        asm volatile("s_waitcnt vmcnt(0) lgkmcnt(0)\n\ts_barrier" ::: "memory");
        STAMP(1);
        bf16x8 xr[28], xt[2], wf[RING];
        f32x16 acc[5];
        const int tok = wid * 32 + r32;
        const int xa = XS0 + tok * 128 + ((hl ^ ((tok >> 1) & 7)) << 4);
        int wa0 = WS0 + (8 * hl + ((lane & 15) >> 2)) * 320 + (lane & 16) * 2 + (lane & 3) * 8, wa1 = wa0 + 61440;
        asm volatile("" : "+v"(wa0), "+v"(wa1));
        auto rdx = [&](int i) { return *(LAS bf16x8*)(smem + ((i >> 2) % 3) * 16384 + (xa ^ ((i & 3) * 32))); };
        auto rdw = [&](int f) { const int i = f / 5, t = f % 5, sl = (i >> 1) % 7;
            return trfrag(smem + (sl < 6 ? wa0 + sl * 10240 : wa1) + (i & 1) * 5120 + t * 64, 1280); };
        unsigned fl = 0;
        auto rdflag = [&](int u) { fl = __hip_atomic_load((LAS unsigned*)(smem + C_F + u * 4), RLX_WG); };
        auto check = [&](int u) {
            if (__builtin_amdgcn_readfirstlane(fl) != 15) {
#pragma unroll 1
                for (unsigned spins = 0; spins < LSPIN_CAP; ++spins) { rdflag(u); if (__builtin_amdgcn_readfirstlane(fl) == 15) break; __builtin_amdgcn_s_sleep(1); }
            }
            FENCE(); };
#pragma unroll
        for (int i = 0; i < 4; ++i) xr[i] = rdx(i);
#pragma unroll
        for (int f = 0; f < WIN; ++f) wf[f] = rdw(f);
        SB();
        SFOR(f, 200)
            constexpr int i = f / 5, t = f % 5;
            if constexpr (f % 20 == 10 - WIN) lds_set(smem + C_P + wid * 4, (i >> 1) + 1);
            if constexpr (f % 20 == 10) rdflag(f / 20 + 1);
            if constexpr (f % 20 == 20 - WIN && f < 190) check(f / 20 + 1);
            if constexpr (f + WIN < 200) wf[(f + WIN) % RING] = rdw(f + WIN);
            if constexpr (f % 20 >= 16 && f < 120) xr[(f / 20 + 1) * 4 + f % 20 - 16] = rdx((f / 20 + 1) * 4 + f % 20 - 16);
            if constexpr (t == 1 && i >= 27 && i < 39) xt[(i + 1) & 1] = rdx(i + 1);
            SB();
            if constexpr (i == 0) MFMA32Z(acc[t], wf[f % RING], xr[0]);
            else if constexpr (i < 28) MFMA32I(acc[t], wf[f % RING], xr[i]);
            else MFMA32I(acc[t], wf[f % RING], xt[i & 1]);
            SB();
        SEND
        MFMA_DRAIN();
        WAITV(0);
        STAMP(2);
#pragma unroll
        for (int t = 0; t < 5; ++t)
#pragma unroll
            for (int b = 0; b < 4; ++b) {
                const int g0 = 8 * t + 2 * b, g1 = g0 + 1;
                const int o0 = g0 < 20 ? kq_base(g0) + tok * (g0 % 5 < 4 ? 16 : 8) : VOFF + (g0 - 20) * 8 + tok * VP;
                const int o1 = g1 < 20 ? kq_base(g1) + tok * (g1 % 5 < 4 ? 16 : 8) : VOFF + (g1 - 20) * 8 + tok * VP;
                *(LAS u32x2*)(smem + OWN + (hl ? o1 : o0)) = (u32x2){pk2(acc[t][4 * b], acc[t][4 * b + 1]), pk2(acc[t][4 * b + 2], acc[t][4 * b + 3])};
            }
        if (hl == 0) *(LAS u32x2*)(smem + OWN + VOFF + tok * VP + 160) = (u32x2){0x3F803F80u, 0x3F803F80u};
        WAITL();
        lds_set(smem + C_E + wid * 4, 1);
        STAMP(3);
        int l2 = tid; asm volatile("" : "+v"(l2)); l2 &= 63;
        int qa0 = (8 * (l2 >> 5) + 2 * ((l2 & 15) >> 2)) * 160 + (l2 & 16) * 2 + (l2 & 3) * 8, qa1 = qa0 + WS0 + 10240;
        asm volatile("" : "+v"(qa0), "+v"(qa1));
        auto rdq = [&](int f) { const int i = f / 3, t = f % 3, j = i >> 2, sl = (20 + j) % 7;
            return trfrag(smem + (j >= 7 ? qa0 + XS0 + (j - 7) * 10240 : sl == 0 ? qa0 + WS0 : qa1 + (sl - 1) * 10240) + (i & 3) * 2560 + t * 64, 160); };
        check(10);
        xt[0] = rdx(28);
#pragma unroll
        for (int f = 0; f < WIN; ++f) wf[f] = rdq(f);
        SB();
        SFOR(f, 120)
            constexpr int i = f / 3, t = f % 3, j = i >> 2;
            if constexpr (f % 24 == 12 && j < 9) rdflag(11 + j / 2);
            if constexpr (f % 24 == 12 - WIN && (j == 2 || j >= 6)) lds_set(smem + C_P + wid * 4, 21 + j);
            if constexpr (f % 24 == 24 - WIN && j < 9) check(11 + j / 2);
            if constexpr (f + WIN < 120) wf[(f + WIN) % RING] = rdq(f + WIN);
            if constexpr (t == 0 && i < 11) xt[(i + 1) & 1] = rdx(28 + i + 1);
            SB();
            if constexpr (i == 0) MFMA32Z(acc[t], wf[f % RING], xt[0]);
            else if constexpr (i < 12) MFMA32I(acc[t], wf[f % RING], xt[i & 1]);
            else MFMA32I(acc[t], wf[f % RING], xr[i - 12]);
            SB();
        SEND
        MFMA_DRAIN();
        STAMP(4);
        wait_min4(smem + C_P, 29);
#pragma unroll
        for (int t = 0; t < 3; ++t)
#pragma unroll
            for (int b = 0; b < 4; ++b)
                if (t < 2 || b < 2) {
                    const int g0 = 8 * t + 2 * b, g1 = g0 + 1;
                    const int o0 = kq_base(g0) + tok * (g0 % 5 < 4 ? 16 : 8), o1 = kq_base(g1) + tok * (g1 % 5 < 4 ? 16 : 8);
                    *(LAS u32x2*)(smem + QST + (hl ? o1 : o0)) =
                        (u32x2){pk2(acc[t][4 * b] * QSCALE, acc[t][4 * b + 1] * QSCALE), pk2(acc[t][4 * b + 2] * QSCALE, acc[t][4 * b + 3] * QSCALE)};
                }
        STAMP(5);
    } else {
        const int hw = wid - 4;
        int known = 0;
        __builtin_amdgcn_s_setprio(3);
        auto lds_or = [&](int off, unsigned v) { FENCE(); if (lane == 0) __hip_atomic_fetch_or((LAS unsigned*)(smem + off), v, RLX_WG); FENCE(); };
        unsigned wo[5], qo[5];
#pragma unroll
        for (int q = 0; q < 5; ++q) { wo[q] = w_off(q) + (hw & 1) * (16 * 3840) + (4 - q) * 1024;
            const int c = q * 64 + lane, rho = c / 10; qo[q] = (unsigned)(((hw & 1) * 32 + (rho & ~7) + 4 * (rho & 1) + ((rho >> 1) & 3)) * 3840 + (c % 10) * 16 + (4 - q) * 1024); }
        auto done = [&](int n) { lds_or(C_F + n * 4, 1u << hw); };
        const unsigned xo1 = (xo ^ 64) + 9216, xo2 = xo + 2 * 9216, xo3 = (xo ^ 64) + 3 * 9216;
        auto round_x = [&](int n) { dma16x4(xo, xo1, xo2, xo3, xb + kt_of(n) * 128 + hw * (32 * 1280), lds0 + XS0 + (n % 3) * 16384 + hw * 4096); };
        auto round_w = [&](int n) {
            const int u = 2 * n + (hw >> 1);
            const unsigned dst = lds0 + (u < 27 ? WS0 + (u % 7) * 10240 : XS0 + (u - 27) * 10240) + (hw & 1) * 5120 + 2048;
            if (u < 20) dma16x5(wo, wb + (size_t)(kt_of(u >> 1) * 64 + (u & 1) * 32) * 3840 - 2048, dst);
            else dma16x5(qo, wb + (size_t)kt_of(u < 23 ? u - 13 : u - 23) * (64 * 3840) - 2048, dst); };
        round_x(1); round_w(1);
        WAITV(9);
        BAR_L();
#pragma unroll 1
        for (int n = ABL4N; n <= 12; ++n) {
            const int need = 2 * n - 5;
            if (need > known) known = min4(smem + C_P);
            if (need > known) { WAITV(0); done(n - 1); known = wait_min4(smem + C_P, need); }
            if (n < 10) round_x(n);
            round_w(n);
            if (n < 10) WAITV(9); else WAITV(5);
            done(n - 1);
        }
        WAITV(0); done(12);
        STAMP(1);
        const u64 tag = rdtag();
        auto a_chunk = [&](int a) { int o;
            if (a < 320) { const int hd = a / 80, rem = a % 80; o = hd * 5120 + (rem < 32 ? (96 + rem) * 16 : rem < 64 ? 2048 + (64 + rem) * 16 : 4096 + 768 + (rem - 64) * 16); }
            else o = VOFF + 96 * VP + (a - 320) * 16;
            return o; };
        if (rt < 15) {
            const __amdgpu_buffer_rsrc_t kvr = __builtin_amdgcn_make_buffer_rsrc(ws + WS_KV + me * KVG, 0, KVB, 0x00020000);
            wait_min4(smem + C_E, 1);
            STAMP(2);
            if (!isA) { u32x4 v[11];
#pragma unroll
                for (int j = 0; j < 11; ++j) if (j < 10 || hw == 3) v[j] = *(LAS u32x4*)(smem + OWN + hw * 10240 + j * 1024 + lane * 16);
#pragma unroll
                for (int j = 0; j < 11; ++j) if (j < 10 || hw == 3) __builtin_amdgcn_raw_buffer_store_b128(v[j], kvr, hw * 10240 + j * 1024 + lane * 16, 0, 16);
            } else {
#pragma unroll
                for (int j = 0; j < 3; ++j) { const int a = (4 * j + hw) * 64 + lane;
                    if (a < 656) { const int o = a_chunk(a); __builtin_amdgcn_raw_buffer_store_b128(*(LAS u32x4*)(smem + OWN + o), kvr, o, 0, 16); } }
            }
            STAMP(3);
        }
        wait_min4(smem + C_P, 23);
        round_w(13); round_w(14);
        WAITV(10);
        if (rt < 15) {
            unsigned old = 0;
            if (lane == 0) old = __hip_atomic_fetch_add((LAS unsigned*)(smem + C_CNT), 1u, RLX_WG);
            if (lane == 0 && old == 3) __hip_atomic_store((GAS u64*)(ws + WS_FKV + me * 128), tag, RLX_AGENT);
            STAMP(4);
        }
        asm volatile("s_waitcnt vmcnt(6)" ::: "memory"); WAITV(5); done(13); WAITV(0); done(14);
        poll_flags((GAS u64*)(ws + WS_FKV + (me - 8 * (lane + 1)) * 128), lane < nnb, tag);
        STAMP(5);
        if (!isA) {
            const int n = hw >> 1, half = hw & 1;
            if (n < nnb) {
                const __amdgpu_buffer_rsrc_t nbr = __builtin_amdgcn_make_buffer_rsrc(ws + WS_KV + (me - 8 * (n + 1)) * KVG, 0, KVB, 0x00020000);
                u32x4 v[21];
#pragma unroll
                for (int j = 0; j < 21; ++j) if (j < 20 || half) v[j] = __builtin_amdgcn_raw_buffer_load_b128(nbr, half * 20480 + j * 1024 + lane * 16, 0, 16);
                wait_min4(smem + C_P, 27);
#pragma unroll
                for (int j = 0; j < 21; ++j) if (j < 20 || half) *(LAS u32x4*)(smem + KV0 + (1 - n) * KVB + half * 20480 + j * 1024 + lane * 16) = v[j];
            }
        } else if (nnb) {
            const __amdgpu_buffer_rsrc_t nbr = __builtin_amdgcn_make_buffer_rsrc(ws + WS_KV + (me - 8) * KVG, 0, KVB, 0x00020000);
            u32x4 v[3];
#pragma unroll
            for (int j = 0; j < 3; ++j) { const int a = (4 * j + hw) * 64 + lane; if (a < 656) v[j] = __builtin_amdgcn_raw_buffer_load_b128(nbr, a_chunk(a), 0, 16); }
            wait_min4(smem + C_P, 27);
#pragma unroll
            for (int j = 0; j < 3; ++j) { const int a = (4 * j + hw) * 64 + lane; if (a < 656) *(LAS u32x4*)(smem + KV0 + KVB + a_chunk(a)) = v[j]; }
        }
        if (hw == 3) { wait_min4(smem + C_P, 27); if (lane < 2) *(LAS u32x2*)(smem + ONE + lane * 5120) = (u32x2){0x3F803F80u, 0x3F803F80u}; }
        __builtin_amdgcn_s_setprio(0);
        STAMP(6);
    }
    const int qi = wid & 3, hp = wid >> 2;
    const int ud = qi + 8;
    const bool far = isA ? (4 * rt + qi >= 1) : (4 * rt + qi >= 8);
    const int u0 = isA ? (far ? ud - 1 : ud) : (far ? qi : 8 - 4 * rt);
    f32x16 cd, cf;
    auto masks = [&](int q) {
      const unsigned low = (2u << q) - 1u;
      unsigned vd = low, vf = ~low;
      if (isA) { const u64 w = 0x5555ull << (q + 18);
          vd = (unsigned)(w >> 32); vf = (unsigned)w; }
      if (!far) vf = ~0u;
      const unsigned id = ~vd >> (4 * hl), jf = ~vf >> (4 * hl);
#pragma unroll
      for (int r = 0; r < 16; ++r) {
          const int c0 = (r & 3) + 8 * (r >> 2);
          cd[r] = __uint_as_float((unsigned)__builtin_amdgcn_sbfe((int)id, c0, 1) & 0xFF800000u);
          cf[r] = __uint_as_float((unsigned)__builtin_amdgcn_sbfe((int)jf, c0, 1) & 0xFF800000u);
      } };
    masks(r32);
    unsigned oo[5]; u32x4 wreg[13];
#pragma unroll
    for (int q = 0; q < 5; ++q) { const int c = q * 64 + lane, rho = c / 10;
        oo[q] = (unsigned)((wid * 80 + (rho & ~7) + 4 * (rho & 1) + ((rho >> 1) & 3)) * 1280 + hg * 160 + (c % 10) * 16); }
    auto ldwo = [&](int i) { wreg[i] = __builtin_amdgcn_raw_buffer_load_b128(wor, oo[i % 5] + (i / 5) * (32 * 1280), 0, 0); };
    BAR_L();
    STAMP(7);
    u32x2 ob[2][3];
    {
        bf16x8 q1[2]; u32x2 q2[2];
#pragma unroll
        for (int e = 0; e < 2; ++e) {
            q1[e] = *(LAS bf16x8*)(smem + QST + (2 * hp + e) * 5120 + hl * 2048 + (qi * 32 + r32) * 16);
            q2[e] = *(LAS u32x2*)(smem + QST + (2 * hp + e) * 5120 + 4096 + (qi * 32 + r32) * 8);
            if (hl) q2[e] = (u32x2){0u, 0u};
        }
        const int kb1 = KV0 + hp * 10240 + hl * 2048 + r32 * 16;
        const int kb2 = hl ? ONE : KV0 + hp * 10240 + 4096 + r32 * 8, nh = hl ^ 1;
        const int vrow = KV0 + VOFF + (4 * hl + ((lane & 15) >> 2)) * VP;
        const bool ones = (lane & 16) && (lane & 3);
        const int vb0 = vrow + (ones ? 160 : hp * 80 + (lane & 16) * 2 + (lane & 3) * 8), vb1 = vb0 + (ones ? 0 : 40);
        struct KF { bf16x8 a; s16x4 b; };
        struct VF { bf16x8 a, b; };
        auto rdk = [&](int u, int e) { const int so = (u >> 2) * KVB, tw = u & 3; KF k;
            k.a = *(LAS bf16x8*)(smem + kb1 + so + tw * 512 + e * 5120); k.b = *(LAS s16x4*)(smem + __umul24(so + tw * 256, nh) + kb2 + e * 5120); return k; };
        auto rdv = [&](int u, int e) { LAS char* p = smem + (e ? vb1 : vb0) + (u >> 2) * KVB + (u & 3) * (32 * VP); VF v;
            v.a = trfrag(p, 8 * VP); v.b = trfrag(p + 16 * VP, 8 * VP); return v; };
        auto qk = [&](const KF& k, int e, const f32x16& c) { return MFMA32K8(k.b, __builtin_bit_cast(s16x4, q2[e]), MFMA32(k.a, q1[e], c)); };
        const f32x16 zero = {};
        auto max16 = [&](const f32x16& s, float m) {
#pragma unroll
            for (int r = 0; r < 16; ++r) m = __builtin_elementwise_maximum(m, s[r]);
            return m; };
        auto set_ref = [&](float m, int e) { const unsigned nb = pk2(-m, 0.f); if (hl) q2[e][0] = nb; };
        auto ex4 = [&](f32x16& s, int b) {
#pragma unroll
            for (int r = 0; r < 4; ++r) s[b + r] = __builtin_amdgcn_exp2f(s[b + r]); };
        auto pk8 = [&](const f32x16& s, int b) { return __builtin_bit_cast(bf16x8, (u32x4){pk2(s[b], s[b + 1]), pk2(s[b + 2], s[b + 3]), pk2(s[b + 4], s[b + 5]), pk2(s[b + 6], s[b + 7])}); };
        auto phase = [&](auto nx, f32x16& cur, const VF& v, f32x16& o, f32x16& nxt, const KF& k, int e) __attribute__((always_inline)) {
            constexpr int NX = decltype(nx)::value;
            SB(); ex4(cur, 0); SB();
            if constexpr (NX) nxt = MFMA32(k.a, q1[e], NX == 1 ? zero : NX == 2 ? cf : cd);
            SB(); ex4(cur, 4); { const bf16x8 p = pk8(cur, 0); SB(); o = MFMA32(v.a, p, o); }
            SB(); ex4(cur, 8); SB();
            if constexpr (NX) nxt = MFMA32K8(k.b, __builtin_bit_cast(s16x4, q2[e]), nxt);
            SB(); ex4(cur, 12); { const bf16x8 p = pk8(cur, 8); SB(); o = MFMA32(v.b, p, o); }
            SB(); };
        f32x16 o0, o1, sa, sb; KF kx; VF vx;
        auto step = [&](auto m0, auto m1, int u) __attribute__((always_inline)) {
            constexpr int M1 = decltype(m1)::value;
            KF ky; if constexpr (M1) ky = rdk(u + 1, 0);
            const VF vy = rdv(u, 1);
            phase(m0, sa, vx, o0, sb, kx, 1);
            if constexpr (M1) { kx = rdk(u + 1, 1); vx = rdv(u + 1, 0); }
            phase(m1, sb, vy, o1, sa, ky, 0); };
        auto run = [&](auto fast) __attribute__((always_inline)) {
            o0 = zero; o1 = zero;
            kx = rdk(u0, 1); vx = rdv(u0, 0);
            if constexpr (decltype(fast)::value) {
                sa = qk(rdk(u0, 0), 0, cf);
                SFOR(it, 9)
                    if constexpr (it < 7) { ldwo(2 * it); if constexpr (it < 6) ldwo(2 * it + 1); }
                    step(IC<it == 0 ? 2 : it == 8 ? 3 : 1>{}, IC<it == 8 ? 0 : it == 7 ? 3 : 1>{}, u0 + it);
                SEND
            } else {
                if (u0 < ud) {
                    sa = qk(rdk(u0, 0), 0, cf);
                    if (u0 + 1 < ud) {
                        step(IC<2>{}, IC<1>{}, u0);
#pragma unroll 1
                        for (int u = u0 + 1; u + 1 < ud; ++u) step(IC<1>{}, IC<1>{}, u);
                        step(IC<1>{}, IC<3>{}, ud - 1);
                    } else step(IC<2>{}, IC<3>{}, u0);
                } else sa = qk(rdk(ud, 0), 0, cd);
                step(IC<3>{}, IC<0>{}, ud);
            } };
        { const f32x16 s0 = qk(rdk(ud, 0), 0, cd), s1 = qk(rdk(ud, 1), 1, cd);
          set_ref(halfmax(max16(s0, s0[0])), 0); set_ref(halfmax(max16(s1, s1[0])), 1); }
        if (u0 + 8 == ud) run(IC<1>{});
        else {
#pragma unroll
            for (int i = 0; i < 13; ++i) ldwo(i);
            run(IC<0>{});
        }
        if (!__all(o0[12] < 1.152921504606847e18f && o1[12] < 1.152921504606847e18f)) {
            { int q = r32; asm volatile("" : "+v"(q)); masks(q); }
#pragma unroll
            for (int e = 0; e < 2; ++e) {
                if (hl) q2[e][0] = 0u;
                const f32x16 s = qk(rdk(ud, e), e, cd);
                float m = max16(s, s[0]);
                if (u0 < ud) m = max16(qk(rdk(u0, e), e, cf), m);
#pragma unroll 1
                for (int u = u0 + 1; u < ud; ++u) m = max16(qk(rdk(u, e), e, zero), m);
                set_ref(halfmax(m), e);
            }
#pragma unroll
            for (int e = 0; e < 2; ++e) {
                f32x16 o = zero;
#pragma unroll 1
                for (int u = u0; u <= ud; ++u) { f32x16 t = u == ud ? qk(rdk(u, e), e, cd) : u == u0 ? qk(rdk(u, e), e, cf) : qk(rdk(u, e), e, zero);
                    const VF v = rdv(u, e); ex4(t, 0); ex4(t, 4); ex4(t, 8); ex4(t, 12);
                    o = MFMA32(v.a, pk8(t, 0), o); o = MFMA32(v.b, pk8(t, 8), o); }
                if (e) o1 = o; else o0 = o;
            }
        }
        const float rl0 = __builtin_amdgcn_rcpf(o0[12]), rl1 = __builtin_amdgcn_rcpf(o1[12]);
#pragma unroll
        for (int j = 0; j < 3; ++j) {
            ob[0][j] = (u32x2){pk2(o0[4 * j] * rl0, o0[4 * j + 1] * rl0), pk2(o0[4 * j + 2] * rl0, o0[4 * j + 3] * rl0)};
            ob[1][j] = (u32x2){pk2(o1[4 * j] * rl1, o1[4 * j + 1] * rl1), pk2(o1[4 * j + 2] * rl1, o1[4 * j + 3] * rl1)}; }
    }
    STAMP(8);
    const __amdgpu_buffer_rsrc_t orr = __builtin_amdgcn_make_buffer_rsrc(ws + WS_O + (br * 16 + rt) * OG, 0, 4 * 40960, 0x00020000);
    { const unsigned obase = (unsigned)(qi * 40960 + (hg * 10 + hp * 5) * 512 + r32 * 16);
      { auto rx = __builtin_amdgcn_permlane32_swap(ob[0][0][0], ob[0][1][0], false, false);
        auto ry = __builtin_amdgcn_permlane32_swap(ob[0][0][1], ob[0][1][1], false, false);
        __builtin_amdgcn_raw_buffer_store_b128((u32x4){rx[0], ry[0], rx[1], ry[1]}, orr, obase + hl * 512, 0, 16); }
      if (hl == 0) __builtin_amdgcn_raw_buffer_store_b128((u32x4){ob[0][2][0], ob[0][2][1], ob[1][0][0], ob[1][0][1]}, orr, obase + 1024, 0, 16);
      { auto fx = __builtin_amdgcn_permlane32_swap(ob[1][0][0], ob[1][1][0], false, false);
        auto fy = __builtin_amdgcn_permlane32_swap(ob[1][0][1], ob[1][1][1], false, false);
        auto sx = __builtin_amdgcn_permlane32_swap(ob[1][1][0], ob[1][2][0], false, false);
        auto sy = __builtin_amdgcn_permlane32_swap(ob[1][1][1], ob[1][2][1], false, false);
        __builtin_amdgcn_raw_buffer_store_b128((u32x4){fx[1], fy[1], sx[0], sy[0]}, orr, obase + 1536 + hl * 512, 0, 16); } }
    const u64 tag = rdtag();
    if (wid == 0 && lane == 0) { GAS unsigned* d = (GAS unsigned*)(ws + WS_DBG + bx * 128);
        __hip_atomic_store(d + 0, dst_[0], RLX_AGENT); __hip_atomic_store(d + 1, dst_[1], RLX_AGENT); __hip_atomic_store(d + 2, dst_[2], RLX_AGENT);
        __hip_atomic_store(d + 3, dst_[7], RLX_AGENT); __hip_atomic_store(d + 4, dst_[8], RLX_AGENT); }
    WAITV(0);
    { unsigned old = 0;
      if (lane == 0) old = __hip_atomic_fetch_add((LAS unsigned*)(smem + C_CNT + 4), 1u, RLX_WG);
      if (lane == 0 && old == 7) __hip_atomic_store((GAS u64*)(ws + WS_FO + me * 128), tag, RLX_AGENT); }
    STAMP(9);
    BAR_L();
#pragma unroll
    for (int i = 0; i < 13; ++i) if (i < 12 || lane < 32) *(LAS u32x4*)(smem + WOB + wid * 12800 + i * 1024 + lane * 16) = wreg[i];
    BAR_L();
    STAMP(10);
    poll_flags((GAS u64*)(ws + WS_FO + ((me & ~7) + (lane & 7)) * 128), lane < 8, tag);
    STAMP(11);
    f32x16 oc[3];
    { bf16x8 af[6], of[20];
      const int aa = WOB + hp * 51200 + (8 * hl + 2 * ((lane & 15) >> 2)) * 160 + (lane & 16) * 2 + (lane & 3) * 8;
      auto rda = [&](int f) { return trfrag(smem + aa + (f / 3) * 2560 + (f % 3) * 64, 160); };
      auto ldo = [&](int k) { of[k] = __builtin_bit_cast(bf16x8, __builtin_amdgcn_raw_buffer_load_b128(orr, qi * 40960 + (hp * 20 + k) * 1024 + lane * 16, 0, 16)); };
#pragma unroll
      for (int k = 0; k < OPF; ++k) ldo(k);
#pragma unroll
      for (int f = 0; f < 4; ++f) af[f] = rda(f);
      SB();
      SFOR(f, 60)
          if constexpr (f % 3 == 0 && f / 3 + OPF < 20) ldo(f / 3 + OPF);
          if constexpr (f + 4 < 60) af[(f + 4) % 6] = rda(f + 4);
          SB();
          if constexpr (f < 3) MFMA32Z(oc[f], af[f % 6], of[0]); else MFMA32I(oc[f % 3], af[f % 6], of[f / 3]);
          SB();
      SEND }
    MFMA_DRAIN();
    STAMP(12);
    { LAS char* mine = smem + RED + wid * 6144 + lane * 16; LAS char* other = smem + RED + (wid ^ 4) * 6144 + lane * 16;
      if (hp) {
#pragma unroll
          for (int b = 0; b < 4; ++b) *(LAS f32x4*)(mine + b * 1024) = (f32x4){oc[0][4 * b], oc[0][4 * b + 1], oc[0][4 * b + 2], oc[0][4 * b + 3]};
#pragma unroll
          for (int b = 0; b < 2; ++b) *(LAS f32x4*)(mine + (4 + b) * 1024) = (f32x4){oc[2][4 * b], oc[2][4 * b + 1], oc[2][4 * b + 2], oc[2][4 * b + 3]};
      } else {
#pragma unroll
          for (int b = 0; b < 4; ++b) *(LAS f32x4*)(mine + b * 1024) = (f32x4){oc[1][4 * b], oc[1][4 * b + 1], oc[1][4 * b + 2], oc[1][4 * b + 3]};
      }
      BAR_L();
      char* orow = out + (size_t)(rt * 128 + qi * 32 + r32) * 2560 + br * 1280 + hg * 160 + hl * 16;
      auto fin = [&](const f32x16& c, int b, int slot) { const f32x4 x = *(LAS f32x4*)(other + slot * 1024);
          return (u32x2){pk2(c[4 * b] + x[0], c[4 * b + 1] + x[1]), pk2(c[4 * b + 2] + x[2], c[4 * b + 3] + x[3])}; };
      auto st2 = [&](u32x2 a, u32x2 b, int col) {
          auto rx = __builtin_amdgcn_permlane32_swap(a[0], b[0], false, false); auto ry = __builtin_amdgcn_permlane32_swap(a[1], b[1], false, false);
          *(u32x4*)(orow + col * 2) = (u32x4){rx[0], ry[0], rx[1], ry[1]}; };
      if (hp) { st2(fin(oc[1], 0, 0), fin(oc[1], 1, 1), 32); st2(fin(oc[1], 2, 2), fin(oc[1], 3, 3), 48); }
      else { st2(fin(oc[0], 0, 0), fin(oc[0], 1, 1), 0); st2(fin(oc[0], 2, 2), fin(oc[0], 3, 3), 16); st2(fin(oc[2], 0, 4), fin(oc[2], 1, 5), 64); } }
    if (bx == 0) {
        if (wid < 4) poll_flags((GAS u64*)(ws + WS_FO + tid * 128), true, tag);
        __syncthreads();
        if (tid == 0) __hip_atomic_store((GAS u64*)ws, tag, RLX_AGENT);
        if (wid == 0) {
            const unsigned now = (unsigned)__builtin_amdgcn_s_memrealtime();
            int mn0 = 0x7fffffff, mx0 = -0x7fffffff, a1 = 0, a2 = 0, a3 = 0, a4 = 0, mx8 = -0x7fffffff, b1 = 0;
            for (int i = 0; i < 4; ++i) { const int b = i * 64 + lane; GAS unsigned* d = (GAS unsigned*)(ws + WS_DBG + b * 128);
                const unsigned t0 = __hip_atomic_load(d, RLX_AGENT), t1 = __hip_atomic_load(d + 1, RLX_AGENT), t2 = __hip_atomic_load(d + 2, RLX_AGENT), t7 = __hip_atomic_load(d + 3, RLX_AGENT), t8 = __hip_atomic_load(d + 4, RLX_AGENT);
                const int r0 = (int)(t0 - now); mn0 = min(mn0, r0); mx0 = max(mx0, r0); mx8 = max(mx8, (int)(t8 - now));
                if (i < 2) { a1 += t1 - t0; a2 += t2 - t1; a3 += t7 - t2; a4 += t8 - t7; } else b1 += t2 - t0; }
            for (int m = 32; m >= 1; m >>= 1) { mn0 = min(mn0, __shfl_xor(mn0, m)); mx0 = max(mx0, __shfl_xor(mx0, m)); mx8 = max(mx8, __shfl_xor(mx8, m));
                a1 += __shfl_xor(a1, m); a2 += __shfl_xor(a2, m); a3 += __shfl_xor(a3, m); a4 += __shfl_xor(a4, m); b1 += __shfl_xor(b1, m); }
            int M = DIAG == 0 ? 500 : DIAG == 1 ? mx0 - mn0 : DIAG == 2 ? a1 / 128 : DIAG == 3 ? a2 / 128 : DIAG == 4 ? a3 / 128 : DIAG == 5 ? -mn0 : DIAG == 6 ? a4 / 128 : b1 / 128;
            M = min(max(M, 0), 5000);
            const unsigned until = now + 1000 + 2 * M;
            while ((int)((unsigned)__builtin_amdgcn_s_memrealtime() - until) < 0) __builtin_amdgcn_s_sleep(2);
        }
    }
}

extern "C" void kernel_launch(void* const* d_in, const int* in_sizes, int n_in, void* d_out, int out_size, void* d_ws, size_t ws_size,
                              hipStream_t stream) {
    fused_attn_kernel<<<256, 512, 0, stream>>>((const char*)d_in[0], (const char*)d_in[1], (const char*)d_in[2], (const char*)d_in[3],
                                               (const char*)d_in[4], (const char*)d_in[5], (char*)d_out, (char*)d_ws);
}
```

```cpp
#define ADELAY 0
#include <hip/hip_runtime.h>
#include <hip/hip_bf16.h>
#include <stdint.h>

typedef short bf16x8 __attribute__((ext_vector_type(8)));
typedef short s16x4 __attribute__((ext_vector_type(4)));
typedef float f32x2 __attribute__((ext_vector_type(2)));
typedef float f32x4 __attribute__((ext_vector_type(4)));
typedef float f32x16 __attribute__((ext_vector_type(16)));
typedef unsigned u32x4 __attribute__((ext_vector_type(4)));
typedef unsigned u32x2 __attribute__((ext_vector_type(2)));
typedef __bf16 bfx2 __attribute__((ext_vector_type(2)));
typedef unsigned long long u64;
#define LAS __attribute__((address_space(3)))
#define GAS __attribute__((address_space(1)))
#define DI __device__ __forceinline__
#define RLX_AGENT __ATOMIC_RELAXED, __HIP_MEMORY_SCOPE_AGENT
#define RLX_WG __ATOMIC_RELAXED, __HIP_MEMORY_SCOPE_WORKGROUP

constexpr int LDS_BYTES = 163840;
constexpr int VOFF = 20480, VP = 168;
constexpr int KVB = VOFF + 128 * VP;
constexpr int KV0 = LDS_BYTES - 3 * KVB;
constexpr int OWN = KV0 + 2 * KVB;
constexpr int C_F = 0, C_P = 128, C_E = 144, C_QF = 160, C_RD = 176, C_CNT = 192, C_TAG = 320;
constexpr int N_PUB = C_CNT, N_NB = C_CNT + 4, N_G0 = C_CNT + 8, N_G1 = C_CNT + 12, N_WO = C_CNT + 16, N_OS = C_CNT + 24, N_KV = C_CNT + 32, N_OK = C_CNT + 36;
constexpr int XS0 = 512, WS0 = XS0 + 3 * 16384, QST = 512, ONE = 336, QU27 = XS0 + 16384, QU28 = XS0, QU29 = QU27 + 10240;
static_assert(WS0 + 7 * 10240 <= OWN, "ring overlaps the own image");
constexpr unsigned WS_FKV = 4096, WS_FO = WS_FKV + 256 * 128, WS_KV = 131072;
constexpr unsigned KVG = KVB, OG = 4 * 40960;
constexpr unsigned WS_O = WS_KV + 256 * KVG;
constexpr unsigned WS_END = WS_O + 32 * OG;
constexpr unsigned WS_DBG = WS_END;
constexpr unsigned SPIN_CAP = 400000, LSPIN_CAP = 4000000;
constexpr float QSCALE = 0.22360679774997896f * 1.4426950408889634f;
#define PSLEEP 2
#define PDELAY 1
#define PSTAG 3
#define PFLIP 6
#define OPF 12
#define ABL4N 2
#define WIN 4

#define MFMA32(a, b, c) __builtin_amdgcn_mfma_f32_32x32x16_bf16((a), (b), (c), 0, 0, 0)
#define MFMA32K8(a, b, c) __builtin_amdgcn_mfma_f32_32x32x8bf16_1k((a), (b), (c), 0, 0, 0)
#define MFMA32I(acc, a, b) asm volatile("v_mfma_f32_32x32x16_bf16 %0, %1, %2, %0" : "+v"(acc) : "v"(a), "v"(b))
#define MFMA32Z(acc, a, b) asm volatile("v_mfma_f32_32x32x16_bf16 %0, %1, %2, 0" : "=&v"(acc) : "v"(a), "v"(b))
#define MFMA_DRAIN() asm volatile("s_nop 7\n\ts_nop 4" ::: "memory")
#define WAITV(N) asm volatile("s_waitcnt vmcnt(" #N ")" ::: "memory")
#define WAITL() asm volatile("s_waitcnt lgkmcnt(0)" ::: "memory")
#define BAR_L() asm volatile("s_waitcnt lgkmcnt(0)\n\ts_barrier" ::: "memory")
#define SB() __builtin_amdgcn_sched_barrier(0)
#define FENCE() asm volatile("" ::: "memory")

#define STAMP(i) do {} while (0)

template <int V> struct IC { static constexpr int value = V; };
template <int I, int N, class F> DI void static_for(F&& f) { if constexpr (I < N) { f(IC<I>{}); static_for<I + 1, N>(f); } }
#define SFOR(var, N) static_for<0, N>([&](auto ic_##var) __attribute__((always_inline)) { constexpr int var = decltype(ic_##var)::value;
#define SEND });

DI void dma16(unsigned voff, const char* sbase, unsigned ldsaddr) {
    asm volatile("s_mov_b32 m0, %2\n\ts_nop 0\n\tglobal_load_lds_dwordx4 %0, %1" :: "v"(voff), "s"(sbase), "s"(ldsaddr) : "memory");
}
DI void dma16x4(unsigned v0, unsigned v1, unsigned v2, unsigned v3, const char* sbase, unsigned ldsaddr) {
    asm volatile("s_mov_b32 m0, %5\n\ts_nop 0\n\tglobal_load_lds_dwordx4 %0, %4\n\tglobal_load_lds_dwordx4 %1, %4 offset:1024\n\t"
                 "global_load_lds_dwordx4 %2, %4 offset:2048\n\tglobal_load_lds_dwordx4 %3, %4 offset:3072"
                 :: "v"(v0), "v"(v1), "v"(v2), "v"(v3), "s"(sbase), "s"(ldsaddr) : "memory");
}
DI void dma16x5(const unsigned (&v)[5], const char* sbase, unsigned ldsaddr) {
    asm volatile("s_mov_b32 m0, %6\n\ts_nop 0\n\tglobal_load_lds_dwordx4 %0, %5 offset:-2048\n\tglobal_load_lds_dwordx4 %1, %5 offset:-1024\n\t"
                 "global_load_lds_dwordx4 %2, %5\n\tglobal_load_lds_dwordx4 %3, %5 offset:1024\n\tglobal_load_lds_dwordx4 %4, %5 offset:2048"
                 :: "v"(v[0]), "v"(v[1]), "v"(v[2]), "v"(v[3]), "v"(v[4]), "s"(sbase), "s"(ldsaddr) : "memory");
}
DI void dma16x5_sc1(unsigned voff, const char* sbase, unsigned ldsaddr) {
    asm volatile("s_mov_b32 m0, %2\n\ts_nop 0\n\tglobal_load_lds_dwordx4 %0, %1 offset:-2048 sc1\n\tglobal_load_lds_dwordx4 %0, %1 offset:-1024 sc1\n\t"
                 "global_load_lds_dwordx4 %0, %1 sc1\n\tglobal_load_lds_dwordx4 %0, %1 offset:1024 sc1\n\tglobal_load_lds_dwordx4 %0, %1 offset:2048 sc1"
                 :: "v"(voff), "s"(sbase), "s"(ldsaddr) : "memory");
}
DI void dma16_sc1(unsigned voff, const char* sbase, unsigned ldsaddr) {
    asm volatile("s_mov_b32 m0, %2\n\ts_nop 0\n\tglobal_load_lds_dwordx4 %0, %1 sc1" :: "v"(voff), "s"(sbase), "s"(ldsaddr) : "memory");
}
DI void dma4(const char* p, unsigned ldsaddr) {
    asm volatile("s_mov_b32 m0, %1\n\ts_nop 0\n\tglobal_load_lds_dword %0, off" :: "v"(p), "s"(ldsaddr) : "memory");
}
DI unsigned pk2(float lo, float hi) { f32x2 v = {lo, hi}; return __builtin_bit_cast(unsigned, __builtin_convertvector(v, bfx2)); }
typedef short v4i16_t __attribute__((ext_vector_type(4)));
DI bf16x8 trfrag(LAS char* p, int second) {
    s16x4 a = __builtin_bit_cast(s16x4, __builtin_amdgcn_ds_read_tr16_b64_v4i16((LAS v4i16_t*)p));
    s16x4 b = __builtin_bit_cast(s16x4, __builtin_amdgcn_ds_read_tr16_b64_v4i16((LAS v4i16_t*)(p + second)));
    return __builtin_shufflevector(a, b, 0, 1, 2, 3, 4, 5, 6, 7);
}
constexpr u64 lanes_cut(int r, bool fartile) {
    u64 m = 0;
    for (int l = 0; l < 64; ++l) { const int key = (r & 3) + 8 * (r >> 2) + 4 * (l >> 5), d = (l & 31) - key + (fartile ? 32 : 0);
        const bool ok = d >= 0 && d <= 14 && !(d & 1);
        if (!ok) m |= 1ull << l; }
    return m;
}
DI float halfmax(float m) { auto rr = __builtin_amdgcn_permlane32_swap(__float_as_uint(m), __float_as_uint(m), false, false);
    return __builtin_elementwise_maximum(__uint_as_float(rr[0]), __uint_as_float(rr[1])); }

DI void poll_flags(GAS u64* word, bool active, u64 tag) {
#pragma unroll 1
    for (unsigned spins = 0; spins < SPIN_CAP; ++spins) {
        u64 v = tag;
        if (active) v = __hip_atomic_load(word, RLX_AGENT);
        if (__all(v == tag)) break;
        __builtin_amdgcn_s_sleep(PSLEEP);
    }
    __builtin_amdgcn_fence(__ATOMIC_ACQUIRE, "wavefront");
}
DI void poll_shared(GAS u64* word, bool active, u64 tag, LAS char* ok) {
#pragma unroll 1
    for (unsigned spins = 0; spins < SPIN_CAP; ++spins) {
        if (__builtin_amdgcn_readfirstlane((int)__hip_atomic_load((LAS unsigned*)ok, RLX_WG))) break;
        u64 v = tag;
        if (active) v = __hip_atomic_load(word, RLX_AGENT);
        if (__all(v == tag)) { __hip_atomic_store((LAS unsigned*)ok, 1u, RLX_WG); break; }
        __builtin_amdgcn_s_sleep(PSLEEP);
    }
    __builtin_amdgcn_fence(__ATOMIC_ACQUIRE, "wavefront");
    asm volatile("" ::: "memory");
}
DI int min4(LAS char* p) {
    const u64 a = __hip_atomic_load((LAS u64*)p, RLX_WG), b = __hip_atomic_load((LAS u64*)(p + 8), RLX_WG);
    const unsigned m = min(min((unsigned)a, (unsigned)(a >> 32)), min((unsigned)b, (unsigned)(b >> 32)));
    return __builtin_amdgcn_readfirstlane((int)m);
}
DI int wait_min4(LAS char* p, int need) {
    int v = min4(p);
#pragma unroll 1
    for (unsigned spins = 0; v < need && spins < LSPIN_CAP; ++spins) { __builtin_amdgcn_s_sleep(1); v = min4(p); }
    FENCE();
    return v;
}
DI void wait_word(LAS char* p, unsigned need) {
#pragma unroll 1
    for (unsigned spins = 0; spins < LSPIN_CAP; ++spins) {
        if ((unsigned)__builtin_amdgcn_readfirstlane((int)__hip_atomic_load((LAS unsigned*)p, RLX_WG)) >= need) break;
        __builtin_amdgcn_s_sleep(1); }
    FENCE();
}
DI void lds_inc0(LAS char* p, int lane) { FENCE(); if (lane == 0) (void)__hip_atomic_fetch_add((LAS unsigned*)p, 1u, RLX_WG); FENCE(); }
DI void wait_pair(LAS char* p, unsigned need) {
#pragma unroll 1
    for (unsigned spins = 0; spins < LSPIN_CAP; ++spins) { const u64 v = __hip_atomic_load((LAS u64*)p, RLX_WG);
        if ((unsigned)__builtin_amdgcn_readfirstlane((int)min((unsigned)v, (unsigned)(v >> 32))) >= need) break;
        __builtin_amdgcn_s_sleep(1); }
    FENCE();
}
DI unsigned lds_inc(LAS char* p, int lane) { FENCE(); unsigned old = 0; if (lane == 0) old = __hip_atomic_fetch_add((LAS unsigned*)p, 1u, RLX_WG); FENCE(); return old; }
DI void lds_set(LAS char* p, unsigned v) { FENCE(); __hip_atomic_store((LAS unsigned*)p, v, RLX_WG); FENCE(); }

__global__ __launch_bounds__(512, 2) void fused_attn_kernel(const char* __restrict__ xA, const char* __restrict__ wA, const char* __restrict__ oA,
                                                           const char* __restrict__ xB, const char* __restrict__ wB, const char* __restrict__ oB,
                                                           char* __restrict__ out, char* __restrict__ ws) {
    __shared__ __attribute__((aligned(16))) char smem_[LDS_BYTES];
    LAS char* smem = (LAS char*)smem_;
    const unsigned lds0 = (unsigned)(uintptr_t)smem;
    STAMP(0);
    asm volatile("" :: "s"(xA), "s"(wA), "s"(oA), "s"(xB), "s"(wB), "s"(oB), "s"(out), "s"(ws));
    const int tid = threadIdx.x, lane = tid & 63;
    const int wid = __builtin_amdgcn_readfirstlane(tid >> 6);
    const int bx = blockIdx.x, xcd = bx & 7, idx = bx >> 3;
    const int isA = idx >> 4, rt = (xcd >> 1) * 4 + ((idx >> 2) & 3), hg = (xcd & 1) * 4 + (idx & 3);
    const int br = isA ? 0 : 1;
    const int k0 = isA ? (((rt & 3) * 5 + 1) / 2 + ((hg & 3) * 5 + 1) / 2) % 10 : 0;
    const char* xb = (isA ? xA : xB) + (size_t)rt * (128 * 1280);
    const char* wb = (isA ? wA : wB) + hg * 160;
    const int me = (br * 16 + rt) * 8 + hg;
    const int r32 = lane & 31, hl = lane >> 5;
    auto kt_of = [&](int s) { int k = s + k0; return k >= 10 ? k - 10 : k; };
    if (isA && wid < 4) {
        const int j16 = idx & 15; const unsigned dump = lds0 + OWN + wid * 512;
        const char* wl = wB + (hg >> 2) * 640 + j16 * (4 * 3840);
        const char* xl = xB + (size_t)(rt * 128 + (hg & 3) * 32 + r32) * 1280 + hl * 128;
        auto pfx = [&](int i) { return xl + i * 256; };
        auto pfkv = [&](int i) { int n = i * 64 + lane; n = n < 400 ? n : 399; const int sl = n / 40, r = n % 40, c = r % 10;
            return wl + (sl * 64 + r / 10) * 3840 + 1280 + (c < 5 ? c * 128 : 640 + c * 128); };
        auto pfq = [&](int i) { int n = i * 64 + lane; n = n < 200 ? n : 199; int sl = n / 20 + 8; sl = sl >= 10 ? sl - 10 : sl; const int r = n % 20;
            return wl + (sl * 64 + r / 5) * 3840 + (r % 5) * 128; };
        dma4(wid & 1 ? pfx(wid >> 1) : pfkv(wid >> 1), dump);
        dma4(wid & 1 ? pfx(2 + (wid >> 1)) : pfkv(2 + (wid >> 1)), dump);
        dma4(wid < 3 ? pfkv(4 + wid) : pfx(4), dump);
        dma4(pfq(wid), dump);
        { int n = wid * 64 + lane; n = n < 200 ? n : 199; const int o = (j16 * 40 + n / 5) * 1280 + (hg >> 2) * 640 + (n % 5) * 128;
          dma4(oB + o, dump); dma4(oA + o, dump); }
    }
    if (isA && wid >= 4) { for (int i = 0; i < ADELAY; ++i) __builtin_amdgcn_s_sleep(16); }

    const unsigned xo = (unsigned)((lane >> 3) * 1280 + (((lane & 7) ^ (lane >> 4)) << 4));
    auto w_off = [&](int q) { const int c = q * 64 + lane, row = __umul24(c, 3277) >> 16, j = c - row * 20; return (unsigned)(__umul24(row, 3840) + j * 16 + (j < 10 ? 1280 : 2400)); };
    if (!isA) { const char* xs = xb + k0 * 128 + wid * (16 * 1280);
      dma16(xo, xs, lds0 + XS0 + wid * 2048);
      dma16(xo ^ 64, xs + 8 * 1280, lds0 + XS0 + wid * 2048 + 1024);
      const char* wsrc = wb + (size_t)k0 * (64 * 3840);
      const unsigned wv = w_off(wid < 5 ? wid : wid - 5);
      if (wid < 5) { dma16(wv, wsrc, lds0 + WS0 + wid * 1024); dma16(wv, wsrc + 16 * 3840, lds0 + WS0 + (5 + wid) * 1024);
          if (wid >= 3) { dma16(wv, wsrc + 32 * 3840, lds0 + WS0 + (10 + wid) * 1024); dma16(wv, wsrc + 48 * 3840, lds0 + WS0 + (15 + wid) * 1024); } }
      else { dma16(wv, wsrc + 32 * 3840, lds0 + WS0 + (5 + wid) * 1024); dma16(wv, wsrc + 48 * 3840, lds0 + WS0 + (10 + wid) * 1024); } }
    if (wid == 4 && lane < 2) dma4(ws + lane * 4, lds0 + C_TAG);
    if (tid < 80) *(LAS unsigned*)(smem + tid * 4) = 0u;
    else if (tid < 82) *(LAS unsigned*)(smem + ONE + (tid - 80) * 4) = 0x3F803F80u;
    const int nnb = isA ? (rt > 0 ? 1 : 0) : (rt > 1 ? 2 : rt);
    auto kq_base = [&](int fg) { const int hd = fg / 5, dg = fg % 5; return dg < 4 ? hd * 5120 + (dg >> 1) * 2048 + (dg & 1) * 8 : hd * 5120 + 4096; };

    auto rdtag = [&]() { const u64 t = *(LAS u64*)(smem + C_TAG);
        const u64 e = ((u64)(unsigned)__builtin_amdgcn_readfirstlane((int)(t >> 32)) << 32) | (unsigned)__builtin_amdgcn_readfirstlane((int)t);
        return e * 6364136223846793005ull + 1442695040888963407ull; };
    const __amdgpu_buffer_rsrc_t wor = __builtin_amdgcn_make_buffer_rsrc((void*)(isA ? oA : oB), 0, 640 * 1280, 0x00020000);

    const int hp = wid >> 2, qo = wid & 3, qi = hp ? 3 - qo : qo;
    const int ud = qi + 8;
    const bool far = isA ? (4 * rt + qi >= 1) : (4 * rt + qi >= 8);
    const int u0 = isA ? (far ? ud - 1 : ud) : (far ? qi : 8 - 4 * rt);
    f32x16 cd, cf;
    int dsh, kb1, kb2, nh, vb0, vb1, qb1, qb2; unsigned obase, oo[5];
    auto mask_b = [&](f32x16& c, int r, bool fartile) { const int k = (r & 3) + 8 * (r >> 2); c[r] = __int_as_float(fartile ? ((k - 1) << 23) - dsh : dsh - (k << 23)); };
    auto mask_a = [&](int z) {
      SFOR(r, 16)
          constexpr u64 ad = lanes_cut(r, false), af = lanes_cut(r, true);
          cd[r] = __builtin_amdgcn_inverse_ballot_w64(ad + z) ? -__builtin_inff() : 0.f;
          cf[r] = __builtin_amdgcn_inverse_ballot_w64(af + z) ? -__builtin_inff() : 0.f;
      SEND };
    auto att_setup = [&](int part) {
        int l = tid; asm volatile("" : "+v"(l)); l &= 63;
        const int q = l & 31, h = l >> 5;
        if (part == 0) { dsh = (q - 4 * h) << 23; kb1 = KV0 + hp * 10240 + h * 2048 + q * 16; kb2 = h ? ONE : KV0 + hp * 10240 + 4096 + q * 8; nh = h ^ 1;
            asm volatile("" : "+v"(dsh), "+v"(kb1), "+v"(kb2), "+v"(nh)); }
        if (part == 1) { const bool ones = (l & 16) && (l & 3);
            vb0 = KV0 + VOFF + (4 * h + ((l & 15) >> 2)) * VP + (ones ? 160 : hp * 80 + (l & 16) * 2 + (l & 3) * 8); vb1 = vb0 + (ones ? 0 : 40);
            asm volatile("" : "+v"(vb0), "+v"(vb1)); }
        if (part == 2) { qb1 = QST + hp * 10240 + h * 2048 + (qi * 32 + q) * 16; qb2 = QST + hp * 10240 + 4096 + (qi * 32 + q) * 8;
            obase = (unsigned)(qi * 40960 + hp * 20480 + hg * 2560 + q * 16 + h * 512);
            asm volatile("" : "+v"(qb1), "+v"(qb2), "+v"(obase)); }
        if (part >= 3) {
#pragma unroll
            for (int r = 4 * (part - 3); r < 4 * (part - 2); ++r) mask_b(cd, r, false);
            asm volatile("" : "+v"(cd)); } };
    auto wo_setup = [&](int q) {
        int l = tid; asm volatile("" : "+v"(l)); const int c = q * 64 + (l & 63), rho = __umul24(c, 6554) >> 16;
        oo[q] = (unsigned)(__umul24(qo * 160 + hp * 40 + (rho & ~7) + 4 * (rho & 1) + ((rho >> 1) & 3), 1280) + hg * 160 + (c - rho * 10) * 16);
        asm volatile("" : "+v"(oo[q])); };
    u64 tag;

    if (wid < 4) {
        if (!isA) WAITV(0);
        BAR_L();
        STAMP(1);
        bf16x8 xr[32], xt[2], wf[WIN + 1];
        f32x16 acc[5];
        const int tok = wid * 32 + r32;
        const int xa = XS0 + tok * 128 + ((hl ^ ((tok >> 1) & 7)) << 4);
        int wa0 = WS0 + (8 * hl + ((lane & 15) >> 2)) * 320 + (lane & 16) * 2 + (lane & 3) * 8, wa1 = wa0 + 61440;
        asm volatile("" : "+v"(wa0), "+v"(wa1));
        auto rdx = [&](int i) { return *(LAS bf16x8*)(smem + ((i >> 2) % 3) * 16384 + (xa ^ ((i & 3) * 32))); };
        auto rdw = [&](int f) { const int i = f / 5, t = f % 5, sl = (i >> 1) % 7;
            return trfrag(smem + (sl < 6 ? wa0 + sl * 10240 : wa1) + (i & 1) * 5120 + t * 64, 1280); };
        unsigned fl = 0;
        auto rdflag = [&](int u) { fl = __hip_atomic_load((LAS unsigned*)(smem + C_F + u * 4), RLX_WG); };
        auto check = [&](int u) {
            if (__builtin_amdgcn_readfirstlane(fl) != 15) {
#pragma unroll 1
                for (unsigned spins = 0; spins < LSPIN_CAP; ++spins) { rdflag(u); if (__builtin_amdgcn_readfirstlane(fl) == 15) break; __builtin_amdgcn_s_sleep(1); }
            }
            FENCE(); };
#pragma unroll
        for (int i = 0; i < 4; ++i) xr[i] = rdx(i);
#pragma unroll
        for (int f = 0; f < WIN; ++f) wf[f] = rdw(f);
        SB();
        SFOR(f, 200)
            constexpr int i = f / 5, t = f % 5;
            if constexpr (f % 20 == 10 - WIN) lds_set(smem + C_P + wid * 4, (i >> 1) + 1);
            if constexpr (f == 200 - WIN) lds_set(smem + C_P + wid * 4, 20);
            if constexpr (f % 20 == 10) rdflag(f / 20 + 1);
            if constexpr (f % 20 == 20 - WIN && f < 190) check(f / 20 + 1);
            if constexpr (f + WIN < 200) wf[(f + WIN) % (WIN + 1)] = rdw(f + WIN);
            if constexpr (f % 20 >= 16 && f < 140) xr[(f / 20 + 1) * 4 + f % 20 - 16] = rdx((f / 20 + 1) * 4 + f % 20 - 16);
            if constexpr (t == 1 && i >= 31 && i < 39) xt[(i + 1) & 1] = rdx(i + 1);
            SB();
            if constexpr (i == 0) MFMA32Z(acc[t], wf[f % (WIN + 1)], xr[0]);
            else if constexpr (i < 32) MFMA32I(acc[t], wf[f % (WIN + 1)], xr[i]);
            else MFMA32I(acc[t], wf[f % (WIN + 1)], xt[i & 1]);
            SB();
        SEND
        STAMP(2);
        int l2 = tid; asm volatile("" : "+v"(l2)); l2 &= 63;
        const int h2 = l2 >> 5, tk = wid * 32 + (l2 & 31);
        int eb[5];
        auto ebases = [&](int base) { int l = tid; asm volatile("" : "+v"(l)); const int h = (l >> 5) & 1, k = wid * 32 + (l & 31);
            eb[0] = base + k * 16 + h * 8; eb[1] = base + k * 16 + h * 2040; eb[2] = base + (h ? k * 16 + 1024 : k * 8); eb[3] = base + (h ? k * 8 + 2040 : k * 16);
            asm volatile("" : "+v"(eb[0]), "+v"(eb[1]), "+v"(eb[2]), "+v"(eb[3])); };
        auto kq_addr = [&](int g0) { return eb[g0 % 5 == 1 ? 1 : g0 % 5 == 4 ? 2 : g0 % 5 == 3 ? 3 : 0] + kq_base(g0); };
        ebases(OWN);
        eb[4] = OWN + VOFF + tk * VP + h2 * 8; asm volatile("" : "+v"(eb[4]));
        int qa0 = (8 * h2 + 2 * ((l2 & 15) >> 2)) * 160 + (l2 & 16) * 2 + (l2 & 3) * 8, qa1 = qa0 + WS0 + 10240;
        asm volatile("" : "+v"(qa0), "+v"(qa1));
        auto rdq = [&](int f) { const int i = f / 3, t = f % 3, j = i >> 2, sl = (20 + j) % 7;
            return trfrag(smem + (j >= 7 ? qa0 + (j == 7 ? QU27 : j == 8 ? QU28 : QU29) : sl == 0 ? qa0 + WS0 : qa1 + (sl - 1) * 10240) + (i & 3) * 2560 + t * 64, 160); };
        MFMA_DRAIN();
        WAITV(0);
        { u32x2 pk[20];
#pragma unroll
          for (int n = 0; n < 20; ++n) { const int t = n >> 2, b = n & 3; pk[n] = (u32x2){pk2(acc[t][4 * b], acc[t][4 * b + 1]), pk2(acc[t][4 * b + 2], acc[t][4 * b + 3])}; }
          SB();
#pragma unroll
          for (int n = 0; n < 20; ++n) *(LAS u32x2*)(smem + (n < 10 ? kq_addr(2 * n) : eb[4] + (2 * n - 20) * 8)) = pk[n];
          if (h2 == 0) *(LAS u32x2*)(smem + eb[4] + 160) = (u32x2){0x3F803F80u, 0x3F803F80u}; }
        lds_set(smem + C_E + wid * 4, 1);
        check(10);
        xt[0] = rdx(32);
#pragma unroll
        for (int f = 0; f < WIN; ++f) wf[f] = rdq(f);
        STAMP(3);
        SB();
        u64 pv0 = 0, pv1 = 0;
        SFOR(f, 120)
            constexpr int i = f / 3, t = f % 3, j = i >> 2;
            if constexpr (f % 24 == 12 && j < 9) rdflag(11 + j / 2);
            if constexpr (f % 12 == 12 - WIN && (j == 1 || j == 6 || j == 8)) lds_set(smem + C_P + wid * 4, 21 + j);
            if constexpr (f % 24 == 24 - WIN && j < 9) check(11 + j / 2);
            if constexpr (f + WIN < 120) wf[(f + WIN) % (WIN + 1)] = rdq(f + WIN);
            if constexpr (t == 0 && i < 7) xt[(i + 1) & 1] = rdx(32 + i + 1);
            if constexpr (f >= 90 && f < 100 && !(f & 1)) wo_setup((f - 90) / 2);
            if constexpr (f == 99) tag = rdtag();
            if constexpr (f >= 100 && f < 107) att_setup(f - 100);
            if constexpr (f == 108) ebases(QST);
            if constexpr (f == 116) { pv0 = __hip_atomic_load((LAS u64*)(smem + C_P), RLX_WG); pv1 = __hip_atomic_load((LAS u64*)(smem + C_P + 8), RLX_WG); }
            SB();
            if constexpr (i == 0) MFMA32Z(acc[t], wf[f % (WIN + 1)], xt[0]);
            else if constexpr (i < 8) MFMA32I(acc[t], wf[f % (WIN + 1)], xt[i & 1]);
            else MFMA32I(acc[t], wf[f % (WIN + 1)], xr[i - 8]);
            SB();
        SEND
        STAMP(4);
        if (__builtin_amdgcn_readfirstlane((int)min(min((unsigned)pv0, (unsigned)(pv0 >> 32)), min((unsigned)pv1, (unsigned)(pv1 >> 32)))) < 29) wait_min4(smem + C_P, 29);
        MFMA_DRAIN();
        { acc[0] *= QSCALE; acc[1] *= QSCALE; acc[2] *= QSCALE;
          SB();
          u32x2 pk[10];
#pragma unroll
          for (int n = 0; n < 10; ++n) { const int t = n >> 2, b = n & 3; pk[n] = (u32x2){pk2(acc[t][4 * b], acc[t][4 * b + 1]), pk2(acc[t][4 * b + 2], acc[t][4 * b + 3])}; }
          SB();
#pragma unroll
          for (int n = 0; n < 10; ++n) *(LAS u32x2*)(smem + kq_addr(2 * n)) = pk[n]; }
        lds_set(smem + C_QF + wid * 4, 1);
        __builtin_amdgcn_s_setprio(1);
        STAMP(5);
    } else {
        const int hw = wid - 4;
        int known = 0;
        __builtin_amdgcn_s_setprio(3);
        auto lds_or = [&](int off, unsigned v) { FENCE(); if (lane == 0) __hip_atomic_fetch_or((LAS unsigned*)(smem + off), v, RLX_WG); FENCE(); };
        unsigned wo[5], qo[5];
        auto done = [&](int n) { lds_or(C_F + n * 4, 1u << hw); };
        const unsigned xo1 = (xo ^ 64) + 9216, xo2 = xo + 2 * 9216, xo3 = (xo ^ 64) + 3 * 9216;
        auto round_x = [&](int n) { dma16x4(xo, xo1, xo2, xo3, xb + kt_of(n) * 128 + hw * (32 * 1280), lds0 + XS0 + (n % 3) * 16384 + hw * 4096); };
        auto round_w = [&](int n) {
            const int u = 2 * n + (hw >> 1);
            const unsigned dst = lds0 + (u < 27 ? WS0 + (u % 7) * 10240 : u == 27 ? QU27 : u == 28 ? QU28 : QU29) + (hw & 1) * 5120 + 2048;
            if (u < 20) dma16x5(wo, wb + (size_t)(kt_of(u >> 1) * 64 + (u & 1) * 32) * 3840 - 2048, dst);
            else dma16x5(qo, wb + (size_t)kt_of(u < 22 ? u - 12 : u - 22) * (64 * 3840) - 2048, dst); };
        if (isA) round_x(0);
        round_x(1);
#pragma unroll
        for (int q = 0; q < 5; ++q) wo[q] = w_off(q) + (hw & 1) * (16 * 3840) + (4 - q) * 1024;
        if (isA) { round_w(0); round_w(1); WAITV(5); } else { round_w(1); WAITV(9); }
        BAR_L();
#pragma unroll
        for (int q = 0; q < 5; ++q) { const int c = q * 64 + lane, rho = __umul24(c, 6554) >> 16;
            qo[q] = (unsigned)(__umul24((hw & 1) * 32 + (rho & ~7) + 4 * (rho & 1) + ((rho >> 1) & 3), 3840) + (c - rho * 10) * 16 + (4 - q) * 1024); }
#pragma unroll 1
        for (int n = 2; n <= 13; ++n) {
            const int need = n < 13 ? 2 * n - 5 : 20;
            if (need > known) known = min4(smem + C_P);
            if (need > known) { WAITV(0); done(n - 1); known = wait_min4(smem + C_P, need); }
            if (n < 10) round_x(n);
            round_w(n);
            if (n < 10) WAITV(9); else WAITV(5);
            done(n - 1);
        }
        WAITV(0); done(13);
        STAMP(1);
        tag = rdtag();
        auto a_chunk = [&](int a) { int o;
            if (a < 320) { const int hd = a / 80, rem = a % 80; o = hd * 5120 + (rem < 32 ? (96 + rem) * 16 : rem < 64 ? 2048 + (64 + rem) * 16 : 4096 + 768 + (rem - 64) * 16); }
            else o = VOFF + 96 * VP + (a - 320) * 16;
            return o; };
        if (rt < 15) {
            const __amdgpu_buffer_rsrc_t kvr = __builtin_amdgcn_make_buffer_rsrc(ws + WS_KV + me * KVG, 0, KVB, 0x00020000);
            wait_min4(smem + C_E, 1);
            STAMP(2);
            if (!isA) { u32x4 v[11];
#pragma unroll
                for (int j = 0; j < 11; ++j) if (j < 10 || hw == 3) v[j] = *(LAS u32x4*)(smem + OWN + hw * 10240 + j * 1024 + lane * 16);
#pragma unroll
                for (int j = 0; j < 11; ++j) if (j < 10 || hw == 3) __builtin_amdgcn_raw_buffer_store_b128(v[j], kvr, hw * 10240 + j * 1024 + lane * 16, 0, 16);
            } else {
#pragma unroll
                for (int j = 0; j < 3; ++j) { const int a = (4 * j + hw) * 64 + lane;
                    if (a < 656) { const int o = a_chunk(a); __builtin_amdgcn_raw_buffer_store_b128(*(LAS u32x4*)(smem + OWN + o), kvr, o, 0, 16); } }
            }
            STAMP(3);
        }
        wait_min4(smem + C_P, 22);
        round_w(14);
        WAITV(5);
        if (rt < 15) {
            unsigned old = 0;
            if (lane == 0) old = __hip_atomic_fetch_add((LAS unsigned*)(smem + N_PUB), 1u, RLX_WG);
            if (lane == 0 && old == 3) __hip_atomic_store((GAS u64*)(ws + WS_FKV + me * 128), tag, RLX_AGENT);
            STAMP(4);
        }
        WAITV(0); done(14);
        __builtin_amdgcn_s_setprio(0);
#pragma unroll
        for (int p = 0; p < 7; ++p) att_setup(p);
#pragma unroll
        for (int q = 0; q < 5; ++q) wo_setup(q);
        __builtin_amdgcn_s_setprio(3);
        for (int i = 0; i < hw; ++i) __builtin_amdgcn_s_sleep(PSTAG);
        poll_shared((GAS u64*)(ws + WS_FKV + (me - 8 * (lane + 1)) * 128), lane < nnb, tag, smem + N_KV);
        STAMP(5);
        if (!isA) {
            const int n = hw >> 1, half = hw & 1;
            if (n < nnb) {
                const char* src = ws + WS_KV + (me - 8 * (n + 1)) * KVG + half * 20480; const unsigned dst = lds0 + KV0 + (1 - n) * KVB + half * 20480;
                wait_min4(smem + C_P, 27);
#pragma unroll
                for (int j = 0; j < 4; ++j) dma16x5_sc1(lane * 16, src + j * 5120 + 2048, dst + j * 5120 + 2048);
                if (half) dma16_sc1(lane * 16, src + 20480, dst + 20480);
            }
        } else if (nnb) {
            const __amdgpu_buffer_rsrc_t nbr = __builtin_amdgcn_make_buffer_rsrc(ws + WS_KV + (me - 8) * KVG, 0, KVB, 0x00020000);
            u32x4 v[3];
#pragma unroll
            for (int j = 0; j < 3; ++j) { const int a = (4 * j + hw) * 64 + lane; if (a < 656) v[j] = __builtin_amdgcn_raw_buffer_load_b128(nbr, a_chunk(a), 0, 16); }
            wait_min4(smem + C_P, 27);
#pragma unroll
            for (int j = 0; j < 3; ++j) { const int a = (4 * j + hw) * 64 + lane; if (a < 656) *(LAS u32x4*)(smem + KV0 + KVB + a_chunk(a)) = v[j]; }
        }
        __builtin_amdgcn_s_setprio(2);
        wait_word(smem + C_QF + qi * 4, 1);
        STAMP(6);
    }
    u32x4 wreg[13];
    auto ldwo = [&](int i) { wreg[i] = __builtin_amdgcn_raw_buffer_load_b128(wor, oo[i % 5] + (i / 5) * (32 * 1280) + (i > 6 || (i == 6 && lane >= 16) ? 40 * 1280 : 0), 0, 0); };
    STAMP(7);
    u32x2 ob[2][3];
    {
        bf16x8 q1[2]; u32x2 q2[2];
#pragma unroll
        for (int e = 0; e < 2; ++e) {
            q1[e] = *(LAS bf16x8*)(smem + qb1 + e * 5120);
            q2[e] = *(LAS u32x2*)(smem + qb2 + e * 5120);
            if (hl) q2[e] = (u32x2){0u, 0u};
        }
        if (isA) mask_a(0);
        struct KF { bf16x8 a; s16x4 b; };
        struct VF { bf16x8 a, b; };
        auto rdk = [&](int u, int e) { const int so = (u >> 2) * KVB, tw = u & 3; KF k;
            k.a = *(LAS bf16x8*)(smem + kb1 + so + tw * 512 + e * 5120); k.b = *(LAS s16x4*)(smem + __umul24(so + tw * 256 + e * 5120, nh) + kb2); return k; };
        auto rdv = [&](int u, int e) { LAS char* p = smem + (e ? vb1 : vb0) + (u >> 2) * KVB + (u & 3) * (32 * VP); VF v;
            v.a = trfrag(p, 8 * VP); v.b = trfrag(p + 16 * VP, 8 * VP); return v; };
        auto qk = [&](const KF& k, int e, const f32x16& c) { return MFMA32K8(k.b, __builtin_bit_cast(s16x4, q2[e]), MFMA32(k.a, q1[e], c)); };
        const f32x16 zero = {};
        auto max16 = [&](const f32x16& s, float m) {
#pragma unroll
            for (int r = 0; r < 16; ++r) m = __builtin_elementwise_maximum(m, s[r]);
            return m; };
        auto set_ref = [&](float m, int e) { const unsigned nb = pk2(-m, 0.f); if (hl) q2[e][0] = nb; };
        auto ex4 = [&](f32x16& s, int b) {
#pragma unroll
            for (int r = 0; r < 4; ++r) s[b + r] = __builtin_amdgcn_exp2f(s[b + r]); };
        auto pk8 = [&](const f32x16& s, int b) { return __builtin_bit_cast(bf16x8, (u32x4){pk2(s[b], s[b + 1]), pk2(s[b + 2], s[b + 3]), pk2(s[b + 4], s[b + 5]), pk2(s[b + 6], s[b + 7])}); };
        auto phase = [&](auto nx, f32x16& cur, const VF& v, f32x16& o, f32x16& nxt, const KF& k, int e) __attribute__((always_inline)) {
            constexpr int NX = decltype(nx)::value;
            SB(); ex4(cur, 0); SB();
            if constexpr (NX) nxt = MFMA32(k.a, q1[e], NX == 1 ? zero : NX == 2 ? cf : cd);
            SB(); ex4(cur, 4); { const bf16x8 p = pk8(cur, 0); SB(); o = MFMA32(v.a, p, o); }
            SB(); ex4(cur, 8); SB();
            if constexpr (NX) nxt = MFMA32K8(k.b, __builtin_bit_cast(s16x4, q2[e]), nxt);
            SB(); ex4(cur, 12); { const bf16x8 p = pk8(cur, 8); SB(); o = MFMA32(v.b, p, o); }
            SB(); };
        f32x16 o0, o1, sa, sb; KF kx; VF vx;
        auto step = [&](auto m0, auto m1, int u) __attribute__((always_inline)) {
            constexpr int M1 = decltype(m1)::value;
            KF ky; if constexpr (M1) ky = rdk(u - 1, 0);
            const VF vy = rdv(u, 1);
            phase(m0, sa, vx, o0, sb, kx, 1);
            if constexpr (M1) { kx = rdk(u - 1, 1); vx = rdv(u - 1, 0); }
            phase(m1, sb, vy, o1, sa, ky, 0); };
        auto nbwait = [&](int u) { if (u == 8) wait_word(smem + N_NB, 4); };
        { const KF k0f = rdk(ud, 0); kx = rdk(ud, 1); vx = rdv(ud, 0);
          const f32x16 s0 = qk(k0f, 0, cd), s1 = qk(kx, 1, cd);
          set_ref(halfmax(max16(s0, s0[0])), 0); set_ref(halfmax(max16(s1, s1[0])), 1);
          sa = qk(k0f, 0, cd); }
        if (hp) { WAITV(0); lds_inc0(smem + N_NB, lane); lds_inc0(smem + N_G0, lane); }
        o0 = zero; o1 = zero;
        if (u0 + 8 == ud) {
            SFOR(it, 9)
                if constexpr (it < 7) { ldwo(2 * it); if constexpr (it < 6) ldwo(2 * it + 1); }
                if constexpr (it == PFLIP) { if (hp) __builtin_amdgcn_s_setprio(0); }
                if constexpr (it == 7) {
#pragma unroll
                    for (int r = 0; r < 16; ++r) mask_b(cf, r, true); }
                if constexpr (it < 4) nbwait(ud - it);
                step(IC<it == 0 ? 3 : it == 8 ? 2 : 1>{}, IC<it == 8 ? 0 : it == 7 ? 2 : 1>{}, ud - it);
            SEND
        } else {
            __builtin_amdgcn_s_setprio(0);
#pragma unroll
            for (int i = 0; i < 13; ++i) ldwo(i);
            if (!isA) cf = zero;
            if (u0 < ud) {
                nbwait(ud);
                if (u0 + 1 < ud) {
                    step(IC<3>{}, IC<1>{}, ud);
#pragma unroll 1
                    for (int u = ud - 1; u > u0 + 1; --u) { nbwait(u); step(IC<1>{}, IC<1>{}, u); }
                    nbwait(u0 + 1);
                    step(IC<1>{}, IC<2>{}, u0 + 1);
                } else step(IC<3>{}, IC<2>{}, ud);
                step(IC<2>{}, IC<0>{}, u0);
            } else step(IC<3>{}, IC<0>{}, ud);
        }
        if (!__all(o0[12] < 1.152921504606847e18f && o1[12] < 1.152921504606847e18f)) {
            if (isA) { int z = 0; asm volatile("" : "+s"(z)); mask_a(z); }
            else { asm volatile("" : "+v"(dsh));
#pragma unroll
                for (int r = 0; r < 16; ++r) { mask_b(cd, r, false); mask_b(cf, r, true); }
                if (!far) cf = zero; }
#pragma unroll
            for (int e = 0; e < 2; ++e) {
                if (hl) q2[e][0] = 0u;
                const f32x16 s = qk(rdk(ud, e), e, cd);
                float m = max16(s, s[0]);
                if (u0 < ud) m = max16(qk(rdk(u0, e), e, cf), m);
#pragma unroll 1
                for (int u = u0 + 1; u < ud; ++u) m = max16(qk(rdk(u, e), e, zero), m);
                set_ref(halfmax(m), e);
            }
#pragma unroll
            for (int e = 0; e < 2; ++e) {
                f32x16 o = zero;
#pragma unroll 1
                for (int u = u0; u <= ud; ++u) { f32x16 t = u == ud ? qk(rdk(u, e), e, cd) : u == u0 ? qk(rdk(u, e), e, cf) : qk(rdk(u, e), e, zero);
                    const VF v = rdv(u, e); ex4(t, 0); ex4(t, 4); ex4(t, 8); ex4(t, 12);
                    o = MFMA32(v.a, pk8(t, 0), o); o = MFMA32(v.b, pk8(t, 8), o); }
                if (e) o1 = o; else o0 = o;
            }
        }
        const float rl0 = __builtin_amdgcn_rcpf(o0[12]), rl1 = __builtin_amdgcn_rcpf(o1[12]);
        SB();
#pragma unroll
        for (int r = 0; r < 12; ++r) { o0[r] *= rl0; o1[r] *= rl1; }
        SB();
#pragma unroll
        for (int j = 0; j < 3; ++j) {
            ob[0][j] = (u32x2){pk2(o0[4 * j], o0[4 * j + 1]), pk2(o0[4 * j + 2], o0[4 * j + 3])};
            ob[1][j] = (u32x2){pk2(o1[4 * j], o1[4 * j + 1]), pk2(o1[4 * j + 2], o1[4 * j + 3])}; }
        SB();
    }
    STAMP(8);
    const __amdgpu_buffer_rsrc_t orr = __builtin_amdgcn_make_buffer_rsrc(ws + WS_O + (br * 16 + rt) * OG, 0, 4 * 40960, 0x00020000);
    {
      { auto rx = __builtin_amdgcn_permlane32_swap(ob[0][0][0], ob[0][1][0], false, false);
        auto ry = __builtin_amdgcn_permlane32_swap(ob[0][0][1], ob[0][1][1], false, false);
        __builtin_amdgcn_raw_buffer_store_b128((u32x4){rx[0], ry[0], rx[1], ry[1]}, orr, obase, 0, 16); }
      if (hl == 0) __builtin_amdgcn_raw_buffer_store_b128((u32x4){ob[0][2][0], ob[0][2][1], ob[1][0][0], ob[1][0][1]}, orr, obase + 1024, 0, 16);
      { auto fx = __builtin_amdgcn_permlane32_swap(ob[1][0][0], ob[1][1][0], false, false);
        auto fy = __builtin_amdgcn_permlane32_swap(ob[1][0][1], ob[1][1][1], false, false);
        auto sx = __builtin_amdgcn_permlane32_swap(ob[1][1][0], ob[1][2][0], false, false);
        auto sy = __builtin_amdgcn_permlane32_swap(ob[1][1][1], ob[1][2][1], false, false);
        __builtin_amdgcn_raw_buffer_store_b128((u32x4){fx[1], fy[1], sx[0], sy[0]}, orr, obase + 1536, 0, 16); } }
    if (lane == 0) { (void)__hip_atomic_fetch_add((LAS unsigned*)(smem + N_G1), 1u, RLX_WG);
        if (!hp) (void)__hip_atomic_fetch_add((LAS unsigned*)(smem + N_G0), 1u, RLX_WG); }
    FENCE();
    STAMP(6);
    auto signal_o = [&]() {
        if (lds_inc(smem + N_OS + hp * 4, lane) == 3 && lane == 0) __hip_atomic_store((GAS u64*)(ws + WS_FO + ((me >> 3) * 2 + hp) * 128 + hg * 8), tag, RLX_AGENT); };
    __builtin_amdgcn_s_setprio(0);
    WAITV(0); signal_o();
    STAMP(9);
    wait_word(smem + (hp ? N_G1 : N_G0), 8);
    const int p1 = hp ? 30720 : 35840, p2 = hp ? 65536 : 43520;
    const int wb0 = hp ? KV0 + 10240 : 512, wb1 = hp ? KV0 + KVB + 10240 - 30720 : KV0 - 35840, wb2 = KV0 + KVB - 43520;
#pragma unroll
    for (int i = 0; i < 13; ++i) { const int L = qo * 12800 + i * 1024;
        if (i < 12 || lane < 32) *(LAS u32x4*)(smem + (L + (L < p1 ? wb0 : L < p2 ? wb1 : wb2)) + lane * 16) = wreg[i]; }
    lds_inc0(smem + N_WO + hp * 4, lane);
    STAMP(10);
    __builtin_amdgcn_s_sleep(PDELAY);
    for (int i = 0; i < qo; ++i) __builtin_amdgcn_s_sleep(PSTAG);
    poll_shared((GAS u64*)(ws + WS_FO + ((me >> 3) * 2 + hp) * 128 + (lane & 7) * 8), lane < 8, tag, smem + N_OK + hp * 4);
    STAMP(11);
    f32x16 oc[3]; f32x4 ps[10];
    LAS char* red = smem + OWN + qo * 10240 + lane * 16;
    { bf16x8 af[5], of[20];
      const int la = (8 * hl + 2 * ((lane & 15) >> 2)) * 160 + (lane & 16) * 2 + (lane & 3) * 8;
      const int aa0 = wb0 + la, aa1 = (hp ? wb1 : wb0) + la, aa2 = wb1 + la, aa3 = (hp ? wb1 : wb2) + la;
      auto rda = [&](int f) { return trfrag(smem + (f < 36 ? aa0 : f < 42 ? aa1 : f < 51 ? aa2 : aa3) + (f / 3) * 2560 + (f % 3) * 64, 160); };
      auto ldo = [&](int k) { of[k] = __builtin_bit_cast(bf16x8, __builtin_amdgcn_raw_buffer_load_b128(orr, qo * 40960 + (hp * 20 + k) * 1024 + lane * 16, 0, 16)); };
#pragma unroll
      for (int k = 0; k < OPF; ++k) ldo(k);
      wait_word(smem + N_WO + hp * 4, 4);
#pragma unroll
      for (int f = 0; f < 4; ++f) af[f] = rda(f);
      SB();
      SFOR(f, 60)
          if constexpr (f % 3 == 0 && f / 3 + OPF < 20) ldo(f / 3 + OPF);
          if constexpr (f + 4 < 60) af[(f + 4) % 5] = rda(f + 4);
          if constexpr (f == 57) { if (hp) { wait_word(smem + C_RD + qo * 4, 1);
#pragma unroll
              for (int b = 0; b < 10; ++b) ps[b] = *(LAS f32x4*)(red + b * 1024); } }
          SB();
          if constexpr (f < 3) MFMA32Z(oc[f], af[f % 5], of[0]); else MFMA32I(oc[f % 3], af[f % 5], of[f / 3]);
          SB();
      SEND }
    MFMA_DRAIN();
    STAMP(12);
    { if (!hp) {
          wait_word(smem + N_G1, 8);
#pragma unroll
          for (int b = 0; b < 10; ++b) *(LAS f32x4*)(red + b * 1024) = (f32x4){oc[b >> 2][4 * (b & 3)], oc[b >> 2][4 * (b & 3) + 1], oc[b >> 2][4 * (b & 3) + 2], oc[b >> 2][4 * (b & 3) + 3]};
          lds_set(smem + C_RD + qo * 4, 1);
      } else {
          char* orow = out + (size_t)(rt * 128 + qo * 32 + r32) * 2560 + br * 1280 + hg * 160 + hl * 16;
#pragma unroll
          for (int b = 0; b < 10; ++b)
#pragma unroll
              for (int r = 0; r < 4; ++r) ps[b][r] += oc[b >> 2][4 * (b & 3) + r];
          SB();
          u32x2 pk[10];
#pragma unroll
          for (int b = 0; b < 10; ++b) pk[b] = (u32x2){pk2(ps[b][0], ps[b][1]), pk2(ps[b][2], ps[b][3])};
          SB();
          u32x4 st[5];
#pragma unroll
          for (int b = 0; b < 10; b += 2) {
              auto rx = __builtin_amdgcn_permlane32_swap(pk[b][0], pk[b + 1][0], false, false); auto ry = __builtin_amdgcn_permlane32_swap(pk[b][1], pk[b + 1][1], false, false);
              st[b >> 1] = (u32x4){rx[0], ry[0], rx[1], ry[1]}; }
          SB();
#pragma unroll
          for (int b = 0; b < 5; ++b) *(u32x4*)(orow + b * 32) = st[b];
      } }
    if (bx == 0 && wid == 0) {
#pragma unroll 1
        for (int i = 0; i < 4; ++i) poll_flags((GAS u64*)(ws + WS_FO + (i * 8 + (lane >> 3)) * 256 + (lane & 7) * 8), true, tag);
        if (lane == 0) __hip_atomic_store((GAS u64*)ws, tag, RLX_AGENT);
    }
}

extern "C" void kernel_launch(void* const* d_in, const int* in_sizes, int n_in, void* d_out, int out_size, void* d_ws, size_t ws_size,
                              hipStream_t stream) {
    fused_attn_kernel<<<256, 512, 0, stream>>>((const char*)d_in[0], (const char*)d_in[1], (const char*)d_in[2], (const char*)d_in[3],
                                               (const char*)d_in[4], (const char*)d_in[5], (char*)d_out, (char*)d_ws);
}
```

```cpp
#define ADELAY 0
#define DIAG 0
#include <hip/hip_runtime.h>
#include <hip/hip_bf16.h>
#include <stdint.h>

typedef short bf16x8 __attribute__((ext_vector_type(8)));
typedef short s16x4 __attribute__((ext_vector_type(4)));
typedef float f32x2 __attribute__((ext_vector_type(2)));
typedef float f32x4 __attribute__((ext_vector_type(4)));
typedef float f32x16 __attribute__((ext_vector_type(16)));
typedef unsigned u32x4 __attribute__((ext_vector_type(4)));
typedef unsigned u32x2 __attribute__((ext_vector_type(2)));
typedef __bf16 bfx2 __attribute__((ext_vector_type(2)));
typedef unsigned long long u64;
#define LAS __attribute__((address_space(3)))
#define GAS __attribute__((address_space(1)))
#define DI __device__ __forceinline__
#define RLX_AGENT __ATOMIC_RELAXED, __HIP_MEMORY_SCOPE_AGENT
#define RLX_WG __ATOMIC_RELAXED, __HIP_MEMORY_SCOPE_WORKGROUP

constexpr int LDS_BYTES = 163840;
constexpr int VOFF = 20480, VP = 168;
constexpr int KVB = VOFF + 128 * VP;
constexpr int KV0 = LDS_BYTES - 3 * KVB;
constexpr int OWN = KV0 + 2 * KVB;
constexpr int C_F = 0, C_P = 128, C_E = 144, C_QF = 160, C_RD = 176, C_CNT = 192, C_TAG = 320;
constexpr int N_PUB = C_CNT, N_NB = C_CNT + 4, N_G0 = C_CNT + 8, N_G1 = C_CNT + 12, N_WO = C_CNT + 16, N_OS = C_CNT + 24, N_KV = C_CNT + 32, N_OK = C_CNT + 36;
constexpr int XS0 = 512, WS0 = XS0 + 3 * 16384, QST = 512, ONE = 336, QU27 = XS0 + 16384, QU28 = XS0, QU29 = QU27 + 10240;
static_assert(WS0 + 7 * 10240 <= OWN, "ring overlaps the own image");
constexpr unsigned WS_FKV = 4096, WS_FO = WS_FKV + 256 * 128, WS_KV = 131072;
constexpr unsigned KVG = KVB, OG = 4 * 40960;
constexpr unsigned WS_O = WS_KV + 256 * KVG;
constexpr unsigned WS_END = WS_O + 32 * OG;
constexpr unsigned WS_DBG = WS_END;
constexpr unsigned SPIN_CAP = 400000, LSPIN_CAP = 4000000;
constexpr float QSCALE = 0.22360679774997896f * 1.4426950408889634f;
#define PSLEEP 2
#define PDELAY 1
#define PSTAG 3
#define PFLIP 6
#define OPF 12
#define ABL4N 2
#define WIN 4

#define MFMA32(a, b, c) __builtin_amdgcn_mfma_f32_32x32x16_bf16((a), (b), (c), 0, 0, 0)
#define MFMA32K8(a, b, c) __builtin_amdgcn_mfma_f32_32x32x8bf16_1k((a), (b), (c), 0, 0, 0)
#define MFMA32I(acc, a, b) asm volatile("v_mfma_f32_32x32x16_bf16 %0, %1, %2, %0" : "+v"(acc) : "v"(a), "v"(b))
#define MFMA32Z(acc, a, b) asm volatile("v_mfma_f32_32x32x16_bf16 %0, %1, %2, 0" : "=&v"(acc) : "v"(a), "v"(b))
#define MFMA_DRAIN() asm volatile("s_nop 7\n\ts_nop 4" ::: "memory")
#define WAITV(N) asm volatile("s_waitcnt vmcnt(" #N ")" ::: "memory")
#define WAITL() asm volatile("s_waitcnt lgkmcnt(0)" ::: "memory")
#define BAR_L() asm volatile("s_waitcnt lgkmcnt(0)\n\ts_barrier" ::: "memory")
#define SB() __builtin_amdgcn_sched_barrier(0)
#define FENCE() asm volatile("" ::: "memory")

#define STAMP(i) do { if ((i) == 0 || (i) == 1 || (i) == 2 || (i) == 7 || (i) == 8) { SB(); dst_[i] = (unsigned)__builtin_amdgcn_s_memrealtime(); SB(); } } while (0)

template <int V> struct IC { static constexpr int value = V; };
template <int I, int N, class F> DI void static_for(F&& f) { if constexpr (I < N) { f(IC<I>{}); static_for<I + 1, N>(f); } }
#define SFOR(var, N) static_for<0, N>([&](auto ic_##var) __attribute__((always_inline)) { constexpr int var = decltype(ic_##var)::value;
#define SEND });

DI void dma16(unsigned voff, const char* sbase, unsigned ldsaddr) {
    asm volatile("s_mov_b32 m0, %2\n\ts_nop 0\n\tglobal_load_lds_dwordx4 %0, %1" :: "v"(voff), "s"(sbase), "s"(ldsaddr) : "memory");
}
DI void dma16x4(unsigned v0, unsigned v1, unsigned v2, unsigned v3, const char* sbase, unsigned ldsaddr) {
    asm volatile("s_mov_b32 m0, %5\n\ts_nop 0\n\tglobal_load_lds_dwordx4 %0, %4\n\tglobal_load_lds_dwordx4 %1, %4 offset:1024\n\t"
                 "global_load_lds_dwordx4 %2, %4 offset:2048\n\tglobal_load_lds_dwordx4 %3, %4 offset:3072"
                 :: "v"(v0), "v"(v1), "v"(v2), "v"(v3), "s"(sbase), "s"(ldsaddr) : "memory");
}
DI void dma16x5(const unsigned (&v)[5], const char* sbase, unsigned ldsaddr) {
    asm volatile("s_mov_b32 m0, %6\n\ts_nop 0\n\tglobal_load_lds_dwordx4 %0, %5 offset:-2048\n\tglobal_load_lds_dwordx4 %1, %5 offset:-1024\n\t"
                 "global_load_lds_dwordx4 %2, %5\n\tglobal_load_lds_dwordx4 %3, %5 offset:1024\n\tglobal_load_lds_dwordx4 %4, %5 offset:2048"
                 :: "v"(v[0]), "v"(v[1]), "v"(v[2]), "v"(v[3]), "v"(v[4]), "s"(sbase), "s"(ldsaddr) : "memory");
}
DI void dma16x5_sc1(unsigned voff, const char* sbase, unsigned ldsaddr) {
    asm volatile("s_mov_b32 m0, %2\n\ts_nop 0\n\tglobal_load_lds_dwordx4 %0, %1 offset:-2048 sc1\n\tglobal_load_lds_dwordx4 %0, %1 offset:-1024 sc1\n\t"
                 "global_load_lds_dwordx4 %0, %1 sc1\n\tglobal_load_lds_dwordx4 %0, %1 offset:1024 sc1\n\tglobal_load_lds_dwordx4 %0, %1 offset:2048 sc1"
                 :: "v"(voff), "s"(sbase), "s"(ldsaddr) : "memory");
}
DI void dma16_sc1(unsigned voff, const char* sbase, unsigned ldsaddr) {
    asm volatile("s_mov_b32 m0, %2\n\ts_nop 0\n\tglobal_load_lds_dwordx4 %0, %1 sc1" :: "v"(voff), "s"(sbase), "s"(ldsaddr) : "memory");
}
DI void dma4(const char* p, unsigned ldsaddr) {
    asm volatile("s_mov_b32 m0, %1\n\ts_nop 0\n\tglobal_load_lds_dword %0, off" :: "v"(p), "s"(ldsaddr) : "memory");
}
DI unsigned pk2(float lo, float hi) { f32x2 v = {lo, hi}; return __builtin_bit_cast(unsigned, __builtin_convertvector(v, bfx2)); }
typedef short v4i16_t __attribute__((ext_vector_type(4)));
DI bf16x8 trfrag(LAS char* p, int second) {
    s16x4 a = __builtin_bit_cast(s16x4, __builtin_amdgcn_ds_read_tr16_b64_v4i16((LAS v4i16_t*)p));
    s16x4 b = __builtin_bit_cast(s16x4, __builtin_amdgcn_ds_read_tr16_b64_v4i16((LAS v4i16_t*)(p + second)));
    return __builtin_shufflevector(a, b, 0, 1, 2, 3, 4, 5, 6, 7);
}
constexpr u64 lanes_cut(int r, bool fartile) {
    u64 m = 0;
    for (int l = 0; l < 64; ++l) { const int key = (r & 3) + 8 * (r >> 2) + 4 * (l >> 5), d = (l & 31) - key + (fartile ? 32 : 0);
        const bool ok = d >= 0 && d <= 14 && !(d & 1);
        if (!ok) m |= 1ull << l; }
    return m;
}
DI float halfmax(float m) { auto rr = __builtin_amdgcn_permlane32_swap(__float_as_uint(m), __float_as_uint(m), false, false);
    return __builtin_elementwise_maximum(__uint_as_float(rr[0]), __uint_as_float(rr[1])); }

DI void poll_flags(GAS u64* word, bool active, u64 tag) {
#pragma unroll 1
    for (unsigned spins = 0; spins < SPIN_CAP; ++spins) {
        u64 v = tag;
        if (active) v = __hip_atomic_load(word, RLX_AGENT);
        if (__all(v == tag)) break;
        __builtin_amdgcn_s_sleep(PSLEEP);
    }
    __builtin_amdgcn_fence(__ATOMIC_ACQUIRE, "wavefront");
}
DI void poll_shared(GAS u64* word, bool active, u64 tag, LAS char* ok) {
#pragma unroll 1
    for (unsigned spins = 0; spins < SPIN_CAP; ++spins) {
        if (__builtin_amdgcn_readfirstlane((int)__hip_atomic_load((LAS unsigned*)ok, RLX_WG))) break;
        u64 v = tag;
        if (active) v = __hip_atomic_load(word, RLX_AGENT);
        if (__all(v == tag)) { __hip_atomic_store((LAS unsigned*)ok, 1u, RLX_WG); break; }
        __builtin_amdgcn_s_sleep(PSLEEP);
    }
    __builtin_amdgcn_fence(__ATOMIC_ACQUIRE, "wavefront");
    asm volatile("" ::: "memory");
}
DI int min4(LAS char* p) {
    const u64 a = __hip_atomic_load((LAS u64*)p, RLX_WG), b = __hip_atomic_load((LAS u64*)(p + 8), RLX_WG);
    const unsigned m = min(min((unsigned)a, (unsigned)(a >> 32)), min((unsigned)b, (unsigned)(b >> 32)));
    return __builtin_amdgcn_readfirstlane((int)m);
}
DI int wait_min4(LAS char* p, int need) {
    int v = min4(p);
#pragma unroll 1
    for (unsigned spins = 0; v < need && spins < LSPIN_CAP; ++spins) { __builtin_amdgcn_s_sleep(1); v = min4(p); }
    FENCE();
    return v;
}
DI void wait_word(LAS char* p, unsigned need) {
#pragma unroll 1
    for (unsigned spins = 0; spins < LSPIN_CAP; ++spins) {
        if ((unsigned)__builtin_amdgcn_readfirstlane((int)__hip_atomic_load((LAS unsigned*)p, RLX_WG)) >= need) break;
        __builtin_amdgcn_s_sleep(1); }
    FENCE();
}
DI void lds_inc0(LAS char* p, int lane) { FENCE(); if (lane == 0) (void)__hip_atomic_fetch_add((LAS unsigned*)p, 1u, RLX_WG); FENCE(); }
DI void wait_pair(LAS char* p, unsigned need) {
#pragma unroll 1
    for (unsigned spins = 0; spins < LSPIN_CAP; ++spins) { const u64 v = __hip_atomic_load((LAS u64*)p, RLX_WG);
        if ((unsigned)__builtin_amdgcn_readfirstlane((int)min((unsigned)v, (unsigned)(v >> 32))) >= need) break;
        __builtin_amdgcn_s_sleep(1); }
    FENCE();
}
DI unsigned lds_inc(LAS char* p, int lane) { FENCE(); unsigned old = 0; if (lane == 0) old = __hip_atomic_fetch_add((LAS unsigned*)p, 1u, RLX_WG); FENCE(); return old; }
DI void lds_set(LAS char* p, unsigned v) { FENCE(); __hip_atomic_store((LAS unsigned*)p, v, RLX_WG); FENCE(); }

__global__ __launch_bounds__(512, 2) void fused_attn_kernel(const char* __restrict__ xA, const char* __restrict__ wA, const char* __restrict__ oA,
                                                           const char* __restrict__ xB, const char* __restrict__ wB, const char* __restrict__ oB,
                                                           char* __restrict__ out, char* __restrict__ ws) {
    __shared__ __attribute__((aligned(16))) char smem_[LDS_BYTES];
    LAS char* smem = (LAS char*)smem_;
    const unsigned lds0 = (unsigned)(uintptr_t)smem;
    unsigned dst_[9];
    STAMP(0);
    asm volatile("" :: "s"(xA), "s"(wA), "s"(oA), "s"(xB), "s"(wB), "s"(oB), "s"(out), "s"(ws));
    const int tid = threadIdx.x, lane = tid & 63;
    const int wid = __builtin_amdgcn_readfirstlane(tid >> 6);
    const int bx = blockIdx.x, xcd = bx & 7, idx = bx >> 3;
    const int isA = idx >> 4, rt = (xcd >> 1) * 4 + ((idx >> 2) & 3), hg = (xcd & 1) * 4 + (idx & 3);
    const int br = isA ? 0 : 1;
    const int k0 = isA ? (((rt & 3) * 5 + 1) / 2 + ((hg & 3) * 5 + 1) / 2) % 10 : 0;
    const char* xb = (isA ? xA : xB) + (size_t)rt * (128 * 1280);
    const char* wb = (isA ? wA : wB) + hg * 160;
    const int me = (br * 16 + rt) * 8 + hg;
    const int r32 = lane & 31, hl = lane >> 5;
    auto kt_of = [&](int s) { int k = s + k0; return k >= 10 ? k - 10 : k; };
    if (isA && wid < 4) {
        const int j16 = idx & 15; const unsigned dump = lds0 + OWN + wid * 512;
        const char* wl = wB + (hg >> 2) * 640 + j16 * (4 * 3840);
        const char* xl = xB + (size_t)(rt * 128 + (hg & 3) * 32 + r32) * 1280 + hl * 128;
        auto pfx = [&](int i) { return xl + i * 256; };
        auto pfkv = [&](int i) { int n = i * 64 + lane; n = n < 400 ? n : 399; const int sl = n / 40, r = n % 40, c = r % 10;
            return wl + (sl * 64 + r / 10) * 3840 + 1280 + (c < 5 ? c * 128 : 640 + c * 128); };
        auto pfq = [&](int i) { int n = i * 64 + lane; n = n < 200 ? n : 199; int sl = n / 20 + 8; sl = sl >= 10 ? sl - 10 : sl; const int r = n % 20;
            return wl + (sl * 64 + r / 5) * 3840 + (r % 5) * 128; };
        dma4(wid & 1 ? pfx(wid >> 1) : pfkv(wid >> 1), dump);
        dma4(wid & 1 ? pfx(2 + (wid >> 1)) : pfkv(2 + (wid >> 1)), dump);
        dma4(wid < 3 ? pfkv(4 + wid) : pfx(4), dump);
        dma4(pfq(wid), dump);
        { int n = wid * 64 + lane; n = n < 200 ? n : 199; const int o = (j16 * 40 + n / 5) * 1280 + (hg >> 2) * 640 + (n % 5) * 128;
          dma4(oB + o, dump); dma4(oA + o, dump); }
    }
    if (isA && wid >= 4) { for (int i = 0; i < ADELAY; ++i) __builtin_amdgcn_s_sleep(16); }

    const unsigned xo = (unsigned)((lane >> 3) * 1280 + (((lane & 7) ^ (lane >> 4)) << 4));
    auto w_off = [&](int q) { const int c = q * 64 + lane, row = __umul24(c, 3277) >> 16, j = c - row * 20; return (unsigned)(__umul24(row, 3840) + j * 16 + (j < 10 ? 1280 : 2400)); };
    if (!isA) { const char* xs = xb + k0 * 128 + wid * (16 * 1280);
      dma16(xo, xs, lds0 + XS0 + wid * 2048);
      dma16(xo ^ 64, xs + 8 * 1280, lds0 + XS0 + wid * 2048 + 1024);
      const char* wsrc = wb + (size_t)k0 * (64 * 3840);
      const unsigned wv = w_off(wid < 5 ? wid : wid - 5);
      if (wid < 5) { dma16(wv, wsrc, lds0 + WS0 + wid * 1024); dma16(wv, wsrc + 16 * 3840, lds0 + WS0 + (5 + wid) * 1024);
          if (wid >= 3) { dma16(wv, wsrc + 32 * 3840, lds0 + WS0 + (10 + wid) * 1024); dma16(wv, wsrc + 48 * 3840, lds0 + WS0 + (15 + wid) * 1024); } }
      else { dma16(wv, wsrc + 32 * 3840, lds0 + WS0 + (5 + wid) * 1024); dma16(wv, wsrc + 48 * 3840, lds0 + WS0 + (10 + wid) * 1024); } }
    if (wid == 4 && lane < 2) dma4(ws + lane * 4, lds0 + C_TAG);
    if (tid < 80) *(LAS unsigned*)(smem + tid * 4) = 0u;
    else if (tid < 82) *(LAS unsigned*)(smem + ONE + (tid - 80) * 4) = 0x3F803F80u;
    const int nnb = isA ? (rt > 0 ? 1 : 0) : (rt > 1 ? 2 : rt);
    auto kq_base = [&](int fg) { const int hd = fg / 5, dg = fg % 5; return dg < 4 ? hd * 5120 + (dg >> 1) * 2048 + (dg & 1) * 8 : hd * 5120 + 4096; };

    auto rdtag = [&]() { const u64 t = *(LAS u64*)(smem + C_TAG);
        const u64 e = ((u64)(unsigned)__builtin_amdgcn_readfirstlane((int)(t >> 32)) << 32) | (unsigned)__builtin_amdgcn_readfirstlane((int)t);
        return e * 6364136223846793005ull + 1442695040888963407ull; };
    const __amdgpu_buffer_rsrc_t wor = __builtin_amdgcn_make_buffer_rsrc((void*)(isA ? oA : oB), 0, 640 * 1280, 0x00020000);

    const int hp = wid >> 2, qo = wid & 3, qi = hp ? 3 - qo : qo;
    const int ud = qi + 8;
    const bool far = isA ? (4 * rt + qi >= 1) : (4 * rt + qi >= 8);
    const int u0 = isA ? (far ? ud - 1 : ud) : (far ? qi : 8 - 4 * rt);
    f32x16 cd, cf;
    int dsh, kb1, kb2, nh, vb0, vb1, qb1, qb2; unsigned obase, oo[5];
    auto mask_b = [&](f32x16& c, int r, bool fartile) { const int k = (r & 3) + 8 * (r >> 2); c[r] = __int_as_float(fartile ? ((k - 1) << 23) - dsh : dsh - (k << 23)); };
    auto mask_a = [&](int z) {
      SFOR(r, 16)
          constexpr u64 ad = lanes_cut(r, false), af = lanes_cut(r, true);
          cd[r] = __builtin_amdgcn_inverse_ballot_w64(ad + z) ? -__builtin_inff() : 0.f;
          cf[r] = __builtin_amdgcn_inverse_ballot_w64(af + z) ? -__builtin_inff() : 0.f;
      SEND };
    auto att_setup = [&](int part) {
        int l = tid; asm volatile("" : "+v"(l)); l &= 63;
        const int q = l & 31, h = l >> 5;
        if (part == 0) { dsh = (q - 4 * h) << 23; kb1 = KV0 + hp * 10240 + h * 2048 + q * 16; kb2 = h ? ONE : KV0 + hp * 10240 + 4096 + q * 8; nh = h ^ 1;
            asm volatile("" : "+v"(dsh), "+v"(kb1), "+v"(kb2), "+v"(nh)); }
        if (part == 1) { const bool ones = (l & 16) && (l & 3);
            vb0 = KV0 + VOFF + (4 * h + ((l & 15) >> 2)) * VP + (ones ? 160 : hp * 80 + (l & 16) * 2 + (l & 3) * 8); vb1 = vb0 + (ones ? 0 : 40);
            asm volatile("" : "+v"(vb0), "+v"(vb1)); }
        if (part == 2) { qb1 = QST + hp * 10240 + h * 2048 + (qi * 32 + q) * 16; qb2 = QST + hp * 10240 + 4096 + (qi * 32 + q) * 8;
            obase = (unsigned)(qi * 40960 + hp * 20480 + hg * 2560 + q * 16 + h * 512);
            asm volatile("" : "+v"(qb1), "+v"(qb2), "+v"(obase)); }
        if (part >= 3) {
#pragma unroll
            for (int r = 4 * (part - 3); r < 4 * (part - 2); ++r) mask_b(cd, r, false);
            asm volatile("" : "+v"(cd)); } };
    auto wo_setup = [&](int q) {
        int l = tid; asm volatile("" : "+v"(l)); const int c = q * 64 + (l & 63), rho = __umul24(c, 6554) >> 16;
        oo[q] = (unsigned)(__umul24(qo * 160 + hp * 40 + (rho & ~7) + 4 * (rho & 1) + ((rho >> 1) & 3), 1280) + hg * 160 + (c - rho * 10) * 16);
        asm volatile("" : "+v"(oo[q])); };
    u64 tag;

    if (wid < 4) {
        if (!isA) WAITV(0);
        BAR_L();
        STAMP(1);
        bf16x8 xr[32], xt[2], wf[WIN + 1];
        f32x16 acc[5];
        const int tok = wid * 32 + r32;
        const int xa = XS0 + tok * 128 + ((hl ^ ((tok >> 1) & 7)) << 4);
        int wa0 = WS0 + (8 * hl + ((lane & 15) >> 2)) * 320 + (lane & 16) * 2 + (lane & 3) * 8, wa1 = wa0 + 61440;
        asm volatile("" : "+v"(wa0), "+v"(wa1));
        auto rdx = [&](int i) { return *(LAS bf16x8*)(smem + ((i >> 2) % 3) * 16384 + (xa ^ ((i & 3) * 32))); };
        auto rdw = [&](int f) { const int i = f / 5, t = f % 5, sl = (i >> 1) % 7;
            return trfrag(smem + (sl < 6 ? wa0 + sl * 10240 : wa1) + (i & 1) * 5120 + t * 64, 1280); };
        unsigned fl = 0;
        auto rdflag = [&](int u) { fl = __hip_atomic_load((LAS unsigned*)(smem + C_F + u * 4), RLX_WG); };
        auto check = [&](int u) {
            if (__builtin_amdgcn_readfirstlane(fl) != 15) {
#pragma unroll 1
                for (unsigned spins = 0; spins < LSPIN_CAP; ++spins) { rdflag(u); if (__builtin_amdgcn_readfirstlane(fl) == 15) break; __builtin_amdgcn_s_sleep(1); }
            }
            FENCE(); };
#pragma unroll
        for (int i = 0; i < 4; ++i) xr[i] = rdx(i);
#pragma unroll
        for (int f = 0; f < WIN; ++f) wf[f] = rdw(f);
        SB();
        SFOR(f, 200)
            constexpr int i = f / 5, t = f % 5;
            if constexpr (f % 20 == 10 - WIN) lds_set(smem + C_P + wid * 4, (i >> 1) + 1);
            if constexpr (f == 200 - WIN) lds_set(smem + C_P + wid * 4, 20);
            if constexpr (f % 20 == 10) rdflag(f / 20 + 1);
            if constexpr (f % 20 == 20 - WIN && f < 190) check(f / 20 + 1);
            if constexpr (f + WIN < 200) wf[(f + WIN) % (WIN + 1)] = rdw(f + WIN);
            if constexpr (f % 20 >= 16 && f < 140) xr[(f / 20 + 1) * 4 + f % 20 - 16] = rdx((f / 20 + 1) * 4 + f % 20 - 16);
            if constexpr (t == 1 && i >= 31 && i < 39) xt[(i + 1) & 1] = rdx(i + 1);
            SB();
            if constexpr (i == 0) MFMA32Z(acc[t], wf[f % (WIN + 1)], xr[0]);
            else if constexpr (i < 32) MFMA32I(acc[t], wf[f % (WIN + 1)], xr[i]);
            else MFMA32I(acc[t], wf[f % (WIN + 1)], xt[i & 1]);
            SB();
        SEND
        STAMP(2);
        int l2 = tid; asm volatile("" : "+v"(l2)); l2 &= 63;
        const int h2 = l2 >> 5, tk = wid * 32 + (l2 & 31);
        int eb[5];
        auto ebases = [&](int base) { int l = tid; asm volatile("" : "+v"(l)); const int h = (l >> 5) & 1, k = wid * 32 + (l & 31);
            eb[0] = base + k * 16 + h * 8; eb[1] = base + k * 16 + h * 2040; eb[2] = base + (h ? k * 16 + 1024 : k * 8); eb[3] = base + (h ? k * 8 + 2040 : k * 16);
            asm volatile("" : "+v"(eb[0]), "+v"(eb[1]), "+v"(eb[2]), "+v"(eb[3])); };
        auto kq_addr = [&](int g0) { return eb[g0 % 5 == 1 ? 1 : g0 % 5 == 4 ? 2 : g0 % 5 == 3 ? 3 : 0] + kq_base(g0); };
        ebases(OWN);
        eb[4] = OWN + VOFF + tk * VP + h2 * 8; asm volatile("" : "+v"(eb[4]));
        int qa0 = (8 * h2 + 2 * ((l2 & 15) >> 2)) * 160 + (l2 & 16) * 2 + (l2 & 3) * 8, qa1 = qa0 + WS0 + 10240;
        asm volatile("" : "+v"(qa0), "+v"(qa1));
        auto rdq = [&](int f) { const int i = f / 3, t = f % 3, j = i >> 2, sl = (20 + j) % 7;
            return trfrag(smem + (j >= 7 ? qa0 + (j == 7 ? QU27 : j == 8 ? QU28 : QU29) : sl == 0 ? qa0 + WS0 : qa1 + (sl - 1) * 10240) + (i & 3) * 2560 + t * 64, 160); };
        MFMA_DRAIN();
        WAITV(0);
        { u32x2 pk[20];
#pragma unroll
          for (int n = 0; n < 20; ++n) { const int t = n >> 2, b = n & 3; pk[n] = (u32x2){pk2(acc[t][4 * b], acc[t][4 * b + 1]), pk2(acc[t][4 * b + 2], acc[t][4 * b + 3])}; }
          SB();
#pragma unroll
          for (int n = 0; n < 20; ++n) *(LAS u32x2*)(smem + (n < 10 ? kq_addr(2 * n) : eb[4] + (2 * n - 20) * 8)) = pk[n];
          if (h2 == 0) *(LAS u32x2*)(smem + eb[4] + 160) = (u32x2){0x3F803F80u, 0x3F803F80u}; }
        lds_set(smem + C_E + wid * 4, 1);
        check(10);
        xt[0] = rdx(32);
#pragma unroll
        for (int f = 0; f < WIN; ++f) wf[f] = rdq(f);
        STAMP(3);
        SB();
        u64 pv0 = 0, pv1 = 0;
        SFOR(f, 120)
            constexpr int i = f / 3, t = f % 3, j = i >> 2;
            if constexpr (f % 24 == 12 && j < 9) rdflag(11 + j / 2);
            if constexpr (f % 12 == 12 - WIN && (j == 1 || j == 6 || j == 8)) lds_set(smem + C_P + wid * 4, 21 + j);
            if constexpr (f % 24 == 24 - WIN && j < 9) check(11 + j / 2);
            if constexpr (f + WIN < 120) wf[(f + WIN) % (WIN + 1)] = rdq(f + WIN);
            if constexpr (t == 0 && i < 7) xt[(i + 1) & 1] = rdx(32 + i + 1);
            if constexpr (f >= 90 && f < 100 && !(f & 1)) wo_setup((f - 90) / 2);
            if constexpr (f == 99) tag = rdtag();
            if constexpr (f >= 100 && f < 107) att_setup(f - 100);
            if constexpr (f == 108) ebases(QST);
            if constexpr (f == 116) { pv0 = __hip_atomic_load((LAS u64*)(smem + C_P), RLX_WG); pv1 = __hip_atomic_load((LAS u64*)(smem + C_P + 8), RLX_WG); }
            SB();
            if constexpr (i == 0) MFMA32Z(acc[t], wf[f % (WIN + 1)], xt[0]);
            else if constexpr (i < 8) MFMA32I(acc[t], wf[f % (WIN + 1)], xt[i & 1]);
            else MFMA32I(acc[t], wf[f % (WIN + 1)], xr[i - 8]);
            SB();
        SEND
        STAMP(4);
        if (__builtin_amdgcn_readfirstlane((int)min(min((unsigned)pv0, (unsigned)(pv0 >> 32)), min((unsigned)pv1, (unsigned)(pv1 >> 32)))) < 29) wait_min4(smem + C_P, 29);
        MFMA_DRAIN();
        { acc[0] *= QSCALE; acc[1] *= QSCALE; acc[2] *= QSCALE;
          SB();
          u32x2 pk[10];
#pragma unroll
          for (int n = 0; n < 10; ++n) { const int t = n >> 2, b = n & 3; pk[n] = (u32x2){pk2(acc[t][4 * b], acc[t][4 * b + 1]), pk2(acc[t][4 * b + 2], acc[t][4 * b + 3])}; }
          SB();
#pragma unroll
          for (int n = 0; n < 10; ++n) *(LAS u32x2*)(smem + kq_addr(2 * n)) = pk[n]; }
        lds_set(smem + C_QF + wid * 4, 1);
        __builtin_amdgcn_s_setprio(1);
        STAMP(5);
    } else {
        const int hw = wid - 4;
        int known = 0;
        __builtin_amdgcn_s_setprio(3);
        auto lds_or = [&](int off, unsigned v) { FENCE(); if (lane == 0) __hip_atomic_fetch_or((LAS unsigned*)(smem + off), v, RLX_WG); FENCE(); };
        unsigned wo[5], qo[5];
        auto done = [&](int n) { lds_or(C_F + n * 4, 1u << hw); };
        const unsigned xo1 = (xo ^ 64) + 9216, xo2 = xo + 2 * 9216, xo3 = (xo ^ 64) + 3 * 9216;
        auto round_x = [&](int n) { dma16x4(xo, xo1, xo2, xo3, xb + kt_of(n) * 128 + hw * (32 * 1280), lds0 + XS0 + (n % 3) * 16384 + hw * 4096); };
        auto round_w = [&](int n) {
            const int u = 2 * n + (hw >> 1);
            const unsigned dst = lds0 + (u < 27 ? WS0 + (u % 7) * 10240 : u == 27 ? QU27 : u == 28 ? QU28 : QU29) + (hw & 1) * 5120 + 2048;
            if (u < 20) dma16x5(wo, wb + (size_t)(kt_of(u >> 1) * 64 + (u & 1) * 32) * 3840 - 2048, dst);
            else dma16x5(qo, wb + (size_t)kt_of(u < 22 ? u - 12 : u - 22) * (64 * 3840) - 2048, dst); };
        if (isA) round_x(0);
        round_x(1);
#pragma unroll
        for (int q = 0; q < 5; ++q) wo[q] = w_off(q) + (hw & 1) * (16 * 3840) + (4 - q) * 1024;
        if (isA) { round_w(0); round_w(1); WAITV(5); } else { round_w(1); WAITV(9); }
        BAR_L();
#pragma unroll
        for (int q = 0; q < 5; ++q) { const int c = q * 64 + lane, rho = __umul24(c, 6554) >> 16;
            qo[q] = (unsigned)(__umul24((hw & 1) * 32 + (rho & ~7) + 4 * (rho & 1) + ((rho >> 1) & 3), 3840) + (c - rho * 10) * 16 + (4 - q) * 1024); }
#pragma unroll 1
        for (int n = 2; n <= 13; ++n) {
            const int need = n < 13 ? 2 * n - 5 : 20;
            if (need > known) known = min4(smem + C_P);
            if (need > known) { WAITV(0); done(n - 1); known = wait_min4(smem + C_P, need); }
            if (n < 10) round_x(n);
            round_w(n);
            if (n < 10) WAITV(9); else WAITV(5);
            done(n - 1);
        }
        WAITV(0); done(13);
        STAMP(1);
        tag = rdtag();
        auto a_chunk = [&](int a) { int o;
            if (a < 320) { const int hd = a / 80, rem = a % 80; o = hd * 5120 + (rem < 32 ? (96 + rem) * 16 : rem < 64 ? 2048 + (64 + rem) * 16 : 4096 + 768 + (rem - 64) * 16); }
            else o = VOFF + 96 * VP + (a - 320) * 16;
            return o; };
        if (rt < 15) {
            const __amdgpu_buffer_rsrc_t kvr = __builtin_amdgcn_make_buffer_rsrc(ws + WS_KV + me * KVG, 0, KVB, 0x00020000);
            wait_min4(smem + C_E, 1);
            STAMP(2);
            if (!isA) { u32x4 v[11];
#pragma unroll
                for (int j = 0; j < 11; ++j) if (j < 10 || hw == 3) v[j] = *(LAS u32x4*)(smem + OWN + hw * 10240 + j * 1024 + lane * 16);
#pragma unroll
                for (int j = 0; j < 11; ++j) if (j < 10 || hw == 3) __builtin_amdgcn_raw_buffer_store_b128(v[j], kvr, hw * 10240 + j * 1024 + lane * 16, 0, 16);
            } else {
#pragma unroll
                for (int j = 0; j < 3; ++j) { const int a = (4 * j + hw) * 64 + lane;
                    if (a < 656) { const int o = a_chunk(a); __builtin_amdgcn_raw_buffer_store_b128(*(LAS u32x4*)(smem + OWN + o), kvr, o, 0, 16); } }
            }
            STAMP(3);
        }
        wait_min4(smem + C_P, 22);
        round_w(14);
        WAITV(5);
        if (rt < 15) {
            unsigned old = 0;
            if (lane == 0) old = __hip_atomic_fetch_add((LAS unsigned*)(smem + N_PUB), 1u, RLX_WG);
            if (lane == 0 && old == 3) __hip_atomic_store((GAS u64*)(ws + WS_FKV + me * 128), tag, RLX_AGENT);
            STAMP(4);
        }
        WAITV(0); done(14);
        __builtin_amdgcn_s_setprio(0);
#pragma unroll
        for (int p = 0; p < 7; ++p) att_setup(p);
#pragma unroll
        for (int q = 0; q < 5; ++q) wo_setup(q);
        __builtin_amdgcn_s_setprio(3);
        for (int i = 0; i < hw; ++i) __builtin_amdgcn_s_sleep(PSTAG);
        poll_shared((GAS u64*)(ws + WS_FKV + (me - 8 * (lane + 1)) * 128), lane < nnb, tag, smem + N_KV);
        STAMP(5);
        if (!isA) {
            const int n = hw >> 1, half = hw & 1;
            if (n < nnb) {
                const char* src = ws + WS_KV + (me - 8 * (n + 1)) * KVG + half * 20480; const unsigned dst = lds0 + KV0 + (1 - n) * KVB + half * 20480;
                wait_min4(smem + C_P, 27);
#pragma unroll
                for (int j = 0; j < 4; ++j) dma16x5_sc1(lane * 16, src + j * 5120 + 2048, dst + j * 5120 + 2048);
                if (half) dma16_sc1(lane * 16, src + 20480, dst + 20480);
            }
        } else if (nnb) {
            const __amdgpu_buffer_rsrc_t nbr = __builtin_amdgcn_make_buffer_rsrc(ws + WS_KV + (me - 8) * KVG, 0, KVB, 0x00020000);
            u32x4 v[3];
#pragma unroll
            for (int j = 0; j < 3; ++j) { const int a = (4 * j + hw) * 64 + lane; if (a < 656) v[j] = __builtin_amdgcn_raw_buffer_load_b128(nbr, a_chunk(a), 0, 16); }
            wait_min4(smem + C_P, 27);
#pragma unroll
            for (int j = 0; j < 3; ++j) { const int a = (4 * j + hw) * 64 + lane; if (a < 656) *(LAS u32x4*)(smem + KV0 + KVB + a_chunk(a)) = v[j]; }
        }
        __builtin_amdgcn_s_setprio(2);
        wait_word(smem + C_QF + qi * 4, 1);
        STAMP(6);
    }
    u32x4 wreg[13];
    auto ldwo = [&](int i) { wreg[i] = __builtin_amdgcn_raw_buffer_load_b128(wor, oo[i % 5] + (i / 5) * (32 * 1280) + (i > 6 || (i == 6 && lane >= 16) ? 40 * 1280 : 0), 0, 0); };
    STAMP(7);
    u32x2 ob[2][3];
    {
        bf16x8 q1[2]; u32x2 q2[2];
#pragma unroll
        for (int e = 0; e < 2; ++e) {
            q1[e] = *(LAS bf16x8*)(smem + qb1 + e * 5120);
            q2[e] = *(LAS u32x2*)(smem + qb2 + e * 5120);
            if (hl) q2[e] = (u32x2){0u, 0u};
        }
        if (isA) mask_a(0);
        struct KF { bf16x8 a; s16x4 b; };
        struct VF { bf16x8 a, b; };
        auto rdk = [&](int u, int e) { const int so = (u >> 2) * KVB, tw = u & 3; KF k;
            k.a = *(LAS bf16x8*)(smem + kb1 + so + tw * 512 + e * 5120); k.b = *(LAS s16x4*)(smem + __umul24(so + tw * 256 + e * 5120, nh) + kb2); return k; };
        auto rdv = [&](int u, int e) { LAS char* p = smem + (e ? vb1 : vb0) + (u >> 2) * KVB + (u & 3) * (32 * VP); VF v;
            v.a = trfrag(p, 8 * VP); v.b = trfrag(p + 16 * VP, 8 * VP); return v; };
        auto qk = [&](const KF& k, int e, const f32x16& c) { return MFMA32K8(k.b, __builtin_bit_cast(s16x4, q2[e]), MFMA32(k.a, q1[e], c)); };
        const f32x16 zero = {};
        auto max16 = [&](const f32x16& s, float m) {
#pragma unroll
            for (int r = 0; r < 16; ++r) m = __builtin_elementwise_maximum(m, s[r]);
            return m; };
        auto set_ref = [&](float m, int e) { const unsigned nb = pk2(-m, 0.f); if (hl) q2[e][0] = nb; };
        auto ex4 = [&](f32x16& s, int b) {
#pragma unroll
            for (int r = 0; r < 4; ++r) s[b + r] = __builtin_amdgcn_exp2f(s[b + r]); };
        auto pk8 = [&](const f32x16& s, int b) { return __builtin_bit_cast(bf16x8, (u32x4){pk2(s[b], s[b + 1]), pk2(s[b + 2], s[b + 3]), pk2(s[b + 4], s[b + 5]), pk2(s[b + 6], s[b + 7])}); };
        auto phase = [&](auto nx, f32x16& cur, const VF& v, f32x16& o, f32x16& nxt, const KF& k, int e) __attribute__((always_inline)) {
            constexpr int NX = decltype(nx)::value;
            SB(); ex4(cur, 0); SB();
            if constexpr (NX) nxt = MFMA32(k.a, q1[e], NX == 1 ? zero : NX == 2 ? cf : cd);
            SB(); ex4(cur, 4); { const bf16x8 p = pk8(cur, 0); SB(); o = MFMA32(v.a, p, o); }
            SB(); ex4(cur, 8); SB();
            if constexpr (NX) nxt = MFMA32K8(k.b, __builtin_bit_cast(s16x4, q2[e]), nxt);
            SB(); ex4(cur, 12); { const bf16x8 p = pk8(cur, 8); SB(); o = MFMA32(v.b, p, o); }
            SB(); };
        f32x16 o0, o1, sa, sb; KF kx; VF vx;
        auto step = [&](auto m0, auto m1, int u) __attribute__((always_inline)) {
            constexpr int M1 = decltype(m1)::value;
            KF ky; if constexpr (M1) ky = rdk(u - 1, 0);
            const VF vy = rdv(u, 1);
            phase(m0, sa, vx, o0, sb, kx, 1);
            if constexpr (M1) { kx = rdk(u - 1, 1); vx = rdv(u - 1, 0); }
            phase(m1, sb, vy, o1, sa, ky, 0); };
        auto nbwait = [&](int u) { if (u == 8) wait_word(smem + N_NB, 4); };
        { const KF k0f = rdk(ud, 0); kx = rdk(ud, 1); vx = rdv(ud, 0);
          const f32x16 s0 = qk(k0f, 0, cd), s1 = qk(kx, 1, cd);
          set_ref(halfmax(max16(s0, s0[0])), 0); set_ref(halfmax(max16(s1, s1[0])), 1);
          sa = qk(k0f, 0, cd); }
        if (hp) { WAITV(0); lds_inc0(smem + N_NB, lane); lds_inc0(smem + N_G0, lane); }
        o0 = zero; o1 = zero;
        if (u0 + 8 == ud) {
            SFOR(it, 9)
                if constexpr (it < 7) { ldwo(2 * it); if constexpr (it < 6) ldwo(2 * it + 1); }
                if constexpr (it == PFLIP) { if (hp) __builtin_amdgcn_s_setprio(0); }
                if constexpr (it == 7) {
#pragma unroll
                    for (int r = 0; r < 16; ++r) mask_b(cf, r, true); }
                if constexpr (it < 4) nbwait(ud - it);
                step(IC<it == 0 ? 3 : it == 8 ? 2 : 1>{}, IC<it == 8 ? 0 : it == 7 ? 2 : 1>{}, ud - it);
            SEND
        } else {
            __builtin_amdgcn_s_setprio(0);
#pragma unroll
            for (int i = 0; i < 13; ++i) ldwo(i);
            if (!isA) cf = zero;
            if (u0 < ud) {
                nbwait(ud);
                if (u0 + 1 < ud) {
                    step(IC<3>{}, IC<1>{}, ud);
#pragma unroll 1
                    for (int u = ud - 1; u > u0 + 1; --u) { nbwait(u); step(IC<1>{}, IC<1>{}, u); }
                    nbwait(u0 + 1);
                    step(IC<1>{}, IC<2>{}, u0 + 1);
                } else step(IC<3>{}, IC<2>{}, ud);
                step(IC<2>{}, IC<0>{}, u0);
            } else step(IC<3>{}, IC<0>{}, ud);
        }
        if (!__all(o0[12] < 1.152921504606847e18f && o1[12] < 1.152921504606847e18f)) {
            if (isA) { int z = 0; asm volatile("" : "+s"(z)); mask_a(z); }
            else { asm volatile("" : "+v"(dsh));
#pragma unroll
                for (int r = 0; r < 16; ++r) { mask_b(cd, r, false); mask_b(cf, r, true); }
                if (!far) cf = zero; }
#pragma unroll
            for (int e = 0; e < 2; ++e) {
                if (hl) q2[e][0] = 0u;
                const f32x16 s = qk(rdk(ud, e), e, cd);
                float m = max16(s, s[0]);
                if (u0 < ud) m = max16(qk(rdk(u0, e), e, cf), m);
#pragma unroll 1
                for (int u = u0 + 1; u < ud; ++u) m = max16(qk(rdk(u, e), e, zero), m);
                set_ref(halfmax(m), e);
            }
#pragma unroll
            for (int e = 0; e < 2; ++e) {
                f32x16 o = zero;
#pragma unroll 1
                for (int u = u0; u <= ud; ++u) { f32x16 t = u == ud ? qk(rdk(u, e), e, cd) : u == u0 ? qk(rdk(u, e), e, cf) : qk(rdk(u, e), e, zero);
                    const VF v = rdv(u, e); ex4(t, 0); ex4(t, 4); ex4(t, 8); ex4(t, 12);
                    o = MFMA32(v.a, pk8(t, 0), o); o = MFMA32(v.b, pk8(t, 8), o); }
                if (e) o1 = o; else o0 = o;
            }
        }
        const float rl0 = __builtin_amdgcn_rcpf(o0[12]), rl1 = __builtin_amdgcn_rcpf(o1[12]);
        SB();
#pragma unroll
        for (int r = 0; r < 12; ++r) { o0[r] *= rl0; o1[r] *= rl1; }
        SB();
#pragma unroll
        for (int j = 0; j < 3; ++j) {
            ob[0][j] = (u32x2){pk2(o0[4 * j], o0[4 * j + 1]), pk2(o0[4 * j + 2], o0[4 * j + 3])};
            ob[1][j] = (u32x2){pk2(o1[4 * j], o1[4 * j + 1]), pk2(o1[4 * j + 2], o1[4 * j + 3])}; }
        SB();
    }
    STAMP(8);
    const __amdgpu_buffer_rsrc_t orr = __builtin_amdgcn_make_buffer_rsrc(ws + WS_O + (br * 16 + rt) * OG, 0, 4 * 40960, 0x00020000);
    {
      { auto rx = __builtin_amdgcn_permlane32_swap(ob[0][0][0], ob[0][1][0], false, false);
        auto ry = __builtin_amdgcn_permlane32_swap(ob[0][0][1], ob[0][1][1], false, false);
        __builtin_amdgcn_raw_buffer_store_b128((u32x4){rx[0], ry[0], rx[1], ry[1]}, orr, obase, 0, 16); }
      if (hl == 0) __builtin_amdgcn_raw_buffer_store_b128((u32x4){ob[0][2][0], ob[0][2][1], ob[1][0][0], ob[1][0][1]}, orr, obase + 1024, 0, 16);
      { auto fx = __builtin_amdgcn_permlane32_swap(ob[1][0][0], ob[1][1][0], false, false);
        auto fy = __builtin_amdgcn_permlane32_swap(ob[1][0][1], ob[1][1][1], false, false);
        auto sx = __builtin_amdgcn_permlane32_swap(ob[1][1][0], ob[1][2][0], false, false);
        auto sy = __builtin_amdgcn_permlane32_swap(ob[1][1][1], ob[1][2][1], false, false);
        __builtin_amdgcn_raw_buffer_store_b128((u32x4){fx[1], fy[1], sx[0], sy[0]}, orr, obase + 1536, 0, 16); } }
    if (lane == 0) { (void)__hip_atomic_fetch_add((LAS unsigned*)(smem + N_G1), 1u, RLX_WG);
        if (!hp) (void)__hip_atomic_fetch_add((LAS unsigned*)(smem + N_G0), 1u, RLX_WG); }
    FENCE();
    STAMP(6);
    auto signal_o = [&]() {
        if (lds_inc(smem + N_OS + hp * 4, lane) == 3 && lane == 0) __hip_atomic_store((GAS u64*)(ws + WS_FO + ((me >> 3) * 2 + hp) * 128 + hg * 8), tag, RLX_AGENT); };
    __builtin_amdgcn_s_setprio(0);
    if (wid == 0 && lane == 0) { GAS unsigned* d = (GAS unsigned*)(ws + WS_DBG + bx * 128);
        __hip_atomic_store(d + 0, dst_[0], RLX_AGENT); __hip_atomic_store(d + 1, dst_[1], RLX_AGENT); __hip_atomic_store(d + 2, dst_[2], RLX_AGENT);
        __hip_atomic_store(d + 3, dst_[7], RLX_AGENT); __hip_atomic_store(d + 4, dst_[8], RLX_AGENT); }
    WAITV(0); signal_o();
    STAMP(9);
    wait_word(smem + (hp ? N_G1 : N_G0), 8);
    const int p1 = hp ? 30720 : 35840, p2 = hp ? 65536 : 43520;
    const int wb0 = hp ? KV0 + 10240 : 512, wb1 = hp ? KV0 + KVB + 10240 - 30720 : KV0 - 35840, wb2 = KV0 + KVB - 43520;
#pragma unroll
    for (int i = 0; i < 13; ++i) { const int L = qo * 12800 + i * 1024;
        if (i < 12 || lane < 32) *(LAS u32x4*)(smem + (L + (L < p1 ? wb0 : L < p2 ? wb1 : wb2)) + lane * 16) = wreg[i]; }
    lds_inc0(smem + N_WO + hp * 4, lane);
    STAMP(10);
    __builtin_amdgcn_s_sleep(PDELAY);
    for (int i = 0; i < qo; ++i) __builtin_amdgcn_s_sleep(PSTAG);
    poll_shared((GAS u64*)(ws + WS_FO + ((me >> 3) * 2 + hp) * 128 + (lane & 7) * 8), lane < 8, tag, smem + N_OK + hp * 4);
    STAMP(11);
    f32x16 oc[3]; f32x4 ps[10];
    LAS char* red = smem + OWN + qo * 10240 + lane * 16;
    { bf16x8 af[5], of[20];
      const int la = (8 * hl + 2 * ((lane & 15) >> 2)) * 160 + (lane & 16) * 2 + (lane & 3) * 8;
      const int aa0 = wb0 + la, aa1 = (hp ? wb1 : wb0) + la, aa2 = wb1 + la, aa3 = (hp ? wb1 : wb2) + la;
      auto rda = [&](int f) { return trfrag(smem + (f < 36 ? aa0 : f < 42 ? aa1 : f < 51 ? aa2 : aa3) + (f / 3) * 2560 + (f % 3) * 64, 160); };
      auto ldo = [&](int k) { of[k] = __builtin_bit_cast(bf16x8, __builtin_amdgcn_raw_buffer_load_b128(orr, qo * 40960 + (hp * 20 + k) * 1024 + lane * 16, 0, 16)); };
#pragma unroll
      for (int k = 0; k < OPF; ++k) ldo(k);
      wait_word(smem + N_WO + hp * 4, 4);
#pragma unroll
      for (int f = 0; f < 4; ++f) af[f] = rda(f);
      SB();
      SFOR(f, 60)
          if constexpr (f % 3 == 0 && f / 3 + OPF < 20) ldo(f / 3 + OPF);
          if constexpr (f + 4 < 60) af[(f + 4) % 5] = rda(f + 4);
          if constexpr (f == 57) { if (hp) { wait_word(smem + C_RD + qo * 4, 1);
#pragma unroll
              for (int b = 0; b < 10; ++b) ps[b] = *(LAS f32x4*)(red + b * 1024); } }
          SB();
          if constexpr (f < 3) MFMA32Z(oc[f], af[f % 5], of[0]); else MFMA32I(oc[f % 3], af[f % 5], of[f / 3]);
          SB();
      SEND }
    MFMA_DRAIN();
    STAMP(12);
    { if (!hp) {
          wait_word(smem + N_G1, 8);
#pragma unroll
          for (int b = 0; b < 10; ++b) *(LAS f32x4*)(red + b * 1024) = (f32x4){oc[b >> 2][4 * (b & 3)], oc[b >> 2][4 * (b & 3) + 1], oc[b >> 2][4 * (b & 3) + 2], oc[b >> 2][4 * (b & 3) + 3]};
          lds_set(smem + C_RD + qo * 4, 1);
      } else {
          char* orow = out + (size_t)(rt * 128 + qo * 32 + r32) * 2560 + br * 1280 + hg * 160 + hl * 16;
#pragma unroll
          for (int b = 0; b < 10; ++b)
#pragma unroll
              for (int r = 0; r < 4; ++r) ps[b][r] += oc[b >> 2][4 * (b & 3) + r];
          SB();
          u32x2 pk[10];
#pragma unroll
          for (int b = 0; b < 10; ++b) pk[b] = (u32x2){pk2(ps[b][0], ps[b][1]), pk2(ps[b][2], ps[b][3])};
          SB();
          u32x4 st[5];
#pragma unroll
          for (int b = 0; b < 10; b += 2) {
              auto rx = __builtin_amdgcn_permlane32_swap(pk[b][0], pk[b + 1][0], false, false); auto ry = __builtin_amdgcn_permlane32_swap(pk[b][1], pk[b + 1][1], false, false);
              st[b >> 1] = (u32x4){rx[0], ry[0], rx[1], ry[1]}; }
          SB();
#pragma unroll
          for (int b = 0; b < 5; ++b) *(u32x4*)(orow + b * 32) = st[b];
      } }
    if (bx == 0 && wid == 0) {
#pragma unroll 1
        for (int i = 0; i < 4; ++i) poll_flags((GAS u64*)(ws + WS_FO + (i * 8 + (lane >> 3)) * 256 + (lane & 7) * 8), true, tag);
        if (lane == 0) __hip_atomic_store((GAS u64*)ws, tag, RLX_AGENT);
        {
            const unsigned now = (unsigned)__builtin_amdgcn_s_memrealtime();
            int mn0 = 0x7fffffff, mB8 = -0x7fffffff, mA8 = -0x7fffffff, mB2 = -0x7fffffff, mA2 = -0x7fffffff, mB7 = -0x7fffffff, a1 = 0;
            for (int i = 0; i < 4; ++i) { const int b = i * 64 + lane; GAS unsigned* d = (GAS unsigned*)(ws + WS_DBG + b * 128);
                const unsigned t0 = __hip_atomic_load(d, RLX_AGENT), t1 = __hip_atomic_load(d + 1, RLX_AGENT), t2 = __hip_atomic_load(d + 2, RLX_AGENT), t7 = __hip_atomic_load(d + 3, RLX_AGENT), t8 = __hip_atomic_load(d + 4, RLX_AGENT);
                mn0 = min(mn0, (int)(t0 - now));
                if (i < 2) { a1 += t1 - t0; mB2 = max(mB2, (int)(t2 - now)); mB7 = max(mB7, (int)(t7 - now)); mB8 = max(mB8, (int)(t8 - now)); } else { mA2 = max(mA2, (int)(t2 - now)); mA8 = max(mA8, (int)(t8 - now)); } }
            for (int m = 32; m >= 1; m >>= 1) { mn0 = min(mn0, __shfl_xor(mn0, m)); mB8 = max(mB8, __shfl_xor(mB8, m)); mA8 = max(mA8, __shfl_xor(mA8, m)); mB2 = max(mB2, __shfl_xor(mB2, m));
                mA2 = max(mA2, __shfl_xor(mA2, m)); mB7 = max(mB7, __shfl_xor(mB7, m)); a1 += __shfl_xor(a1, m); }
            int M = DIAG == 0 ? 0 : DIAG == 1 ? a1 / 128 : DIAG == 2 ? mB2 - mn0 : DIAG == 3 ? mB7 - mn0 : DIAG == 4 ? mB8 - mn0 : DIAG == 5 ? mA8 - mn0 : mA2 - mn0;
            M = min(max(M, 0), 5000);
            const unsigned until = now + mn0 + 4000 + M;
            while ((int)((unsigned)__builtin_amdgcn_s_memrealtime() - until) < 0) __builtin_amdgcn_s_sleep(2);
        }
    }
}

extern "C" void kernel_launch(void* const* d_in, const int* in_sizes, int n_in, void* d_out, int out_size, void* d_ws, size_t ws_size,
                              hipStream_t stream) {
    fused_attn_kernel<<<256, 512, 0, stream>>>((const char*)d_in[0], (const char*)d_in[1], (const char*)d_in[2], (const char*)d_in[3],
                                               (const char*)d_in[4], (const char*)d_in[5], (char*)d_out, (char*)d_ws);
}
```

```cpp
#define ADELAY 0
#define DIAG 1
#include <hip/hip_runtime.h>
#include <hip/hip_bf16.h>
#include <stdint.h>

typedef short bf16x8 __attribute__((ext_vector_type(8)));
typedef short s16x4 __attribute__((ext_vector_type(4)));
typedef float f32x2 __attribute__((ext_vector_type(2)));
typedef float f32x4 __attribute__((ext_vector_type(4)));
typedef float f32x16 __attribute__((ext_vector_type(16)));
typedef unsigned u32x4 __attribute__((ext_vector_type(4)));
typedef unsigned u32x2 __attribute__((ext_vector_type(2)));
typedef __bf16 bfx2 __attribute__((ext_vector_type(2)));
typedef unsigned long long u64;
#define LAS __attribute__((address_space(3)))
#define GAS __attribute__((address_space(1)))
#define DI __device__ __forceinline__
#define RLX_AGENT __ATOMIC_RELAXED, __HIP_MEMORY_SCOPE_AGENT
#define RLX_WG __ATOMIC_RELAXED, __HIP_MEMORY_SCOPE_WORKGROUP

constexpr int LDS_BYTES = 163840;
constexpr int VOFF = 20480, VP = 168;
constexpr int KVB = VOFF + 128 * VP;
constexpr int KV0 = LDS_BYTES - 3 * KVB;
constexpr int OWN = KV0 + 2 * KVB;
constexpr int C_F = 0, C_P = 128, C_E = 144, C_QF = 160, C_RD = 176, C_CNT = 192, C_TAG = 320;
constexpr int N_PUB = C_CNT, N_NB = C_CNT + 4, N_G0 = C_CNT + 8, N_G1 = C_CNT + 12, N_WO = C_CNT + 16, N_OS = C_CNT + 24, N_KV = C_CNT + 32, N_OK = C_CNT + 36;
constexpr int XS0 = 512, WS0 = XS0 + 3 * 16384, QST = 512, ONE = 336, QU27 = XS0 + 16384, QU28 = XS0, QU29 = QU27 + 10240;
static_assert(WS0 + 7 * 10240 <= OWN, "ring overlaps the own image");
constexpr unsigned WS_FKV = 4096, WS_FO = WS_FKV + 256 * 128, WS_KV = 131072;
constexpr unsigned KVG = KVB, OG = 4 * 40960;
constexpr unsigned WS_O = WS_KV + 256 * KVG;
constexpr unsigned WS_END = WS_O + 32 * OG;
constexpr unsigned WS_DBG = WS_END;
constexpr unsigned SPIN_CAP = 400000, LSPIN_CAP = 4000000;
constexpr float QSCALE = 0.22360679774997896f * 1.4426950408889634f;
#define PSLEEP 2
#define PDELAY 1
#define PSTAG 3
#define PFLIP 6
#define OPF 12
#define ABL4N 2
#define WIN 4

#define MFMA32(a, b, c) __builtin_amdgcn_mfma_f32_32x32x16_bf16((a), (b), (c), 0, 0, 0)
#define MFMA32K8(a, b, c) __builtin_amdgcn_mfma_f32_32x32x8bf16_1k((a), (b), (c), 0, 0, 0)
#define MFMA32I(acc, a, b) asm volatile("v_mfma_f32_32x32x16_bf16 %0, %1, %2, %0" : "+v"(acc) : "v"(a), "v"(b))
#define MFMA32Z(acc, a, b) asm volatile("v_mfma_f32_32x32x16_bf16 %0, %1, %2, 0" : "=&v"(acc) : "v"(a), "v"(b))
#define MFMA_DRAIN() asm volatile("s_nop 7\n\ts_nop 4" ::: "memory")
#define WAITV(N) asm volatile("s_waitcnt vmcnt(" #N ")" ::: "memory")
#define WAITL() asm volatile("s_waitcnt lgkmcnt(0)" ::: "memory")
#define BAR_L() asm volatile("s_waitcnt lgkmcnt(0)\n\ts_barrier" ::: "memory")
#define SB() __builtin_amdgcn_sched_barrier(0)
#define FENCE() asm volatile("" ::: "memory")

#define STAMP(i) do { if ((i) == 0 || (i) == 8 || (i) == 9 || (i) == 11 || (i) == 12) { SB(); dst_[i] = (unsigned)__builtin_amdgcn_s_memrealtime(); SB(); } } while (0)

template <int V> struct IC { static constexpr int value = V; };
template <int I, int N, class F> DI void static_for(F&& f) { if constexpr (I < N) { f(IC<I>{}); static_for<I + 1, N>(f); } }
#define SFOR(var, N) static_for<0, N>([&](auto ic_##var) __attribute__((always_inline)) { constexpr int var = decltype(ic_##var)::value;
#define SEND });

DI void dma16(unsigned voff, const char* sbase, unsigned ldsaddr) {
    asm volatile("s_mov_b32 m0, %2\n\ts_nop 0\n\tglobal_load_lds_dwordx4 %0, %1" :: "v"(voff), "s"(sbase), "s"(ldsaddr) : "memory");
}
DI void dma16x4(unsigned v0, unsigned v1, unsigned v2, unsigned v3, const char* sbase, unsigned ldsaddr) {
    asm volatile("s_mov_b32 m0, %5\n\ts_nop 0\n\tglobal_load_lds_dwordx4 %0, %4\n\tglobal_load_lds_dwordx4 %1, %4 offset:1024\n\t"
                 "global_load_lds_dwordx4 %2, %4 offset:2048\n\tglobal_load_lds_dwordx4 %3, %4 offset:3072"
                 :: "v"(v0), "v"(v1), "v"(v2), "v"(v3), "s"(sbase), "s"(ldsaddr) : "memory");
}
DI void dma16x5(const unsigned (&v)[5], const char* sbase, unsigned ldsaddr) {
    asm volatile("s_mov_b32 m0, %6\n\ts_nop 0\n\tglobal_load_lds_dwordx4 %0, %5 offset:-2048\n\tglobal_load_lds_dwordx4 %1, %5 offset:-1024\n\t"
                 "global_load_lds_dwordx4 %2, %5\n\tglobal_load_lds_dwordx4 %3, %5 offset:1024\n\tglobal_load_lds_dwordx4 %4, %5 offset:2048"
                 :: "v"(v[0]), "v"(v[1]), "v"(v[2]), "v"(v[3]), "v"(v[4]), "s"(sbase), "s"(ldsaddr) : "memory");
}
DI void dma16x5_sc1(unsigned voff, const char* sbase, unsigned ldsaddr) {
    asm volatile("s_mov_b32 m0, %2\n\ts_nop 0\n\tglobal_load_lds_dwordx4 %0, %1 offset:-2048 sc1\n\tglobal_load_lds_dwordx4 %0, %1 offset:-1024 sc1\n\t"
                 "global_load_lds_dwordx4 %0, %1 sc1\n\tglobal_load_lds_dwordx4 %0, %1 offset:1024 sc1\n\tglobal_load_lds_dwordx4 %0, %1 offset:2048 sc1"
                 :: "v"(voff), "s"(sbase), "s"(ldsaddr) : "memory");
}
DI void dma16_sc1(unsigned voff, const char* sbase, unsigned ldsaddr) {
    asm volatile("s_mov_b32 m0, %2\n\ts_nop 0\n\tglobal_load_lds_dwordx4 %0, %1 sc1" :: "v"(voff), "s"(sbase), "s"(ldsaddr) : "memory");
}
DI void dma4(const char* p, unsigned ldsaddr) {
    asm volatile("s_mov_b32 m0, %1\n\ts_nop 0\n\tglobal_load_lds_dword %0, off" :: "v"(p), "s"(ldsaddr) : "memory");
}
DI unsigned pk2(float lo, float hi) { f32x2 v = {lo, hi}; return __builtin_bit_cast(unsigned, __builtin_convertvector(v, bfx2)); }
typedef short v4i16_t __attribute__((ext_vector_type(4)));
DI bf16x8 trfrag(LAS char* p, int second) {
    s16x4 a = __builtin_bit_cast(s16x4, __builtin_amdgcn_ds_read_tr16_b64_v4i16((LAS v4i16_t*)p));
    s16x4 b = __builtin_bit_cast(s16x4, __builtin_amdgcn_ds_read_tr16_b64_v4i16((LAS v4i16_t*)(p + second)));
    return __builtin_shufflevector(a, b, 0, 1, 2, 3, 4, 5, 6, 7);
}
constexpr u64 lanes_cut(int r, bool fartile) {
    u64 m = 0;
    for (int l = 0; l < 64; ++l) { const int key = (r & 3) + 8 * (r >> 2) + 4 * (l >> 5), d = (l & 31) - key + (fartile ? 32 : 0);
        const bool ok = d >= 0 && d <= 14 && !(d & 1);
        if (!ok) m |= 1ull << l; }
    return m;
}
DI float halfmax(float m) { auto rr = __builtin_amdgcn_permlane32_swap(__float_as_uint(m), __float_as_uint(m), false, false);
    return __builtin_elementwise_maximum(__uint_as_float(rr[0]), __uint_as_float(rr[1])); }

DI void poll_flags(GAS u64* word, bool active, u64 tag) {
#pragma unroll 1
    for (unsigned spins = 0; spins < SPIN_CAP; ++spins) {
        u64 v = tag;
        if (active) v = __hip_atomic_load(word, RLX_AGENT);
        if (__all(v == tag)) break;
        __builtin_amdgcn_s_sleep(PSLEEP);
    }
    __builtin_amdgcn_fence(__ATOMIC_ACQUIRE, "wavefront");
}
DI void poll_shared(GAS u64* word, bool active, u64 tag, LAS char* ok) {
#pragma unroll 1
    for (unsigned spins = 0; spins < SPIN_CAP; ++spins) {
        if (__builtin_amdgcn_readfirstlane((int)__hip_atomic_load((LAS unsigned*)ok, RLX_WG))) break;
        u64 v = tag;
        if (active) v = __hip_atomic_load(word, RLX_AGENT);
        if (__all(v == tag)) { __hip_atomic_store((LAS unsigned*)ok, 1u, RLX_WG); break; }
        __builtin_amdgcn_s_sleep(PSLEEP);
    }
    __builtin_amdgcn_fence(__ATOMIC_ACQUIRE, "wavefront");
    asm volatile("" ::: "memory");
}
DI int min4(LAS char* p) {
    const u64 a = __hip_atomic_load((LAS u64*)p, RLX_WG), b = __hip_atomic_load((LAS u64*)(p + 8), RLX_WG);
    const unsigned m = min(min((unsigned)a, (unsigned)(a >> 32)), min((unsigned)b, (unsigned)(b >> 32)));
    return __builtin_amdgcn_readfirstlane((int)m);
}
DI int wait_min4(LAS char* p, int need) {
    int v = min4(p);
#pragma unroll 1
    for (unsigned spins = 0; v < need && spins < LSPIN_CAP; ++spins) { __builtin_amdgcn_s_sleep(1); v = min4(p); }
    FENCE();
    return v;
}
DI void wait_word(LAS char* p, unsigned need) {
#pragma unroll 1
    for (unsigned spins = 0; spins < LSPIN_CAP; ++spins) {
        if ((unsigned)__builtin_amdgcn_readfirstlane((int)__hip_atomic_load((LAS unsigned*)p, RLX_WG)) >= need) break;
        __builtin_amdgcn_s_sleep(1); }
    FENCE();
}
DI void lds_inc0(LAS char* p, int lane) { FENCE(); if (lane == 0) (void)__hip_atomic_fetch_add((LAS unsigned*)p, 1u, RLX_WG); FENCE(); }
DI void wait_pair(LAS char* p, unsigned need) {
#pragma unroll 1
    for (unsigned spins = 0; spins < LSPIN_CAP; ++spins) { const u64 v = __hip_atomic_load((LAS u64*)p, RLX_WG);
        if ((unsigned)__builtin_amdgcn_readfirstlane((int)min((unsigned)v, (unsigned)(v >> 32))) >= need) break;
        __builtin_amdgcn_s_sleep(1); }
    FENCE();
}
DI unsigned lds_inc(LAS char* p, int lane) { FENCE(); unsigned old = 0; if (lane == 0) old = __hip_atomic_fetch_add((LAS unsigned*)p, 1u, RLX_WG); FENCE(); return old; }
DI void lds_set(LAS char* p, unsigned v) { FENCE(); __hip_atomic_store((LAS unsigned*)p, v, RLX_WG); FENCE(); }

__global__ __launch_bounds__(512, 2) void fused_attn_kernel(const char* __restrict__ xA, const char* __restrict__ wA, const char* __restrict__ oA,
                                                           const char* __restrict__ xB, const char* __restrict__ wB, const char* __restrict__ oB,
                                                           char* __restrict__ out, char* __restrict__ ws) {
    __shared__ __attribute__((aligned(16))) char smem_[LDS_BYTES];
    LAS char* smem = (LAS char*)smem_;
    const unsigned lds0 = (unsigned)(uintptr_t)smem;
    unsigned dst_[13];
    STAMP(0);
    asm volatile("" :: "s"(xA), "s"(wA), "s"(oA), "s"(xB), "s"(wB), "s"(oB), "s"(out), "s"(ws));
    const int tid = threadIdx.x, lane = tid & 63;
    const int wid = __builtin_amdgcn_readfirstlane(tid >> 6);
    const int bx = blockIdx.x, xcd = bx & 7, idx = bx >> 3;
    const int isA = idx >> 4, rt = (xcd >> 1) * 4 + ((idx >> 2) & 3), hg = (xcd & 1) * 4 + (idx & 3);
    const int br = isA ? 0 : 1;
    const int k0 = isA ? (((rt & 3) * 5 + 1) / 2 + ((hg & 3) * 5 + 1) / 2) % 10 : 0;
    const char* xb = (isA ? xA : xB) + (size_t)rt * (128 * 1280);
    const char* wb = (isA ? wA : wB) + hg * 160;
    const int me = (br * 16 + rt) * 8 + hg;
    const int r32 = lane & 31, hl = lane >> 5;
    auto kt_of = [&](int s) { int k = s + k0; return k >= 10 ? k - 10 : k; };
    if (isA && wid < 4) {
        const int j16 = idx & 15; const unsigned dump = lds0 + OWN + wid * 512;
        const char* wl = wB + (hg >> 2) * 640 + j16 * (4 * 3840);
        const char* xl = xB + (size_t)(rt * 128 + (hg & 3) * 32 + r32) * 1280 + hl * 128;
        auto pfx = [&](int i) { return xl + i * 256; };
        auto pfkv = [&](int i) { int n = i * 64 + lane; n = n < 400 ? n : 399; const int sl = n / 40, r = n % 40, c = r % 10;
            return wl + (sl * 64 + r / 10) * 3840 + 1280 + (c < 5 ? c * 128 : 640 + c * 128); };
        auto pfq = [&](int i) { int n = i * 64 + lane; n = n < 200 ? n : 199; int sl = n / 20 + 8; sl = sl >= 10 ? sl - 10 : sl; const int r = n % 20;
            return wl + (sl * 64 + r / 5) * 3840 + (r % 5) * 128; };
        dma4(wid & 1 ? pfx(wid >> 1) : pfkv(wid >> 1), dump);
        dma4(wid & 1 ? pfx(2 + (wid >> 1)) : pfkv(2 + (wid >> 1)), dump);
        dma4(wid < 3 ? pfkv(4 + wid) : pfx(4), dump);
        dma4(pfq(wid), dump);
        { int n = wid * 64 + lane; n = n < 200 ? n : 199; const int o = (j16 * 40 + n / 5) * 1280 + (hg >> 2) * 640 + (n % 5) * 128;
          dma4(oB + o, dump); dma4(oA + o, dump); }
    }
    if (isA && wid >= 4) { for (int i = 0; i < ADELAY; ++i) __builtin_amdgcn_s_sleep(16); }

    const unsigned xo = (unsigned)((lane >> 3) * 1280 + (((lane & 7) ^ (lane >> 4)) << 4));
    auto w_off = [&](int q) { const int c = q * 64 + lane, row = __umul24(c, 3277) >> 16, j = c - row * 20; return (unsigned)(__umul24(row, 3840) + j * 16 + (j < 10 ? 1280 : 2400)); };
    if (!isA) { const char* xs = xb + k0 * 128 + wid * (16 * 1280);
      dma16(xo, xs, lds0 + XS0 + wid * 2048);
      dma16(xo ^ 64, xs + 8 * 1280, lds0 + XS0 + wid * 2048 + 1024);
      const char* wsrc = wb + (size_t)k0 * (64 * 3840);
      const unsigned wv = w_off(wid < 5 ? wid : wid - 5);
      if (wid < 5) { dma16(wv, wsrc, lds0 + WS0 + wid * 1024); dma16(wv, wsrc + 16 * 3840, lds0 + WS0 + (5 + wid) * 1024);
          if (wid >= 3) { dma16(wv, wsrc + 32 * 3840, lds0 + WS0 + (10 + wid) * 1024); dma16(wv, wsrc + 48 * 3840, lds0 + WS0 + (15 + wid) * 1024); } }
      else { dma16(wv, wsrc + 32 * 3840, lds0 + WS0 + (5 + wid) * 1024); dma16(wv, wsrc + 48 * 3840, lds0 + WS0 + (10 + wid) * 1024); } }
    if (wid == 4 && lane < 2) dma4(ws + lane * 4, lds0 + C_TAG);
    if (tid < 80) *(LAS unsigned*)(smem + tid * 4) = 0u;
    else if (tid < 82) *(LAS unsigned*)(smem + ONE + (tid - 80) * 4) = 0x3F803F80u;
    const int nnb = isA ? (rt > 0 ? 1 : 0) : (rt > 1 ? 2 : rt);
    auto kq_base = [&](int fg) { const int hd = fg / 5, dg = fg % 5; return dg < 4 ? hd * 5120 + (dg >> 1) * 2048 + (dg & 1) * 8 : hd * 5120 + 4096; };

    auto rdtag = [&]() { const u64 t = *(LAS u64*)(smem + C_TAG);
        const u64 e = ((u64)(unsigned)__builtin_amdgcn_readfirstlane((int)(t >> 32)) << 32) | (unsigned)__builtin_amdgcn_readfirstlane((int)t);
        return e * 6364136223846793005ull + 1442695040888963407ull; };
    const __amdgpu_buffer_rsrc_t wor = __builtin_amdgcn_make_buffer_rsrc((void*)(isA ? oA : oB), 0, 640 * 1280, 0x00020000);

    const int hp = wid >> 2, qo = wid & 3, qi = hp ? 3 - qo : qo;
    const int ud = qi + 8;
    const bool far = isA ? (4 * rt + qi >= 1) : (4 * rt + qi >= 8);
    const int u0 = isA ? (far ? ud - 1 : ud) : (far ? qi : 8 - 4 * rt);
    f32x16 cd, cf;
    int dsh, kb1, kb2, nh, vb0, vb1, qb1, qb2; unsigned obase, oo[5];
    auto mask_b = [&](f32x16& c, int r, bool fartile) { const int k = (r & 3) + 8 * (r >> 2); c[r] = __int_as_float(fartile ? ((k - 1) << 23) - dsh : dsh - (k << 23)); };
    auto mask_a = [&](int z) {
      SFOR(r, 16)
          constexpr u64 ad = lanes_cut(r, false), af = lanes_cut(r, true);
          cd[r] = __builtin_amdgcn_inverse_ballot_w64(ad + z) ? -__builtin_inff() : 0.f;
          cf[r] = __builtin_amdgcn_inverse_ballot_w64(af + z) ? -__builtin_inff() : 0.f;
      SEND };
    auto att_setup = [&](int part) {
        int l = tid; asm volatile("" : "+v"(l)); l &= 63;
        const int q = l & 31, h = l >> 5;
        if (part == 0) { dsh = (q - 4 * h) << 23; kb1 = KV0 + hp * 10240 + h * 2048 + q * 16; kb2 = h ? ONE : KV0 + hp * 10240 + 4096 + q * 8; nh = h ^ 1;
            asm volatile("" : "+v"(dsh), "+v"(kb1), "+v"(kb2), "+v"(nh)); }
        if (part == 1) { const bool ones = (l & 16) && (l & 3);
            vb0 = KV0 + VOFF + (4 * h + ((l & 15) >> 2)) * VP + (ones ? 160 : hp * 80 + (l & 16) * 2 + (l & 3) * 8); vb1 = vb0 + (ones ? 0 : 40);
            asm volatile("" : "+v"(vb0), "+v"(vb1)); }
        if (part == 2) { qb1 = QST + hp * 10240 + h * 2048 + (qi * 32 + q) * 16; qb2 = QST + hp * 10240 + 4096 + (qi * 32 + q) * 8;
            obase = (unsigned)(qi * 40960 + hp * 20480 + hg * 2560 + q * 16 + h * 512);
            asm volatile("" : "+v"(qb1), "+v"(qb2), "+v"(obase)); }
        if (part >= 3) {
#pragma unroll
            for (int r = 4 * (part - 3); r < 4 * (part - 2); ++r) mask_b(cd, r, false);
            asm volatile("" : "+v"(cd)); } };
    auto wo_setup = [&](int q) {
        int l = tid; asm volatile("" : "+v"(l)); const int c = q * 64 + (l & 63), rho = __umul24(c, 6554) >> 16;
        oo[q] = (unsigned)(__umul24(qo * 160 + hp * 40 + (rho & ~7) + 4 * (rho & 1) + ((rho >> 1) & 3), 1280) + hg * 160 + (c - rho * 10) * 16);
        asm volatile("" : "+v"(oo[q])); };
    u64 tag;

    if (wid < 4) {
        if (!isA) WAITV(0);
        BAR_L();
        STAMP(1);
        bf16x8 xr[32], xt[2], wf[WIN + 1];
        f32x16 acc[5];
        const int tok = wid * 32 + r32;
        const int xa = XS0 + tok * 128 + ((hl ^ ((tok >> 1) & 7)) << 4);
        int wa0 = WS0 + (8 * hl + ((lane & 15) >> 2)) * 320 + (lane & 16) * 2 + (lane & 3) * 8, wa1 = wa0 + 61440;
        asm volatile("" : "+v"(wa0), "+v"(wa1));
        auto rdx = [&](int i) { return *(LAS bf16x8*)(smem + ((i >> 2) % 3) * 16384 + (xa ^ ((i & 3) * 32))); };
        auto rdw = [&](int f) { const int i = f / 5, t = f % 5, sl = (i >> 1) % 7;
            return trfrag(smem + (sl < 6 ? wa0 + sl * 10240 : wa1) + (i & 1) * 5120 + t * 64, 1280); };
        unsigned fl = 0;
        auto rdflag = [&](int u) { fl = __hip_atomic_load((LAS unsigned*)(smem + C_F + u * 4), RLX_WG); };
        auto check = [&](int u) {
            if (__builtin_amdgcn_readfirstlane(fl) != 15) {
#pragma unroll 1
                for (unsigned spins = 0; spins < LSPIN_CAP; ++spins) { rdflag(u); if (__builtin_amdgcn_readfirstlane(fl) == 15) break; __builtin_amdgcn_s_sleep(1); }
            }
            FENCE(); };
#pragma unroll
        for (int i = 0; i < 4; ++i) xr[i] = rdx(i);
#pragma unroll
        for (int f = 0; f < WIN; ++f) wf[f] = rdw(f);
        SB();
        SFOR(f, 200)
            constexpr int i = f / 5, t = f % 5;
            if constexpr (f % 20 == 10 - WIN) lds_set(smem + C_P + wid * 4, (i >> 1) + 1);
            if constexpr (f == 200 - WIN) lds_set(smem + C_P + wid * 4, 20);
            if constexpr (f % 20 == 10) rdflag(f / 20 + 1);
            if constexpr (f % 20 == 20 - WIN && f < 190) check(f / 20 + 1);
            if constexpr (f + WIN < 200) wf[(f + WIN) % (WIN + 1)] = rdw(f + WIN);
            if constexpr (f % 20 >= 16 && f < 140) xr[(f / 20 + 1) * 4 + f % 20 - 16] = rdx((f / 20 + 1) * 4 + f % 20 - 16);
            if constexpr (t == 1 && i >= 31 && i < 39) xt[(i + 1) & 1] = rdx(i + 1);
            SB();
            if constexpr (i == 0) MFMA32Z(acc[t], wf[f % (WIN + 1)], xr[0]);
            else if constexpr (i < 32) MFMA32I(acc[t], wf[f % (WIN + 1)], xr[i]);
            else MFMA32I(acc[t], wf[f % (WIN + 1)], xt[i & 1]);
            SB();
        SEND
        STAMP(2);
        int l2 = tid; asm volatile("" : "+v"(l2)); l2 &= 63;
        const int h2 = l2 >> 5, tk = wid * 32 + (l2 & 31);
        int eb[5];
        auto ebases = [&](int base) { int l = tid; asm volatile("" : "+v"(l)); const int h = (l >> 5) & 1, k = wid * 32 + (l & 31);
            eb[0] = base + k * 16 + h * 8; eb[1] = base + k * 16 + h * 2040; eb[2] = base + (h ? k * 16 + 1024 : k * 8); eb[3] = base + (h ? k * 8 + 2040 : k * 16);
            asm volatile("" : "+v"(eb[0]), "+v"(eb[1]), "+v"(eb[2]), "+v"(eb[3])); };
        auto kq_addr = [&](int g0) { return eb[g0 % 5 == 1 ? 1 : g0 % 5 == 4 ? 2 : g0 % 5 == 3 ? 3 : 0] + kq_base(g0); };
        ebases(OWN);
        eb[4] = OWN + VOFF + tk * VP + h2 * 8; asm volatile("" : "+v"(eb[4]));
        int qa0 = (8 * h2 + 2 * ((l2 & 15) >> 2)) * 160 + (l2 & 16) * 2 + (l2 & 3) * 8, qa1 = qa0 + WS0 + 10240;
        asm volatile("" : "+v"(qa0), "+v"(qa1));
        auto rdq = [&](int f) { const int i = f / 3, t = f % 3, j = i >> 2, sl = (20 + j) % 7;
            return trfrag(smem + (j >= 7 ? qa0 + (j == 7 ? QU27 : j == 8 ? QU28 : QU29) : sl == 0 ? qa0 + WS0 : qa1 + (sl - 1) * 10240) + (i & 3) * 2560 + t * 64, 160); };
        MFMA_DRAIN();
        WAITV(0);
        { u32x2 pk[20];
#pragma unroll
          for (int n = 0; n < 20; ++n) { const int t = n >> 2, b = n & 3; pk[n] = (u32x2){pk2(acc[t][4 * b], acc[t][4 * b + 1]), pk2(acc[t][4 * b + 2], acc[t][4 * b + 3])}; }
          SB();
#pragma unroll
          for (int n = 0; n < 20; ++n) *(LAS u32x2*)(smem + (n < 10 ? kq_addr(2 * n) : eb[4] + (2 * n - 20) * 8)) = pk[n];
          if (h2 == 0) *(LAS u32x2*)(smem + eb[4] + 160) = (u32x2){0x3F803F80u, 0x3F803F80u}; }
        lds_set(smem + C_E + wid * 4, 1);
        check(10);
        xt[0] = rdx(32);
#pragma unroll
        for (int f = 0; f < WIN; ++f) wf[f] = rdq(f);
        STAMP(3);
        SB();
        u64 pv0 = 0, pv1 = 0;
        SFOR(f, 120)
            constexpr int i = f / 3, t = f % 3, j = i >> 2;
            if constexpr (f % 24 == 12 && j < 9) rdflag(11 + j / 2);
            if constexpr (f % 12 == 12 - WIN && (j == 1 || j == 6 || j == 8)) lds_set(smem + C_P + wid * 4, 21 + j);
            if constexpr (f % 24 == 24 - WIN && j < 9) check(11 + j / 2);
            if constexpr (f + WIN < 120) wf[(f + WIN) % (WIN + 1)] = rdq(f + WIN);
            if constexpr (t == 0 && i < 7) xt[(i + 1) & 1] = rdx(32 + i + 1);
            if constexpr (f >= 90 && f < 100 && !(f & 1)) wo_setup((f - 90) / 2);
            if constexpr (f == 99) tag = rdtag();
            if constexpr (f >= 100 && f < 107) att_setup(f - 100);
            if constexpr (f == 108) ebases(QST);
            if constexpr (f == 116) { pv0 = __hip_atomic_load((LAS u64*)(smem + C_P), RLX_WG); pv1 = __hip_atomic_load((LAS u64*)(smem + C_P + 8), RLX_WG); }
            SB();
            if constexpr (i == 0) MFMA32Z(acc[t], wf[f % (WIN + 1)], xt[0]);
            else if constexpr (i < 8) MFMA32I(acc[t], wf[f % (WIN + 1)], xt[i & 1]);
            else MFMA32I(acc[t], wf[f % (WIN + 1)], xr[i - 8]);
            SB();
        SEND
        STAMP(4);
        if (__builtin_amdgcn_readfirstlane((int)min(min((unsigned)pv0, (unsigned)(pv0 >> 32)), min((unsigned)pv1, (unsigned)(pv1 >> 32)))) < 29) wait_min4(smem + C_P, 29);
        MFMA_DRAIN();
        { acc[0] *= QSCALE; acc[1] *= QSCALE; acc[2] *= QSCALE;
          SB();
          u32x2 pk[10];
#pragma unroll
          for (int n = 0; n < 10; ++n) { const int t = n >> 2, b = n & 3; pk[n] = (u32x2){pk2(acc[t][4 * b], acc[t][4 * b + 1]), pk2(acc[t][4 * b + 2], acc[t][4 * b + 3])}; }
          SB();
#pragma unroll
          for (int n = 0; n < 10; ++n) *(LAS u32x2*)(smem + kq_addr(2 * n)) = pk[n]; }
        lds_set(smem + C_QF + wid * 4, 1);
        __builtin_amdgcn_s_setprio(1);
        STAMP(5);
    } else {
        const int hw = wid - 4;
        int known = 0;
        __builtin_amdgcn_s_setprio(3);
        auto lds_or = [&](int off, unsigned v) { FENCE(); if (lane == 0) __hip_atomic_fetch_or((LAS unsigned*)(smem + off), v, RLX_WG); FENCE(); };
        unsigned wo[5], qo[5];
        auto done = [&](int n) { lds_or(C_F + n * 4, 1u << hw); };
        const unsigned xo1 = (xo ^ 64) + 9216, xo2 = xo + 2 * 9216, xo3 = (xo ^ 64) + 3 * 9216;
        auto round_x = [&](int n) { dma16x4(xo, xo1, xo2, xo3, xb + kt_of(n) * 128 + hw * (32 * 1280), lds0 + XS0 + (n % 3) * 16384 + hw * 4096); };
        auto round_w = [&](int n) {
            const int u = 2 * n + (hw >> 1);
            const unsigned dst = lds0 + (u < 27 ? WS0 + (u % 7) * 10240 : u == 27 ? QU27 : u == 28 ? QU28 : QU29) + (hw & 1) * 5120 + 2048;
            if (u < 20) dma16x5(wo, wb + (size_t)(kt_of(u >> 1) * 64 + (u & 1) * 32) * 3840 - 2048, dst);
            else dma16x5(qo, wb + (size_t)kt_of(u < 22 ? u - 12 : u - 22) * (64 * 3840) - 2048, dst); };
        if (isA) round_x(0);
        round_x(1);
#pragma unroll
        for (int q = 0; q < 5; ++q) wo[q] = w_off(q) + (hw & 1) * (16 * 3840) + (4 - q) * 1024;
        if (isA) { round_w(0); round_w(1); WAITV(5); } else { round_w(1); WAITV(9); }
        BAR_L();
#pragma unroll
        for (int q = 0; q < 5; ++q) { const int c = q * 64 + lane, rho = __umul24(c, 6554) >> 16;
            qo[q] = (unsigned)(__umul24((hw & 1) * 32 + (rho & ~7) + 4 * (rho & 1) + ((rho >> 1) & 3), 3840) + (c - rho * 10) * 16 + (4 - q) * 1024); }
#pragma unroll 1
        for (int n = 2; n <= 13; ++n) {
            const int need = n < 13 ? 2 * n - 5 : 20;
            if (need > known) known = min4(smem + C_P);
            if (need > known) { WAITV(0); done(n - 1); known = wait_min4(smem + C_P, need); }
            if (n < 10) round_x(n);
            round_w(n);
            if (n < 10) WAITV(9); else WAITV(5);
            done(n - 1);
        }
        WAITV(0); done(13);
        STAMP(1);
        tag = rdtag();
        auto a_chunk = [&](int a) { int o;
            if (a < 320) { const int hd = a / 80, rem = a % 80; o = hd * 5120 + (rem < 32 ? (96 + rem) * 16 : rem < 64 ? 2048 + (64 + rem) * 16 : 4096 + 768 + (rem - 64) * 16); }
            else o = VOFF + 96 * VP + (a - 320) * 16;
            return o; };
        if (rt < 15) {
            const __amdgpu_buffer_rsrc_t kvr = __builtin_amdgcn_make_buffer_rsrc(ws + WS_KV + me * KVG, 0, KVB, 0x00020000);
            wait_min4(smem + C_E, 1);
            STAMP(2);
            if (!isA) { u32x4 v[11];
#pragma unroll
                for (int j = 0; j < 11; ++j) if (j < 10 || hw == 3) v[j] = *(LAS u32x4*)(smem + OWN + hw * 10240 + j * 1024 + lane * 16);
#pragma unroll
                for (int j = 0; j < 11; ++j) if (j < 10 || hw == 3) __builtin_amdgcn_raw_buffer_store_b128(v[j], kvr, hw * 10240 + j * 1024 + lane * 16, 0, 16);
            } else {
#pragma unroll
                for (int j = 0; j < 3; ++j) { const int a = (4 * j + hw) * 64 + lane;
                    if (a < 656) { const int o = a_chunk(a); __builtin_amdgcn_raw_buffer_store_b128(*(LAS u32x4*)(smem + OWN + o), kvr, o, 0, 16); } }
            }
            STAMP(3);
        }
        wait_min4(smem + C_P, 22);
        round_w(14);
        WAITV(5);
        if (rt < 15) {
            unsigned old = 0;
            if (lane == 0) old = __hip_atomic_fetch_add((LAS unsigned*)(smem + N_PUB), 1u, RLX_WG);
            if (lane == 0 && old == 3) __hip_atomic_store((GAS u64*)(ws + WS_FKV + me * 128), tag, RLX_AGENT);
            STAMP(4);
        }
        WAITV(0); done(14);
        __builtin_amdgcn_s_setprio(0);
#pragma unroll
        for (int p = 0; p < 7; ++p) att_setup(p);
#pragma unroll
        for (int q = 0; q < 5; ++q) wo_setup(q);
        __builtin_amdgcn_s_setprio(3);
        for (int i = 0; i < hw; ++i) __builtin_amdgcn_s_sleep(PSTAG);
        poll_shared((GAS u64*)(ws + WS_FKV + (me - 8 * (lane + 1)) * 128), lane < nnb, tag, smem + N_KV);
        STAMP(5);
        if (!isA) {
            const int n = hw >> 1, half = hw & 1;
            if (n < nnb) {
                const char* src = ws + WS_KV + (me - 8 * (n + 1)) * KVG + half * 20480; const unsigned dst = lds0 + KV0 + (1 - n) * KVB + half * 20480;
                wait_min4(smem + C_P, 27);
#pragma unroll
                for (int j = 0; j < 4; ++j) dma16x5_sc1(lane * 16, src + j * 5120 + 2048, dst + j * 5120 + 2048);
                if (half) dma16_sc1(lane * 16, src + 20480, dst + 20480);
            }
        } else if (nnb) {
            const __amdgpu_buffer_rsrc_t nbr = __builtin_amdgcn_make_buffer_rsrc(ws + WS_KV + (me - 8) * KVG, 0, KVB, 0x00020000);
            u32x4 v[3];
#pragma unroll
            for (int j = 0; j < 3; ++j) { const int a = (4 * j + hw) * 64 + lane; if (a < 656) v[j] = __builtin_amdgcn_raw_buffer_load_b128(nbr, a_chunk(a), 0, 16); }
            wait_min4(smem + C_P, 27);
#pragma unroll
            for (int j = 0; j < 3; ++j) { const int a = (4 * j + hw) * 64 + lane; if (a < 656) *(LAS u32x4*)(smem + KV0 + KVB + a_chunk(a)) = v[j]; }
        }
        __builtin_amdgcn_s_setprio(2);
        wait_word(smem + C_QF + qi * 4, 1);
        STAMP(6);
    }
    u32x4 wreg[13];
    auto ldwo = [&](int i) { wreg[i] = __builtin_amdgcn_raw_buffer_load_b128(wor, oo[i % 5] + (i / 5) * (32 * 1280) + (i > 6 || (i == 6 && lane >= 16) ? 40 * 1280 : 0), 0, 0); };
    STAMP(7);
    u32x2 ob[2][3];
    {
        bf16x8 q1[2]; u32x2 q2[2];
#pragma unroll
        for (int e = 0; e < 2; ++e) {
            q1[e] = *(LAS bf16x8*)(smem + qb1 + e * 5120);
            q2[e] = *(LAS u32x2*)(smem + qb2 + e * 5120);
            if (hl) q2[e] = (u32x2){0u, 0u};
        }
        if (isA) mask_a(0);
        struct KF { bf16x8 a; s16x4 b; };
        struct VF { bf16x8 a, b; };
        auto rdk = [&](int u, int e) { const int so = (u >> 2) * KVB, tw = u & 3; KF k;
            k.a = *(LAS bf16x8*)(smem + kb1 + so + tw * 512 + e * 5120); k.b = *(LAS s16x4*)(smem + __umul24(so + tw * 256 + e * 5120, nh) + kb2); return k; };
        auto rdv = [&](int u, int e) { LAS char* p = smem + (e ? vb1 : vb0) + (u >> 2) * KVB + (u & 3) * (32 * VP); VF v;
            v.a = trfrag(p, 8 * VP); v.b = trfrag(p + 16 * VP, 8 * VP); return v; };
        auto qk = [&](const KF& k, int e, const f32x16& c) { return MFMA32K8(k.b, __builtin_bit_cast(s16x4, q2[e]), MFMA32(k.a, q1[e], c)); };
        const f32x16 zero = {};
        auto max16 = [&](const f32x16& s, float m) {
#pragma unroll
            for (int r = 0; r < 16; ++r) m = __builtin_elementwise_maximum(m, s[r]);
            return m; };
        auto set_ref = [&](float m, int e) { const unsigned nb = pk2(-m, 0.f); if (hl) q2[e][0] = nb; };
        auto ex4 = [&](f32x16& s, int b) {
#pragma unroll
            for (int r = 0; r < 4; ++r) s[b + r] = __builtin_amdgcn_exp2f(s[b + r]); };
        auto pk8 = [&](const f32x16& s, int b) { return __builtin_bit_cast(bf16x8, (u32x4){pk2(s[b], s[b + 1]), pk2(s[b + 2], s[b + 3]), pk2(s[b + 4], s[b + 5]), pk2(s[b + 6], s[b + 7])}); };
        auto phase = [&](auto nx, f32x16& cur, const VF& v, f32x16& o, f32x16& nxt, const KF& k, int e) __attribute__((always_inline)) {
            constexpr int NX = decltype(nx)::value;
            SB(); ex4(cur, 0); SB();
            if constexpr (NX) nxt = MFMA32(k.a, q1[e], NX == 1 ? zero : NX == 2 ? cf : cd);
            SB(); ex4(cur, 4); { const bf16x8 p = pk8(cur, 0); SB(); o = MFMA32(v.a, p, o); }
            SB(); ex4(cur, 8); SB();
            if constexpr (NX) nxt = MFMA32K8(k.b, __builtin_bit_cast(s16x4, q2[e]), nxt);
            SB(); ex4(cur, 12); { const bf16x8 p = pk8(cur, 8); SB(); o = MFMA32(v.b, p, o); }
            SB(); };
        f32x16 o0, o1, sa, sb; KF kx; VF vx;
        auto step = [&](auto m0, auto m1, int u) __attribute__((always_inline)) {
            constexpr int M1 = decltype(m1)::value;
            KF ky; if constexpr (M1) ky = rdk(u - 1, 0);
            const VF vy = rdv(u, 1);
            phase(m0, sa, vx, o0, sb, kx, 1);
            if constexpr (M1) { kx = rdk(u - 1, 1); vx = rdv(u - 1, 0); }
            phase(m1, sb, vy, o1, sa, ky, 0); };
        auto nbwait = [&](int u) { if (u == 8) wait_word(smem + N_NB, 4); };
        { const KF k0f = rdk(ud, 0); kx = rdk(ud, 1); vx = rdv(ud, 0);
          const f32x16 s0 = qk(k0f, 0, cd), s1 = qk(kx, 1, cd);
          set_ref(halfmax(max16(s0, s0[0])), 0); set_ref(halfmax(max16(s1, s1[0])), 1);
          sa = qk(k0f, 0, cd); }
        if (hp) { WAITV(0); lds_inc0(smem + N_NB, lane); lds_inc0(smem + N_G0, lane); }
        o0 = zero; o1 = zero;
        if (u0 + 8 == ud) {
            SFOR(it, 9)
                if constexpr (it < 7) { ldwo(2 * it); if constexpr (it < 6) ldwo(2 * it + 1); }
                if constexpr (it == PFLIP) { if (hp) __builtin_amdgcn_s_setprio(0); }
                if constexpr (it == 7) {
#pragma unroll
                    for (int r = 0; r < 16; ++r) mask_b(cf, r, true); }
                if constexpr (it < 4) nbwait(ud - it);
                step(IC<it == 0 ? 3 : it == 8 ? 2 : 1>{}, IC<it == 8 ? 0 : it == 7 ? 2 : 1>{}, ud - it);
            SEND
        } else {
            __builtin_amdgcn_s_setprio(0);
#pragma unroll
            for (int i = 0; i < 13; ++i) ldwo(i);
            if (!isA) cf = zero;
            if (u0 < ud) {
                nbwait(ud);
                if (u0 + 1 < ud) {
                    step(IC<3>{}, IC<1>{}, ud);
#pragma unroll 1
                    for (int u = ud - 1; u > u0 + 1; --u) { nbwait(u); step(IC<1>{}, IC<1>{}, u); }
                    nbwait(u0 + 1);
                    step(IC<1>{}, IC<2>{}, u0 + 1);
                } else step(IC<3>{}, IC<2>{}, ud);
                step(IC<2>{}, IC<0>{}, u0);
            } else step(IC<3>{}, IC<0>{}, ud);
        }
        if (!__all(o0[12] < 1.152921504606847e18f && o1[12] < 1.152921504606847e18f)) {
            if (isA) { int z = 0; asm volatile("" : "+s"(z)); mask_a(z); }
            else { asm volatile("" : "+v"(dsh));
#pragma unroll
                for (int r = 0; r < 16; ++r) { mask_b(cd, r, false); mask_b(cf, r, true); }
                if (!far) cf = zero; }
#pragma unroll
            for (int e = 0; e < 2; ++e) {
                if (hl) q2[e][0] = 0u;
                const f32x16 s = qk(rdk(ud, e), e, cd);
                float m = max16(s, s[0]);
                if (u0 < ud) m = max16(qk(rdk(u0, e), e, cf), m);
#pragma unroll 1
                for (int u = u0 + 1; u < ud; ++u) m = max16(qk(rdk(u, e), e, zero), m);
                set_ref(halfmax(m), e);
            }
#pragma unroll
            for (int e = 0; e < 2; ++e) {
                f32x16 o = zero;
#pragma unroll 1
                for (int u = u0; u <= ud; ++u) { f32x16 t = u == ud ? qk(rdk(u, e), e, cd) : u == u0 ? qk(rdk(u, e), e, cf) : qk(rdk(u, e), e, zero);
                    const VF v = rdv(u, e); ex4(t, 0); ex4(t, 4); ex4(t, 8); ex4(t, 12);
                    o = MFMA32(v.a, pk8(t, 0), o); o = MFMA32(v.b, pk8(t, 8), o); }
                if (e) o1 = o; else o0 = o;
            }
        }
        const float rl0 = __builtin_amdgcn_rcpf(o0[12]), rl1 = __builtin_amdgcn_rcpf(o1[12]);
        SB();
#pragma unroll
        for (int r = 0; r < 12; ++r) { o0[r] *= rl0; o1[r] *= rl1; }
        SB();
#pragma unroll
        for (int j = 0; j < 3; ++j) {
            ob[0][j] = (u32x2){pk2(o0[4 * j], o0[4 * j + 1]), pk2(o0[4 * j + 2], o0[4 * j + 3])};
            ob[1][j] = (u32x2){pk2(o1[4 * j], o1[4 * j + 1]), pk2(o1[4 * j + 2], o1[4 * j + 3])}; }
        SB();
    }
    STAMP(8);
    const __amdgpu_buffer_rsrc_t orr = __builtin_amdgcn_make_buffer_rsrc(ws + WS_O + (br * 16 + rt) * OG, 0, 4 * 40960, 0x00020000);
    {
      { auto rx = __builtin_amdgcn_permlane32_swap(ob[0][0][0], ob[0][1][0], false, false);
        auto ry = __builtin_amdgcn_permlane32_swap(ob[0][0][1], ob[0][1][1], false, false);
        __builtin_amdgcn_raw_buffer_store_b128((u32x4){rx[0], ry[0], rx[1], ry[1]}, orr, obase, 0, 16); }
      if (hl == 0) __builtin_amdgcn_raw_buffer_store_b128((u32x4){ob[0][2][0], ob[0][2][1], ob[1][0][0], ob[1][0][1]}, orr, obase + 1024, 0, 16);
      { auto fx = __builtin_amdgcn_permlane32_swap(ob[1][0][0], ob[1][1][0], false, false);
        auto fy = __builtin_amdgcn_permlane32_swap(ob[1][0][1], ob[1][1][1], false, false);
        auto sx = __builtin_amdgcn_permlane32_swap(ob[1][1][0], ob[1][2][0], false, false);
        auto sy = __builtin_amdgcn_permlane32_swap(ob[1][1][1], ob[1][2][1], false, false);
        __builtin_amdgcn_raw_buffer_store_b128((u32x4){fx[1], fy[1], sx[0], sy[0]}, orr, obase + 1536, 0, 16); } }
    if (lane == 0) { (void)__hip_atomic_fetch_add((LAS unsigned*)(smem + N_G1), 1u, RLX_WG);
        if (!hp) (void)__hip_atomic_fetch_add((LAS unsigned*)(smem + N_G0), 1u, RLX_WG); }
    FENCE();
    STAMP(6);
    auto signal_o = [&]() {
        if (lds_inc(smem + N_OS + hp * 4, lane) == 3 && lane == 0) __hip_atomic_store((GAS u64*)(ws + WS_FO + ((me >> 3) * 2 + hp) * 128 + hg * 8), tag, RLX_AGENT); };
    __builtin_amdgcn_s_setprio(0);
    WAITV(0); signal_o();
    STAMP(9);
    wait_word(smem + (hp ? N_G1 : N_G0), 8);
    const int p1 = hp ? 30720 : 35840, p2 = hp ? 65536 : 43520;
    const int wb0 = hp ? KV0 + 10240 : 512, wb1 = hp ? KV0 + KVB + 10240 - 30720 : KV0 - 35840, wb2 = KV0 + KVB - 43520;
#pragma unroll
    for (int i = 0; i < 13; ++i) { const int L = qo * 12800 + i * 1024;
        if (i < 12 || lane < 32) *(LAS u32x4*)(smem + (L + (L < p1 ? wb0 : L < p2 ? wb1 : wb2)) + lane * 16) = wreg[i]; }
    lds_inc0(smem + N_WO + hp * 4, lane);
    STAMP(10);
    __builtin_amdgcn_s_sleep(PDELAY);
    for (int i = 0; i < qo; ++i) __builtin_amdgcn_s_sleep(PSTAG);
    poll_shared((GAS u64*)(ws + WS_FO + ((me >> 3) * 2 + hp) * 128 + (lane & 7) * 8), lane < 8, tag, smem + N_OK + hp * 4);
    STAMP(11);
    f32x16 oc[3]; f32x4 ps[10];
    LAS char* red = smem + OWN + qo * 10240 + lane * 16;
    { bf16x8 af[5], of[20];
      const int la = (8 * hl + 2 * ((lane & 15) >> 2)) * 160 + (lane & 16) * 2 + (lane & 3) * 8;
      const int aa0 = wb0 + la, aa1 = (hp ? wb1 : wb0) + la, aa2 = wb1 + la, aa3 = (hp ? wb1 : wb2) + la;
      auto rda = [&](int f) { return trfrag(smem + (f < 36 ? aa0 : f < 42 ? aa1 : f < 51 ? aa2 : aa3) + (f / 3) * 2560 + (f % 3) * 64, 160); };
      auto ldo = [&](int k) { of[k] = __builtin_bit_cast(bf16x8, __builtin_amdgcn_raw_buffer_load_b128(orr, qo * 40960 + (hp * 20 + k) * 1024 + lane * 16, 0, 16)); };
#pragma unroll
      for (int k = 0; k < OPF; ++k) ldo(k);
      wait_word(smem + N_WO + hp * 4, 4);
#pragma unroll
      for (int f = 0; f < 4; ++f) af[f] = rda(f);
      SB();
      SFOR(f, 60)
          if constexpr (f % 3 == 0 && f / 3 + OPF < 20) ldo(f / 3 + OPF);
          if constexpr (f + 4 < 60) af[(f + 4) % 5] = rda(f + 4);
          if constexpr (f == 57) { if (hp) { wait_word(smem + C_RD + qo * 4, 1);
#pragma unroll
              for (int b = 0; b < 10; ++b) ps[b] = *(LAS f32x4*)(red + b * 1024); } }
          SB();
          if constexpr (f < 3) MFMA32Z(oc[f], af[f % 5], of[0]); else MFMA32I(oc[f % 3], af[f % 5], of[f / 3]);
          SB();
      SEND }
    MFMA_DRAIN();
    STAMP(12);
    { if (!hp) {
          wait_word(smem + N_G1, 8);
#pragma unroll
          for (int b = 0; b < 10; ++b) *(LAS f32x4*)(red + b * 1024) = (f32x4){oc[b >> 2][4 * (b & 3)], oc[b >> 2][4 * (b & 3) + 1], oc[b >> 2][4 * (b & 3) + 2], oc[b >> 2][4 * (b & 3) + 3]};
          lds_set(smem + C_RD + qo * 4, 1);
      } else {
          char* orow = out + (size_t)(rt * 128 + qo * 32 + r32) * 2560 + br * 1280 + hg * 160 + hl * 16;
#pragma unroll
          for (int b = 0; b < 10; ++b)
#pragma unroll
              for (int r = 0; r < 4; ++r) ps[b][r] += oc[b >> 2][4 * (b & 3) + r];
          SB();
          u32x2 pk[10];
#pragma unroll
          for (int b = 0; b < 10; ++b) pk[b] = (u32x2){pk2(ps[b][0], ps[b][1]), pk2(ps[b][2], ps[b][3])};
          SB();
          u32x4 st[5];
#pragma unroll
          for (int b = 0; b < 10; b += 2) {
              auto rx = __builtin_amdgcn_permlane32_swap(pk[b][0], pk[b + 1][0], false, false); auto ry = __builtin_amdgcn_permlane32_swap(pk[b][1], pk[b + 1][1], false, false);
              st[b >> 1] = (u32x4){rx[0], ry[0], rx[1], ry[1]}; }
          SB();
#pragma unroll
          for (int b = 0; b < 5; ++b) *(u32x4*)(orow + b * 32) = st[b];
      } }
    if (wid >= 4) { WAITV(0); const unsigned te = (unsigned)__builtin_amdgcn_s_memrealtime();
        if (lane == 0) { GAS unsigned* d = (GAS unsigned*)(ws + WS_DBG + (bx * 4 + qo) * 64);
            __hip_atomic_store(d + 0, dst_[0], RLX_AGENT); __hip_atomic_store(d + 1, dst_[8], RLX_AGENT); __hip_atomic_store(d + 2, dst_[9], RLX_AGENT);
            __hip_atomic_store(d + 3, dst_[11], RLX_AGENT); __hip_atomic_store(d + 4, dst_[12], RLX_AGENT); __hip_atomic_store(d + 5, te, RLX_AGENT);
            WAITV(0); __hip_atomic_store((GAS u64*)(d + 6), tag, RLX_AGENT); } }
    if (bx == 0 && wid == 0) {
#pragma unroll 1
        for (int i = 0; i < 4; ++i) poll_flags((GAS u64*)(ws + WS_FO + (i * 8 + (lane >> 3)) * 256 + (lane & 7) * 8), true, tag);
        if (lane == 0) __hip_atomic_store((GAS u64*)ws, tag, RLX_AGENT);
        {
            for (int i = 0; i < 40; ++i) __builtin_amdgcn_s_sleep(8);
            for (int i = 0; i < 16; ++i) poll_flags((GAS u64*)(ws + WS_DBG + (i * 64 + lane) * 64 + 24), true, tag);
            const unsigned now = (unsigned)__builtin_amdgcn_s_memrealtime();
            int mn0 = 0x7fffffff, mx = -0x7fffffff;
            for (int i = 0; i < 16; ++i) { GAS unsigned* d = (GAS unsigned*)(ws + WS_DBG + (i * 64 + lane) * 64);
                const unsigned t0 = __hip_atomic_load(d, RLX_AGENT), t = __hip_atomic_load(d + DIAG % 6, RLX_AGENT);
                mn0 = min(mn0, (int)(t0 - now));
                if ((i < 8) == (DIAG < 6)) mx = max(mx, (int)(t - now)); }
            for (int m = 32; m >= 1; m >>= 1) { mn0 = min(mn0, __shfl_xor(mn0, m)); mx = max(mx, __shfl_xor(mx, m)); }
            int M = min(max(mx - mn0, 0), 5000);
            const unsigned until = now + mn0 + 4000 + M;
            while ((int)((unsigned)__builtin_amdgcn_s_memrealtime() - until) < 0) __builtin_amdgcn_s_sleep(2);
        }
    }
}

extern "C" void kernel_launch(void* const* d_in, const int* in_sizes, int n_in, void* d_out, int out_size, void* d_ws, size_t ws_size,
                              hipStream_t stream) {
    fused_attn_kernel<<<256, 512, 0, stream>>>((const char*)d_in[0], (const char*)d_in[1], (const char*)d_in[2], (const char*)d_in[3],
                                               (const char*)d_in[4], (const char*)d_in[5], (char*)d_out, (char*)d_ws);
}
```

```cpp
#define ADELAY 0
#define NPOLL 1
#define PDELAY 8
#define PSLEEP 4
#include <hip/hip_runtime.h>
#include <hip/hip_bf16.h>
#include <stdint.h>

typedef short bf16x8 __attribute__((ext_vector_type(8)));
typedef short s16x4 __attribute__((ext_vector_type(4)));
typedef float f32x2 __attribute__((ext_vector_type(2)));
typedef float f32x4 __attribute__((ext_vector_type(4)));
typedef float f32x16 __attribute__((ext_vector_type(16)));
typedef unsigned u32x4 __attribute__((ext_vector_type(4)));
typedef unsigned u32x2 __attribute__((ext_vector_type(2)));
typedef __bf16 bfx2 __attribute__((ext_vector_type(2)));
typedef unsigned long long u64;
#define LAS __attribute__((address_space(3)))
#define GAS __attribute__((address_space(1)))
#define DI __device__ __forceinline__
#define RLX_AGENT __ATOMIC_RELAXED, __HIP_MEMORY_SCOPE_AGENT
#define RLX_WG __ATOMIC_RELAXED, __HIP_MEMORY_SCOPE_WORKGROUP

constexpr int LDS_BYTES = 163840;
constexpr int VOFF = 20480, VP = 168;
constexpr int KVB = VOFF + 128 * VP;
constexpr int KV0 = LDS_BYTES - 3 * KVB;
constexpr int OWN = KV0 + 2 * KVB;
constexpr int C_F = 0, C_P = 128, C_E = 144, C_QF = 160, C_RD = 176, C_CNT = 192, C_TAG = 320;
constexpr int N_PUB = C_CNT, N_NB = C_CNT + 4, N_G0 = C_CNT + 8, N_G1 = C_CNT + 12, N_WO = C_CNT + 16, N_OS = C_CNT + 24, N_KV = C_CNT + 32, N_OK = C_CNT + 36;
constexpr int XS0 = 512, WS0 = XS0 + 3 * 16384, QST = 512, ONE = 336, QU27 = XS0 + 16384, QU28 = XS0, QU29 = QU27 + 10240;
static_assert(WS0 + 7 * 10240 <= OWN, "ring overlaps the own image");
constexpr unsigned WS_FKV = 4096, WS_FO = WS_FKV + 256 * 128, WS_KV = 131072;
constexpr unsigned KVG = KVB, OG = 4 * 40960;
constexpr unsigned WS_O = WS_KV + 256 * KVG;
constexpr unsigned WS_END = WS_O + 32 * OG;
constexpr unsigned WS_DBG = WS_END;
constexpr unsigned SPIN_CAP = 400000, LSPIN_CAP = 4000000;
constexpr float QSCALE = 0.22360679774997896f * 1.4426950408889634f;
#define PSTAG 3
#define PFLIP 6
#define OPF 12
#define ABL4N 2
#define WIN 4

#define MFMA32(a, b, c) __builtin_amdgcn_mfma_f32_32x32x16_bf16((a), (b), (c), 0, 0, 0)
#define MFMA32K8(a, b, c) __builtin_amdgcn_mfma_f32_32x32x8bf16_1k((a), (b), (c), 0, 0, 0)
#define MFMA32I(acc, a, b) asm volatile("v_mfma_f32_32x32x16_bf16 %0, %1, %2, %0" : "+v"(acc) : "v"(a), "v"(b))
#define MFMA32Z(acc, a, b) asm volatile("v_mfma_f32_32x32x16_bf16 %0, %1, %2, 0" : "=&v"(acc) : "v"(a), "v"(b))
#define MFMA_DRAIN() asm volatile("s_nop 7\n\ts_nop 4" ::: "memory")
#define WAITV(N) asm volatile("s_waitcnt vmcnt(" #N ")" ::: "memory")
#define WAITL() asm volatile("s_waitcnt lgkmcnt(0)" ::: "memory")
#define BAR_L() asm volatile("s_waitcnt lgkmcnt(0)\n\ts_barrier" ::: "memory")
#define SB() __builtin_amdgcn_sched_barrier(0)
#define FENCE() asm volatile("" ::: "memory")

#define STAMP(i) do {} while (0)

template <int V> struct IC { static constexpr int value = V; };
template <int I, int N, class F> DI void static_for(F&& f) { if constexpr (I < N) { f(IC<I>{}); static_for<I + 1, N>(f); } }
#define SFOR(var, N) static_for<0, N>([&](auto ic_##var) __attribute__((always_inline)) { constexpr int var = decltype(ic_##var)::value;
#define SEND });

DI void dma16(unsigned voff, const char* sbase, unsigned ldsaddr) {
    asm volatile("s_mov_b32 m0, %2\n\ts_nop 0\n\tglobal_load_lds_dwordx4 %0, %1" :: "v"(voff), "s"(sbase), "s"(ldsaddr) : "memory");
}
DI void dma16x4(unsigned v0, unsigned v1, unsigned v2, unsigned v3, const char* sbase, unsigned ldsaddr) {
    asm volatile("s_mov_b32 m0, %5\n\ts_nop 0\n\tglobal_load_lds_dwordx4 %0, %4\n\tglobal_load_lds_dwordx4 %1, %4 offset:1024\n\t"
                 "global_load_lds_dwordx4 %2, %4 offset:2048\n\tglobal_load_lds_dwordx4 %3, %4 offset:3072"
                 :: "v"(v0), "v"(v1), "v"(v2), "v"(v3), "s"(sbase), "s"(ldsaddr) : "memory");
}
DI void dma16x5(const unsigned (&v)[5], const char* sbase, unsigned ldsaddr) {
    asm volatile("s_mov_b32 m0, %6\n\ts_nop 0\n\tglobal_load_lds_dwordx4 %0, %5 offset:-2048\n\tglobal_load_lds_dwordx4 %1, %5 offset:-1024\n\t"
                 "global_load_lds_dwordx4 %2, %5\n\tglobal_load_lds_dwordx4 %3, %5 offset:1024\n\tglobal_load_lds_dwordx4 %4, %5 offset:2048"
                 :: "v"(v[0]), "v"(v[1]), "v"(v[2]), "v"(v[3]), "v"(v[4]), "s"(sbase), "s"(ldsaddr) : "memory");
}
DI void dma16x5_sc1(unsigned voff, const char* sbase, unsigned ldsaddr) {
    asm volatile("s_mov_b32 m0, %2\n\ts_nop 0\n\tglobal_load_lds_dwordx4 %0, %1 offset:-2048 sc1\n\tglobal_load_lds_dwordx4 %0, %1 offset:-1024 sc1\n\t"
                 "global_load_lds_dwordx4 %0, %1 sc1\n\tglobal_load_lds_dwordx4 %0, %1 offset:1024 sc1\n\tglobal_load_lds_dwordx4 %0, %1 offset:2048 sc1"
                 :: "v"(voff), "s"(sbase), "s"(ldsaddr) : "memory");
}
DI void dma16_sc1(unsigned voff, const char* sbase, unsigned ldsaddr) {
    asm volatile("s_mov_b32 m0, %2\n\ts_nop 0\n\tglobal_load_lds_dwordx4 %0, %1 sc1" :: "v"(voff), "s"(sbase), "s"(ldsaddr) : "memory");
}
DI void dma4(const char* p, unsigned ldsaddr) {
    asm volatile("s_mov_b32 m0, %1\n\ts_nop 0\n\tglobal_load_lds_dword %0, off" :: "v"(p), "s"(ldsaddr) : "memory");
}
DI unsigned pk2(float lo, float hi) { f32x2 v = {lo, hi}; return __builtin_bit_cast(unsigned, __builtin_convertvector(v, bfx2)); }
typedef short v4i16_t __attribute__((ext_vector_type(4)));
DI bf16x8 trfrag(LAS char* p, int second) {
    s16x4 a = __builtin_bit_cast(s16x4, __builtin_amdgcn_ds_read_tr16_b64_v4i16((LAS v4i16_t*)p));
    s16x4 b = __builtin_bit_cast(s16x4, __builtin_amdgcn_ds_read_tr16_b64_v4i16((LAS v4i16_t*)(p + second)));
    return __builtin_shufflevector(a, b, 0, 1, 2, 3, 4, 5, 6, 7);
}
constexpr u64 lanes_cut(int r, bool fartile) {
    u64 m = 0;
    for (int l = 0; l < 64; ++l) { const int key = (r & 3) + 8 * (r >> 2) + 4 * (l >> 5), d = (l & 31) - key + (fartile ? 32 : 0);
        const bool ok = d >= 0 && d <= 14 && !(d & 1);
        if (!ok) m |= 1ull << l; }
    return m;
}
DI float halfmax(float m) { auto rr = __builtin_amdgcn_permlane32_swap(__float_as_uint(m), __float_as_uint(m), false, false);
    return __builtin_elementwise_maximum(__uint_as_float(rr[0]), __uint_as_float(rr[1])); }

DI void poll_flags(GAS u64* word, bool active, u64 tag) {
#pragma unroll 1
    for (unsigned spins = 0; spins < SPIN_CAP; ++spins) {
        u64 v = tag;
        if (active) v = __hip_atomic_load(word, RLX_AGENT);
        if (__all(v == tag)) break;
        __builtin_amdgcn_s_sleep(PSLEEP);
    }
    __builtin_amdgcn_fence(__ATOMIC_ACQUIRE, "wavefront");
}
DI void poll_shared(GAS u64* word, bool active, u64 tag, LAS char* ok) {
#pragma unroll 1
    for (unsigned spins = 0; spins < SPIN_CAP; ++spins) {
        if (__builtin_amdgcn_readfirstlane((int)__hip_atomic_load((LAS unsigned*)ok, RLX_WG))) break;
        u64 v = tag;
        if (active) v = __hip_atomic_load(word, RLX_AGENT);
        if (__all(v == tag)) { __hip_atomic_store((LAS unsigned*)ok, 1u, RLX_WG); break; }
        __builtin_amdgcn_s_sleep(PSLEEP);
    }
    __builtin_amdgcn_fence(__ATOMIC_ACQUIRE, "wavefront");
    asm volatile("" ::: "memory");
}
DI int min4(LAS char* p) {
    const u64 a = __hip_atomic_load((LAS u64*)p, RLX_WG), b = __hip_atomic_load((LAS u64*)(p + 8), RLX_WG);
    const unsigned m = min(min((unsigned)a, (unsigned)(a >> 32)), min((unsigned)b, (unsigned)(b >> 32)));
    return __builtin_amdgcn_readfirstlane((int)m);
}
DI int wait_min4(LAS char* p, int need) {
    int v = min4(p);
#pragma unroll 1
    for (unsigned spins = 0; v < need && spins < LSPIN_CAP; ++spins) { __builtin_amdgcn_s_sleep(1); v = min4(p); }
    FENCE();
    return v;
}
DI void wait_word(LAS char* p, unsigned need) {
#pragma unroll 1
    for (unsigned spins = 0; spins < LSPIN_CAP; ++spins) {
        if ((unsigned)__builtin_amdgcn_readfirstlane((int)__hip_atomic_load((LAS unsigned*)p, RLX_WG)) >= need) break;
        __builtin_amdgcn_s_sleep(1); }
    FENCE();
}
DI void lds_inc0(LAS char* p, int lane) { FENCE(); if (lane == 0) (void)__hip_atomic_fetch_add((LAS unsigned*)p, 1u, RLX_WG); FENCE(); }
DI void wait_pair(LAS char* p, unsigned need) {
#pragma unroll 1
    for (unsigned spins = 0; spins < LSPIN_CAP; ++spins) { const u64 v = __hip_atomic_load((LAS u64*)p, RLX_WG);
        if ((unsigned)__builtin_amdgcn_readfirstlane((int)min((unsigned)v, (unsigned)(v >> 32))) >= need) break;
        __builtin_amdgcn_s_sleep(1); }
    FENCE();
}
DI unsigned lds_inc(LAS char* p, int lane) { FENCE(); unsigned old = 0; if (lane == 0) old = __hip_atomic_fetch_add((LAS unsigned*)p, 1u, RLX_WG); FENCE(); return old; }
DI void lds_set(LAS char* p, unsigned v) { FENCE(); __hip_atomic_store((LAS unsigned*)p, v, RLX_WG); FENCE(); }

__global__ __launch_bounds__(512, 2) void fused_attn_kernel(const char* __restrict__ xA, const char* __restrict__ wA, const char* __restrict__ oA,
                                                           const char* __restrict__ xB, const char* __restrict__ wB, const char* __restrict__ oB,
                                                           char* __restrict__ out, char* __restrict__ ws) {
    __shared__ __attribute__((aligned(16))) char smem_[LDS_BYTES];
    LAS char* smem = (LAS char*)smem_;
    const unsigned lds0 = (unsigned)(uintptr_t)smem;
    STAMP(0);
    asm volatile("" :: "s"(xA), "s"(wA), "s"(oA), "s"(xB), "s"(wB), "s"(oB), "s"(out), "s"(ws));
    const int tid = threadIdx.x, lane = tid & 63;
    const int wid = __builtin_amdgcn_readfirstlane(tid >> 6);
    const int bx = blockIdx.x, xcd = bx & 7, idx = bx >> 3;
    const int isA = idx >> 4, rt = (xcd >> 1) * 4 + ((idx >> 2) & 3), hg = (xcd & 1) * 4 + (idx & 3);
    const int br = isA ? 0 : 1;
    const int k0 = isA ? (((rt & 3) * 5 + 1) / 2 + ((hg & 3) * 5 + 1) / 2) % 10 : 0;
    const char* xb = (isA ? xA : xB) + (size_t)rt * (128 * 1280);
    const char* wb = (isA ? wA : wB) + hg * 160;
    const int me = (br * 16 + rt) * 8 + hg;
    const int r32 = lane & 31, hl = lane >> 5;
    auto kt_of = [&](int s) { int k = s + k0; return k >= 10 ? k - 10 : k; };
    if (isA && wid < 4) {
        const int j16 = idx & 15; const unsigned dump = lds0 + OWN + wid * 512;
        const char* wl = wB + (hg >> 2) * 640 + j16 * (4 * 3840);
        const char* xl = xB + (size_t)(rt * 128 + (hg & 3) * 32 + r32) * 1280 + hl * 128;
        auto pfx = [&](int i) { return xl + i * 256; };
        auto pfkv = [&](int i) { int n = i * 64 + lane; n = n < 400 ? n : 399; const int sl = n / 40, r = n % 40, c = r % 10;
            return wl + (sl * 64 + r / 10) * 3840 + 1280 + (c < 5 ? c * 128 : 640 + c * 128); };
        auto pfq = [&](int i) { int n = i * 64 + lane; n = n < 200 ? n : 199; int sl = n / 20 + 8; sl = sl >= 10 ? sl - 10 : sl; const int r = n % 20;
            return wl + (sl * 64 + r / 5) * 3840 + (r % 5) * 128; };
        dma4(wid & 1 ? pfx(wid >> 1) : pfkv(wid >> 1), dump);
        dma4(wid & 1 ? pfx(2 + (wid >> 1)) : pfkv(2 + (wid >> 1)), dump);
        dma4(wid < 3 ? pfkv(4 + wid) : pfx(4), dump);
        dma4(pfq(wid), dump);
        { int n = wid * 64 + lane; n = n < 200 ? n : 199; const int o = (j16 * 40 + n / 5) * 1280 + (hg >> 2) * 640 + (n % 5) * 128;
          dma4(oB + o, dump); dma4(oA + o, dump); }
    }
    if (isA && wid >= 4) { for (int i = 0; i < ADELAY; ++i) __builtin_amdgcn_s_sleep(16); }

    const unsigned xo = (unsigned)((lane >> 3) * 1280 + (((lane & 7) ^ (lane >> 4)) << 4));
    auto w_off = [&](int q) { const int c = q * 64 + lane, row = __umul24(c, 3277) >> 16, j = c - row * 20; return (unsigned)(__umul24(row, 3840) + j * 16 + (j < 10 ? 1280 : 2400)); };
    if (!isA) { const char* xs = xb + k0 * 128 + wid * (16 * 1280);
      dma16(xo, xs, lds0 + XS0 + wid * 2048);
      dma16(xo ^ 64, xs + 8 * 1280, lds0 + XS0 + wid * 2048 + 1024);
      const char* wsrc = wb + (size_t)k0 * (64 * 3840);
      const unsigned wv = w_off(wid < 5 ? wid : wid - 5);
      if (wid < 5) { dma16(wv, wsrc, lds0 + WS0 + wid * 1024); dma16(wv, wsrc + 16 * 3840, lds0 + WS0 + (5 + wid) * 1024);
          if (wid >= 3) { dma16(wv, wsrc + 32 * 3840, lds0 + WS0 + (10 + wid) * 1024); dma16(wv, wsrc + 48 * 3840, lds0 + WS0 + (15 + wid) * 1024); } }
      else { dma16(wv, wsrc + 32 * 3840, lds0 + WS0 + (5 + wid) * 1024); dma16(wv, wsrc + 48 * 3840, lds0 + WS0 + (10 + wid) * 1024); } }
    if (wid == 4 && lane < 2) dma4(ws + lane * 4, lds0 + C_TAG);
    if (tid < 80) *(LAS unsigned*)(smem + tid * 4) = 0u;
    else if (tid < 82) *(LAS unsigned*)(smem + ONE + (tid - 80) * 4) = 0x3F803F80u;
    const int nnb = isA ? (rt > 0 ? 1 : 0) : (rt > 1 ? 2 : rt);
    auto kq_base = [&](int fg) { const int hd = fg / 5, dg = fg % 5; return dg < 4 ? hd * 5120 + (dg >> 1) * 2048 + (dg & 1) * 8 : hd * 5120 + 4096; };

    auto rdtag = [&]() { const u64 t = *(LAS u64*)(smem + C_TAG);
        const u64 e = ((u64)(unsigned)__builtin_amdgcn_readfirstlane((int)(t >> 32)) << 32) | (unsigned)__builtin_amdgcn_readfirstlane((int)t);
        return e * 6364136223846793005ull + 1442695040888963407ull; };
    const __amdgpu_buffer_rsrc_t wor = __builtin_amdgcn_make_buffer_rsrc((void*)(isA ? oA : oB), 0, 640 * 1280, 0x00020000);

    const int hp = wid >> 2, qo = wid & 3, qi = hp ? 3 - qo : qo;
    const int ud = qi + 8;
    const bool far = isA ? (4 * rt + qi >= 1) : (4 * rt + qi >= 8);
    const int u0 = isA ? (far ? ud - 1 : ud) : (far ? qi : 8 - 4 * rt);
    f32x16 cd, cf;
    int dsh, kb1, kb2, nh, vb0, vb1, qb1, qb2; unsigned obase, oo[5];
    auto mask_b = [&](f32x16& c, int r, bool fartile) { const int k = (r & 3) + 8 * (r >> 2); c[r] = __int_as_float(fartile ? ((k - 1) << 23) - dsh : dsh - (k << 23)); };
    auto mask_a = [&](int z) {
      SFOR(r, 16)
          constexpr u64 ad = lanes_cut(r, false), af = lanes_cut(r, true);
          cd[r] = __builtin_amdgcn_inverse_ballot_w64(ad + z) ? -__builtin_inff() : 0.f;
          cf[r] = __builtin_amdgcn_inverse_ballot_w64(af + z) ? -__builtin_inff() : 0.f;
      SEND };
    auto att_setup = [&](int part) {
        int l = tid; asm volatile("" : "+v"(l)); l &= 63;
        const int q = l & 31, h = l >> 5;
        if (part == 0) { dsh = (q - 4 * h) << 23; kb1 = KV0 + hp * 10240 + h * 2048 + q * 16; kb2 = h ? ONE : KV0 + hp * 10240 + 4096 + q * 8; nh = h ^ 1;
            asm volatile("" : "+v"(dsh), "+v"(kb1), "+v"(kb2), "+v"(nh)); }
        if (part == 1) { const bool ones = (l & 16) && (l & 3);
            vb0 = KV0 + VOFF + (4 * h + ((l & 15) >> 2)) * VP + (ones ? 160 : hp * 80 + (l & 16) * 2 + (l & 3) * 8); vb1 = vb0 + (ones ? 0 : 40);
            asm volatile("" : "+v"(vb0), "+v"(vb1)); }
        if (part == 2) { qb1 = QST + hp * 10240 + h * 2048 + (qi * 32 + q) * 16; qb2 = QST + hp * 10240 + 4096 + (qi * 32 + q) * 8;
            obase = (unsigned)(qi * 40960 + hp * 20480 + hg * 2560 + q * 16 + h * 512);
            asm volatile("" : "+v"(qb1), "+v"(qb2), "+v"(obase)); }
        if (part >= 3) {
#pragma unroll
            for (int r = 4 * (part - 3); r < 4 * (part - 2); ++r) mask_b(cd, r, false);
            asm volatile("" : "+v"(cd)); } };
    auto wo_setup = [&](int q) {
        int l = tid; asm volatile("" : "+v"(l)); const int c = q * 64 + (l & 63), rho = __umul24(c, 6554) >> 16;
        oo[q] = (unsigned)(__umul24(qo * 160 + hp * 40 + (rho & ~7) + 4 * (rho & 1) + ((rho >> 1) & 3), 1280) + hg * 160 + (c - rho * 10) * 16);
        asm volatile("" : "+v"(oo[q])); };
    u64 tag;

    if (wid < 4) {
        if (!isA) WAITV(0);
        BAR_L();
        STAMP(1);
        bf16x8 xr[32], xt[2], wf[WIN + 1];
        f32x16 acc[5];
        const int tok = wid * 32 + r32;
        const int xa = XS0 + tok * 128 + ((hl ^ ((tok >> 1) & 7)) << 4);
        int wa0 = WS0 + (8 * hl + ((lane & 15) >> 2)) * 320 + (lane & 16) * 2 + (lane & 3) * 8, wa1 = wa0 + 61440;
        asm volatile("" : "+v"(wa0), "+v"(wa1));
        auto rdx = [&](int i) { return *(LAS bf16x8*)(smem + ((i >> 2) % 3) * 16384 + (xa ^ ((i & 3) * 32))); };
        auto rdw = [&](int f) { const int i = f / 5, t = f % 5, sl = (i >> 1) % 7;
            return trfrag(smem + (sl < 6 ? wa0 + sl * 10240 : wa1) + (i & 1) * 5120 + t * 64, 1280); };
        unsigned fl = 0;
        auto rdflag = [&](int u) { fl = __hip_atomic_load((LAS unsigned*)(smem + C_F + u * 4), RLX_WG); };
        auto check = [&](int u) {
            if (__builtin_amdgcn_readfirstlane(fl) != 15) {
#pragma unroll 1
                for (unsigned spins = 0; spins < LSPIN_CAP; ++spins) { rdflag(u); if (__builtin_amdgcn_readfirstlane(fl) == 15) break; __builtin_amdgcn_s_sleep(1); }
            }
            FENCE(); };
#pragma unroll
        for (int i = 0; i < 4; ++i) xr[i] = rdx(i);
#pragma unroll
        for (int f = 0; f < WIN; ++f) wf[f] = rdw(f);
        SB();
        SFOR(f, 200)
            constexpr int i = f / 5, t = f % 5;
            if constexpr (f % 20 == 10 - WIN) lds_set(smem + C_P + wid * 4, (i >> 1) + 1);
            if constexpr (f == 200 - WIN) lds_set(smem + C_P + wid * 4, 20);
            if constexpr (f % 20 == 10) rdflag(f / 20 + 1);
            if constexpr (f % 20 == 20 - WIN && f < 190) check(f / 20 + 1);
            if constexpr (f + WIN < 200) wf[(f + WIN) % (WIN + 1)] = rdw(f + WIN);
            if constexpr (f % 20 >= 16 && f < 140) xr[(f / 20 + 1) * 4 + f % 20 - 16] = rdx((f / 20 + 1) * 4 + f % 20 - 16);
            if constexpr (t == 1 && i >= 31 && i < 39) xt[(i + 1) & 1] = rdx(i + 1);
            SB();
            if constexpr (i == 0) MFMA32Z(acc[t], wf[f % (WIN + 1)], xr[0]);
            else if constexpr (i < 32) MFMA32I(acc[t], wf[f % (WIN + 1)], xr[i]);
            else MFMA32I(acc[t], wf[f % (WIN + 1)], xt[i & 1]);
            SB();
        SEND
        STAMP(2);
        int l2 = tid; asm volatile("" : "+v"(l2)); l2 &= 63;
        const int h2 = l2 >> 5, tk = wid * 32 + (l2 & 31);
        int eb[5];
        auto ebases = [&](int base) { int l = tid; asm volatile("" : "+v"(l)); const int h = (l >> 5) & 1, k = wid * 32 + (l & 31);
            eb[0] = base + k * 16 + h * 8; eb[1] = base + k * 16 + h * 2040; eb[2] = base + (h ? k * 16 + 1024 : k * 8); eb[3] = base + (h ? k * 8 + 2040 : k * 16);
            asm volatile("" : "+v"(eb[0]), "+v"(eb[1]), "+v"(eb[2]), "+v"(eb[3])); };
        auto kq_addr = [&](int g0) { return eb[g0 % 5 == 1 ? 1 : g0 % 5 == 4 ? 2 : g0 % 5 == 3 ? 3 : 0] + kq_base(g0); };
        ebases(OWN);
        eb[4] = OWN + VOFF + tk * VP + h2 * 8; asm volatile("" : "+v"(eb[4]));
        int qa0 = (8 * h2 + 2 * ((l2 & 15) >> 2)) * 160 + (l2 & 16) * 2 + (l2 & 3) * 8, qa1 = qa0 + WS0 + 10240;
        asm volatile("" : "+v"(qa0), "+v"(qa1));
        auto rdq = [&](int f) { const int i = f / 3, t = f % 3, j = i >> 2, sl = (20 + j) % 7;
            return trfrag(smem + (j >= 7 ? qa0 + (j == 7 ? QU27 : j == 8 ? QU28 : QU29) : sl == 0 ? qa0 + WS0 : qa1 + (sl - 1) * 10240) + (i & 3) * 2560 + t * 64, 160); };
        MFMA_DRAIN();
        WAITV(0);
        { u32x2 pk[20];
#pragma unroll
          for (int n = 0; n < 20; ++n) { const int t = n >> 2, b = n & 3; pk[n] = (u32x2){pk2(acc[t][4 * b], acc[t][4 * b + 1]), pk2(acc[t][4 * b + 2], acc[t][4 * b + 3])}; }
          SB();
#pragma unroll
          for (int n = 0; n < 20; ++n) *(LAS u32x2*)(smem + (n < 10 ? kq_addr(2 * n) : eb[4] + (2 * n - 20) * 8)) = pk[n];
          if (h2 == 0) *(LAS u32x2*)(smem + eb[4] + 160) = (u32x2){0x3F803F80u, 0x3F803F80u}; }
        lds_set(smem + C_E + wid * 4, 1);
        check(10);
        xt[0] = rdx(32);
#pragma unroll
        for (int f = 0; f < WIN; ++f) wf[f] = rdq(f);
        STAMP(3);
        SB();
        u64 pv0 = 0, pv1 = 0;
        SFOR(f, 120)
            constexpr int i = f / 3, t = f % 3, j = i >> 2;
            if constexpr (f % 24 == 12 && j < 9) rdflag(11 + j / 2);
            if constexpr (f % 12 == 12 - WIN && (j == 1 || j == 6 || j == 8)) lds_set(smem + C_P + wid * 4, 21 + j);
            if constexpr (f % 24 == 24 - WIN && j < 9) check(11 + j / 2);
            if constexpr (f + WIN < 120) wf[(f + WIN) % (WIN + 1)] = rdq(f + WIN);
            if constexpr (t == 0 && i < 7) xt[(i + 1) & 1] = rdx(32 + i + 1);
            if constexpr (f >= 90 && f < 100 && !(f & 1)) wo_setup((f - 90) / 2);
            if constexpr (f == 99) tag = rdtag();
            if constexpr (f >= 100 && f < 107) att_setup(f - 100);
            if constexpr (f == 108) ebases(QST);
            if constexpr (f == 116) { pv0 = __hip_atomic_load((LAS u64*)(smem + C_P), RLX_WG); pv1 = __hip_atomic_load((LAS u64*)(smem + C_P + 8), RLX_WG); }
            SB();
            if constexpr (i == 0) MFMA32Z(acc[t], wf[f % (WIN + 1)], xt[0]);
            else if constexpr (i < 8) MFMA32I(acc[t], wf[f % (WIN + 1)], xt[i & 1]);
            else MFMA32I(acc[t], wf[f % (WIN + 1)], xr[i - 8]);
            SB();
        SEND
        STAMP(4);
        if (__builtin_amdgcn_readfirstlane((int)min(min((unsigned)pv0, (unsigned)(pv0 >> 32)), min((unsigned)pv1, (unsigned)(pv1 >> 32)))) < 29) wait_min4(smem + C_P, 29);
        MFMA_DRAIN();
        { acc[0] *= QSCALE; acc[1] *= QSCALE; acc[2] *= QSCALE;
          SB();
          u32x2 pk[10];
#pragma unroll
          for (int n = 0; n < 10; ++n) { const int t = n >> 2, b = n & 3; pk[n] = (u32x2){pk2(acc[t][4 * b], acc[t][4 * b + 1]), pk2(acc[t][4 * b + 2], acc[t][4 * b + 3])}; }
          SB();
#pragma unroll
          for (int n = 0; n < 10; ++n) *(LAS u32x2*)(smem + kq_addr(2 * n)) = pk[n]; }
        lds_set(smem + C_QF + wid * 4, 1);
        __builtin_amdgcn_s_setprio(1);
        STAMP(5);
    } else {
        const int hw = wid - 4;
        int known = 0;
        __builtin_amdgcn_s_setprio(3);
        auto lds_or = [&](int off, unsigned v) { FENCE(); if (lane == 0) __hip_atomic_fetch_or((LAS unsigned*)(smem + off), v, RLX_WG); FENCE(); };
        unsigned wo[5], qo[5];
        auto done = [&](int n) { lds_or(C_F + n * 4, 1u << hw); };
        const unsigned xo1 = (xo ^ 64) + 9216, xo2 = xo + 2 * 9216, xo3 = (xo ^ 64) + 3 * 9216;
        auto round_x = [&](int n) { dma16x4(xo, xo1, xo2, xo3, xb + kt_of(n) * 128 + hw * (32 * 1280), lds0 + XS0 + (n % 3) * 16384 + hw * 4096); };
        auto round_w = [&](int n) {
            const int u = 2 * n + (hw >> 1);
            const unsigned dst = lds0 + (u < 27 ? WS0 + (u % 7) * 10240 : u == 27 ? QU27 : u == 28 ? QU28 : QU29) + (hw & 1) * 5120 + 2048;
            if (u < 20) dma16x5(wo, wb + (size_t)(kt_of(u >> 1) * 64 + (u & 1) * 32) * 3840 - 2048, dst);
            else dma16x5(qo, wb + (size_t)kt_of(u < 22 ? u - 12 : u - 22) * (64 * 3840) - 2048, dst); };
        if (isA) round_x(0);
        round_x(1);
#pragma unroll
        for (int q = 0; q < 5; ++q) wo[q] = w_off(q) + (hw & 1) * (16 * 3840) + (4 - q) * 1024;
        if (isA) { round_w(0); round_w(1); WAITV(5); } else { round_w(1); WAITV(9); }
        BAR_L();
#pragma unroll
        for (int q = 0; q < 5; ++q) { const int c = q * 64 + lane, rho = __umul24(c, 6554) >> 16;
            qo[q] = (unsigned)(__umul24((hw & 1) * 32 + (rho & ~7) + 4 * (rho & 1) + ((rho >> 1) & 3), 3840) + (c - rho * 10) * 16 + (4 - q) * 1024); }
#pragma unroll 1
        for (int n = 2; n <= 13; ++n) {
            const int need = n < 13 ? 2 * n - 5 : 20;
            if (need > known) known = min4(smem + C_P);
            if (need > known) { WAITV(0); done(n - 1); known = wait_min4(smem + C_P, need); }
            if (n < 10) round_x(n);
            round_w(n);
            if (n < 10) WAITV(9); else WAITV(5);
            done(n - 1);
        }
        WAITV(0); done(13);
        STAMP(1);
        tag = rdtag();
        auto a_chunk = [&](int a) { int o;
            if (a < 320) { const int hd = a / 80, rem = a % 80; o = hd * 5120 + (rem < 32 ? (96 + rem) * 16 : rem < 64 ? 2048 + (64 + rem) * 16 : 4096 + 768 + (rem - 64) * 16); }
            else o = VOFF + 96 * VP + (a - 320) * 16;
            return o; };
        if (rt < 15) {
            const __amdgpu_buffer_rsrc_t kvr = __builtin_amdgcn_make_buffer_rsrc(ws + WS_KV + me * KVG, 0, KVB, 0x00020000);
            wait_min4(smem + C_E, 1);
            STAMP(2);
            if (!isA) { u32x4 v[11];
#pragma unroll
                for (int j = 0; j < 11; ++j) if (j < 10 || hw == 3) v[j] = *(LAS u32x4*)(smem + OWN + hw * 10240 + j * 1024 + lane * 16);
#pragma unroll
                for (int j = 0; j < 11; ++j) if (j < 10 || hw == 3) __builtin_amdgcn_raw_buffer_store_b128(v[j], kvr, hw * 10240 + j * 1024 + lane * 16, 0, 16);
            } else {
#pragma unroll
                for (int j = 0; j < 3; ++j) { const int a = (4 * j + hw) * 64 + lane;
                    if (a < 656) { const int o = a_chunk(a); __builtin_amdgcn_raw_buffer_store_b128(*(LAS u32x4*)(smem + OWN + o), kvr, o, 0, 16); } }
            }
            STAMP(3);
        }
        wait_min4(smem + C_P, 22);
        round_w(14);
        WAITV(5);
        if (rt < 15) {
            unsigned old = 0;
            if (lane == 0) old = __hip_atomic_fetch_add((LAS unsigned*)(smem + N_PUB), 1u, RLX_WG);
            if (lane == 0 && old == 3) __hip_atomic_store((GAS u64*)(ws + WS_FKV + me * 128), tag, RLX_AGENT);
            STAMP(4);
        }
        WAITV(0); done(14);
        __builtin_amdgcn_s_setprio(0);
#pragma unroll
        for (int p = 0; p < 7; ++p) att_setup(p);
#pragma unroll
        for (int q = 0; q < 5; ++q) wo_setup(q);
        __builtin_amdgcn_s_setprio(3);
        if (hw < NPOLL) {
            for (int i = 0; i < hw; ++i) __builtin_amdgcn_s_sleep(PSTAG);
            poll_shared((GAS u64*)(ws + WS_FKV + (me - 8 * (lane + 1)) * 128), lane < nnb, tag, smem + N_KV);
        } else wait_word(smem + N_KV, 1);
        STAMP(5);
        if (!isA) {
            const int n = hw >> 1, half = hw & 1;
            if (n < nnb) {
                const char* src = ws + WS_KV + (me - 8 * (n + 1)) * KVG + half * 20480; const unsigned dst = lds0 + KV0 + (1 - n) * KVB + half * 20480;
                wait_min4(smem + C_P, 27);
#pragma unroll
                for (int j = 0; j < 4; ++j) dma16x5_sc1(lane * 16, src + j * 5120 + 2048, dst + j * 5120 + 2048);
                if (half) dma16_sc1(lane * 16, src + 20480, dst + 20480);
            }
        } else if (nnb) {
            const __amdgpu_buffer_rsrc_t nbr = __builtin_amdgcn_make_buffer_rsrc(ws + WS_KV + (me - 8) * KVG, 0, KVB, 0x00020000);
            u32x4 v[3];
#pragma unroll
            for (int j = 0; j < 3; ++j) { const int a = (4 * j + hw) * 64 + lane; if (a < 656) v[j] = __builtin_amdgcn_raw_buffer_load_b128(nbr, a_chunk(a), 0, 16); }
            wait_min4(smem + C_P, 27);
#pragma unroll
            for (int j = 0; j < 3; ++j) { const int a = (4 * j + hw) * 64 + lane; if (a < 656) *(LAS u32x4*)(smem + KV0 + KVB + a_chunk(a)) = v[j]; }
        }
        __builtin_amdgcn_s_setprio(2);
        wait_word(smem + C_QF + qi * 4, 1);
        STAMP(6);
    }
    u32x4 wreg[13];
    auto ldwo = [&](int i) { wreg[i] = __builtin_amdgcn_raw_buffer_load_b128(wor, oo[i % 5] + (i / 5) * (32 * 1280) + (i > 6 || (i == 6 && lane >= 16) ? 40 * 1280 : 0), 0, 0); };
    STAMP(7);
    u32x2 ob[2][3];
    {
        bf16x8 q1[2]; u32x2 q2[2];
#pragma unroll
        for (int e = 0; e < 2; ++e) {
            q1[e] = *(LAS bf16x8*)(smem + qb1 + e * 5120);
            q2[e] = *(LAS u32x2*)(smem + qb2 + e * 5120);
            if (hl) q2[e] = (u32x2){0u, 0u};
        }
        if (isA) mask_a(0);
        struct KF { bf16x8 a; s16x4 b; };
        struct VF { bf16x8 a, b; };
        auto rdk = [&](int u, int e) { const int so = (u >> 2) * KVB, tw = u & 3; KF k;
            k.a = *(LAS bf16x8*)(smem + kb1 + so + tw * 512 + e * 5120); k.b = *(LAS s16x4*)(smem + __umul24(so + tw * 256 + e * 5120, nh) + kb2); return k; };
        auto rdv = [&](int u, int e) { LAS char* p = smem + (e ? vb1 : vb0) + (u >> 2) * KVB + (u & 3) * (32 * VP); VF v;
            v.a = trfrag(p, 8 * VP); v.b = trfrag(p + 16 * VP, 8 * VP); return v; };
        auto qk = [&](const KF& k, int e, const f32x16& c) { return MFMA32K8(k.b, __builtin_bit_cast(s16x4, q2[e]), MFMA32(k.a, q1[e], c)); };
        const f32x16 zero = {};
        auto max16 = [&](const f32x16& s, float m) {
#pragma unroll
            for (int r = 0; r < 16; ++r) m = __builtin_elementwise_maximum(m, s[r]);
            return m; };
        auto set_ref = [&](float m, int e) { const unsigned nb = pk2(-m, 0.f); if (hl) q2[e][0] = nb; };
        auto ex4 = [&](f32x16& s, int b) {
#pragma unroll
            for (int r = 0; r < 4; ++r) s[b + r] = __builtin_amdgcn_exp2f(s[b + r]); };
        auto pk8 = [&](const f32x16& s, int b) { return __builtin_bit_cast(bf16x8, (u32x4){pk2(s[b], s[b + 1]), pk2(s[b + 2], s[b + 3]), pk2(s[b + 4], s[b + 5]), pk2(s[b + 6], s[b + 7])}); };
        auto phase = [&](auto nx, f32x16& cur, const VF& v, f32x16& o, f32x16& nxt, const KF& k, int e) __attribute__((always_inline)) {
            constexpr int NX = decltype(nx)::value;
            SB(); ex4(cur, 0); SB();
            if constexpr (NX) nxt = MFMA32(k.a, q1[e], NX == 1 ? zero : NX == 2 ? cf : cd);
            SB(); ex4(cur, 4); { const bf16x8 p = pk8(cur, 0); SB(); o = MFMA32(v.a, p, o); }
            SB(); ex4(cur, 8); SB();
            if constexpr (NX) nxt = MFMA32K8(k.b, __builtin_bit_cast(s16x4, q2[e]), nxt);
            SB(); ex4(cur, 12); { const bf16x8 p = pk8(cur, 8); SB(); o = MFMA32(v.b, p, o); }
            SB(); };
        f32x16 o0, o1, sa, sb; KF kx; VF vx;
        auto step = [&](auto m0, auto m1, int u) __attribute__((always_inline)) {
            constexpr int M1 = decltype(m1)::value;
            KF ky; if constexpr (M1) ky = rdk(u - 1, 0);
            const VF vy = rdv(u, 1);
            phase(m0, sa, vx, o0, sb, kx, 1);
            if constexpr (M1) { kx = rdk(u - 1, 1); vx = rdv(u - 1, 0); }
            phase(m1, sb, vy, o1, sa, ky, 0); };
        auto nbwait = [&](int u) { if (u == 8) wait_word(smem + N_NB, 4); };
        { const KF k0f = rdk(ud, 0); kx = rdk(ud, 1); vx = rdv(ud, 0);
          const f32x16 s0 = qk(k0f, 0, cd), s1 = qk(kx, 1, cd);
          set_ref(halfmax(max16(s0, s0[0])), 0); set_ref(halfmax(max16(s1, s1[0])), 1);
          sa = qk(k0f, 0, cd); }
        if (hp) { WAITV(0); lds_inc0(smem + N_NB, lane); lds_inc0(smem + N_G0, lane); }
        o0 = zero; o1 = zero;
        if (u0 + 8 == ud) {
            SFOR(it, 9)
                if constexpr (it < 7) { ldwo(2 * it); if constexpr (it < 6) ldwo(2 * it + 1); }
                if constexpr (it == PFLIP) { if (hp) __builtin_amdgcn_s_setprio(0); }
                if constexpr (it == 7) {
#pragma unroll
                    for (int r = 0; r < 16; ++r) mask_b(cf, r, true); }
                if constexpr (it < 4) nbwait(ud - it);
                step(IC<it == 0 ? 3 : it == 8 ? 2 : 1>{}, IC<it == 8 ? 0 : it == 7 ? 2 : 1>{}, ud - it);
            SEND
        } else {
            __builtin_amdgcn_s_setprio(0);
#pragma unroll
            for (int i = 0; i < 13; ++i) ldwo(i);
            if (!isA) cf = zero;
            if (u0 < ud) {
                nbwait(ud);
                if (u0 + 1 < ud) {
                    step(IC<3>{}, IC<1>{}, ud);
#pragma unroll 1
                    for (int u = ud - 1; u > u0 + 1; --u) { nbwait(u); step(IC<1>{}, IC<1>{}, u); }
                    nbwait(u0 + 1);
                    step(IC<1>{}, IC<2>{}, u0 + 1);
                } else step(IC<3>{}, IC<2>{}, ud);
                step(IC<2>{}, IC<0>{}, u0);
            } else step(IC<3>{}, IC<0>{}, ud);
        }
        if (!__all(o0[12] < 1.152921504606847e18f && o1[12] < 1.152921504606847e18f)) {
            if (isA) { int z = 0; asm volatile("" : "+s"(z)); mask_a(z); }
            else { asm volatile("" : "+v"(dsh));
#pragma unroll
                for (int r = 0; r < 16; ++r) { mask_b(cd, r, false); mask_b(cf, r, true); }
                if (!far) cf = zero; }
#pragma unroll
            for (int e = 0; e < 2; ++e) {
                if (hl) q2[e][0] = 0u;
                const f32x16 s = qk(rdk(ud, e), e, cd);
                float m = max16(s, s[0]);
                if (u0 < ud) m = max16(qk(rdk(u0, e), e, cf), m);
#pragma unroll 1
                for (int u = u0 + 1; u < ud; ++u) m = max16(qk(rdk(u, e), e, zero), m);
                set_ref(halfmax(m), e);
            }
#pragma unroll
            for (int e = 0; e < 2; ++e) {
                f32x16 o = zero;
#pragma unroll 1
                for (int u = u0; u <= ud; ++u) { f32x16 t = u == ud ? qk(rdk(u, e), e, cd) : u == u0 ? qk(rdk(u, e), e, cf) : qk(rdk(u, e), e, zero);
                    const VF v = rdv(u, e); ex4(t, 0); ex4(t, 4); ex4(t, 8); ex4(t, 12);
                    o = MFMA32(v.a, pk8(t, 0), o); o = MFMA32(v.b, pk8(t, 8), o); }
                if (e) o1 = o; else o0 = o;
            }
        }
        const float rl0 = __builtin_amdgcn_rcpf(o0[12]), rl1 = __builtin_amdgcn_rcpf(o1[12]);
        SB();
#pragma unroll
        for (int r = 0; r < 12; ++r) { o0[r] *= rl0; o1[r] *= rl1; }
        SB();
#pragma unroll
        for (int j = 0; j < 3; ++j) {
            ob[0][j] = (u32x2){pk2(o0[4 * j], o0[4 * j + 1]), pk2(o0[4 * j + 2], o0[4 * j + 3])};
            ob[1][j] = (u32x2){pk2(o1[4 * j], o1[4 * j + 1]), pk2(o1[4 * j + 2], o1[4 * j + 3])}; }
        SB();
    }
    STAMP(8);
    const __amdgpu_buffer_rsrc_t orr = __builtin_amdgcn_make_buffer_rsrc(ws + WS_O + (br * 16 + rt) * OG, 0, 4 * 40960, 0x00020000);
    {
      { auto rx = __builtin_amdgcn_permlane32_swap(ob[0][0][0], ob[0][1][0], false, false);
        auto ry = __builtin_amdgcn_permlane32_swap(ob[0][0][1], ob[0][1][1], false, false);
        __builtin_amdgcn_raw_buffer_store_b128((u32x4){rx[0], ry[0], rx[1], ry[1]}, orr, obase, 0, 16); }
      if (hl == 0) __builtin_amdgcn_raw_buffer_store_b128((u32x4){ob[0][2][0], ob[0][2][1], ob[1][0][0], ob[1][0][1]}, orr, obase + 1024, 0, 16);
      { auto fx = __builtin_amdgcn_permlane32_swap(ob[1][0][0], ob[1][1][0], false, false);
        auto fy = __builtin_amdgcn_permlane32_swap(ob[1][0][1], ob[1][1][1], false, false);
        auto sx = __builtin_amdgcn_permlane32_swap(ob[1][1][0], ob[1][2][0], false, false);
        auto sy = __builtin_amdgcn_permlane32_swap(ob[1][1][1], ob[1][2][1], false, false);
        __builtin_amdgcn_raw_buffer_store_b128((u32x4){fx[1], fy[1], sx[0], sy[0]}, orr, obase + 1536, 0, 16); } }
    if (lane == 0) { (void)__hip_atomic_fetch_add((LAS unsigned*)(smem + N_G1), 1u, RLX_WG);
        if (!hp) (void)__hip_atomic_fetch_add((LAS unsigned*)(smem + N_G0), 1u, RLX_WG); }
    FENCE();
    STAMP(6);
    auto signal_o = [&]() {
        if (lds_inc(smem + N_OS + hp * 4, lane) == 3 && lane == 0) __hip_atomic_store((GAS u64*)(ws + WS_FO + (me * 2 + hp) * 128), tag, RLX_AGENT); };
    __builtin_amdgcn_s_setprio(0);
    WAITV(0); signal_o();
    STAMP(9);
    wait_word(smem + (hp ? N_G1 : N_G0), 8);
    const int p1 = hp ? 30720 : 35840, p2 = hp ? 65536 : 43520;
    const int wb0 = hp ? KV0 + 10240 : 512, wb1 = hp ? KV0 + KVB + 10240 - 30720 : KV0 - 35840, wb2 = KV0 + KVB - 43520;
#pragma unroll
    for (int i = 0; i < 13; ++i) { const int L = qo * 12800 + i * 1024;
        if (i < 12 || lane < 32) *(LAS u32x4*)(smem + (L + (L < p1 ? wb0 : L < p2 ? wb1 : wb2)) + lane * 16) = wreg[i]; }
    lds_inc0(smem + N_WO + hp * 4, lane);
    STAMP(10);
    __builtin_amdgcn_s_sleep(PDELAY);
    if (qo < NPOLL) {
        for (int i = 0; i < qo; ++i) __builtin_amdgcn_s_sleep(PSTAG);
        poll_shared((GAS u64*)(ws + WS_FO + (((me & ~7) + (lane & 7)) * 2 + hp) * 128), lane < 8, tag, smem + N_OK + hp * 4);
    } else wait_word(smem + N_OK + hp * 4, 1);
    STAMP(11);
    f32x16 oc[3]; f32x4 ps[10];
    LAS char* red = smem + OWN + qo * 10240 + lane * 16;
    { bf16x8 af[5], of[20];
      const int la = (8 * hl + 2 * ((lane & 15) >> 2)) * 160 + (lane & 16) * 2 + (lane & 3) * 8;
      const int aa0 = wb0 + la, aa1 = (hp ? wb1 : wb0) + la, aa2 = wb1 + la, aa3 = (hp ? wb1 : wb2) + la;
      auto rda = [&](int f) { return trfrag(smem + (f < 36 ? aa0 : f < 42 ? aa1 : f < 51 ? aa2 : aa3) + (f / 3) * 2560 + (f % 3) * 64, 160); };
      auto ldo = [&](int k) { of[k] = __builtin_bit_cast(bf16x8, __builtin_amdgcn_raw_buffer_load_b128(orr, qo * 40960 + (hp * 20 + k) * 1024 + lane * 16, 0, 16)); };
#pragma unroll
      for (int k = 0; k < OPF; ++k) ldo(k);
      wait_word(smem + N_WO + hp * 4, 4);
#pragma unroll
      for (int f = 0; f < 4; ++f) af[f] = rda(f);
      SB();
      SFOR(f, 60)
          if constexpr (f % 3 == 0 && f / 3 + OPF < 20) ldo(f / 3 + OPF);
          if constexpr (f + 4 < 60) af[(f + 4) % 5] = rda(f + 4);
          if constexpr (f == 57) { if (hp) { wait_word(smem + C_RD + qo * 4, 1);
#pragma unroll
              for (int b = 0; b < 10; ++b) ps[b] = *(LAS f32x4*)(red + b * 1024); } }
          SB();
          if constexpr (f < 3) MFMA32Z(oc[f], af[f % 5], of[0]); else MFMA32I(oc[f % 3], af[f % 5], of[f / 3]);
          SB();
      SEND }
    MFMA_DRAIN();
    STAMP(12);
    { if (!hp) {
          wait_word(smem + N_G1, 8);
#pragma unroll
          for (int b = 0; b < 10; ++b) *(LAS f32x4*)(red + b * 1024) = (f32x4){oc[b >> 2][4 * (b & 3)], oc[b >> 2][4 * (b & 3) + 1], oc[b >> 2][4 * (b & 3) + 2], oc[b >> 2][4 * (b & 3) + 3]};
          lds_set(smem + C_RD + qo * 4, 1);
      } else {
          char* orow = out + (size_t)(rt * 128 + qo * 32 + r32) * 2560 + br * 1280 + hg * 160 + hl * 16;
#pragma unroll
          for (int b = 0; b < 10; ++b)
#pragma unroll
              for (int r = 0; r < 4; ++r) ps[b][r] += oc[b >> 2][4 * (b & 3) + r];
          SB();
          u32x2 pk[10];
#pragma unroll
          for (int b = 0; b < 10; ++b) pk[b] = (u32x2){pk2(ps[b][0], ps[b][1]), pk2(ps[b][2], ps[b][3])};
          SB();
          u32x4 st[5];
#pragma unroll
          for (int b = 0; b < 10; b += 2) {
              auto rx = __builtin_amdgcn_permlane32_swap(pk[b][0], pk[b + 1][0], false, false); auto ry = __builtin_amdgcn_permlane32_swap(pk[b][1], pk[b + 1][1], false, false);
              st[b >> 1] = (u32x4){rx[0], ry[0], rx[1], ry[1]}; }
          SB();
#pragma unroll
          for (int b = 0; b < 5; ++b) *(u32x4*)(orow + b * 32) = st[b];
      } }
    if (bx == 0 && wid == 0) {
#pragma unroll 1
        for (int i = 0; i < 4; ++i) poll_flags((GAS u64*)(ws + WS_FO + (i * 64 + lane) * 256), true, tag);
        if (lane == 0) __hip_atomic_store((GAS u64*)ws, tag, RLX_AGENT);
    }
}

extern "C" void kernel_launch(void* const* d_in, const int* in_sizes, int n_in, void* d_out, int out_size, void* d_ws, size_t ws_size,
                              hipStream_t stream) {
    fused_attn_kernel<<<256, 512, 0, stream>>>((const char*)d_in[0], (const char*)d_in[1], (const char*)d_in[2], (const char*)d_in[3],
                                               (const char*)d_in[4], (const char*)d_in[5], (char*)d_out, (char*)d_ws);
}
```

```cpp
#define ADELAY 0
#define NOWARM 1
#include <hip/hip_runtime.h>
#include <hip/hip_bf16.h>
#include <stdint.h>

typedef short bf16x8 __attribute__((ext_vector_type(8)));
typedef short s16x4 __attribute__((ext_vector_type(4)));
typedef float f32x2 __attribute__((ext_vector_type(2)));
typedef float f32x4 __attribute__((ext_vector_type(4)));
typedef float f32x16 __attribute__((ext_vector_type(16)));
typedef unsigned u32x4 __attribute__((ext_vector_type(4)));
typedef unsigned u32x2 __attribute__((ext_vector_type(2)));
typedef __bf16 bfx2 __attribute__((ext_vector_type(2)));
typedef unsigned long long u64;
#define LAS __attribute__((address_space(3)))
#define GAS __attribute__((address_space(1)))
#define DI __device__ __forceinline__
#define RLX_AGENT __ATOMIC_RELAXED, __HIP_MEMORY_SCOPE_AGENT
#define RLX_WG __ATOMIC_RELAXED, __HIP_MEMORY_SCOPE_WORKGROUP

constexpr int LDS_BYTES = 163840;
constexpr int VOFF = 20480, VP = 168;
constexpr int KVB = VOFF + 128 * VP;
constexpr int KV0 = LDS_BYTES - 3 * KVB;
constexpr int OWN = KV0 + 2 * KVB;
constexpr int C_F = 0, C_P = 128, C_E = 144, C_QF = 160, C_RD = 176, C_CNT = 192, C_TAG = 320;
constexpr int N_PUB = C_CNT, N_NB = C_CNT + 4, N_G0 = C_CNT + 8, N_G1 = C_CNT + 12, N_WO = C_CNT + 16, N_OS = C_CNT + 24, N_KV = C_CNT + 32, N_OK = C_CNT + 36;
constexpr int XS0 = 512, WS0 = XS0 + 3 * 16384, QST = 512, ONE = 336, QU27 = XS0 + 16384, QU28 = XS0, QU29 = QU27 + 10240;
static_assert(WS0 + 7 * 10240 <= OWN, "ring overlaps the own image");
constexpr unsigned WS_FKV = 4096, WS_FO = WS_FKV + 256 * 128, WS_KV = 131072;
constexpr unsigned KVG = KVB, OG = 4 * 40960;
constexpr unsigned WS_O = WS_KV + 256 * KVG;
constexpr unsigned WS_END = WS_O + 32 * OG;
constexpr unsigned WS_DBG = WS_END;
constexpr unsigned SPIN_CAP = 400000, LSPIN_CAP = 4000000;
constexpr float QSCALE = 0.22360679774997896f * 1.4426950408889634f;
#define PSLEEP 2
#define PDELAY 1
#define WAUX 0
#define NPOLL 1
#define PSTAG 3
#define PFLIP 6
#define OPF 12
#define ABL4N 2
#define WIN 4

#define MFMA32(a, b, c) __builtin_amdgcn_mfma_f32_32x32x16_bf16((a), (b), (c), 0, 0, 0)
#define MFMA32K8(a, b, c) __builtin_amdgcn_mfma_f32_32x32x8bf16_1k((a), (b), (c), 0, 0, 0)
#define MFMA32I(acc, a, b) asm volatile("v_mfma_f32_32x32x16_bf16 %0, %1, %2, %0" : "+v"(acc) : "v"(a), "v"(b))
#define MFMA32Z(acc, a, b) asm volatile("v_mfma_f32_32x32x16_bf16 %0, %1, %2, 0" : "=&v"(acc) : "v"(a), "v"(b))
#define MFMA_DRAIN() asm volatile("s_nop 7\n\ts_nop 4" ::: "memory")
#define WAITV(N) asm volatile("s_waitcnt vmcnt(" #N ")" ::: "memory")
#define WAITL() asm volatile("s_waitcnt lgkmcnt(0)" ::: "memory")
#define BAR_L() asm volatile("s_waitcnt lgkmcnt(0)\n\ts_barrier" ::: "memory")
#define SB() __builtin_amdgcn_sched_barrier(0)
#define FENCE() asm volatile("" ::: "memory")

#define STAMP(i) do {} while (0)

template <int V> struct IC { static constexpr int value = V; };
template <int I, int N, class F> DI void static_for(F&& f) { if constexpr (I < N) { f(IC<I>{}); static_for<I + 1, N>(f); } }
#define SFOR(var, N) static_for<0, N>([&](auto ic_##var) __attribute__((always_inline)) { constexpr int var = decltype(ic_##var)::value;
#define SEND });

DI void dma16(unsigned voff, const char* sbase, unsigned ldsaddr) {
    asm volatile("s_mov_b32 m0, %2\n\ts_nop 0\n\tglobal_load_lds_dwordx4 %0, %1" :: "v"(voff), "s"(sbase), "s"(ldsaddr) : "memory");
}
DI void dma16x4(unsigned v0, unsigned v1, unsigned v2, unsigned v3, const char* sbase, unsigned ldsaddr) {
    asm volatile("s_mov_b32 m0, %5\n\ts_nop 0\n\tglobal_load_lds_dwordx4 %0, %4\n\tglobal_load_lds_dwordx4 %1, %4 offset:1024\n\t"
                 "global_load_lds_dwordx4 %2, %4 offset:2048\n\tglobal_load_lds_dwordx4 %3, %4 offset:3072"
                 :: "v"(v0), "v"(v1), "v"(v2), "v"(v3), "s"(sbase), "s"(ldsaddr) : "memory");
}
DI void dma16x5(const unsigned (&v)[5], const char* sbase, unsigned ldsaddr) {
    asm volatile("s_mov_b32 m0, %6\n\ts_nop 0\n\tglobal_load_lds_dwordx4 %0, %5 offset:-2048\n\tglobal_load_lds_dwordx4 %1, %5 offset:-1024\n\t"
                 "global_load_lds_dwordx4 %2, %5\n\tglobal_load_lds_dwordx4 %3, %5 offset:1024\n\tglobal_load_lds_dwordx4 %4, %5 offset:2048"
                 :: "v"(v[0]), "v"(v[1]), "v"(v[2]), "v"(v[3]), "v"(v[4]), "s"(sbase), "s"(ldsaddr) : "memory");
}
DI void dma16x5_sc1(unsigned voff, const char* sbase, unsigned ldsaddr) {
    asm volatile("s_mov_b32 m0, %2\n\ts_nop 0\n\tglobal_load_lds_dwordx4 %0, %1 offset:-2048 sc1\n\tglobal_load_lds_dwordx4 %0, %1 offset:-1024 sc1\n\t"
                 "global_load_lds_dwordx4 %0, %1 sc1\n\tglobal_load_lds_dwordx4 %0, %1 offset:1024 sc1\n\tglobal_load_lds_dwordx4 %0, %1 offset:2048 sc1"
                 :: "v"(voff), "s"(sbase), "s"(ldsaddr) : "memory");
}
DI void dma16_sc1(unsigned voff, const char* sbase, unsigned ldsaddr) {
    asm volatile("s_mov_b32 m0, %2\n\ts_nop 0\n\tglobal_load_lds_dwordx4 %0, %1 sc1" :: "v"(voff), "s"(sbase), "s"(ldsaddr) : "memory");
}
DI void dma4(const char* p, unsigned ldsaddr) {
    asm volatile("s_mov_b32 m0, %1\n\ts_nop 0\n\tglobal_load_lds_dword %0, off" :: "v"(p), "s"(ldsaddr) : "memory");
}
DI unsigned pk2(float lo, float hi) { f32x2 v = {lo, hi}; return __builtin_bit_cast(unsigned, __builtin_convertvector(v, bfx2)); }
typedef short v4i16_t __attribute__((ext_vector_type(4)));
DI bf16x8 trfrag(LAS char* p, int second) {
    s16x4 a = __builtin_bit_cast(s16x4, __builtin_amdgcn_ds_read_tr16_b64_v4i16((LAS v4i16_t*)p));
    s16x4 b = __builtin_bit_cast(s16x4, __builtin_amdgcn_ds_read_tr16_b64_v4i16((LAS v4i16_t*)(p + second)));
    return __builtin_shufflevector(a, b, 0, 1, 2, 3, 4, 5, 6, 7);
}
constexpr u64 lanes_cut(int r, bool fartile) {
    u64 m = 0;
    for (int l = 0; l < 64; ++l) { const int key = (r & 3) + 8 * (r >> 2) + 4 * (l >> 5), d = (l & 31) - key + (fartile ? 32 : 0);
        const bool ok = d >= 0 && d <= 14 && !(d & 1);
        if (!ok) m |= 1ull << l; }
    return m;
}
DI float halfmax(float m) { auto rr = __builtin_amdgcn_permlane32_swap(__float_as_uint(m), __float_as_uint(m), false, false);
    return __builtin_elementwise_maximum(__uint_as_float(rr[0]), __uint_as_float(rr[1])); }

DI void poll_flags(GAS u64* word, bool active, u64 tag) {
#pragma unroll 1
    for (unsigned spins = 0; spins < SPIN_CAP; ++spins) {
        u64 v = tag;
        if (active) v = __hip_atomic_load(word, RLX_AGENT);
        if (__all(v == tag)) break;
        __builtin_amdgcn_s_sleep(PSLEEP);
    }
    __builtin_amdgcn_fence(__ATOMIC_ACQUIRE, "wavefront");
}
DI void poll_shared(GAS u64* word, bool active, u64 tag, LAS char* ok) {
#pragma unroll 1
    for (unsigned spins = 0; spins < SPIN_CAP; ++spins) {
        if (__builtin_amdgcn_readfirstlane((int)__hip_atomic_load((LAS unsigned*)ok, RLX_WG))) break;
        u64 v = tag;
        if (active) v = __hip_atomic_load(word, RLX_AGENT);
        if (__all(v == tag)) { __hip_atomic_store((LAS unsigned*)ok, 1u, RLX_WG); break; }
        __builtin_amdgcn_s_sleep(PSLEEP);
    }
    __builtin_amdgcn_fence(__ATOMIC_ACQUIRE, "wavefront");
    asm volatile("" ::: "memory");
}
DI int min4(LAS char* p) {
    const u64 a = __hip_atomic_load((LAS u64*)p, RLX_WG), b = __hip_atomic_load((LAS u64*)(p + 8), RLX_WG);
    const unsigned m = min(min((unsigned)a, (unsigned)(a >> 32)), min((unsigned)b, (unsigned)(b >> 32)));
    return __builtin_amdgcn_readfirstlane((int)m);
}
DI int wait_min4(LAS char* p, int need) {
    int v = min4(p);
#pragma unroll 1
    for (unsigned spins = 0; v < need && spins < LSPIN_CAP; ++spins) { __builtin_amdgcn_s_sleep(1); v = min4(p); }
    FENCE();
    return v;
}
DI void wait_word(LAS char* p, unsigned need) {
#pragma unroll 1
    for (unsigned spins = 0; spins < LSPIN_CAP; ++spins) {
        if ((unsigned)__builtin_amdgcn_readfirstlane((int)__hip_atomic_load((LAS unsigned*)p, RLX_WG)) >= need) break;
        __builtin_amdgcn_s_sleep(1); }
    FENCE();
}
DI void lds_inc0(LAS char* p, int lane) { FENCE(); if (lane == 0) (void)__hip_atomic_fetch_add((LAS unsigned*)p, 1u, RLX_WG); FENCE(); }
DI void wait_pair(LAS char* p, unsigned need) {
#pragma unroll 1
    for (unsigned spins = 0; spins < LSPIN_CAP; ++spins) { const u64 v = __hip_atomic_load((LAS u64*)p, RLX_WG);
        if ((unsigned)__builtin_amdgcn_readfirstlane((int)min((unsigned)v, (unsigned)(v >> 32))) >= need) break;
        __builtin_amdgcn_s_sleep(1); }
    FENCE();
}
DI unsigned lds_inc(LAS char* p, int lane) { FENCE(); unsigned old = 0; if (lane == 0) old = __hip_atomic_fetch_add((LAS unsigned*)p, 1u, RLX_WG); FENCE(); return old; }
DI void lds_set(LAS char* p, unsigned v) { FENCE(); __hip_atomic_store((LAS unsigned*)p, v, RLX_WG); FENCE(); }

__global__ __launch_bounds__(512, 2) void fused_attn_kernel(const char* __restrict__ xA, const char* __restrict__ wA, const char* __restrict__ oA,
                                                           const char* __restrict__ xB, const char* __restrict__ wB, const char* __restrict__ oB,
                                                           char* __restrict__ out, char* __restrict__ ws) {
    __shared__ __attribute__((aligned(16))) char smem_[LDS_BYTES];
    LAS char* smem = (LAS char*)smem_;
    const unsigned lds0 = (unsigned)(uintptr_t)smem;
    STAMP(0);
    asm volatile("" :: "s"(xA), "s"(wA), "s"(oA), "s"(xB), "s"(wB), "s"(oB), "s"(out), "s"(ws));
    const int tid = threadIdx.x, lane = tid & 63;
    const int wid = __builtin_amdgcn_readfirstlane(tid >> 6);
    const int bx = blockIdx.x, xcd = bx & 7, idx = bx >> 3;
    const int isA = idx >> 4, rt = (xcd >> 1) * 4 + ((idx >> 2) & 3), hg = (xcd & 1) * 4 + (idx & 3);
    const int br = isA ? 0 : 1;
    const int k0 = isA ? (((rt & 3) * 5 + 1) / 2 + ((hg & 3) * 5 + 1) / 2) % 10 : 0;
    const char* xb = (isA ? xA : xB) + (size_t)rt * (128 * 1280);
    const char* wb = (isA ? wA : wB) + hg * 160;
    const int me = (br * 16 + rt) * 8 + hg;
    const int r32 = lane & 31, hl = lane >> 5;
    auto kt_of = [&](int s) { int k = s + k0; return k >= 10 ? k - 10 : k; };
    if (isA && wid < 4) {
        const int j16 = idx & 15; const unsigned dump = lds0 + OWN + wid * 512;
        const char* wl = wB + (hg >> 2) * 640 + j16 * (4 * 3840);
        const char* xl = xB + (size_t)(rt * 128 + (hg & 3) * 32 + r32) * 1280 + hl * 128;
        auto pfx = [&](int i) { return xl + i * 256; };
        auto pfkv = [&](int i) { int n = i * 64 + lane; n = n < 400 ? n : 399; const int sl = n / 40, r = n % 40, c = r % 10;
            return wl + (sl * 64 + r / 10) * 3840 + 1280 + (c < 5 ? c * 128 : 640 + c * 128); };
        auto pfq = [&](int i) { int n = i * 64 + lane; n = n < 200 ? n : 199; int sl = n / 20 + 8; sl = sl >= 10 ? sl - 10 : sl; const int r = n % 20;
            return wl + (sl * 64 + r / 5) * 3840 + (r % 5) * 128; };
        dma4(wid & 1 ? pfx(wid >> 1) : pfkv(wid >> 1), dump);
        dma4(wid & 1 ? pfx(2 + (wid >> 1)) : pfkv(2 + (wid >> 1)), dump);
        dma4(wid < 3 ? pfkv(4 + wid) : pfx(4), dump);
        dma4(pfq(wid), dump);
        { int n = wid * 64 + lane; n = n < 200 ? n : 199; const int o = (j16 * 40 + n / 5) * 1280 + (hg >> 2) * 640 + (n % 5) * 128;
          dma4(oB + o, dump); dma4(oA + o, dump); }
    }
    if (isA && wid >= 4) { for (int i = 0; i < ADELAY; ++i) __builtin_amdgcn_s_sleep(16); }

    const unsigned xo = (unsigned)((lane >> 3) * 1280 + (((lane & 7) ^ (lane >> 4)) << 4));
    auto w_off = [&](int q) { const int c = q * 64 + lane, row = __umul24(c, 3277) >> 16, j = c - row * 20; return (unsigned)(__umul24(row, 3840) + j * 16 + (j < 10 ? 1280 : 2400)); };
    if (!isA) { const char* xs = xb + k0 * 128 + wid * (16 * 1280);
      dma16(xo, xs, lds0 + XS0 + wid * 2048);
      dma16(xo ^ 64, xs + 8 * 1280, lds0 + XS0 + wid * 2048 + 1024);
      const char* wsrc = wb + (size_t)k0 * (64 * 3840);
      const unsigned wv = w_off(wid < 5 ? wid : wid - 5);
      if (wid < 5) { dma16(wv, wsrc, lds0 + WS0 + wid * 1024); dma16(wv, wsrc + 16 * 3840, lds0 + WS0 + (5 + wid) * 1024);
          if (wid >= 3) { dma16(wv, wsrc + 32 * 3840, lds0 + WS0 + (10 + wid) * 1024); dma16(wv, wsrc + 48 * 3840, lds0 + WS0 + (15 + wid) * 1024); } }
      else { dma16(wv, wsrc + 32 * 3840, lds0 + WS0 + (5 + wid) * 1024); dma16(wv, wsrc + 48 * 3840, lds0 + WS0 + (10 + wid) * 1024); } }
    if (wid == 4 && lane < 2) dma4(ws + lane * 4, lds0 + C_TAG);
    if (tid < 80) *(LAS unsigned*)(smem + tid * 4) = 0u;
    else if (tid < 82) *(LAS unsigned*)(smem + ONE + (tid - 80) * 4) = 0x3F803F80u;
    const int nnb = isA ? (rt > 0 ? 1 : 0) : (rt > 1 ? 2 : rt);
    auto kq_base = [&](int fg) { const int hd = fg / 5, dg = fg % 5; return dg < 4 ? hd * 5120 + (dg >> 1) * 2048 + (dg & 1) * 8 : hd * 5120 + 4096; };

    auto rdtag = [&]() { const u64 t = *(LAS u64*)(smem + C_TAG);
        const u64 e = ((u64)(unsigned)__builtin_amdgcn_readfirstlane((int)(t >> 32)) << 32) | (unsigned)__builtin_amdgcn_readfirstlane((int)t);
        return e * 6364136223846793005ull + 1442695040888963407ull; };
    const __amdgpu_buffer_rsrc_t wor = __builtin_amdgcn_make_buffer_rsrc((void*)(isA ? oA : oB), 0, 640 * 1280, 0x00020000);

    const int hp = wid >> 2, qo = wid & 3, qi = hp ? 3 - qo : qo;
    const int ud = qi + 8;
    const bool far = isA ? (4 * rt + qi >= 1) : (4 * rt + qi >= 8);
    const int u0 = isA ? (far ? ud - 1 : ud) : (far ? qi : 8 - 4 * rt);
    f32x16 cd, cf;
    int dsh, kb1, kb2, nh, vb0, vb1, qb1, qb2; unsigned obase, oo[5];
    auto mask_b = [&](f32x16& c, int r, bool fartile) { const int k = (r & 3) + 8 * (r >> 2); c[r] = __int_as_float(fartile ? ((k - 1) << 23) - dsh : dsh - (k << 23)); };
    auto mask_a = [&](int z) {
      SFOR(r, 16)
          constexpr u64 ad = lanes_cut(r, false), af = lanes_cut(r, true);
          cd[r] = __builtin_amdgcn_inverse_ballot_w64(ad + z) ? -__builtin_inff() : 0.f;
          cf[r] = __builtin_amdgcn_inverse_ballot_w64(af + z) ? -__builtin_inff() : 0.f;
      SEND };
    auto att_setup = [&](int part) {
        int l = tid; asm volatile("" : "+v"(l)); l &= 63;
        const int q = l & 31, h = l >> 5;
        if (part == 0) { dsh = (q - 4 * h) << 23; kb1 = KV0 + hp * 10240 + h * 2048 + q * 16; kb2 = h ? ONE : KV0 + hp * 10240 + 4096 + q * 8; nh = h ^ 1;
            asm volatile("" : "+v"(dsh), "+v"(kb1), "+v"(kb2), "+v"(nh)); }
        if (part == 1) { const bool ones = (l & 16) && (l & 3);
            vb0 = KV0 + VOFF + (4 * h + ((l & 15) >> 2)) * VP + (ones ? 160 : hp * 80 + (l & 16) * 2 + (l & 3) * 8); vb1 = vb0 + (ones ? 0 : 40);
            asm volatile("" : "+v"(vb0), "+v"(vb1)); }
        if (part == 2) { qb1 = QST + hp * 10240 + h * 2048 + (qi * 32 + q) * 16; qb2 = QST + hp * 10240 + 4096 + (qi * 32 + q) * 8;
            obase = (unsigned)(qi * 40960 + hp * 20480 + hg * 2560 + q * 16 + h * 512);
            asm volatile("" : "+v"(qb1), "+v"(qb2), "+v"(obase)); }
        if (part >= 3) {
#pragma unroll
            for (int r = 4 * (part - 3); r < 4 * (part - 2); ++r) mask_b(cd, r, false);
            asm volatile("" : "+v"(cd)); } };
    auto wo_setup = [&](int q) {
        int l = tid; asm volatile("" : "+v"(l)); const int c = q * 64 + (l & 63), rho = __umul24(c, 6554) >> 16;
        oo[q] = (unsigned)(__umul24(qo * 160 + hp * 40 + (rho & ~7) + 4 * (rho & 1) + ((rho >> 1) & 3), 1280) + hg * 160 + (c - rho * 10) * 16);
        asm volatile("" : "+v"(oo[q])); };
    u64 tag;

    if (wid < 4) {
        if (!isA) WAITV(0);
        BAR_L();
        STAMP(1);
        bf16x8 xr[32], xt[2], wf[WIN + 1];
        f32x16 acc[5];
        const int tok = wid * 32 + r32;
        const int xa = XS0 + tok * 128 + ((hl ^ ((tok >> 1) & 7)) << 4);
        int wa0 = WS0 + (8 * hl + ((lane & 15) >> 2)) * 320 + (lane & 16) * 2 + (lane & 3) * 8, wa1 = wa0 + 61440;
        asm volatile("" : "+v"(wa0), "+v"(wa1));
        auto rdx = [&](int i) { return *(LAS bf16x8*)(smem + ((i >> 2) % 3) * 16384 + (xa ^ ((i & 3) * 32))); };
        auto rdw = [&](int f) { const int i = f / 5, t = f % 5, sl = (i >> 1) % 7;
            return trfrag(smem + (sl < 6 ? wa0 + sl * 10240 : wa1) + (i & 1) * 5120 + t * 64, 1280); };
        unsigned fl = 0;
        auto rdflag = [&](int u) { fl = __hip_atomic_load((LAS unsigned*)(smem + C_F + u * 4), RLX_WG); };
        auto check = [&](int u) {
            if (__builtin_amdgcn_readfirstlane(fl) != 15) {
#pragma unroll 1
                for (unsigned spins = 0; spins < LSPIN_CAP; ++spins) { rdflag(u); if (__builtin_amdgcn_readfirstlane(fl) == 15) break; __builtin_amdgcn_s_sleep(1); }
            }
            FENCE(); };
#pragma unroll
        for (int i = 0; i < 4; ++i) xr[i] = rdx(i);
#pragma unroll
        for (int f = 0; f < WIN; ++f) wf[f] = rdw(f);
        SB();
        SFOR(f, 200)
            constexpr int i = f / 5, t = f % 5;
            if constexpr (f % 20 == 10 - WIN) lds_set(smem + C_P + wid * 4, (i >> 1) + 1);
            if constexpr (f == 200 - WIN) lds_set(smem + C_P + wid * 4, 20);
            if constexpr (f % 20 == 10) rdflag(f / 20 + 1);
            if constexpr (f % 20 == 20 - WIN && f < 190) check(f / 20 + 1);
            if constexpr (f + WIN < 200) wf[(f + WIN) % (WIN + 1)] = rdw(f + WIN);
            if constexpr (f % 20 >= 16 && f < 140) xr[(f / 20 + 1) * 4 + f % 20 - 16] = rdx((f / 20 + 1) * 4 + f % 20 - 16);
            if constexpr (t == 1 && i >= 31 && i < 39) xt[(i + 1) & 1] = rdx(i + 1);
            SB();
            if constexpr (i == 0) MFMA32Z(acc[t], wf[f % (WIN + 1)], xr[0]);
            else if constexpr (i < 32) MFMA32I(acc[t], wf[f % (WIN + 1)], xr[i]);
            else MFMA32I(acc[t], wf[f % (WIN + 1)], xt[i & 1]);
            SB();
        SEND
        STAMP(2);
        int l2 = tid; asm volatile("" : "+v"(l2)); l2 &= 63;
        const int h2 = l2 >> 5, tk = wid * 32 + (l2 & 31);
        int eb[5];
        auto ebases = [&](int base) { int l = tid; asm volatile("" : "+v"(l)); const int h = (l >> 5) & 1, k = wid * 32 + (l & 31);
            eb[0] = base + k * 16 + h * 8; eb[1] = base + k * 16 + h * 2040; eb[2] = base + (h ? k * 16 + 1024 : k * 8); eb[3] = base + (h ? k * 8 + 2040 : k * 16);
            asm volatile("" : "+v"(eb[0]), "+v"(eb[1]), "+v"(eb[2]), "+v"(eb[3])); };
        auto kq_addr = [&](int g0) { return eb[g0 % 5 == 1 ? 1 : g0 % 5 == 4 ? 2 : g0 % 5 == 3 ? 3 : 0] + kq_base(g0); };
        ebases(OWN);
        eb[4] = OWN + VOFF + tk * VP + h2 * 8; asm volatile("" : "+v"(eb[4]));
        int qa0 = (8 * h2 + 2 * ((l2 & 15) >> 2)) * 160 + (l2 & 16) * 2 + (l2 & 3) * 8, qa1 = qa0 + WS0 + 10240;
        asm volatile("" : "+v"(qa0), "+v"(qa1));
        auto rdq = [&](int f) { const int i = f / 3, t = f % 3, j = i >> 2, sl = (20 + j) % 7;
            return trfrag(smem + (j >= 7 ? qa0 + (j == 7 ? QU27 : j == 8 ? QU28 : QU29) : sl == 0 ? qa0 + WS0 : qa1 + (sl - 1) * 10240) + (i & 3) * 2560 + t * 64, 160); };
        MFMA_DRAIN();
        WAITV(0);
        { u32x2 pk[20];
#pragma unroll
          for (int n = 0; n < 20; ++n) { const int t = n >> 2, b = n & 3; pk[n] = (u32x2){pk2(acc[t][4 * b], acc[t][4 * b + 1]), pk2(acc[t][4 * b + 2], acc[t][4 * b + 3])}; }
          SB();
#pragma unroll
          for (int n = 0; n < 20; ++n) *(LAS u32x2*)(smem + (n < 10 ? kq_addr(2 * n) : eb[4] + (2 * n - 20) * 8)) = pk[n];
          if (h2 == 0) *(LAS u32x2*)(smem + eb[4] + 160) = (u32x2){0x3F803F80u, 0x3F803F80u}; }
        lds_set(smem + C_E + wid * 4, 1);
        check(10);
        xt[0] = rdx(32);
#pragma unroll
        for (int f = 0; f < WIN; ++f) wf[f] = rdq(f);
        STAMP(3);
        SB();
        u64 pv0 = 0, pv1 = 0;
        SFOR(f, 120)
            constexpr int i = f / 3, t = f % 3, j = i >> 2;
            if constexpr (f % 24 == 12 && j < 9) rdflag(11 + j / 2);
            if constexpr (f % 12 == 12 - WIN && (j == 1 || j == 6 || j == 8)) lds_set(smem + C_P + wid * 4, 21 + j);
            if constexpr (f % 24 == 24 - WIN && j < 9) check(11 + j / 2);
            if constexpr (f + WIN < 120) wf[(f + WIN) % (WIN + 1)] = rdq(f + WIN);
            if constexpr (t == 0 && i < 7) xt[(i + 1) & 1] = rdx(32 + i + 1);
            if constexpr (f >= 90 && f < 100 && !(f & 1)) wo_setup((f - 90) / 2);
            if constexpr (f == 99) tag = rdtag();
            if constexpr (f >= 100 && f < 107) att_setup(f - 100);
            if constexpr (f == 108) ebases(QST);
            if constexpr (f == 116) { pv0 = __hip_atomic_load((LAS u64*)(smem + C_P), RLX_WG); pv1 = __hip_atomic_load((LAS u64*)(smem + C_P + 8), RLX_WG); }
            SB();
            if constexpr (i == 0) MFMA32Z(acc[t], wf[f % (WIN + 1)], xt[0]);
            else if constexpr (i < 8) MFMA32I(acc[t], wf[f % (WIN + 1)], xt[i & 1]);
            else MFMA32I(acc[t], wf[f % (WIN + 1)], xr[i - 8]);
            SB();
        SEND
        STAMP(4);
        if (__builtin_amdgcn_readfirstlane((int)min(min((unsigned)pv0, (unsigned)(pv0 >> 32)), min((unsigned)pv1, (unsigned)(pv1 >> 32)))) < 29) wait_min4(smem + C_P, 29);
        MFMA_DRAIN();
        { acc[0] *= QSCALE; acc[1] *= QSCALE; acc[2] *= QSCALE;
          SB();
          u32x2 pk[10];
#pragma unroll
          for (int n = 0; n < 10; ++n) { const int t = n >> 2, b = n & 3; pk[n] = (u32x2){pk2(acc[t][4 * b], acc[t][4 * b + 1]), pk2(acc[t][4 * b + 2], acc[t][4 * b + 3])}; }
          SB();
#pragma unroll
          for (int n = 0; n < 10; ++n) *(LAS u32x2*)(smem + kq_addr(2 * n)) = pk[n]; }
        lds_set(smem + C_QF + wid * 4, 1);
        __builtin_amdgcn_s_setprio(1);
        STAMP(5);
    } else {
        const int hw = wid - 4;
        int known = 0;
        __builtin_amdgcn_s_setprio(3);
        auto lds_or = [&](int off, unsigned v) { FENCE(); if (lane == 0) __hip_atomic_fetch_or((LAS unsigned*)(smem + off), v, RLX_WG); FENCE(); };
        unsigned wo[5], qo[5];
        auto done = [&](int n) { lds_or(C_F + n * 4, 1u << hw); };
        const unsigned xo1 = (xo ^ 64) + 9216, xo2 = xo + 2 * 9216, xo3 = (xo ^ 64) + 3 * 9216;
        auto round_x = [&](int n) { dma16x4(xo, xo1, xo2, xo3, xb + kt_of(n) * 128 + hw * (32 * 1280), lds0 + XS0 + (n % 3) * 16384 + hw * 4096); };
        auto round_w = [&](int n) {
            const int u = 2 * n + (hw >> 1);
            const unsigned dst = lds0 + (u < 27 ? WS0 + (u % 7) * 10240 : u == 27 ? QU27 : u == 28 ? QU28 : QU29) + (hw & 1) * 5120 + 2048;
            if (u < 20) dma16x5(wo, wb + (size_t)(kt_of(u >> 1) * 64 + (u & 1) * 32) * 3840 - 2048, dst);
            else dma16x5(qo, wb + (size_t)kt_of(u < 22 ? u - 12 : u - 22) * (64 * 3840) - 2048, dst); };
        if (isA) round_x(0);
        round_x(1);
#pragma unroll
        for (int q = 0; q < 5; ++q) wo[q] = w_off(q) + (hw & 1) * (16 * 3840) + (4 - q) * 1024;
        if (isA) { round_w(0); round_w(1); WAITV(5); } else { round_w(1); WAITV(9); }
        BAR_L();
#pragma unroll
        for (int q = 0; q < 5; ++q) { const int c = q * 64 + lane, rho = __umul24(c, 6554) >> 16;
            qo[q] = (unsigned)(__umul24((hw & 1) * 32 + (rho & ~7) + 4 * (rho & 1) + ((rho >> 1) & 3), 3840) + (c - rho * 10) * 16 + (4 - q) * 1024); }
#pragma unroll 1
        for (int n = 2; n <= 13; ++n) {
            const int need = n < 13 ? 2 * n - 5 : 20;
            if (need > known) known = min4(smem + C_P);
            if (need > known) { WAITV(0); done(n - 1); known = wait_min4(smem + C_P, need); }
            if (n < 10) round_x(n);
            round_w(n);
            if (n < 10) WAITV(9); else WAITV(5);
            done(n - 1);
        }
        WAITV(0); done(13);
        STAMP(1);
        tag = rdtag();
        auto a_chunk = [&](int a) { int o;
            if (a < 320) { const int hd = a / 80, rem = a % 80; o = hd * 5120 + (rem < 32 ? (96 + rem) * 16 : rem < 64 ? 2048 + (64 + rem) * 16 : 4096 + 768 + (rem - 64) * 16); }
            else o = VOFF + 96 * VP + (a - 320) * 16;
            return o; };
        if (rt < 15) {
            const __amdgpu_buffer_rsrc_t kvr = __builtin_amdgcn_make_buffer_rsrc(ws + WS_KV + me * KVG, 0, KVB, 0x00020000);
            wait_min4(smem + C_E, 1);
            STAMP(2);
            if (!isA) { u32x4 v[11];
#pragma unroll
                for (int j = 0; j < 11; ++j) if (j < 10 || hw == 3) v[j] = *(LAS u32x4*)(smem + OWN + hw * 10240 + j * 1024 + lane * 16);
#pragma unroll
                for (int j = 0; j < 11; ++j) if (j < 10 || hw == 3) __builtin_amdgcn_raw_buffer_store_b128(v[j], kvr, hw * 10240 + j * 1024 + lane * 16, 0, 16);
            } else {
#pragma unroll
                for (int j = 0; j < 3; ++j) { const int a = (4 * j + hw) * 64 + lane;
                    if (a < 656) { const int o = a_chunk(a); __builtin_amdgcn_raw_buffer_store_b128(*(LAS u32x4*)(smem + OWN + o), kvr, o, 0, 16); } }
            }
            STAMP(3);
        }
        wait_min4(smem + C_P, 22);
        round_w(14);
        WAITV(5);
        if (rt < 15) {
            unsigned old = 0;
            if (lane == 0) old = __hip_atomic_fetch_add((LAS unsigned*)(smem + N_PUB), 1u, RLX_WG);
            if (lane == 0 && old == 3) __hip_atomic_store((GAS u64*)(ws + WS_FKV + me * 128), tag, RLX_AGENT);
            STAMP(4);
        }
        WAITV(0); done(14);
        __builtin_amdgcn_s_setprio(0);
#pragma unroll
        for (int p = 0; p < 7; ++p) att_setup(p);
#pragma unroll
        for (int q = 0; q < 5; ++q) wo_setup(q);
        __builtin_amdgcn_s_setprio(3);
        if (hw < NPOLL) {
            for (int i = 0; i < hw; ++i) __builtin_amdgcn_s_sleep(PSTAG);
            poll_shared((GAS u64*)(ws + WS_FKV + (me - 8 * (lane + 1)) * 128), lane < nnb, tag, smem + N_KV);
        } else wait_word(smem + N_KV, 1);
        STAMP(5);
        if (!isA) {
            const int n = hw >> 1, half = hw & 1;
            if (n < nnb) {
                const char* src = ws + WS_KV + (me - 8 * (n + 1)) * KVG + half * 20480; const unsigned dst = lds0 + KV0 + (1 - n) * KVB + half * 20480;
                wait_min4(smem + C_P, 27);
#pragma unroll
                for (int j = 0; j < 4; ++j) dma16x5_sc1(lane * 16, src + j * 5120 + 2048, dst + j * 5120 + 2048);
                if (half) dma16_sc1(lane * 16, src + 20480, dst + 20480);
            }
        } else if (nnb) {
            const __amdgpu_buffer_rsrc_t nbr = __builtin_amdgcn_make_buffer_rsrc(ws + WS_KV + (me - 8) * KVG, 0, KVB, 0x00020000);
            u32x4 v[3];
#pragma unroll
            for (int j = 0; j < 3; ++j) { const int a = (4 * j + hw) * 64 + lane; if (a < 656) v[j] = __builtin_amdgcn_raw_buffer_load_b128(nbr, a_chunk(a), 0, 16); }
            wait_min4(smem + C_P, 27);
#pragma unroll
            for (int j = 0; j < 3; ++j) { const int a = (4 * j + hw) * 64 + lane; if (a < 656) *(LAS u32x4*)(smem + KV0 + KVB + a_chunk(a)) = v[j]; }
        }
        __builtin_amdgcn_s_setprio(2);
        wait_word(smem + C_QF + qi * 4, 1);
        STAMP(6);
    }
    u32x4 wreg[13];
    auto ldwo = [&](int i) { wreg[i] = __builtin_amdgcn_raw_buffer_load_b128(wor, oo[i % 5] + (i / 5) * (32 * 1280) + (i > 6 || (i == 6 && lane >= 16) ? 40 * 1280 : 0), 0, 0); };
    const __amdgpu_buffer_rsrc_t orr = __builtin_amdgcn_make_buffer_rsrc(ws + WS_O + (br * 16 + rt) * OG, 0, 4 * 40960, 0x00020000);
    unsigned warm0 = 0, warm1 = 0;
    STAMP(7);
    u32x2 ob[2][3];
    {
        bf16x8 q1[2]; u32x2 q2[2];
#pragma unroll
        for (int e = 0; e < 2; ++e) {
            q1[e] = *(LAS bf16x8*)(smem + qb1 + e * 5120);
            q2[e] = *(LAS u32x2*)(smem + qb2 + e * 5120);
            if (hl) q2[e] = (u32x2){0u, 0u};
        }
        if (isA) mask_a(0);
        struct KF { bf16x8 a; s16x4 b; };
        struct VF { bf16x8 a, b; };
        auto rdk = [&](int u, int e) { const int so = (u >> 2) * KVB, tw = u & 3; KF k;
            k.a = *(LAS bf16x8*)(smem + kb1 + so + tw * 512 + e * 5120); k.b = *(LAS s16x4*)(smem + __umul24(so + tw * 256 + e * 5120, nh) + kb2); return k; };
        auto rdv = [&](int u, int e) { LAS char* p = smem + (e ? vb1 : vb0) + (u >> 2) * KVB + (u & 3) * (32 * VP); VF v;
            v.a = trfrag(p, 8 * VP); v.b = trfrag(p + 16 * VP, 8 * VP); return v; };
        auto qk = [&](const KF& k, int e, const f32x16& c) { return MFMA32K8(k.b, __builtin_bit_cast(s16x4, q2[e]), MFMA32(k.a, q1[e], c)); };
        const f32x16 zero = {};
        auto max16 = [&](const f32x16& s, float m) {
#pragma unroll
            for (int r = 0; r < 16; ++r) m = __builtin_elementwise_maximum(m, s[r]);
            return m; };
        auto set_ref = [&](float m, int e) { const unsigned nb = pk2(-m, 0.f); if (hl) q2[e][0] = nb; };
        auto ex4 = [&](f32x16& s, int b) {
#pragma unroll
            for (int r = 0; r < 4; ++r) s[b + r] = __builtin_amdgcn_exp2f(s[b + r]); };
        auto pk8 = [&](const f32x16& s, int b) { return __builtin_bit_cast(bf16x8, (u32x4){pk2(s[b], s[b + 1]), pk2(s[b + 2], s[b + 3]), pk2(s[b + 4], s[b + 5]), pk2(s[b + 6], s[b + 7])}); };
        auto phase = [&](auto nx, f32x16& cur, const VF& v, f32x16& o, f32x16& nxt, const KF& k, int e) __attribute__((always_inline)) {
            constexpr int NX = decltype(nx)::value;
            SB(); ex4(cur, 0); SB();
            if constexpr (NX) nxt = MFMA32(k.a, q1[e], NX == 1 ? zero : NX == 2 ? cf : cd);
            SB(); ex4(cur, 4); { const bf16x8 p = pk8(cur, 0); SB(); o = MFMA32(v.a, p, o); }
            SB(); ex4(cur, 8); SB();
            if constexpr (NX) nxt = MFMA32K8(k.b, __builtin_bit_cast(s16x4, q2[e]), nxt);
            SB(); ex4(cur, 12); { const bf16x8 p = pk8(cur, 8); SB(); o = MFMA32(v.b, p, o); }
            SB(); };
        f32x16 o0, o1, sa, sb; KF kx; VF vx;
        auto step = [&](auto m0, auto m1, int u) __attribute__((always_inline)) {
            constexpr int M1 = decltype(m1)::value;
            KF ky; if constexpr (M1) ky = rdk(u - 1, 0);
            const VF vy = rdv(u, 1);
            phase(m0, sa, vx, o0, sb, kx, 1);
            if constexpr (M1) { kx = rdk(u - 1, 1); vx = rdv(u - 1, 0); }
            phase(m1, sb, vy, o1, sa, ky, 0); };
        auto nbwait = [&](int u) { if (u == 8) wait_word(smem + N_NB, 4); };
        { const KF k0f = rdk(ud, 0); kx = rdk(ud, 1); vx = rdv(ud, 0);
          const f32x16 s0 = qk(k0f, 0, cd), s1 = qk(kx, 1, cd);
          set_ref(halfmax(max16(s0, s0[0])), 0); set_ref(halfmax(max16(s1, s1[0])), 1);
          sa = qk(k0f, 0, cd); }
        if (hp) { WAITV(0); lds_inc0(smem + N_NB, lane); lds_inc0(smem + N_G0, lane); }
        o0 = zero; o1 = zero;
        if (u0 + 8 == ud) {
            SFOR(it, 9)
                if constexpr (it < 7) { ldwo(2 * it); if constexpr (it < 6) ldwo(2 * it + 1); }
                if constexpr (it == PFLIP) { if (hp) __builtin_amdgcn_s_setprio(0); }
                if constexpr (it == 7) {
#pragma unroll
                    for (int r = 0; r < 16; ++r) mask_b(cf, r, true); }
                if constexpr (it < 4) nbwait(ud - it);
                step(IC<it == 0 ? 3 : it == 8 ? 2 : 1>{}, IC<it == 8 ? 0 : it == 7 ? 2 : 1>{}, ud - it);
            SEND
        } else {
            __builtin_amdgcn_s_setprio(0);
#pragma unroll
            for (int i = 0; i < 13; ++i) ldwo(i);
            if (!isA) cf = zero;
            if (u0 < ud) {
                nbwait(ud);
                if (u0 + 1 < ud) {
                    step(IC<3>{}, IC<1>{}, ud);
#pragma unroll 1
                    for (int u = ud - 1; u > u0 + 1; --u) { nbwait(u); step(IC<1>{}, IC<1>{}, u); }
                    nbwait(u0 + 1);
                    step(IC<1>{}, IC<2>{}, u0 + 1);
                } else step(IC<3>{}, IC<2>{}, ud);
                step(IC<2>{}, IC<0>{}, u0);
            } else step(IC<3>{}, IC<0>{}, ud);
        }
        if (!__all(o0[12] < 1.152921504606847e18f && o1[12] < 1.152921504606847e18f)) {
            if (isA) { int z = 0; asm volatile("" : "+s"(z)); mask_a(z); }
            else { asm volatile("" : "+v"(dsh));
#pragma unroll
                for (int r = 0; r < 16; ++r) { mask_b(cd, r, false); mask_b(cf, r, true); }
                if (!far) cf = zero; }
#pragma unroll
            for (int e = 0; e < 2; ++e) {
                if (hl) q2[e][0] = 0u;
                const f32x16 s = qk(rdk(ud, e), e, cd);
                float m = max16(s, s[0]);
                if (u0 < ud) m = max16(qk(rdk(u0, e), e, cf), m);
#pragma unroll 1
                for (int u = u0 + 1; u < ud; ++u) m = max16(qk(rdk(u, e), e, zero), m);
                set_ref(halfmax(m), e);
            }
#pragma unroll
            for (int e = 0; e < 2; ++e) {
                f32x16 o = zero;
#pragma unroll 1
                for (int u = u0; u <= ud; ++u) { f32x16 t = u == ud ? qk(rdk(u, e), e, cd) : u == u0 ? qk(rdk(u, e), e, cf) : qk(rdk(u, e), e, zero);
                    const VF v = rdv(u, e); ex4(t, 0); ex4(t, 4); ex4(t, 8); ex4(t, 12);
                    o = MFMA32(v.a, pk8(t, 0), o); o = MFMA32(v.b, pk8(t, 8), o); }
                if (e) o1 = o; else o0 = o;
            }
        }
        const float rl0 = __builtin_amdgcn_rcpf(o0[12]), rl1 = __builtin_amdgcn_rcpf(o1[12]);
        SB();
#pragma unroll
        for (int r = 0; r < 12; ++r) { o0[r] *= rl0; o1[r] *= rl1; }
        SB();
#pragma unroll
        for (int j = 0; j < 3; ++j) {
            ob[0][j] = (u32x2){pk2(o0[4 * j], o0[4 * j + 1]), pk2(o0[4 * j + 2], o0[4 * j + 3])};
            ob[1][j] = (u32x2){pk2(o1[4 * j], o1[4 * j + 1]), pk2(o1[4 * j + 2], o1[4 * j + 3])}; }
        SB();
    }
    STAMP(8);
    {
      { auto rx = __builtin_amdgcn_permlane32_swap(ob[0][0][0], ob[0][1][0], false, false);
        auto ry = __builtin_amdgcn_permlane32_swap(ob[0][0][1], ob[0][1][1], false, false);
        __builtin_amdgcn_raw_buffer_store_b128((u32x4){rx[0], ry[0], rx[1], ry[1]}, orr, obase, 0, 16); }
      if (hl == 0) __builtin_amdgcn_raw_buffer_store_b128((u32x4){ob[0][2][0], ob[0][2][1], ob[1][0][0], ob[1][0][1]}, orr, obase + 1024, 0, 16);
      { auto fx = __builtin_amdgcn_permlane32_swap(ob[1][0][0], ob[1][1][0], false, false);
        auto fy = __builtin_amdgcn_permlane32_swap(ob[1][0][1], ob[1][1][1], false, false);
        auto sx = __builtin_amdgcn_permlane32_swap(ob[1][1][0], ob[1][2][0], false, false);
        auto sy = __builtin_amdgcn_permlane32_swap(ob[1][1][1], ob[1][2][1], false, false);
        __builtin_amdgcn_raw_buffer_store_b128((u32x4){fx[1], fy[1], sx[0], sy[0]}, orr, obase + 1536, 0, 16); } }
    if (lane == 0) { (void)__hip_atomic_fetch_add((LAS unsigned*)(smem + N_G1), 1u, RLX_WG);
        if (!hp) (void)__hip_atomic_fetch_add((LAS unsigned*)(smem + N_G0), 1u, RLX_WG); }
    FENCE();
    STAMP(6);
    auto signal_o = [&]() {
        if (lds_inc(smem + N_OS + hp * 4, lane) == 3 && lane == 0) __hip_atomic_store((GAS u64*)(ws + WS_FO + (me * 2 + hp) * 128), tag, RLX_AGENT); };
    __builtin_amdgcn_s_setprio(0);
    WAITV(0); signal_o();
    STAMP(9);
    wait_word(smem + (hp ? N_G1 : N_G0), 8);
    const int p1 = hp ? 30720 : 35840, p2 = hp ? 65536 : 43520;
    const int wb0 = hp ? KV0 + 10240 : 512, wb1 = hp ? KV0 + KVB + 10240 - 30720 : KV0 - 35840, wb2 = KV0 + KVB - 43520;
#pragma unroll
    for (int i = 0; i < 13; ++i) { const int L = qo * 12800 + i * 1024;
        if (i < 12 || lane < 32) *(LAS u32x4*)(smem + (L + (L < p1 ? wb0 : L < p2 ? wb1 : wb2)) + lane * 16) = wreg[i]; }
    lds_inc0(smem + N_WO + hp * 4, lane);
    STAMP(10);
    __builtin_amdgcn_s_sleep(PDELAY);
    if (qo < NPOLL) {
        for (int i = 0; i < qo; ++i) __builtin_amdgcn_s_sleep(PSTAG);
        poll_shared((GAS u64*)(ws + WS_FO + (((me & ~7) + (lane & 7)) * 2 + hp) * 128), lane < 8, tag, smem + N_OK + hp * 4);
    } else wait_word(smem + N_OK + hp * 4, 1);
    STAMP(11);
    f32x16 oc[3]; f32x4 ps[10];
    LAS char* red = smem + OWN + qo * 10240 + lane * 16;
    { bf16x8 af[5], of[20];
      const int la = (8 * hl + 2 * ((lane & 15) >> 2)) * 160 + (lane & 16) * 2 + (lane & 3) * 8;
      const int aa0 = wb0 + la, aa1 = (hp ? wb1 : wb0) + la, aa2 = wb1 + la, aa3 = (hp ? wb1 : wb2) + la;
      auto rda = [&](int f) { return trfrag(smem + (f < 36 ? aa0 : f < 42 ? aa1 : f < 51 ? aa2 : aa3) + (f / 3) * 2560 + (f % 3) * 64, 160); };
      auto ldo = [&](int k) { of[k] = __builtin_bit_cast(bf16x8, __builtin_amdgcn_raw_buffer_load_b128(orr, qo * 40960 + (hp * 20 + k) * 1024 + lane * 16, 0, 16)); };
#pragma unroll
      for (int k = 0; k < OPF; ++k) ldo(k);
      wait_word(smem + N_WO + hp * 4, 4);
#pragma unroll
      for (int f = 0; f < 4; ++f) af[f] = rda(f);
      SB();
      SFOR(f, 60)
          if constexpr (f % 3 == 0 && f / 3 + OPF < 20) ldo(f / 3 + OPF);
          if constexpr (f + 4 < 60) af[(f + 4) % 5] = rda(f + 4);
          if constexpr (f == 57) { if (hp) { wait_word(smem + C_RD + qo * 4, 1);
#pragma unroll
              for (int b = 0; b < 10; ++b) ps[b] = *(LAS f32x4*)(red + b * 1024); } }
          SB();
          if constexpr (f < 3) MFMA32Z(oc[f], af[f % 5], of[0]); else MFMA32I(oc[f % 3], af[f % 5], of[f / 3]);
          SB();
      SEND }
    MFMA_DRAIN();
    STAMP(12);
    { if (!hp) {
          wait_word(smem + N_G1, 8);
#pragma unroll
          for (int b = 0; b < 10; ++b) *(LAS f32x4*)(red + b * 1024) = (f32x4){oc[b >> 2][4 * (b & 3)], oc[b >> 2][4 * (b & 3) + 1], oc[b >> 2][4 * (b & 3) + 2], oc[b >> 2][4 * (b & 3) + 3]};
          lds_set(smem + C_RD + qo * 4, 1);
      } else {
          char* orow = out + (size_t)(rt * 128 + qo * 32 + r32) * 2560 + br * 1280 + hg * 160 + hl * 16;
#pragma unroll
          for (int b = 0; b < 10; ++b)
#pragma unroll
              for (int r = 0; r < 4; ++r) ps[b][r] += oc[b >> 2][4 * (b & 3) + r];
          SB();
          u32x2 pk[10];
#pragma unroll
          for (int b = 0; b < 10; ++b) pk[b] = (u32x2){pk2(ps[b][0], ps[b][1]), pk2(ps[b][2], ps[b][3])};
          SB();
          u32x4 st[5];
#pragma unroll
          for (int b = 0; b < 10; b += 2) {
              auto rx = __builtin_amdgcn_permlane32_swap(pk[b][0], pk[b + 1][0], false, false); auto ry = __builtin_amdgcn_permlane32_swap(pk[b][1], pk[b + 1][1], false, false);
              st[b >> 1] = (u32x4){rx[0], ry[0], rx[1], ry[1]}; }
          SB();
#pragma unroll
          for (int b = 0; b < 5; ++b) *(u32x4*)(orow + b * 32) = st[b];
      } }
    if (bx == 0 && wid == 0) {
#pragma unroll 1
        for (int i = 0; i < 4; ++i) poll_flags((GAS u64*)(ws + WS_FO + (i * 64 + lane) * 256), true, tag);
        if (lane == 0) __hip_atomic_store((GAS u64*)ws, tag, RLX_AGENT);
    }
}

extern "C" void kernel_launch(void* const* d_in, const int* in_sizes, int n_in, void* d_out, int out_size, void* d_ws, size_t ws_size,
                              hipStream_t stream) {
    fused_attn_kernel<<<256, 512, 0, stream>>>((const char*)d_in[0], (const char*)d_in[1], (const char*)d_in[2], (const char*)d_in[3],
                                               (const char*)d_in[4], (const char*)d_in[5], (char*)d_out, (char*)d_ws);
}
```

```cpp
#define PDELAY 14
#define PSLEEP 4
#include <hip/hip_runtime.h>
#include <hip/hip_bf16.h>
#include <stdint.h>

typedef short bf16x8 __attribute__((ext_vector_type(8)));
typedef short s16x4 __attribute__((ext_vector_type(4)));
typedef float f32x2 __attribute__((ext_vector_type(2)));
typedef float f32x4 __attribute__((ext_vector_type(4)));
typedef float f32x16 __attribute__((ext_vector_type(16)));
typedef unsigned u32x4 __attribute__((ext_vector_type(4)));
typedef unsigned u32x2 __attribute__((ext_vector_type(2)));
typedef __bf16 bfx2 __attribute__((ext_vector_type(2)));
typedef unsigned long long u64;
#define LAS __attribute__((address_space(3)))
#define GAS __attribute__((address_space(1)))
#define DI __device__ __forceinline__
#define RLX_AGENT __ATOMIC_RELAXED, __HIP_MEMORY_SCOPE_AGENT
#define RLX_WG __ATOMIC_RELAXED, __HIP_MEMORY_SCOPE_WORKGROUP

constexpr int LDS_BYTES = 163840;
constexpr int VOFF = 20480, VP = 168;
constexpr int KVB = VOFF + 128 * VP;
constexpr int KV0 = LDS_BYTES - 3 * KVB;
constexpr int OWN = KV0 + 2 * KVB;
constexpr int C_F = 0, C_P = 128, C_E = 144, C_QF = 160, C_RD = 176, C_CNT = 192, C_TAG = 320;
constexpr int N_PUB = C_CNT, N_NB = C_CNT + 4, N_G0 = C_CNT + 8, N_G1 = C_CNT + 12, N_WO = C_CNT + 16, N_OS = C_CNT + 24, N_KV = C_CNT + 32, N_OK = C_CNT + 36;
constexpr int XS0 = 512, WS0 = XS0 + 3 * 16384, QST = 512, ONE = 336, QU27 = XS0 + 16384, QU28 = XS0, QU29 = QU27 + 10240;
static_assert(WS0 + 7 * 10240 <= OWN, "ring overlaps the own image");
constexpr unsigned WS_FKV = 4096, WS_FO = WS_FKV + 256 * 128, WS_KV = 131072;
constexpr unsigned KVG = KVB, OG = 4 * 40960;
constexpr unsigned WS_O = WS_KV + 256 * KVG;
constexpr unsigned WS_END = WS_O + 32 * OG;
constexpr unsigned WS_DBG = WS_END;
constexpr unsigned SPIN_CAP = 400000, LSPIN_CAP = 4000000;
constexpr float QSCALE = 0.22360679774997896f * 1.4426950408889634f;
#define KDELAY 0
#define NPOLL 1
#define PSTAG 3
#define PFLIP 6
#define OPF 12
#define ABL4N 2
#define WIN 4
#define ADELAY 0

#define MFMA32(a, b, c) __builtin_amdgcn_mfma_f32_32x32x16_bf16((a), (b), (c), 0, 0, 0)
#define MFMA32K8(a, b, c) __builtin_amdgcn_mfma_f32_32x32x8bf16_1k((a), (b), (c), 0, 0, 0)
#define MFMA32I(acc, a, b) asm volatile("v_mfma_f32_32x32x16_bf16 %0, %1, %2, %0" : "+v"(acc) : "v"(a), "v"(b))
#define MFMA32Z(acc, a, b) asm volatile("v_mfma_f32_32x32x16_bf16 %0, %1, %2, 0" : "=&v"(acc) : "v"(a), "v"(b))
#define MFMA_DRAIN() asm volatile("s_nop 7\n\ts_nop 4" ::: "memory")
#define WAITV(N) asm volatile("s_waitcnt vmcnt(" #N ")" ::: "memory")
#define WAITL() asm volatile("s_waitcnt lgkmcnt(0)" ::: "memory")
#define BAR_L() asm volatile("s_waitcnt lgkmcnt(0)\n\ts_barrier" ::: "memory")
#define SB() __builtin_amdgcn_sched_barrier(0)
#define FENCE() asm volatile("" ::: "memory")

#define STAMP(i) do {} while (0)

template <int V> struct IC { static constexpr int value = V; };
template <int I, int N, class F> DI void static_for(F&& f) { if constexpr (I < N) { f(IC<I>{}); static_for<I + 1, N>(f); } }
#define SFOR(var, N) static_for<0, N>([&](auto ic_##var) __attribute__((always_inline)) { constexpr int var = decltype(ic_##var)::value;
#define SEND });

DI void dma16(unsigned voff, const char* sbase, unsigned ldsaddr) {
    asm volatile("s_mov_b32 m0, %2\n\ts_nop 0\n\tglobal_load_lds_dwordx4 %0, %1" :: "v"(voff), "s"(sbase), "s"(ldsaddr) : "memory");
}
DI void dma16x4(unsigned v0, unsigned v1, unsigned v2, unsigned v3, const char* sbase, unsigned ldsaddr) {
    asm volatile("s_mov_b32 m0, %5\n\ts_nop 0\n\tglobal_load_lds_dwordx4 %0, %4\n\tglobal_load_lds_dwordx4 %1, %4 offset:1024\n\t"
                 "global_load_lds_dwordx4 %2, %4 offset:2048\n\tglobal_load_lds_dwordx4 %3, %4 offset:3072"
                 :: "v"(v0), "v"(v1), "v"(v2), "v"(v3), "s"(sbase), "s"(ldsaddr) : "memory");
}
DI void dma16x5(const unsigned (&v)[5], const char* sbase, unsigned ldsaddr) {
    asm volatile("s_mov_b32 m0, %6\n\ts_nop 0\n\tglobal_load_lds_dwordx4 %0, %5 offset:-2048\n\tglobal_load_lds_dwordx4 %1, %5 offset:-1024\n\t"
                 "global_load_lds_dwordx4 %2, %5\n\tglobal_load_lds_dwordx4 %3, %5 offset:1024\n\tglobal_load_lds_dwordx4 %4, %5 offset:2048"
                 :: "v"(v[0]), "v"(v[1]), "v"(v[2]), "v"(v[3]), "v"(v[4]), "s"(sbase), "s"(ldsaddr) : "memory");
}
DI void dma16x5_sc1(unsigned voff, const char* sbase, unsigned ldsaddr) {
    asm volatile("s_mov_b32 m0, %2\n\ts_nop 0\n\tglobal_load_lds_dwordx4 %0, %1 offset:-2048 sc1\n\tglobal_load_lds_dwordx4 %0, %1 offset:-1024 sc1\n\t"
                 "global_load_lds_dwordx4 %0, %1 sc1\n\tglobal_load_lds_dwordx4 %0, %1 offset:1024 sc1\n\tglobal_load_lds_dwordx4 %0, %1 offset:2048 sc1"
                 :: "v"(voff), "s"(sbase), "s"(ldsaddr) : "memory");
}
DI void dma16_sc1(unsigned voff, const char* sbase, unsigned ldsaddr) {
    asm volatile("s_mov_b32 m0, %2\n\ts_nop 0\n\tglobal_load_lds_dwordx4 %0, %1 sc1" :: "v"(voff), "s"(sbase), "s"(ldsaddr) : "memory");
}
DI void dma4(const char* p, unsigned ldsaddr) {
    asm volatile("s_mov_b32 m0, %1\n\ts_nop 0\n\tglobal_load_lds_dword %0, off" :: "v"(p), "s"(ldsaddr) : "memory");
}
DI unsigned pk2(float lo, float hi) { f32x2 v = {lo, hi}; return __builtin_bit_cast(unsigned, __builtin_convertvector(v, bfx2)); }
typedef short v4i16_t __attribute__((ext_vector_type(4)));
DI bf16x8 trfrag(LAS char* p, int second) {
    s16x4 a = __builtin_bit_cast(s16x4, __builtin_amdgcn_ds_read_tr16_b64_v4i16((LAS v4i16_t*)p));
    s16x4 b = __builtin_bit_cast(s16x4, __builtin_amdgcn_ds_read_tr16_b64_v4i16((LAS v4i16_t*)(p + second)));
    return __builtin_shufflevector(a, b, 0, 1, 2, 3, 4, 5, 6, 7);
}
constexpr u64 lanes_cut(int r, bool fartile) {
    u64 m = 0;
    for (int l = 0; l < 64; ++l) { const int key = (r & 3) + 8 * (r >> 2) + 4 * (l >> 5), d = (l & 31) - key + (fartile ? 32 : 0);
        const bool ok = d >= 0 && d <= 14 && !(d & 1);
        if (!ok) m |= 1ull << l; }
    return m;
}
DI float halfmax(float m) { auto rr = __builtin_amdgcn_permlane32_swap(__float_as_uint(m), __float_as_uint(m), false, false);
    return __builtin_elementwise_maximum(__uint_as_float(rr[0]), __uint_as_float(rr[1])); }

DI void poll_flags(GAS u64* word, bool active, u64 tag) {
#pragma unroll 1
    for (unsigned spins = 0; spins < SPIN_CAP; ++spins) {
        u64 v = tag;
        if (active) v = __hip_atomic_load(word, RLX_AGENT);
        if (__all(v == tag)) break;
        __builtin_amdgcn_s_sleep(PSLEEP);
    }
    __builtin_amdgcn_fence(__ATOMIC_ACQUIRE, "wavefront");
}
DI void poll_shared(GAS u64* word, bool active, u64 tag, LAS char* ok) {
#pragma unroll 1
    for (unsigned spins = 0; spins < SPIN_CAP; ++spins) {
        if (__builtin_amdgcn_readfirstlane((int)__hip_atomic_load((LAS unsigned*)ok, RLX_WG))) break;
        u64 v = tag;
        if (active) v = __hip_atomic_load(word, RLX_AGENT);
        if (__all(v == tag)) { __hip_atomic_store((LAS unsigned*)ok, 1u, RLX_WG); break; }
        __builtin_amdgcn_s_sleep(PSLEEP);
    }
    __builtin_amdgcn_fence(__ATOMIC_ACQUIRE, "wavefront");
    asm volatile("" ::: "memory");
}
DI int min4(LAS char* p) {
    const u64 a = __hip_atomic_load((LAS u64*)p, RLX_WG), b = __hip_atomic_load((LAS u64*)(p + 8), RLX_WG);
    const unsigned m = min(min((unsigned)a, (unsigned)(a >> 32)), min((unsigned)b, (unsigned)(b >> 32)));
    return __builtin_amdgcn_readfirstlane((int)m);
}
DI int wait_min4(LAS char* p, int need) {
    int v = min4(p);
#pragma unroll 1
    for (unsigned spins = 0; v < need && spins < LSPIN_CAP; ++spins) { __builtin_amdgcn_s_sleep(1); v = min4(p); }
    FENCE();
    return v;
}
DI void wait_word(LAS char* p, unsigned need) {
#pragma unroll 1
    for (unsigned spins = 0; spins < LSPIN_CAP; ++spins) {
        if ((unsigned)__builtin_amdgcn_readfirstlane((int)__hip_atomic_load((LAS unsigned*)p, RLX_WG)) >= need) break;
        __builtin_amdgcn_s_sleep(1); }
    FENCE();
}
DI void lds_inc0(LAS char* p, int lane) { FENCE(); if (lane == 0) (void)__hip_atomic_fetch_add((LAS unsigned*)p, 1u, RLX_WG); FENCE(); }
DI void wait_pair(LAS char* p, unsigned need) {
#pragma unroll 1
    for (unsigned spins = 0; spins < LSPIN_CAP; ++spins) { const u64 v = __hip_atomic_load((LAS u64*)p, RLX_WG);
        if ((unsigned)__builtin_amdgcn_readfirstlane((int)min((unsigned)v, (unsigned)(v >> 32))) >= need) break;
        __builtin_amdgcn_s_sleep(1); }
    FENCE();
}
DI unsigned lds_inc(LAS char* p, int lane) { FENCE(); unsigned old = 0; if (lane == 0) old = __hip_atomic_fetch_add((LAS unsigned*)p, 1u, RLX_WG); FENCE(); return old; }
DI void lds_set(LAS char* p, unsigned v) { FENCE(); __hip_atomic_store((LAS unsigned*)p, v, RLX_WG); FENCE(); }

__global__ __launch_bounds__(512, 2) void fused_attn_kernel(const char* __restrict__ xA, const char* __restrict__ wA, const char* __restrict__ oA,
                                                           const char* __restrict__ xB, const char* __restrict__ wB, const char* __restrict__ oB,
                                                           char* __restrict__ out, char* __restrict__ ws) {
    __shared__ __attribute__((aligned(16))) char smem_[LDS_BYTES];
    LAS char* smem = (LAS char*)smem_;
    const unsigned lds0 = (unsigned)(uintptr_t)smem;
    STAMP(0);
    asm volatile("" :: "s"(xA), "s"(wA), "s"(oA), "s"(xB), "s"(wB), "s"(oB), "s"(out), "s"(ws));
    const int tid = threadIdx.x, lane = tid & 63;
    const int wid = __builtin_amdgcn_readfirstlane(tid >> 6);
    const int bx = blockIdx.x, xcd = bx & 7, idx = bx >> 3;
    const int isA = idx >> 4, rt = (xcd >> 1) * 4 + ((idx >> 2) & 3), hg = (xcd & 1) * 4 + (idx & 3);
    const int br = isA ? 0 : 1;
    const int k0 = isA ? (((rt & 3) * 5 + 1) / 2 + ((hg & 3) * 5 + 1) / 2) % 10 : 0;
    const char* xb = (isA ? xA : xB) + (size_t)rt * (128 * 1280);
    const char* wb = (isA ? wA : wB) + hg * 160;
    const int me = (br * 16 + rt) * 8 + hg;
    const int r32 = lane & 31, hl = lane >> 5;
    auto kt_of = [&](int s) { int k = s + k0; return k >= 10 ? k - 10 : k; };
    if (isA && wid < 4) {
        const int j16 = idx & 15; const unsigned dump = lds0 + OWN + wid * 512;
        const char* wl = wB + (hg >> 2) * 640 + j16 * (4 * 3840);
        const char* xl = xB + (size_t)(rt * 128 + (hg & 3) * 32 + r32) * 1280 + hl * 128;
        auto pfx = [&](int i) { return xl + i * 256; };
        auto pfkv = [&](int i) { int n = i * 64 + lane; n = n < 400 ? n : 399; const int sl = n / 40, r = n % 40, c = r % 10;
            return wl + (sl * 64 + r / 10) * 3840 + 1280 + (c < 5 ? c * 128 : 640 + c * 128); };
        auto pfq = [&](int i) { int n = i * 64 + lane; n = n < 200 ? n : 199; int sl = n / 20 + 8; sl = sl >= 10 ? sl - 10 : sl; const int r = n % 20;
            return wl + (sl * 64 + r / 5) * 3840 + (r % 5) * 128; };
        dma4(wid & 1 ? pfx(wid >> 1) : pfkv(wid >> 1), dump);
        dma4(wid & 1 ? pfx(2 + (wid >> 1)) : pfkv(2 + (wid >> 1)), dump);
        dma4(wid < 3 ? pfkv(4 + wid) : pfx(4), dump);
        dma4(pfq(wid), dump);
        { int n = wid * 64 + lane; n = n < 200 ? n : 199; const int o = (j16 * 40 + n / 5) * 1280 + (hg >> 2) * 640 + (n % 5) * 128;
          dma4(oB + o, dump); dma4(oA + o, dump); }
    }
    if (isA && wid >= 4) { for (int i = 0; i < ADELAY; ++i) __builtin_amdgcn_s_sleep(16); }

    const unsigned xo = (unsigned)((lane >> 3) * 1280 + (((lane & 7) ^ (lane >> 4)) << 4));
    auto w_off = [&](int q) { const int c = q * 64 + lane, row = __umul24(c, 3277) >> 16, j = c - row * 20; return (unsigned)(__umul24(row, 3840) + j * 16 + (j < 10 ? 1280 : 2400)); };
    if (!isA) { const char* xs = xb + k0 * 128 + wid * (16 * 1280);
      dma16(xo, xs, lds0 + XS0 + wid * 2048);
      dma16(xo ^ 64, xs + 8 * 1280, lds0 + XS0 + wid * 2048 + 1024);
      const char* wsrc = wb + (size_t)k0 * (64 * 3840);
      const unsigned wv = w_off(wid < 5 ? wid : wid - 5);
      if (wid < 5) { dma16(wv, wsrc, lds0 + WS0 + wid * 1024); dma16(wv, wsrc + 16 * 3840, lds0 + WS0 + (5 + wid) * 1024);
          if (wid >= 3) { dma16(wv, wsrc + 32 * 3840, lds0 + WS0 + (10 + wid) * 1024); dma16(wv, wsrc + 48 * 3840, lds0 + WS0 + (15 + wid) * 1024); } }
      else { dma16(wv, wsrc + 32 * 3840, lds0 + WS0 + (5 + wid) * 1024); dma16(wv, wsrc + 48 * 3840, lds0 + WS0 + (10 + wid) * 1024); } }
    if (wid == 4 && lane < 2) dma4(ws + lane * 4, lds0 + C_TAG);
    if (tid < 80) *(LAS unsigned*)(smem + tid * 4) = 0u;
    else if (tid < 82) *(LAS unsigned*)(smem + ONE + (tid - 80) * 4) = 0x3F803F80u;
    const int nnb = isA ? (rt > 0 ? 1 : 0) : (rt > 1 ? 2 : rt);
    auto kq_base = [&](int fg) { const int hd = fg / 5, dg = fg % 5; return dg < 4 ? hd * 5120 + (dg >> 1) * 2048 + (dg & 1) * 8 : hd * 5120 + 4096; };

    auto rdtag = [&]() { const u64 t = *(LAS u64*)(smem + C_TAG);
        const u64 e = ((u64)(unsigned)__builtin_amdgcn_readfirstlane((int)(t >> 32)) << 32) | (unsigned)__builtin_amdgcn_readfirstlane((int)t);
        return e * 6364136223846793005ull + 1442695040888963407ull; };
    const __amdgpu_buffer_rsrc_t wor = __builtin_amdgcn_make_buffer_rsrc((void*)(isA ? oA : oB), 0, 640 * 1280, 0x00020000);

    const int hp = wid >> 2, qo = wid & 3, qi = hp ? 3 - qo : qo;
    const int ud = qi + 8;
    const bool far = isA ? (4 * rt + qi >= 1) : (4 * rt + qi >= 8);
    const int u0 = isA ? (far ? ud - 1 : ud) : (far ? qi : 8 - 4 * rt);
    f32x16 cd, cf;
    int dsh, kb1, kb2, nh, vb0, vb1, qb1, qb2; unsigned obase, oo[5];
    auto mask_b = [&](f32x16& c, int r, bool fartile) { const int k = (r & 3) + 8 * (r >> 2); c[r] = __int_as_float(fartile ? ((k - 1) << 23) - dsh : dsh - (k << 23)); };
    auto mask_a = [&](int z) {
      SFOR(r, 16)
          constexpr u64 ad = lanes_cut(r, false), af = lanes_cut(r, true);
          cd[r] = __builtin_amdgcn_inverse_ballot_w64(ad + z) ? -__builtin_inff() : 0.f;
          cf[r] = __builtin_amdgcn_inverse_ballot_w64(af + z) ? -__builtin_inff() : 0.f;
      SEND };
    auto att_setup = [&](int part) {
        int l = tid; asm volatile("" : "+v"(l)); l &= 63;
        const int q = l & 31, h = l >> 5;
        if (part == 0) { dsh = (q - 4 * h) << 23; kb1 = KV0 + hp * 10240 + h * 2048 + q * 16; kb2 = h ? ONE : KV0 + hp * 10240 + 4096 + q * 8; nh = h ^ 1;
            asm volatile("" : "+v"(dsh), "+v"(kb1), "+v"(kb2), "+v"(nh)); }
        if (part == 1) { const bool ones = (l & 16) && (l & 3);
            vb0 = KV0 + VOFF + (4 * h + ((l & 15) >> 2)) * VP + (ones ? 160 : hp * 80 + (l & 16) * 2 + (l & 3) * 8); vb1 = vb0 + (ones ? 0 : 40);
            asm volatile("" : "+v"(vb0), "+v"(vb1)); }
        if (part == 2) { qb1 = QST + hp * 10240 + h * 2048 + (qi * 32 + q) * 16; qb2 = QST + hp * 10240 + 4096 + (qi * 32 + q) * 8;
            obase = (unsigned)(qi * 40960 + hp * 20480 + hg * 2560 + q * 16 + h * 512);
            asm volatile("" : "+v"(qb1), "+v"(qb2), "+v"(obase)); }
        if (part >= 3) {
#pragma unroll
            for (int r = 4 * (part - 3); r < 4 * (part - 2); ++r) mask_b(cd, r, false);
            asm volatile("" : "+v"(cd)); } };
    auto wo_setup = [&](int q) {
        int l = tid; asm volatile("" : "+v"(l)); const int c = q * 64 + (l & 63), rho = __umul24(c, 6554) >> 16;
        oo[q] = (unsigned)(__umul24(qo * 160 + hp * 40 + (rho & ~7) + 4 * (rho & 1) + ((rho >> 1) & 3), 1280) + hg * 160 + (c - rho * 10) * 16);
        asm volatile("" : "+v"(oo[q])); };
    u64 tag;

    if (wid < 4) {
        if (!isA) WAITV(0);
        BAR_L();
        STAMP(1);
        bf16x8 xr[32], xt[2], wf[WIN + 1];
        f32x16 acc[5];
        const int tok = wid * 32 + r32;
        const int xa = XS0 + tok * 128 + ((hl ^ ((tok >> 1) & 7)) << 4);
        int wa0 = WS0 + (8 * hl + ((lane & 15) >> 2)) * 320 + (lane & 16) * 2 + (lane & 3) * 8, wa1 = wa0 + 61440;
        asm volatile("" : "+v"(wa0), "+v"(wa1));
        auto rdx = [&](int i) { return *(LAS bf16x8*)(smem + ((i >> 2) % 3) * 16384 + (xa ^ ((i & 3) * 32))); };
        auto rdw = [&](int f) { const int i = f / 5, t = f % 5, sl = (i >> 1) % 7;
            return trfrag(smem + (sl < 6 ? wa0 + sl * 10240 : wa1) + (i & 1) * 5120 + t * 64, 1280); };
        unsigned fl = 0;
        auto rdflag = [&](int u) { fl = __hip_atomic_load((LAS unsigned*)(smem + C_F + u * 4), RLX_WG); };
        auto check = [&](int u) {
            if (__builtin_amdgcn_readfirstlane(fl) != 15) {
#pragma unroll 1
                for (unsigned spins = 0; spins < LSPIN_CAP; ++spins) { rdflag(u); if (__builtin_amdgcn_readfirstlane(fl) == 15) break; __builtin_amdgcn_s_sleep(1); }
            }
            FENCE(); };
#pragma unroll
        for (int i = 0; i < 4; ++i) xr[i] = rdx(i);
#pragma unroll
        for (int f = 0; f < WIN; ++f) wf[f] = rdw(f);
        SB();
        SFOR(f, 200)
            constexpr int i = f / 5, t = f % 5;
            if constexpr (f % 20 == 10 - WIN) lds_set(smem + C_P + wid * 4, (i >> 1) + 1);
            if constexpr (f == 200 - WIN) lds_set(smem + C_P + wid * 4, 20);
            if constexpr (f % 20 == 10) rdflag(f / 20 + 1);
            if constexpr (f % 20 == 20 - WIN && f < 190) check(f / 20 + 1);
            if constexpr (f + WIN < 200) wf[(f + WIN) % (WIN + 1)] = rdw(f + WIN);
            if constexpr (f % 20 >= 16 && f < 140) xr[(f / 20 + 1) * 4 + f % 20 - 16] = rdx((f / 20 + 1) * 4 + f % 20 - 16);
            if constexpr (t == 1 && i >= 31 && i < 39) xt[(i + 1) & 1] = rdx(i + 1);
            SB();
            if constexpr (i == 0) MFMA32Z(acc[t], wf[f % (WIN + 1)], xr[0]);
            else if constexpr (i < 32) MFMA32I(acc[t], wf[f % (WIN + 1)], xr[i]);
            else MFMA32I(acc[t], wf[f % (WIN + 1)], xt[i & 1]);
            SB();
        SEND
        STAMP(2);
        int l2 = tid; asm volatile("" : "+v"(l2)); l2 &= 63;
        const int h2 = l2 >> 5, tk = wid * 32 + (l2 & 31);
        int eb[5];
        auto ebases = [&](int base) { int l = tid; asm volatile("" : "+v"(l)); const int h = (l >> 5) & 1, k = wid * 32 + (l & 31);
            eb[0] = base + k * 16 + h * 8; eb[1] = base + k * 16 + h * 2040; eb[2] = base + (h ? k * 16 + 1024 : k * 8); eb[3] = base + (h ? k * 8 + 2040 : k * 16);
            asm volatile("" : "+v"(eb[0]), "+v"(eb[1]), "+v"(eb[2]), "+v"(eb[3])); };
        auto kq_addr = [&](int g0) { return eb[g0 % 5 == 1 ? 1 : g0 % 5 == 4 ? 2 : g0 % 5 == 3 ? 3 : 0] + kq_base(g0); };
        ebases(OWN);
        eb[4] = OWN + VOFF + tk * VP + h2 * 8; asm volatile("" : "+v"(eb[4]));
        int qa0 = (8 * h2 + 2 * ((l2 & 15) >> 2)) * 160 + (l2 & 16) * 2 + (l2 & 3) * 8, qa1 = qa0 + WS0 + 10240;
        asm volatile("" : "+v"(qa0), "+v"(qa1));
        auto rdq = [&](int f) { const int i = f / 3, t = f % 3, j = i >> 2, sl = (20 + j) % 7;
            return trfrag(smem + (j >= 7 ? qa0 + (j == 7 ? QU27 : j == 8 ? QU28 : QU29) : sl == 0 ? qa0 + WS0 : qa1 + (sl - 1) * 10240) + (i & 3) * 2560 + t * 64, 160); };
        MFMA_DRAIN();
        WAITV(0);
        { u32x2 pk[20];
#pragma unroll
          for (int n = 0; n < 20; ++n) { const int t = n >> 2, b = n & 3; pk[n] = (u32x2){pk2(acc[t][4 * b], acc[t][4 * b + 1]), pk2(acc[t][4 * b + 2], acc[t][4 * b + 3])}; }
          SB();
#pragma unroll
          for (int n = 0; n < 20; ++n) *(LAS u32x2*)(smem + (n < 10 ? kq_addr(2 * n) : eb[4] + (2 * n - 20) * 8)) = pk[n];
          if (h2 == 0) *(LAS u32x2*)(smem + eb[4] + 160) = (u32x2){0x3F803F80u, 0x3F803F80u}; }
        lds_set(smem + C_E + wid * 4, 1);
        check(10);
        xt[0] = rdx(32);
#pragma unroll
        for (int f = 0; f < WIN; ++f) wf[f] = rdq(f);
        STAMP(3);
        SB();
        u64 pv0 = 0, pv1 = 0;
        SFOR(f, 120)
            constexpr int i = f / 3, t = f % 3, j = i >> 2;
            if constexpr (f % 24 == 12 && j < 9) rdflag(11 + j / 2);
            if constexpr (f % 12 == 12 - WIN && (j == 1 || j == 6 || j == 8)) lds_set(smem + C_P + wid * 4, 21 + j);
            if constexpr (f % 24 == 24 - WIN && j < 9) check(11 + j / 2);
            if constexpr (f + WIN < 120) wf[(f + WIN) % (WIN + 1)] = rdq(f + WIN);
            if constexpr (t == 0 && i < 7) xt[(i + 1) & 1] = rdx(32 + i + 1);
            if constexpr (f >= 90 && f < 100 && !(f & 1)) wo_setup((f - 90) / 2);
            if constexpr (f == 99) tag = rdtag();
            if constexpr (f >= 100 && f < 107) att_setup(f - 100);
            if constexpr (f == 108) ebases(QST);
            if constexpr (f == 116) { pv0 = __hip_atomic_load((LAS u64*)(smem + C_P), RLX_WG); pv1 = __hip_atomic_load((LAS u64*)(smem + C_P + 8), RLX_WG); }
            SB();
            if constexpr (i == 0) MFMA32Z(acc[t], wf[f % (WIN + 1)], xt[0]);
            else if constexpr (i < 8) MFMA32I(acc[t], wf[f % (WIN + 1)], xt[i & 1]);
            else MFMA32I(acc[t], wf[f % (WIN + 1)], xr[i - 8]);
            SB();
        SEND
        STAMP(4);
        if (__builtin_amdgcn_readfirstlane((int)min(min((unsigned)pv0, (unsigned)(pv0 >> 32)), min((unsigned)pv1, (unsigned)(pv1 >> 32)))) < 29) wait_min4(smem + C_P, 29);
        MFMA_DRAIN();
        { acc[0] *= QSCALE; acc[1] *= QSCALE; acc[2] *= QSCALE;
          SB();
          u32x2 pk[10];
#pragma unroll
          for (int n = 0; n < 10; ++n) { const int t = n >> 2, b = n & 3; pk[n] = (u32x2){pk2(acc[t][4 * b], acc[t][4 * b + 1]), pk2(acc[t][4 * b + 2], acc[t][4 * b + 3])}; }
          SB();
#pragma unroll
          for (int n = 0; n < 10; ++n) *(LAS u32x2*)(smem + kq_addr(2 * n)) = pk[n]; }
        lds_set(smem + C_QF + wid * 4, 1);
        __builtin_amdgcn_s_setprio(1);
        STAMP(5);
    } else {
        const int hw = wid - 4;
        int known = 0;
        __builtin_amdgcn_s_setprio(3);
        auto lds_or = [&](int off, unsigned v) { FENCE(); if (lane == 0) __hip_atomic_fetch_or((LAS unsigned*)(smem + off), v, RLX_WG); FENCE(); };
        unsigned wo[5], qo[5];
        auto done = [&](int n) { lds_or(C_F + n * 4, 1u << hw); };
        const unsigned xo1 = (xo ^ 64) + 9216, xo2 = xo + 2 * 9216, xo3 = (xo ^ 64) + 3 * 9216;
        auto round_x = [&](int n) { dma16x4(xo, xo1, xo2, xo3, xb + kt_of(n) * 128 + hw * (32 * 1280), lds0 + XS0 + (n % 3) * 16384 + hw * 4096); };
        auto round_w = [&](int n) {
            const int u = 2 * n + (hw >> 1);
            const unsigned dst = lds0 + (u < 27 ? WS0 + (u % 7) * 10240 : u == 27 ? QU27 : u == 28 ? QU28 : QU29) + (hw & 1) * 5120 + 2048;
            if (u < 20) dma16x5(wo, wb + (size_t)(kt_of(u >> 1) * 64 + (u & 1) * 32) * 3840 - 2048, dst);
            else dma16x5(qo, wb + (size_t)kt_of(u < 22 ? u - 12 : u - 22) * (64 * 3840) - 2048, dst); };
        if (isA) round_x(0);
        round_x(1);
#pragma unroll
        for (int q = 0; q < 5; ++q) wo[q] = w_off(q) + (hw & 1) * (16 * 3840) + (4 - q) * 1024;
        if (isA) { round_w(0); round_w(1); WAITV(5); } else { round_w(1); WAITV(9); }
        BAR_L();
#pragma unroll
        for (int q = 0; q < 5; ++q) { const int c = q * 64 + lane, rho = __umul24(c, 6554) >> 16;
            qo[q] = (unsigned)(__umul24((hw & 1) * 32 + (rho & ~7) + 4 * (rho & 1) + ((rho >> 1) & 3), 3840) + (c - rho * 10) * 16 + (4 - q) * 1024); }
#pragma unroll 1
        for (int n = 2; n <= 13; ++n) {
            const int need = n < 13 ? 2 * n - 5 : 20;
            if (need > known) known = min4(smem + C_P);
            if (need > known) { WAITV(0); done(n - 1); known = wait_min4(smem + C_P, need); }
            if (n < 10) round_x(n);
            round_w(n);
            if (n < 10) WAITV(9); else WAITV(5);
            done(n - 1);
        }
        WAITV(0); done(13);
        STAMP(1);
        tag = rdtag();
        auto a_chunk = [&](int a) { int o;
            if (a < 320) { const int hd = a / 80, rem = a % 80; o = hd * 5120 + (rem < 32 ? (96 + rem) * 16 : rem < 64 ? 2048 + (64 + rem) * 16 : 4096 + 768 + (rem - 64) * 16); }
            else o = VOFF + 96 * VP + (a - 320) * 16;
            return o; };
        if (rt < 15) {
            const __amdgpu_buffer_rsrc_t kvr = __builtin_amdgcn_make_buffer_rsrc(ws + WS_KV + me * KVG, 0, KVB, 0x00020000);
            wait_min4(smem + C_E, 1);
            STAMP(2);
            if (!isA) { u32x4 v[11];
#pragma unroll
                for (int j = 0; j < 11; ++j) if (j < 10 || hw == 3) v[j] = *(LAS u32x4*)(smem + OWN + hw * 10240 + j * 1024 + lane * 16);
#pragma unroll
                for (int j = 0; j < 11; ++j) if (j < 10 || hw == 3) __builtin_amdgcn_raw_buffer_store_b128(v[j], kvr, hw * 10240 + j * 1024 + lane * 16, 0, 16);
            } else {
#pragma unroll
                for (int j = 0; j < 3; ++j) { const int a = (4 * j + hw) * 64 + lane;
                    if (a < 656) { const int o = a_chunk(a); __builtin_amdgcn_raw_buffer_store_b128(*(LAS u32x4*)(smem + OWN + o), kvr, o, 0, 16); } }
            }
            STAMP(3);
        }
        wait_min4(smem + C_P, 22);
        round_w(14);
        WAITV(5);
        if (rt < 15) {
            unsigned old = 0;
            if (lane == 0) old = __hip_atomic_fetch_add((LAS unsigned*)(smem + N_PUB), 1u, RLX_WG);
            if (lane == 0 && old == 3) __hip_atomic_store((GAS u64*)(ws + WS_FKV + me * 128), tag, RLX_AGENT);
            STAMP(4);
        }
        WAITV(0); done(14);
        __builtin_amdgcn_s_setprio(0);
#pragma unroll
        for (int p = 0; p < 7; ++p) att_setup(p);
#pragma unroll
        for (int q = 0; q < 5; ++q) wo_setup(q);
        __builtin_amdgcn_s_setprio(3);
        if (hw < NPOLL) {
            __builtin_amdgcn_s_sleep(KDELAY);
            for (int i = 0; i < hw; ++i) __builtin_amdgcn_s_sleep(PSTAG);
            poll_shared((GAS u64*)(ws + WS_FKV + (me - 8 * (lane + 1)) * 128), lane < nnb, tag, smem + N_KV);
        } else wait_word(smem + N_KV, 1);
        STAMP(5);
        if (!isA) {
            const int n = hw >> 1, half = hw & 1;
            if (n < nnb) {
                const char* src = ws + WS_KV + (me - 8 * (n + 1)) * KVG + half * 20480; const unsigned dst = lds0 + KV0 + (1 - n) * KVB + half * 20480;
                wait_min4(smem + C_P, 27);
#pragma unroll
                for (int j = 0; j < 4; ++j) dma16x5_sc1(lane * 16, src + j * 5120 + 2048, dst + j * 5120 + 2048);
                if (half) dma16_sc1(lane * 16, src + 20480, dst + 20480);
            }
        } else if (nnb) {
            const __amdgpu_buffer_rsrc_t nbr = __builtin_amdgcn_make_buffer_rsrc(ws + WS_KV + (me - 8) * KVG, 0, KVB, 0x00020000);
            u32x4 v[3];
#pragma unroll
            for (int j = 0; j < 3; ++j) { const int a = (4 * j + hw) * 64 + lane; if (a < 656) v[j] = __builtin_amdgcn_raw_buffer_load_b128(nbr, a_chunk(a), 0, 16); }
            wait_min4(smem + C_P, 27);
#pragma unroll
            for (int j = 0; j < 3; ++j) { const int a = (4 * j + hw) * 64 + lane; if (a < 656) *(LAS u32x4*)(smem + KV0 + KVB + a_chunk(a)) = v[j]; }
        }
        __builtin_amdgcn_s_setprio(2);
        wait_word(smem + C_QF + qi * 4, 1);
        STAMP(6);
    }
    u32x4 wreg[13];
    auto ldwo = [&](int i) { wreg[i] = __builtin_amdgcn_raw_buffer_load_b128(wor, oo[i % 5] + (i / 5) * (32 * 1280) + (i > 6 || (i == 6 && lane >= 16) ? 40 * 1280 : 0), 0, 0); };
    STAMP(7);
    u32x2 ob[2][3];
    {
        bf16x8 q1[2]; u32x2 q2[2];
#pragma unroll
        for (int e = 0; e < 2; ++e) {
            q1[e] = *(LAS bf16x8*)(smem + qb1 + e * 5120);
            q2[e] = *(LAS u32x2*)(smem + qb2 + e * 5120);
            if (hl) q2[e] = (u32x2){0u, 0u};
        }
        if (isA) mask_a(0);
        struct KF { bf16x8 a; s16x4 b; };
        struct VF { bf16x8 a, b; };
        auto rdk = [&](int u, int e) { const int so = (u >> 2) * KVB, tw = u & 3; KF k;
            k.a = *(LAS bf16x8*)(smem + kb1 + so + tw * 512 + e * 5120); k.b = *(LAS s16x4*)(smem + __umul24(so + tw * 256 + e * 5120, nh) + kb2); return k; };
        auto rdv = [&](int u, int e) { LAS char* p = smem + (e ? vb1 : vb0) + (u >> 2) * KVB + (u & 3) * (32 * VP); VF v;
            v.a = trfrag(p, 8 * VP); v.b = trfrag(p + 16 * VP, 8 * VP); return v; };
        auto qk = [&](const KF& k, int e, const f32x16& c) { return MFMA32K8(k.b, __builtin_bit_cast(s16x4, q2[e]), MFMA32(k.a, q1[e], c)); };
        const f32x16 zero = {};
        auto max16 = [&](const f32x16& s, float m) {
#pragma unroll
            for (int r = 0; r < 16; ++r) m = __builtin_elementwise_maximum(m, s[r]);
            return m; };
        auto set_ref = [&](float m, int e) { const unsigned nb = pk2(-m, 0.f); if (hl) q2[e][0] = nb; };
        auto ex4 = [&](f32x16& s, int b) {
#pragma unroll
            for (int r = 0; r < 4; ++r) s[b + r] = __builtin_amdgcn_exp2f(s[b + r]); };
        auto pk8 = [&](const f32x16& s, int b) { return __builtin_bit_cast(bf16x8, (u32x4){pk2(s[b], s[b + 1]), pk2(s[b + 2], s[b + 3]), pk2(s[b + 4], s[b + 5]), pk2(s[b + 6], s[b + 7])}); };
        auto phase = [&](auto nx, f32x16& cur, const VF& v, f32x16& o, f32x16& nxt, const KF& k, int e) __attribute__((always_inline)) {
            constexpr int NX = decltype(nx)::value;
            SB(); ex4(cur, 0); SB();
            if constexpr (NX) nxt = MFMA32(k.a, q1[e], NX == 1 ? zero : NX == 2 ? cf : cd);
            SB(); ex4(cur, 4); { const bf16x8 p = pk8(cur, 0); SB(); o = MFMA32(v.a, p, o); }
            SB(); ex4(cur, 8); SB();
            if constexpr (NX) nxt = MFMA32K8(k.b, __builtin_bit_cast(s16x4, q2[e]), nxt);
            SB(); ex4(cur, 12); { const bf16x8 p = pk8(cur, 8); SB(); o = MFMA32(v.b, p, o); }
            SB(); };
        f32x16 o0, o1, sa, sb; KF kx; VF vx;
        auto step = [&](auto m0, auto m1, int u) __attribute__((always_inline)) {
            constexpr int M1 = decltype(m1)::value;
            KF ky; if constexpr (M1) ky = rdk(u - 1, 0);
            const VF vy = rdv(u, 1);
            phase(m0, sa, vx, o0, sb, kx, 1);
            if constexpr (M1) { kx = rdk(u - 1, 1); vx = rdv(u - 1, 0); }
            phase(m1, sb, vy, o1, sa, ky, 0); };
        auto nbwait = [&](int u) { if (u == 8) wait_word(smem + N_NB, 4); };
        { const KF k0f = rdk(ud, 0); kx = rdk(ud, 1); vx = rdv(ud, 0);
          const f32x16 s0 = qk(k0f, 0, cd), s1 = qk(kx, 1, cd);
          set_ref(halfmax(max16(s0, s0[0])), 0); set_ref(halfmax(max16(s1, s1[0])), 1);
          sa = qk(k0f, 0, cd); }
        if (hp) { WAITV(0); lds_inc0(smem + N_NB, lane); lds_inc0(smem + N_G0, lane); }
        o0 = zero; o1 = zero;
        if (u0 + 8 == ud) {
            SFOR(it, 9)
                if constexpr (it < 7) { ldwo(2 * it); if constexpr (it < 6) ldwo(2 * it + 1); }
                if constexpr (it == PFLIP) { if (hp) __builtin_amdgcn_s_setprio(0); }
                if constexpr (it == 7) {
#pragma unroll
                    for (int r = 0; r < 16; ++r) mask_b(cf, r, true); }
                if constexpr (it < 4) nbwait(ud - it);
                step(IC<it == 0 ? 3 : it == 8 ? 2 : 1>{}, IC<it == 8 ? 0 : it == 7 ? 2 : 1>{}, ud - it);
            SEND
        } else {
            __builtin_amdgcn_s_setprio(0);
#pragma unroll
            for (int i = 0; i < 13; ++i) ldwo(i);
            if (!isA) cf = zero;
            if (u0 < ud) {
                nbwait(ud);
                if (u0 + 1 < ud) {
                    step(IC<3>{}, IC<1>{}, ud);
#pragma unroll 1
                    for (int u = ud - 1; u > u0 + 1; --u) { nbwait(u); step(IC<1>{}, IC<1>{}, u); }
                    nbwait(u0 + 1);
                    step(IC<1>{}, IC<2>{}, u0 + 1);
                } else step(IC<3>{}, IC<2>{}, ud);
                step(IC<2>{}, IC<0>{}, u0);
            } else step(IC<3>{}, IC<0>{}, ud);
        }
        if (!__all(o0[12] < 1.152921504606847e18f && o1[12] < 1.152921504606847e18f)) {
            if (isA) { int z = 0; asm volatile("" : "+s"(z)); mask_a(z); }
            else { asm volatile("" : "+v"(dsh));
#pragma unroll
                for (int r = 0; r < 16; ++r) { mask_b(cd, r, false); mask_b(cf, r, true); }
                if (!far) cf = zero; }
#pragma unroll
            for (int e = 0; e < 2; ++e) {
                if (hl) q2[e][0] = 0u;
                const f32x16 s = qk(rdk(ud, e), e, cd);
                float m = max16(s, s[0]);
                if (u0 < ud) m = max16(qk(rdk(u0, e), e, cf), m);
#pragma unroll 1
                for (int u = u0 + 1; u < ud; ++u) m = max16(qk(rdk(u, e), e, zero), m);
                set_ref(halfmax(m), e);
            }
#pragma unroll
            for (int e = 0; e < 2; ++e) {
                f32x16 o = zero;
#pragma unroll 1
                for (int u = u0; u <= ud; ++u) { f32x16 t = u == ud ? qk(rdk(u, e), e, cd) : u == u0 ? qk(rdk(u, e), e, cf) : qk(rdk(u, e), e, zero);
                    const VF v = rdv(u, e); ex4(t, 0); ex4(t, 4); ex4(t, 8); ex4(t, 12);
                    o = MFMA32(v.a, pk8(t, 0), o); o = MFMA32(v.b, pk8(t, 8), o); }
                if (e) o1 = o; else o0 = o;
            }
        }
        const float rl0 = __builtin_amdgcn_rcpf(o0[12]), rl1 = __builtin_amdgcn_rcpf(o1[12]);
        SB();
#pragma unroll
        for (int r = 0; r < 12; ++r) { o0[r] *= rl0; o1[r] *= rl1; }
        SB();
#pragma unroll
        for (int j = 0; j < 3; ++j) {
            ob[0][j] = (u32x2){pk2(o0[4 * j], o0[4 * j + 1]), pk2(o0[4 * j + 2], o0[4 * j + 3])};
            ob[1][j] = (u32x2){pk2(o1[4 * j], o1[4 * j + 1]), pk2(o1[4 * j + 2], o1[4 * j + 3])}; }
        SB();
    }
    STAMP(8);
    const __amdgpu_buffer_rsrc_t orr = __builtin_amdgcn_make_buffer_rsrc(ws + WS_O + (br * 16 + rt) * OG, 0, 4 * 40960, 0x00020000);
    {
      { auto rx = __builtin_amdgcn_permlane32_swap(ob[0][0][0], ob[0][1][0], false, false);
        auto ry = __builtin_amdgcn_permlane32_swap(ob[0][0][1], ob[0][1][1], false, false);
        __builtin_amdgcn_raw_buffer_store_b128((u32x4){rx[0], ry[0], rx[1], ry[1]}, orr, obase, 0, 16); }
      if (hl == 0) __builtin_amdgcn_raw_buffer_store_b128((u32x4){ob[0][2][0], ob[0][2][1], ob[1][0][0], ob[1][0][1]}, orr, obase + 1024, 0, 16);
      { auto fx = __builtin_amdgcn_permlane32_swap(ob[1][0][0], ob[1][1][0], false, false);
        auto fy = __builtin_amdgcn_permlane32_swap(ob[1][0][1], ob[1][1][1], false, false);
        auto sx = __builtin_amdgcn_permlane32_swap(ob[1][1][0], ob[1][2][0], false, false);
        auto sy = __builtin_amdgcn_permlane32_swap(ob[1][1][1], ob[1][2][1], false, false);
        __builtin_amdgcn_raw_buffer_store_b128((u32x4){fx[1], fy[1], sx[0], sy[0]}, orr, obase + 1536, 0, 16); } }
    if (lane == 0) { (void)__hip_atomic_fetch_add((LAS unsigned*)(smem + N_G1), 1u, RLX_WG);
        if (!hp) (void)__hip_atomic_fetch_add((LAS unsigned*)(smem + N_G0), 1u, RLX_WG); }
    FENCE();
    STAMP(6);
    auto signal_o = [&]() {
        if (lds_inc(smem + N_OS + hp * 4, lane) == 3 && lane == 0) __hip_atomic_store((GAS u64*)(ws + WS_FO + (me * 2 + hp) * 128), tag, RLX_AGENT); };
    __builtin_amdgcn_s_setprio(0);
    WAITV(0); signal_o();
    STAMP(9);
    wait_word(smem + (hp ? N_G1 : N_G0), 8);
    const int p1 = hp ? 30720 : 35840, p2 = hp ? 65536 : 43520;
    const int wb0 = hp ? KV0 + 10240 : 512, wb1 = hp ? KV0 + KVB + 10240 - 30720 : KV0 - 35840, wb2 = KV0 + KVB - 43520;
#pragma unroll
    for (int i = 0; i < 13; ++i) { const int L = qo * 12800 + i * 1024;
        if (i < 12 || lane < 32) *(LAS u32x4*)(smem + (L + (L < p1 ? wb0 : L < p2 ? wb1 : wb2)) + lane * 16) = wreg[i]; }
    lds_inc0(smem + N_WO + hp * 4, lane);
    STAMP(10);
    __builtin_amdgcn_s_sleep(PDELAY);
    if (qo < NPOLL) {
        for (int i = 0; i < qo; ++i) __builtin_amdgcn_s_sleep(PSTAG);
        poll_shared((GAS u64*)(ws + WS_FO + (((me & ~7) + (lane & 7)) * 2 + hp) * 128), lane < 8, tag, smem + N_OK + hp * 4);
    } else wait_word(smem + N_OK + hp * 4, 1);
    STAMP(11);
    f32x16 oc[3]; f32x4 ps[10];
    LAS char* red = smem + OWN + qo * 10240 + lane * 16;
    { bf16x8 af[5], of[20];
      const int la = (8 * hl + 2 * ((lane & 15) >> 2)) * 160 + (lane & 16) * 2 + (lane & 3) * 8;
      const int aa0 = wb0 + la, aa1 = (hp ? wb1 : wb0) + la, aa2 = wb1 + la, aa3 = (hp ? wb1 : wb2) + la;
      auto rda = [&](int f) { return trfrag(smem + (f < 36 ? aa0 : f < 42 ? aa1 : f < 51 ? aa2 : aa3) + (f / 3) * 2560 + (f % 3) * 64, 160); };
      auto ldo = [&](int k) { of[k] = __builtin_bit_cast(bf16x8, __builtin_amdgcn_raw_buffer_load_b128(orr, qo * 40960 + (hp * 20 + k) * 1024 + lane * 16, 0, 16)); };
#pragma unroll
      for (int k = 0; k < OPF; ++k) ldo(k);
      wait_word(smem + N_WO + hp * 4, 4);
#pragma unroll
      for (int f = 0; f < 4; ++f) af[f] = rda(f);
      SB();
      SFOR(f, 60)
          if constexpr (f % 3 == 0 && f / 3 + OPF < 20) ldo(f / 3 + OPF);
          if constexpr (f + 4 < 60) af[(f + 4) % 5] = rda(f + 4);
          if constexpr (f == 57) { if (hp) { wait_word(smem + C_RD + qo * 4, 1);
#pragma unroll
              for (int b = 0; b < 10; ++b) ps[b] = *(LAS f32x4*)(red + b * 1024); } }
          SB();
          if constexpr (f < 3) MFMA32Z(oc[f], af[f % 5], of[0]); else MFMA32I(oc[f % 3], af[f % 5], of[f / 3]);
          SB();
      SEND }
    MFMA_DRAIN();
    STAMP(12);
    { if (!hp) {
          wait_word(smem + N_G1, 8);
#pragma unroll
          for (int b = 0; b < 10; ++b) *(LAS f32x4*)(red + b * 1024) = (f32x4){oc[b >> 2][4 * (b & 3)], oc[b >> 2][4 * (b & 3) + 1], oc[b >> 2][4 * (b & 3) + 2], oc[b >> 2][4 * (b & 3) + 3]};
          lds_set(smem + C_RD + qo * 4, 1);
      } else {
          char* orow = out + (size_t)(rt * 128 + qo * 32 + r32) * 2560 + br * 1280 + hg * 160 + hl * 16;
#pragma unroll
          for (int b = 0; b < 10; ++b)
#pragma unroll
              for (int r = 0; r < 4; ++r) ps[b][r] += oc[b >> 2][4 * (b & 3) + r];
          SB();
          u32x2 pk[10];
#pragma unroll
          for (int b = 0; b < 10; ++b) pk[b] = (u32x2){pk2(ps[b][0], ps[b][1]), pk2(ps[b][2], ps[b][3])};
          SB();
          u32x4 st[5];
#pragma unroll
          for (int b = 0; b < 10; b += 2) {
              auto rx = __builtin_amdgcn_permlane32_swap(pk[b][0], pk[b + 1][0], false, false); auto ry = __builtin_amdgcn_permlane32_swap(pk[b][1], pk[b + 1][1], false, false);
              st[b >> 1] = (u32x4){rx[0], ry[0], rx[1], ry[1]}; }
          SB();
#pragma unroll
          for (int b = 0; b < 5; ++b) *(u32x4*)(orow + b * 32) = st[b];
      } }
    if (bx == 0 && wid == 0) {
#pragma unroll 1
        for (int i = 0; i < 4; ++i) poll_flags((GAS u64*)(ws + WS_FO + (i * 64 + lane) * 256), true, tag);
        if (lane == 0) __hip_atomic_store((GAS u64*)ws, tag, RLX_AGENT);
    }
}

extern "C" void kernel_launch(void* const* d_in, const int* in_sizes, int n_in, void* d_out, int out_size, void* d_ws, size_t ws_size,
                              hipStream_t stream) {
    fused_attn_kernel<<<256, 512, 0, stream>>>((const char*)d_in[0], (const char*)d_in[1], (const char*)d_in[2], (const char*)d_in[3],
                                               (const char*)d_in[4], (const char*)d_in[5], (char*)d_out, (char*)d_ws);
}
```

```cpp
#include <hip/hip_runtime.h>
#include <hip/hip_bf16.h>
#include <stdint.h>

typedef short bf16x8 __attribute__((ext_vector_type(8)));
typedef short s16x4 __attribute__((ext_vector_type(4)));
typedef float f32x2 __attribute__((ext_vector_type(2)));
typedef float f32x4 __attribute__((ext_vector_type(4)));
typedef float f32x16 __attribute__((ext_vector_type(16)));
typedef unsigned u32x4 __attribute__((ext_vector_type(4)));
typedef unsigned u32x2 __attribute__((ext_vector_type(2)));
typedef __bf16 bfx2 __attribute__((ext_vector_type(2)));
typedef unsigned long long u64;
#define LAS __attribute__((address_space(3)))
#define GAS __attribute__((address_space(1)))
#define DI __device__ __forceinline__
#define RLX_AGENT __ATOMIC_RELAXED, __HIP_MEMORY_SCOPE_AGENT
#define RLX_WG __ATOMIC_RELAXED, __HIP_MEMORY_SCOPE_WORKGROUP

constexpr int LDS_BYTES = 163840;
constexpr int VOFF = 20480, VP = 168;
constexpr int KVB = VOFF + 128 * VP;
constexpr int KV0 = LDS_BYTES - 3 * KVB;
constexpr int OWN = KV0 + 2 * KVB;
constexpr int C_F = 0, C_P = 128, C_E = 144, C_QF = 160, C_RD = 176, C_CNT = 192, C_TAG = 320;
constexpr int N_PUB = C_CNT, N_NB = C_CNT + 4, N_G0 = C_CNT + 8, N_G1 = C_CNT + 12, N_WO = C_CNT + 16, N_OS = C_CNT + 24, N_KV = C_CNT + 32, N_OK = C_CNT + 36;
constexpr int XS0 = 512, WS0 = XS0 + 3 * 16384, QST = 512, ONE = 336, QU27 = XS0 + 16384, QU28 = XS0, QU29 = QU27 + 10240;
static_assert(WS0 + 7 * 10240 <= OWN, "ring overlaps the own image");
constexpr unsigned WS_FKV = 4096, WS_FO = WS_FKV + 256 * 128, WS_KV = 131072;
constexpr unsigned KVG = KVB, OG = 4 * 40960;
constexpr unsigned WS_O = WS_KV + 256 * KVG;
constexpr unsigned WS_END = WS_O + 32 * OG;
constexpr unsigned WS_DBG = WS_END;
constexpr unsigned SPIN_CAP = 400000, LSPIN_CAP = 4000000;
constexpr float QSCALE = 0.22360679774997896f * 1.4426950408889634f;
#define PSLEEP 2
#define PDELAY 1
#define KDELAY 0
#define NPOLL 1
#define PSTAG 3
#define PFLIP 6
#define OPF 12
#define ABL4N 2
#define WIN 4
#define ADELAY 0

#define MFMA32(a, b, c) __builtin_amdgcn_mfma_f32_32x32x16_bf16((a), (b), (c), 0, 0, 0)
#define MFMA32K8(a, b, c) __builtin_amdgcn_mfma_f32_32x32x8bf16_1k((a), (b), (c), 0, 0, 0)
#define MFMA32I(acc, a, b) asm volatile("v_mfma_f32_32x32x16_bf16 %0, %1, %2, %0" : "+v"(acc) : "v"(a), "v"(b))
#define MFMA32Z(acc, a, b) asm volatile("v_mfma_f32_32x32x16_bf16 %0, %1, %2, 0" : "=&v"(acc) : "v"(a), "v"(b))
#define MFMA_DRAIN() asm volatile("s_nop 7\n\ts_nop 4" ::: "memory")
#define WAITV(N) asm volatile("s_waitcnt vmcnt(" #N ")" ::: "memory")
#define WAITL() asm volatile("s_waitcnt lgkmcnt(0)" ::: "memory")
#define BAR_L() asm volatile("s_waitcnt lgkmcnt(0)\n\ts_barrier" ::: "memory")
#define SB() __builtin_amdgcn_sched_barrier(0)
#define FENCE() asm volatile("" ::: "memory")

#define STAMP(i) do {} while (0)

template <int V> struct IC { static constexpr int value = V; };
template <int I, int N, class F> DI void static_for(F&& f) { if constexpr (I < N) { f(IC<I>{}); static_for<I + 1, N>(f); } }
#define SFOR(var, N) static_for<0, N>([&](auto ic_##var) __attribute__((always_inline)) { constexpr int var = decltype(ic_##var)::value;
#define SEND });

DI void dma16(unsigned voff, const char* sbase, unsigned ldsaddr) {
    asm volatile("s_mov_b32 m0, %2\n\ts_nop 0\n\tglobal_load_lds_dwordx4 %0, %1" :: "v"(voff), "s"(sbase), "s"(ldsaddr) : "memory");
}
DI void dma16x4(unsigned v0, unsigned v1, unsigned v2, unsigned v3, const char* sbase, unsigned ldsaddr) {
    asm volatile("s_mov_b32 m0, %5\n\ts_nop 0\n\tglobal_load_lds_dwordx4 %0, %4\n\tglobal_load_lds_dwordx4 %1, %4 offset:1024\n\t"
                 "global_load_lds_dwordx4 %2, %4 offset:2048\n\tglobal_load_lds_dwordx4 %3, %4 offset:3072"
                 :: "v"(v0), "v"(v1), "v"(v2), "v"(v3), "s"(sbase), "s"(ldsaddr) : "memory");
}
DI void dma16x5(const unsigned (&v)[5], const char* sbase, unsigned ldsaddr) {
    asm volatile("s_mov_b32 m0, %6\n\ts_nop 0\n\tglobal_load_lds_dwordx4 %0, %5 offset:-2048\n\tglobal_load_lds_dwordx4 %1, %5 offset:-1024\n\t"
                 "global_load_lds_dwordx4 %2, %5\n\tglobal_load_lds_dwordx4 %3, %5 offset:1024\n\tglobal_load_lds_dwordx4 %4, %5 offset:2048"
                 :: "v"(v[0]), "v"(v[1]), "v"(v[2]), "v"(v[3]), "v"(v[4]), "s"(sbase), "s"(ldsaddr) : "memory");
}
DI void dma16x5_sc1(unsigned voff, const char* sbase, unsigned ldsaddr) {
    asm volatile("s_mov_b32 m0, %2\n\ts_nop 0\n\tglobal_load_lds_dwordx4 %0, %1 offset:-2048 sc1\n\tglobal_load_lds_dwordx4 %0, %1 offset:-1024 sc1\n\t"
                 "global_load_lds_dwordx4 %0, %1 sc1\n\tglobal_load_lds_dwordx4 %0, %1 offset:1024 sc1\n\tglobal_load_lds_dwordx4 %0, %1 offset:2048 sc1"
                 :: "v"(voff), "s"(sbase), "s"(ldsaddr) : "memory");
}
DI void dma16_sc1(unsigned voff, const char* sbase, unsigned ldsaddr) {
    asm volatile("s_mov_b32 m0, %2\n\ts_nop 0\n\tglobal_load_lds_dwordx4 %0, %1 sc1" :: "v"(voff), "s"(sbase), "s"(ldsaddr) : "memory");
}
DI void dma4(const char* p, unsigned ldsaddr) {
    asm volatile("s_mov_b32 m0, %1\n\ts_nop 0\n\tglobal_load_lds_dword %0, off" :: "v"(p), "s"(ldsaddr) : "memory");
}
DI unsigned pk2(float lo, float hi) { f32x2 v = {lo, hi}; return __builtin_bit_cast(unsigned, __builtin_convertvector(v, bfx2)); }
typedef short v4i16_t __attribute__((ext_vector_type(4)));
DI bf16x8 trfrag(LAS char* p, int second) {
    s16x4 a = __builtin_bit_cast(s16x4, __builtin_amdgcn_ds_read_tr16_b64_v4i16((LAS v4i16_t*)p));
    s16x4 b = __builtin_bit_cast(s16x4, __builtin_amdgcn_ds_read_tr16_b64_v4i16((LAS v4i16_t*)(p + second)));
    return __builtin_shufflevector(a, b, 0, 1, 2, 3, 4, 5, 6, 7);
}
constexpr u64 lanes_cut(int r, bool fartile) {
    u64 m = 0;
    for (int l = 0; l < 64; ++l) { const int key = (r & 3) + 8 * (r >> 2) + 4 * (l >> 5), d = (l & 31) - key + (fartile ? 32 : 0);
        const bool ok = d >= 0 && d <= 14 && !(d & 1);
        if (!ok) m |= 1ull << l; }
    return m;
}
DI float halfmax(float m) { auto rr = __builtin_amdgcn_permlane32_swap(__float_as_uint(m), __float_as_uint(m), false, false);
    return __builtin_elementwise_maximum(__uint_as_float(rr[0]), __uint_as_float(rr[1])); }

DI void poll_flags(GAS u64* word, bool active, u64 tag) {
#pragma unroll 1
    for (unsigned spins = 0; spins < SPIN_CAP; ++spins) {
        u64 v = tag;
        if (active) v = __hip_atomic_load(word, RLX_AGENT);
        if (__all(v == tag)) break;
        __builtin_amdgcn_s_sleep(PSLEEP);
    }
    __builtin_amdgcn_fence(__ATOMIC_ACQUIRE, "wavefront");
}
DI void poll_shared(GAS u64* word, bool active, u64 tag, LAS char* ok) {
#pragma unroll 1
    for (unsigned spins = 0; spins < SPIN_CAP; ++spins) {
        if (__builtin_amdgcn_readfirstlane((int)__hip_atomic_load((LAS unsigned*)ok, RLX_WG))) break;
        u64 v = tag;
        if (active) v = __hip_atomic_load(word, RLX_AGENT);
        if (__all(v == tag)) { __hip_atomic_store((LAS unsigned*)ok, 1u, RLX_WG); break; }
        __builtin_amdgcn_s_sleep(PSLEEP);
    }
    __builtin_amdgcn_fence(__ATOMIC_ACQUIRE, "wavefront");
    asm volatile("" ::: "memory");
}
DI int min4(LAS char* p) {
    const u64 a = __hip_atomic_load((LAS u64*)p, RLX_WG), b = __hip_atomic_load((LAS u64*)(p + 8), RLX_WG);
    const unsigned m = min(min((unsigned)a, (unsigned)(a >> 32)), min((unsigned)b, (unsigned)(b >> 32)));
    return __builtin_amdgcn_readfirstlane((int)m);
}
DI int wait_min4(LAS char* p, int need) {
    int v = min4(p);
#pragma unroll 1
    for (unsigned spins = 0; v < need && spins < LSPIN_CAP; ++spins) { __builtin_amdgcn_s_sleep(1); v = min4(p); }
    FENCE();
    return v;
}
DI void wait_word(LAS char* p, unsigned need) {
#pragma unroll 1
    for (unsigned spins = 0; spins < LSPIN_CAP; ++spins) {
        if ((unsigned)__builtin_amdgcn_readfirstlane((int)__hip_atomic_load((LAS unsigned*)p, RLX_WG)) >= need) break;
        __builtin_amdgcn_s_sleep(1); }
    FENCE();
}
DI void lds_inc0(LAS char* p, int lane) { FENCE(); if (lane == 0) (void)__hip_atomic_fetch_add((LAS unsigned*)p, 1u, RLX_WG); FENCE(); }
DI void wait_pair(LAS char* p, unsigned need) {
#pragma unroll 1
    for (unsigned spins = 0; spins < LSPIN_CAP; ++spins) { const u64 v = __hip_atomic_load((LAS u64*)p, RLX_WG);
        if ((unsigned)__builtin_amdgcn_readfirstlane((int)min((unsigned)v, (unsigned)(v >> 32))) >= need) break;
        __builtin_amdgcn_s_sleep(1); }
    FENCE();
}
DI unsigned lds_inc(LAS char* p, int lane) { FENCE(); unsigned old = 0; if (lane == 0) old = __hip_atomic_fetch_add((LAS unsigned*)p, 1u, RLX_WG); FENCE(); return old; }
DI void lds_set(LAS char* p, unsigned v) { FENCE(); __hip_atomic_store((LAS unsigned*)p, v, RLX_WG); FENCE(); }

__global__ __launch_bounds__(512, 2) void fused_attn_kernel(const char* __restrict__ xA, const char* __restrict__ wA, const char* __restrict__ oA,
                                                           const char* __restrict__ xB, const char* __restrict__ wB, const char* __restrict__ oB,
                                                           char* __restrict__ out, char* __restrict__ ws) {
    __shared__ __attribute__((aligned(16))) char smem_[LDS_BYTES];
    LAS char* smem = (LAS char*)smem_;
    const unsigned lds0 = (unsigned)(uintptr_t)smem;
    STAMP(0);
    asm volatile("" :: "s"(xA), "s"(wA), "s"(oA), "s"(xB), "s"(wB), "s"(oB), "s"(out), "s"(ws));
    const int tid = threadIdx.x, lane = tid & 63;
    const int wid = __builtin_amdgcn_readfirstlane(tid >> 6);
    const int bx = blockIdx.x, xcd = bx & 7, idx = bx >> 3;
    const int isA = idx >> 4, rt = (xcd >> 1) * 4 + ((idx >> 2) & 3), hg = (xcd & 1) * 4 + (idx & 3);
    const int br = isA ? 0 : 1;
    const int k0 = isA ? (((rt & 3) * 5 + 1) / 2 + ((hg & 3) * 5 + 1) / 2) % 10 : 0;
    const char* xb = (isA ? xA : xB) + (size_t)rt * (128 * 1280);
    const char* wb = (isA ? wA : wB) + hg * 160;
    const int me = (br * 16 + rt) * 8 + hg;
    const int r32 = lane & 31, hl = lane >> 5;
    auto kt_of = [&](int s) { int k = s + k0; return k >= 10 ? k - 10 : k; };
    if (isA && wid < 4) {
        const int j16 = idx & 15; const unsigned dump = lds0 + OWN + wid * 512;
        const char* wl = wB + (hg >> 2) * 640 + j16 * (4 * 3840);
        const char* xl = xB + (size_t)(rt * 128 + (hg & 3) * 32 + r32) * 1280 + hl * 128;
        auto pfx = [&](int i) { return xl + i * 256; };
        auto pfkv = [&](int i) { int n = i * 64 + lane; n = n < 400 ? n : 399; const int sl = n / 40, r = n % 40, c = r % 10;
            return wl + (sl * 64 + r / 10) * 3840 + 1280 + (c < 5 ? c * 128 : 640 + c * 128); };
        auto pfq = [&](int i) { int n = i * 64 + lane; n = n < 200 ? n : 199; int sl = n / 20 + 8; sl = sl >= 10 ? sl - 10 : sl; const int r = n % 20;
            return wl + (sl * 64 + r / 5) * 3840 + (r % 5) * 128; };
        dma4(wid & 1 ? pfx(wid >> 1) : pfkv(wid >> 1), dump);
        dma4(wid & 1 ? pfx(2 + (wid >> 1)) : pfkv(2 + (wid >> 1)), dump);
        dma4(wid < 3 ? pfkv(4 + wid) : pfx(4), dump);
        dma4(pfq(wid), dump);
        { int n = wid * 64 + lane; n = n < 200 ? n : 199; const int o = (j16 * 40 + n / 5) * 1280 + (hg >> 2) * 640 + (n % 5) * 128;
          dma4(oB + o, dump); dma4(oA + o, dump); }
    }
    if (isA && wid >= 4) { for (int i = 0; i < ADELAY; ++i) __builtin_amdgcn_s_sleep(16); }

    const unsigned xo = (unsigned)((lane >> 3) * 1280 + (((lane & 7) ^ (lane >> 4)) << 4));
    auto w_off = [&](int q) { const int c = q * 64 + lane, row = __umul24(c, 3277) >> 16, j = c - row * 20; return (unsigned)(__umul24(row, 3840) + j * 16 + (j < 10 ? 1280 : 2400)); };
    if (!isA) { const char* xs = xb + k0 * 128 + wid * (16 * 1280);
      dma16(xo, xs, lds0 + XS0 + wid * 2048);
      dma16(xo ^ 64, xs + 8 * 1280, lds0 + XS0 + wid * 2048 + 1024);
      const char* wsrc = wb + (size_t)k0 * (64 * 3840);
      const unsigned wv = w_off(wid < 5 ? wid : wid - 5);
      if (wid < 5) { dma16(wv, wsrc, lds0 + WS0 + wid * 1024); dma16(wv, wsrc + 16 * 3840, lds0 + WS0 + (5 + wid) * 1024);
          if (wid >= 3) { dma16(wv, wsrc + 32 * 3840, lds0 + WS0 + (10 + wid) * 1024); dma16(wv, wsrc + 48 * 3840, lds0 + WS0 + (15 + wid) * 1024); } }
      else { dma16(wv, wsrc + 32 * 3840, lds0 + WS0 + (5 + wid) * 1024); dma16(wv, wsrc + 48 * 3840, lds0 + WS0 + (10 + wid) * 1024); } }
    if (wid == 4 && lane < 2) dma4(ws + lane * 4, lds0 + C_TAG);
    if (tid < 80) *(LAS unsigned*)(smem + tid * 4) = 0u;
    else if (tid < 82) *(LAS unsigned*)(smem + ONE + (tid - 80) * 4) = 0x3F803F80u;
    const int nnb = isA ? (rt > 0 ? 1 : 0) : (rt > 1 ? 2 : rt);
    auto kq_base = [&](int fg) { const int hd = fg / 5, dg = fg % 5; return dg < 4 ? hd * 5120 + (dg >> 1) * 2048 + (dg & 1) * 8 : hd * 5120 + 4096; };

    auto rdtag = [&]() { const u64 t = *(LAS u64*)(smem + C_TAG);
        const u64 e = ((u64)(unsigned)__builtin_amdgcn_readfirstlane((int)(t >> 32)) << 32) | (unsigned)__builtin_amdgcn_readfirstlane((int)t);
        return e * 6364136223846793005ull + 1442695040888963407ull; };
    const __amdgpu_buffer_rsrc_t wor = __builtin_amdgcn_make_buffer_rsrc((void*)(isA ? oA : oB), 0, 640 * 1280, 0x00020000);

    const int hp = wid >> 2, qo = wid & 3, qi = hp ? 3 - qo : qo;
    const int ud = qi + 8;
    const bool far = isA ? (4 * rt + qi >= 1) : (4 * rt + qi >= 8);
    const int u0 = isA ? (far ? ud - 1 : ud) : (far ? qi : 8 - 4 * rt);
    f32x16 cd, cf;
    int dsh, kb1, kb2, nh, vb0, vb1, qb1, qb2; unsigned obase, oo[5];
    auto mask_b = [&](f32x16& c, int r, bool fartile) { const int k = (r & 3) + 8 * (r >> 2); c[r] = __int_as_float(fartile ? ((k - 1) << 23) - dsh : dsh - (k << 23)); };
    auto mask_a = [&](int z) {
      SFOR(r, 16)
          constexpr u64 ad = lanes_cut(r, false), af = lanes_cut(r, true);
          cd[r] = __builtin_amdgcn_inverse_ballot_w64(ad + z) ? -__builtin_inff() : 0.f;
          cf[r] = __builtin_amdgcn_inverse_ballot_w64(af + z) ? -__builtin_inff() : 0.f;
      SEND };
    auto att_setup = [&](int part) {
        int l = tid; asm volatile("" : "+v"(l)); l &= 63;
        const int q = l & 31, h = l >> 5;
        if (part == 0) { dsh = (q - 4 * h) << 23; kb1 = KV0 + hp * 10240 + h * 2048 + q * 16; kb2 = h ? ONE : KV0 + hp * 10240 + 4096 + q * 8; nh = h ^ 1;
            asm volatile("" : "+v"(dsh), "+v"(kb1), "+v"(kb2), "+v"(nh)); }
        if (part == 1) { const bool ones = (l & 16) && (l & 3);
            vb0 = KV0 + VOFF + (4 * h + ((l & 15) >> 2)) * VP + (ones ? 160 : hp * 80 + (l & 16) * 2 + (l & 3) * 8); vb1 = vb0 + (ones ? 0 : 40);
            asm volatile("" : "+v"(vb0), "+v"(vb1)); }
        if (part == 2) { qb1 = QST + hp * 10240 + h * 2048 + (qi * 32 + q) * 16; qb2 = QST + hp * 10240 + 4096 + (qi * 32 + q) * 8;
            obase = (unsigned)(qi * 40960 + hp * 20480 + hg * 2560 + q * 16 + h * 512);
            asm volatile("" : "+v"(qb1), "+v"(qb2), "+v"(obase)); }
        if (part >= 3) {
#pragma unroll
            for (int r = 4 * (part - 3); r < 4 * (part - 2); ++r) mask_b(cd, r, false);
            asm volatile("" : "+v"(cd)); } };
    auto wo_setup = [&](int q) {
        int l = tid; asm volatile("" : "+v"(l)); const int c = q * 64 + (l & 63), rho = __umul24(c, 6554) >> 16;
        oo[q] = (unsigned)(__umul24(qo * 160 + hp * 40 + (rho & ~7) + 4 * (rho & 1) + ((rho >> 1) & 3), 1280) + hg * 160 + (c - rho * 10) * 16);
        asm volatile("" : "+v"(oo[q])); };
    u64 tag;

    if (wid < 4) {
        if (!isA) WAITV(0);
        BAR_L();
        STAMP(1);
        bf16x8 xr[32], xt[2], wf[WIN + 1];
        f32x16 acc[5];
        const int tok = wid * 32 + r32;
        const int xa = XS0 + tok * 128 + ((hl ^ ((tok >> 1) & 7)) << 4);
        int wa0 = WS0 + (8 * hl + ((lane & 15) >> 2)) * 320 + (lane & 16) * 2 + (lane & 3) * 8, wa1 = wa0 + 61440;
        asm volatile("" : "+v"(wa0), "+v"(wa1));
        auto rdx = [&](int i) { return *(LAS bf16x8*)(smem + ((i >> 2) % 3) * 16384 + (xa ^ ((i & 3) * 32))); };
        auto rdw = [&](int f) { const int i = f / 5, t = f % 5, sl = (i >> 1) % 7;
            return trfrag(smem + (sl < 6 ? wa0 + sl * 10240 : wa1) + (i & 1) * 5120 + t * 64, 1280); };
        unsigned fl = 0;
        auto rdflag = [&](int u) { fl = __hip_atomic_load((LAS unsigned*)(smem + C_F + u * 4), RLX_WG); };
        auto check = [&](int u) {
            if (__builtin_amdgcn_readfirstlane(fl) != 15) {
#pragma unroll 1
                for (unsigned spins = 0; spins < LSPIN_CAP; ++spins) { rdflag(u); if (__builtin_amdgcn_readfirstlane(fl) == 15) break; __builtin_amdgcn_s_sleep(1); }
            }
            FENCE(); };
#pragma unroll
        for (int i = 0; i < 4; ++i) xr[i] = rdx(i);
#pragma unroll
        for (int f = 0; f < WIN; ++f) wf[f] = rdw(f);
        SB();
        SFOR(f, 200)
            constexpr int i = f / 5, t = f % 5;
            if constexpr (f % 20 == 10 - WIN) lds_set(smem + C_P + wid * 4, (i >> 1) + 1);
            if constexpr (f == 200 - WIN) lds_set(smem + C_P + wid * 4, 20);
            if constexpr (f % 20 == 10) rdflag(f / 20 + 1);
            if constexpr (f % 20 == 20 - WIN && f < 190) check(f / 20 + 1);
            if constexpr (f + WIN < 200) wf[(f + WIN) % (WIN + 1)] = rdw(f + WIN);
            if constexpr (f % 20 >= 16 && f < 140) xr[(f / 20 + 1) * 4 + f % 20 - 16] = rdx((f / 20 + 1) * 4 + f % 20 - 16);
            if constexpr (t == 1 && i >= 31 && i < 39) xt[(i + 1) & 1] = rdx(i + 1);
            SB();
            if constexpr (i == 0) MFMA32Z(acc[t], wf[f % (WIN + 1)], xr[0]);
            else if constexpr (i < 32) MFMA32I(acc[t], wf[f % (WIN + 1)], xr[i]);
            else MFMA32I(acc[t], wf[f % (WIN + 1)], xt[i & 1]);
            SB();
        SEND
        STAMP(2);
        int l2 = tid; asm volatile("" : "+v"(l2)); l2 &= 63;
        const int h2 = l2 >> 5, tk = wid * 32 + (l2 & 31);
        int eb[5];
        auto ebases = [&](int base) { int l = tid; asm volatile("" : "+v"(l)); const int h = (l >> 5) & 1, k = wid * 32 + (l & 31);
            eb[0] = base + k * 16 + h * 8; eb[1] = base + k * 16 + h * 2040; eb[2] = base + (h ? k * 16 + 1024 : k * 8); eb[3] = base + (h ? k * 8 + 2040 : k * 16);
            asm volatile("" : "+v"(eb[0]), "+v"(eb[1]), "+v"(eb[2]), "+v"(eb[3])); };
        auto kq_addr = [&](int g0) { return eb[g0 % 5 == 1 ? 1 : g0 % 5 == 4 ? 2 : g0 % 5 == 3 ? 3 : 0] + kq_base(g0); };
        ebases(OWN);
        eb[4] = OWN + VOFF + tk * VP + h2 * 8; asm volatile("" : "+v"(eb[4]));
        int qa0 = (8 * h2 + 2 * ((l2 & 15) >> 2)) * 160 + (l2 & 16) * 2 + (l2 & 3) * 8, qa1 = qa0 + WS0 + 10240;
        asm volatile("" : "+v"(qa0), "+v"(qa1));
        auto rdq = [&](int f) { const int i = f / 3, t = f % 3, j = i >> 2, sl = (20 + j) % 7;
            return trfrag(smem + (j >= 7 ? qa0 + (j == 7 ? QU27 : j == 8 ? QU28 : QU29) : sl == 0 ? qa0 + WS0 : qa1 + (sl - 1) * 10240) + (i & 3) * 2560 + t * 64, 160); };
        MFMA_DRAIN();
        WAITV(0);
        { u32x2 pk[20];
#pragma unroll
          for (int n = 0; n < 20; ++n) { const int t = n >> 2, b = n & 3; pk[n] = (u32x2){pk2(acc[t][4 * b], acc[t][4 * b + 1]), pk2(acc[t][4 * b + 2], acc[t][4 * b + 3])}; }
          SB();
#pragma unroll
          for (int n = 0; n < 20; ++n) *(LAS u32x2*)(smem + (n < 10 ? kq_addr(2 * n) : eb[4] + (2 * n - 20) * 8)) = pk[n];
          if (h2 == 0) *(LAS u32x2*)(smem + eb[4] + 160) = (u32x2){0x3F803F80u, 0x3F803F80u}; }
        lds_set(smem + C_E + wid * 4, 1);
        check(10);
        xt[0] = rdx(32);
#pragma unroll
        for (int f = 0; f < WIN; ++f) wf[f] = rdq(f);
        STAMP(3);
        SB();
        u64 pv0 = 0, pv1 = 0;
        SFOR(f, 120)
            constexpr int i = f / 3, t = f % 3, j = i >> 2;
            if constexpr (f % 24 == 12 && j < 9) rdflag(11 + j / 2);
            if constexpr (f % 12 == 12 - WIN && (j == 1 || j == 6 || j == 8)) lds_set(smem + C_P + wid * 4, 21 + j);
            if constexpr (f % 24 == 24 - WIN && j < 9) check(11 + j / 2);
            if constexpr (f + WIN < 120) wf[(f + WIN) % (WIN + 1)] = rdq(f + WIN);
            if constexpr (t == 0 && i < 7) xt[(i + 1) & 1] = rdx(32 + i + 1);
            if constexpr (f >= 90 && f < 100 && !(f & 1)) wo_setup((f - 90) / 2);
            if constexpr (f == 99) tag = rdtag();
            if constexpr (f >= 100 && f < 107) att_setup(f - 100);
            if constexpr (f == 108) ebases(QST);
            if constexpr (f == 116) { pv0 = __hip_atomic_load((LAS u64*)(smem + C_P), RLX_WG); pv1 = __hip_atomic_load((LAS u64*)(smem + C_P + 8), RLX_WG); }
            SB();
            if constexpr (i == 0) MFMA32Z(acc[t], wf[f % (WIN + 1)], xt[0]);
            else if constexpr (i < 8) MFMA32I(acc[t], wf[f % (WIN + 1)], xt[i & 1]);
            else MFMA32I(acc[t], wf[f % (WIN + 1)], xr[i - 8]);
            SB();
        SEND
        STAMP(4);
        if (__builtin_amdgcn_readfirstlane((int)min(min((unsigned)pv0, (unsigned)(pv0 >> 32)), min((unsigned)pv1, (unsigned)(pv1 >> 32)))) < 29) wait_min4(smem + C_P, 29);
        MFMA_DRAIN();
        { acc[0] *= QSCALE; acc[1] *= QSCALE; acc[2] *= QSCALE;
          SB();
          u32x2 pk[10];
#pragma unroll
          for (int n = 0; n < 10; ++n) { const int t = n >> 2, b = n & 3; pk[n] = (u32x2){pk2(acc[t][4 * b], acc[t][4 * b + 1]), pk2(acc[t][4 * b + 2], acc[t][4 * b + 3])}; }
          SB();
#pragma unroll
          for (int n = 0; n < 10; ++n) *(LAS u32x2*)(smem + kq_addr(2 * n)) = pk[n]; }
        lds_set(smem + C_QF + wid * 4, 1);
        __builtin_amdgcn_s_setprio(1);
        STAMP(5);
    } else {
        const int hw = wid - 4;
        int known = 0;
        __builtin_amdgcn_s_setprio(3);
        auto lds_or = [&](int off, unsigned v) { FENCE(); if (lane == 0) __hip_atomic_fetch_or((LAS unsigned*)(smem + off), v, RLX_WG); FENCE(); };
        unsigned wo[5], qo[5];
        auto done = [&](int n) { lds_or(C_F + n * 4, 1u << hw); };
        const unsigned xo1 = (xo ^ 64) + 9216, xo2 = xo + 2 * 9216, xo3 = (xo ^ 64) + 3 * 9216;
        auto round_x = [&](int n) { dma16x4(xo, xo1, xo2, xo3, xb + kt_of(n) * 128 + hw * (32 * 1280), lds0 + XS0 + (n % 3) * 16384 + hw * 4096); };
        auto round_w = [&](int n) {
            const int u = 2 * n + (hw >> 1);
            const unsigned dst = lds0 + (u < 27 ? WS0 + (u % 7) * 10240 : u == 27 ? QU27 : u == 28 ? QU28 : QU29) + (hw & 1) * 5120 + 2048;
            if (u < 20) dma16x5(wo, wb + (size_t)(kt_of(u >> 1) * 64 + (u & 1) * 32) * 3840 - 2048, dst);
            else dma16x5(qo, wb + (size_t)kt_of(u < 22 ? u - 12 : u - 22) * (64 * 3840) - 2048, dst); };
        if (isA) round_x(0);
        round_x(1);
#pragma unroll
        for (int q = 0; q < 5; ++q) wo[q] = w_off(q) + (hw & 1) * (16 * 3840) + (4 - q) * 1024;
        if (isA) { round_w(0); round_w(1); WAITV(5); } else { round_w(1); WAITV(9); }
        BAR_L();
#pragma unroll
        for (int q = 0; q < 5; ++q) { const int c = q * 64 + lane, rho = __umul24(c, 6554) >> 16;
            qo[q] = (unsigned)(__umul24((hw & 1) * 32 + (rho & ~7) + 4 * (rho & 1) + ((rho >> 1) & 3), 3840) + (c - rho * 10) * 16 + (4 - q) * 1024); }
#pragma unroll 1
        for (int n = 2; n <= 13; ++n) {
            const int need = n < 13 ? 2 * n - 5 : 20;
            if (need > known) known = min4(smem + C_P);
            if (need > known) { WAITV(0); done(n - 1); known = wait_min4(smem + C_P, need); }
            if (n < 10) round_x(n);
            round_w(n);
            if (n < 10) WAITV(9); else WAITV(5);
            done(n - 1);
        }
        WAITV(0); done(13);
        STAMP(1);
        tag = rdtag();
        auto a_chunk = [&](int a) { int o;
            if (a < 320) { const int hd = a / 80, rem = a % 80; o = hd * 5120 + (rem < 32 ? (96 + rem) * 16 : rem < 64 ? 2048 + (64 + rem) * 16 : 4096 + 768 + (rem - 64) * 16); }
            else o = VOFF + 96 * VP + (a - 320) * 16;
            return o; };
        if (rt < 15) {
            const __amdgpu_buffer_rsrc_t kvr = __builtin_amdgcn_make_buffer_rsrc(ws + WS_KV + me * KVG, 0, KVB, 0x00020000);
            wait_min4(smem + C_E, 1);
            STAMP(2);
            if (!isA) { u32x4 v[11];
#pragma unroll
                for (int j = 0; j < 11; ++j) if (j < 10 || hw == 3) v[j] = *(LAS u32x4*)(smem + OWN + hw * 10240 + j * 1024 + lane * 16);
#pragma unroll
                for (int j = 0; j < 11; ++j) if (j < 10 || hw == 3) __builtin_amdgcn_raw_buffer_store_b128(v[j], kvr, hw * 10240 + j * 1024 + lane * 16, 0, 16);
            } else {
#pragma unroll
                for (int j = 0; j < 3; ++j) { const int a = (4 * j + hw) * 64 + lane;
                    if (a < 656) { const int o = a_chunk(a); __builtin_amdgcn_raw_buffer_store_b128(*(LAS u32x4*)(smem + OWN + o), kvr, o, 0, 16); } }
            }
            STAMP(3);
        }
        if (rt < 15) {
            unsigned old = 0;
            if (lane == 0) old = __hip_atomic_fetch_add((LAS unsigned*)(smem + N_PUB), 1u, RLX_WG);
            if (lane == 0 && old == 3) __hip_atomic_store((GAS u64*)(ws + WS_FKV + me * 128), tag, RLX_AGENT);
            STAMP(4);
        }
        wait_min4(smem + C_P, 22);
        round_w(14);
        WAITV(0); done(14);
        __builtin_amdgcn_s_setprio(0);
#pragma unroll
        for (int p = 0; p < 7; ++p) att_setup(p);
#pragma unroll
        for (int q = 0; q < 5; ++q) wo_setup(q);
        __builtin_amdgcn_s_setprio(3);
        if (hw < NPOLL) {
            __builtin_amdgcn_s_sleep(KDELAY);
            for (int i = 0; i < hw; ++i) __builtin_amdgcn_s_sleep(PSTAG);
            poll_shared((GAS u64*)(ws + WS_FKV + (me - 8 * (lane + 1)) * 128), lane < nnb, tag, smem + N_KV);
        } else wait_word(smem + N_KV, 1);
        STAMP(5);
        if (!isA) {
            const int n = hw >> 1, half = hw & 1;
            if (n < nnb) {
                const char* src = ws + WS_KV + (me - 8 * (n + 1)) * KVG + half * 20480; const unsigned dst = lds0 + KV0 + (1 - n) * KVB + half * 20480;
                wait_min4(smem + C_P, 27);
#pragma unroll
                for (int j = 0; j < 4; ++j) dma16x5_sc1(lane * 16, src + j * 5120 + 2048, dst + j * 5120 + 2048);
                if (half) dma16_sc1(lane * 16, src + 20480, dst + 20480);
            }
        } else if (nnb) {
            const __amdgpu_buffer_rsrc_t nbr = __builtin_amdgcn_make_buffer_rsrc(ws + WS_KV + (me - 8) * KVG, 0, KVB, 0x00020000);
            u32x4 v[3];
#pragma unroll
            for (int j = 0; j < 3; ++j) { const int a = (4 * j + hw) * 64 + lane; if (a < 656) v[j] = __builtin_amdgcn_raw_buffer_load_b128(nbr, a_chunk(a), 0, 16); }
            wait_min4(smem + C_P, 27);
#pragma unroll
            for (int j = 0; j < 3; ++j) { const int a = (4 * j + hw) * 64 + lane; if (a < 656) *(LAS u32x4*)(smem + KV0 + KVB + a_chunk(a)) = v[j]; }
        }
        __builtin_amdgcn_s_setprio(2);
        wait_word(smem + C_QF + qi * 4, 1);
        STAMP(6);
    }
    u32x4 wreg[13];
    auto ldwo = [&](int i) { wreg[i] = __builtin_amdgcn_raw_buffer_load_b128(wor, oo[i % 5] + (i / 5) * (32 * 1280) + (i > 6 || (i == 6 && lane >= 16) ? 40 * 1280 : 0), 0, 0); };
    STAMP(7);
    u32x2 ob[2][3];
    {
        bf16x8 q1[2]; u32x2 q2[2];
#pragma unroll
        for (int e = 0; e < 2; ++e) {
            q1[e] = *(LAS bf16x8*)(smem + qb1 + e * 5120);
            q2[e] = *(LAS u32x2*)(smem + qb2 + e * 5120);
            if (hl) q2[e] = (u32x2){0u, 0u};
        }
        if (isA) mask_a(0);
        struct KF { bf16x8 a; s16x4 b; };
        struct VF { bf16x8 a, b; };
        auto rdk = [&](int u, int e) { const int so = (u >> 2) * KVB, tw = u & 3; KF k;
            k.a = *(LAS bf16x8*)(smem + kb1 + so + tw * 512 + e * 5120); k.b = *(LAS s16x4*)(smem + __umul24(so + tw * 256 + e * 5120, nh) + kb2); return k; };
        auto rdv = [&](int u, int e) { LAS char* p = smem + (e ? vb1 : vb0) + (u >> 2) * KVB + (u & 3) * (32 * VP); VF v;
            v.a = trfrag(p, 8 * VP); v.b = trfrag(p + 16 * VP, 8 * VP); return v; };
        auto qk = [&](const KF& k, int e, const f32x16& c) { return MFMA32K8(k.b, __builtin_bit_cast(s16x4, q2[e]), MFMA32(k.a, q1[e], c)); };
        const f32x16 zero = {};
        auto max16 = [&](const f32x16& s, float m) {
#pragma unroll
            for (int r = 0; r < 16; ++r) m = __builtin_elementwise_maximum(m, s[r]);
            return m; };
        auto set_ref = [&](float m, int e) { const unsigned nb = pk2(-m, 0.f); if (hl) q2[e][0] = nb; };
        auto ex4 = [&](f32x16& s, int b) {
#pragma unroll
            for (int r = 0; r < 4; ++r) s[b + r] = __builtin_amdgcn_exp2f(s[b + r]); };
        auto pk8 = [&](const f32x16& s, int b) { return __builtin_bit_cast(bf16x8, (u32x4){pk2(s[b], s[b + 1]), pk2(s[b + 2], s[b + 3]), pk2(s[b + 4], s[b + 5]), pk2(s[b + 6], s[b + 7])}); };
        auto phase = [&](auto nx, f32x16& cur, const VF& v, f32x16& o, f32x16& nxt, const KF& k, int e) __attribute__((always_inline)) {
            constexpr int NX = decltype(nx)::value;
            SB(); ex4(cur, 0); SB();
            if constexpr (NX) nxt = MFMA32(k.a, q1[e], NX == 1 ? zero : NX == 2 ? cf : cd);
            SB(); ex4(cur, 4); { const bf16x8 p = pk8(cur, 0); SB(); o = MFMA32(v.a, p, o); }
            SB(); ex4(cur, 8); SB();
            if constexpr (NX) nxt = MFMA32K8(k.b, __builtin_bit_cast(s16x4, q2[e]), nxt);
            SB(); ex4(cur, 12); { const bf16x8 p = pk8(cur, 8); SB(); o = MFMA32(v.b, p, o); }
            SB(); };
        f32x16 o0, o1, sa, sb; KF kx; VF vx;
        auto step = [&](auto m0, auto m1, int u) __attribute__((always_inline)) {
            constexpr int M1 = decltype(m1)::value;
            KF ky; if constexpr (M1) ky = rdk(u - 1, 0);
            const VF vy = rdv(u, 1);
            phase(m0, sa, vx, o0, sb, kx, 1);
            if constexpr (M1) { kx = rdk(u - 1, 1); vx = rdv(u - 1, 0); }
            phase(m1, sb, vy, o1, sa, ky, 0); };
        auto nbwait = [&](int u) { if (u == 8) wait_word(smem + N_NB, 4); };
        { const KF k0f = rdk(ud, 0); kx = rdk(ud, 1); vx = rdv(ud, 0);
          const f32x16 s0 = qk(k0f, 0, cd), s1 = qk(kx, 1, cd);
          set_ref(halfmax(max16(s0, s0[0])), 0); set_ref(halfmax(max16(s1, s1[0])), 1);
          sa = qk(k0f, 0, cd); }
        if (hp) { WAITV(0); lds_inc0(smem + N_NB, lane); lds_inc0(smem + N_G0, lane); }
        o0 = zero; o1 = zero;
        if (u0 + 8 == ud) {
            SFOR(it, 9)
                if constexpr (it < 7) { ldwo(2 * it); if constexpr (it < 6) ldwo(2 * it + 1); }
                if constexpr (it == PFLIP) { if (hp) __builtin_amdgcn_s_setprio(0); }
                if constexpr (it == 7) {
#pragma unroll
                    for (int r = 0; r < 16; ++r) mask_b(cf, r, true); }
                if constexpr (it < 4) nbwait(ud - it);
                step(IC<it == 0 ? 3 : it == 8 ? 2 : 1>{}, IC<it == 8 ? 0 : it == 7 ? 2 : 1>{}, ud - it);
            SEND
        } else {
            __builtin_amdgcn_s_setprio(0);
#pragma unroll
            for (int i = 0; i < 13; ++i) ldwo(i);
            if (!isA) cf = zero;
            if (u0 < ud) {
                nbwait(ud);
                if (u0 + 1 < ud) {
                    step(IC<3>{}, IC<1>{}, ud);
#pragma unroll 1
                    for (int u = ud - 1; u > u0 + 1; --u) { nbwait(u); step(IC<1>{}, IC<1>{}, u); }
                    nbwait(u0 + 1);
                    step(IC<1>{}, IC<2>{}, u0 + 1);
                } else step(IC<3>{}, IC<2>{}, ud);
                step(IC<2>{}, IC<0>{}, u0);
            } else step(IC<3>{}, IC<0>{}, ud);
        }
        if (!__all(o0[12] < 1.152921504606847e18f && o1[12] < 1.152921504606847e18f)) {
            if (isA) { int z = 0; asm volatile("" : "+s"(z)); mask_a(z); }
            else { asm volatile("" : "+v"(dsh));
#pragma unroll
                for (int r = 0; r < 16; ++r) { mask_b(cd, r, false); mask_b(cf, r, true); }
                if (!far) cf = zero; }
#pragma unroll
            for (int e = 0; e < 2; ++e) {
                if (hl) q2[e][0] = 0u;
                const f32x16 s = qk(rdk(ud, e), e, cd);
                float m = max16(s, s[0]);
                if (u0 < ud) m = max16(qk(rdk(u0, e), e, cf), m);
#pragma unroll 1
                for (int u = u0 + 1; u < ud; ++u) m = max16(qk(rdk(u, e), e, zero), m);
                set_ref(halfmax(m), e);
            }
#pragma unroll
            for (int e = 0; e < 2; ++e) {
                f32x16 o = zero;
#pragma unroll 1
                for (int u = u0; u <= ud; ++u) { f32x16 t = u == ud ? qk(rdk(u, e), e, cd) : u == u0 ? qk(rdk(u, e), e, cf) : qk(rdk(u, e), e, zero);
                    const VF v = rdv(u, e); ex4(t, 0); ex4(t, 4); ex4(t, 8); ex4(t, 12);
                    o = MFMA32(v.a, pk8(t, 0), o); o = MFMA32(v.b, pk8(t, 8), o); }
                if (e) o1 = o; else o0 = o;
            }
        }
        const float rl0 = __builtin_amdgcn_rcpf(o0[12]), rl1 = __builtin_amdgcn_rcpf(o1[12]);
        SB();
#pragma unroll
        for (int r = 0; r < 12; ++r) { o0[r] *= rl0; o1[r] *= rl1; }
        SB();
#pragma unroll
        for (int j = 0; j < 3; ++j) {
            ob[0][j] = (u32x2){pk2(o0[4 * j], o0[4 * j + 1]), pk2(o0[4 * j + 2], o0[4 * j + 3])};
            ob[1][j] = (u32x2){pk2(o1[4 * j], o1[4 * j + 1]), pk2(o1[4 * j + 2], o1[4 * j + 3])}; }
        SB();
    }
    STAMP(8);
    const __amdgpu_buffer_rsrc_t orr = __builtin_amdgcn_make_buffer_rsrc(ws + WS_O + (br * 16 + rt) * OG, 0, 4 * 40960, 0x00020000);
    {
      { auto rx = __builtin_amdgcn_permlane32_swap(ob[0][0][0], ob[0][1][0], false, false);
        auto ry = __builtin_amdgcn_permlane32_swap(ob[0][0][1], ob[0][1][1], false, false);
        __builtin_amdgcn_raw_buffer_store_b128((u32x4){rx[0], ry[0], rx[1], ry[1]}, orr, obase, 0, 16); }
      if (hl == 0) __builtin_amdgcn_raw_buffer_store_b128((u32x4){ob[0][2][0], ob[0][2][1], ob[1][0][0], ob[1][0][1]}, orr, obase + 1024, 0, 16);
      { auto fx = __builtin_amdgcn_permlane32_swap(ob[1][0][0], ob[1][1][0], false, false);
        auto fy = __builtin_amdgcn_permlane32_swap(ob[1][0][1], ob[1][1][1], false, false);
        auto sx = __builtin_amdgcn_permlane32_swap(ob[1][1][0], ob[1][2][0], false, false);
        auto sy = __builtin_amdgcn_permlane32_swap(ob[1][1][1], ob[1][2][1], false, false);
        __builtin_amdgcn_raw_buffer_store_b128((u32x4){fx[1], fy[1], sx[0], sy[0]}, orr, obase + 1536, 0, 16); } }
    if (lane == 0) { (void)__hip_atomic_fetch_add((LAS unsigned*)(smem + N_G1), 1u, RLX_WG);
        if (!hp) (void)__hip_atomic_fetch_add((LAS unsigned*)(smem + N_G0), 1u, RLX_WG); }
    FENCE();
    STAMP(6);
    auto signal_o = [&]() {
        if (lds_inc(smem + N_OS + hp * 4, lane) == 3 && lane == 0) __hip_atomic_store((GAS u64*)(ws + WS_FO + (me * 2 + hp) * 128), tag, RLX_AGENT); };
    __builtin_amdgcn_s_setprio(0);
    signal_o();
    STAMP(9);
    wait_word(smem + (hp ? N_G1 : N_G0), 8);
    const int p1 = hp ? 30720 : 35840, p2 = hp ? 65536 : 43520;
    const int wb0 = hp ? KV0 + 10240 : 512, wb1 = hp ? KV0 + KVB + 10240 - 30720 : KV0 - 35840, wb2 = KV0 + KVB - 43520;
#pragma unroll
    for (int i = 0; i < 13; ++i) { const int L = qo * 12800 + i * 1024;
        if (i < 12 || lane < 32) *(LAS u32x4*)(smem + (L + (L < p1 ? wb0 : L < p2 ? wb1 : wb2)) + lane * 16) = wreg[i]; }
    lds_inc0(smem + N_WO + hp * 4, lane);
    STAMP(10);
    __builtin_amdgcn_s_sleep(PDELAY);
    if (qo < NPOLL) {
        for (int i = 0; i < qo; ++i) __builtin_amdgcn_s_sleep(PSTAG);
        poll_shared((GAS u64*)(ws + WS_FO + (((me & ~7) + (lane & 7)) * 2 + hp) * 128), lane < 8, tag, smem + N_OK + hp * 4);
    } else wait_word(smem + N_OK + hp * 4, 1);
    STAMP(11);
    f32x16 oc[3]; f32x4 ps[10];
    LAS char* red = smem + OWN + qo * 10240 + lane * 16;
    { bf16x8 af[5], of[20];
      const int la = (8 * hl + 2 * ((lane & 15) >> 2)) * 160 + (lane & 16) * 2 + (lane & 3) * 8;
      const int aa0 = wb0 + la, aa1 = (hp ? wb1 : wb0) + la, aa2 = wb1 + la, aa3 = (hp ? wb1 : wb2) + la;
      auto rda = [&](int f) { return trfrag(smem + (f < 36 ? aa0 : f < 42 ? aa1 : f < 51 ? aa2 : aa3) + (f / 3) * 2560 + (f % 3) * 64, 160); };
      auto ldo = [&](int k) { of[k] = __builtin_bit_cast(bf16x8, __builtin_amdgcn_raw_buffer_load_b128(orr, qo * 40960 + (hp * 20 + k) * 1024 + lane * 16, 0, 16)); };
#pragma unroll
      for (int k = 0; k < OPF; ++k) ldo(k);
      wait_word(smem + N_WO + hp * 4, 4);
#pragma unroll
      for (int f = 0; f < 4; ++f) af[f] = rda(f);
      SB();
      SFOR(f, 60)
          if constexpr (f % 3 == 0 && f / 3 + OPF < 20) ldo(f / 3 + OPF);
          if constexpr (f + 4 < 60) af[(f + 4) % 5] = rda(f + 4);
          if constexpr (f == 57) { if (hp) { wait_word(smem + C_RD + qo * 4, 1);
#pragma unroll
              for (int b = 0; b < 10; ++b) ps[b] = *(LAS f32x4*)(red + b * 1024); } }
          SB();
          if constexpr (f < 3) MFMA32Z(oc[f], af[f % 5], of[0]); else MFMA32I(oc[f % 3], af[f % 5], of[f / 3]);
          SB();
      SEND }
    MFMA_DRAIN();
    STAMP(12);
    { if (!hp) {
          wait_word(smem + N_G1, 8);
#pragma unroll
          for (int b = 0; b < 10; ++b) *(LAS f32x4*)(red + b * 1024) = (f32x4){oc[b >> 2][4 * (b & 3)], oc[b >> 2][4 * (b & 3) + 1], oc[b >> 2][4 * (b & 3) + 2], oc[b >> 2][4 * (b & 3) + 3]};
          lds_set(smem + C_RD + qo * 4, 1);
      } else {
          char* orow = out + (size_t)(rt * 128 + qo * 32 + r32) * 2560 + br * 1280 + hg * 160 + hl * 16;
#pragma unroll
          for (int b = 0; b < 10; ++b)
#pragma unroll
              for (int r = 0; r < 4; ++r) ps[b][r] += oc[b >> 2][4 * (b & 3) + r];
          SB();
          u32x2 pk[10];
#pragma unroll
          for (int b = 0; b < 10; ++b) pk[b] = (u32x2){pk2(ps[b][0], ps[b][1]), pk2(ps[b][2], ps[b][3])};
          SB();
          u32x4 st[5];
#pragma unroll
          for (int b = 0; b < 10; b += 2) {
              auto rx = __builtin_amdgcn_permlane32_swap(pk[b][0], pk[b + 1][0], false, false); auto ry = __builtin_amdgcn_permlane32_swap(pk[b][1], pk[b + 1][1], false, false);
              st[b >> 1] = (u32x4){rx[0], ry[0], rx[1], ry[1]}; }
          SB();
#pragma unroll
          for (int b = 0; b < 5; ++b) *(u32x4*)(orow + b * 32) = st[b];
      } }
    if (bx == 0 && wid == 0) {
#pragma unroll 1
        for (int i = 0; i < 4; ++i) poll_flags((GAS u64*)(ws + WS_FO + (i * 64 + lane) * 256), true, tag);
        if (lane == 0) __hip_atomic_store((GAS u64*)ws, tag, RLX_AGENT);
    }
}

extern "C" void kernel_launch(void* const* d_in, const int* in_sizes, int n_in, void* d_out, int out_size, void* d_ws, size_t ws_size,
                              hipStream_t stream) {
    fused_attn_kernel<<<256, 512, 0, stream>>>((const char*)d_in[0], (const char*)d_in[1], (const char*)d_in[2], (const char*)d_in[3],
                                               (const char*)d_in[4], (const char*)d_in[5], (char*)d_out, (char*)d_ws);
}
```

```cpp
#define PFLIP 3
#define DEFER 1
#define KEEPP 1
#define OPF 20
#include <hip/hip_runtime.h>
#include <hip/hip_bf16.h>
#include <stdint.h>

typedef short bf16x8 __attribute__((ext_vector_type(8)));
typedef short s16x4 __attribute__((ext_vector_type(4)));
typedef float f32x2 __attribute__((ext_vector_type(2)));
typedef float f32x4 __attribute__((ext_vector_type(4)));
typedef float f32x16 __attribute__((ext_vector_type(16)));
typedef unsigned u32x4 __attribute__((ext_vector_type(4)));
typedef unsigned u32x2 __attribute__((ext_vector_type(2)));
typedef __bf16 bfx2 __attribute__((ext_vector_type(2)));
typedef unsigned long long u64;
#define LAS __attribute__((address_space(3)))
#define GAS __attribute__((address_space(1)))
#define DI __device__ __forceinline__
#define RLX_AGENT __ATOMIC_RELAXED, __HIP_MEMORY_SCOPE_AGENT
#define RLX_WG __ATOMIC_RELAXED, __HIP_MEMORY_SCOPE_WORKGROUP

constexpr int LDS_BYTES = 163840;
constexpr int VOFF = 20480, VP = 168;
constexpr int KVB = VOFF + 128 * VP;
constexpr int KV0 = LDS_BYTES - 3 * KVB;
constexpr int OWN = KV0 + 2 * KVB;
constexpr int C_F = 0, C_P = 128, C_E = 144, C_QF = 160, C_RD = 176, C_CNT = 192, C_TAG = 320;
constexpr int N_PUB = C_CNT, N_NB = C_CNT + 4, N_G0 = C_CNT + 8, N_G1 = C_CNT + 12, N_WO = C_CNT + 16, N_OS = C_CNT + 24, N_KV = C_CNT + 32, N_OK = C_CNT + 36;
constexpr int XS0 = 512, WS0 = XS0 + 3 * 16384, QST = 512, ONE = 336, QU27 = XS0 + 16384, QU28 = XS0, QU29 = QU27 + 10240;
static_assert(WS0 + 7 * 10240 <= OWN, "ring overlaps the own image");
constexpr unsigned WS_FKV = 4096, WS_FO = WS_FKV + 256 * 128, WS_KV = 131072;
constexpr unsigned KVG = KVB, OG = 4 * 40960;
constexpr unsigned WS_O = WS_KV + 256 * KVG;
constexpr unsigned WS_END = WS_O + 32 * OG;
constexpr unsigned WS_DBG = WS_END;
constexpr unsigned SPIN_CAP = 400000, LSPIN_CAP = 4000000;
constexpr float QSCALE = 0.22360679774997896f * 1.4426950408889634f;
#define PSLEEP 2
#define PDELAY 1
#define KDELAY 0
#define NPOLL 1
#define PSTAG 3
#define ABL4N 2
#define WIN 4
#define ADELAY 0

#define MFMA32(a, b, c) __builtin_amdgcn_mfma_f32_32x32x16_bf16((a), (b), (c), 0, 0, 0)
#define MFMA32K8(a, b, c) __builtin_amdgcn_mfma_f32_32x32x8bf16_1k((a), (b), (c), 0, 0, 0)
#define MFMA32I(acc, a, b) asm volatile("v_mfma_f32_32x32x16_bf16 %0, %1, %2, %0" : "+v"(acc) : "v"(a), "v"(b))
#define MFMA32Z(acc, a, b) asm volatile("v_mfma_f32_32x32x16_bf16 %0, %1, %2, 0" : "=&v"(acc) : "v"(a), "v"(b))
#define MFMA_DRAIN() asm volatile("s_nop 7\n\ts_nop 4" ::: "memory")
#define WAITV(N) asm volatile("s_waitcnt vmcnt(" #N ")" ::: "memory")
#define WAITL() asm volatile("s_waitcnt lgkmcnt(0)" ::: "memory")
#define BAR_L() asm volatile("s_waitcnt lgkmcnt(0)\n\ts_barrier" ::: "memory")
#define SB() __builtin_amdgcn_sched_barrier(0)
#define FENCE() asm volatile("" ::: "memory")

#define STAMP(i) do {} while (0)

template <int V> struct IC { static constexpr int value = V; };
template <int I, int N, class F> DI void static_for(F&& f) { if constexpr (I < N) { f(IC<I>{}); static_for<I + 1, N>(f); } }
#define SFOR(var, N) static_for<0, N>([&](auto ic_##var) __attribute__((always_inline)) { constexpr int var = decltype(ic_##var)::value;
#define SEND });

DI void dma16(unsigned voff, const char* sbase, unsigned ldsaddr) {
    asm volatile("s_mov_b32 m0, %2\n\ts_nop 0\n\tglobal_load_lds_dwordx4 %0, %1" :: "v"(voff), "s"(sbase), "s"(ldsaddr) : "memory");
}
DI void dma16x4(unsigned v0, unsigned v1, unsigned v2, unsigned v3, const char* sbase, unsigned ldsaddr) {
    asm volatile("s_mov_b32 m0, %5\n\ts_nop 0\n\tglobal_load_lds_dwordx4 %0, %4\n\tglobal_load_lds_dwordx4 %1, %4 offset:1024\n\t"
                 "global_load_lds_dwordx4 %2, %4 offset:2048\n\tglobal_load_lds_dwordx4 %3, %4 offset:3072"
                 :: "v"(v0), "v"(v1), "v"(v2), "v"(v3), "s"(sbase), "s"(ldsaddr) : "memory");
}
DI void dma16x5(const unsigned (&v)[5], const char* sbase, unsigned ldsaddr) {
    asm volatile("s_mov_b32 m0, %6\n\ts_nop 0\n\tglobal_load_lds_dwordx4 %0, %5 offset:-2048\n\tglobal_load_lds_dwordx4 %1, %5 offset:-1024\n\t"
                 "global_load_lds_dwordx4 %2, %5\n\tglobal_load_lds_dwordx4 %3, %5 offset:1024\n\tglobal_load_lds_dwordx4 %4, %5 offset:2048"
                 :: "v"(v[0]), "v"(v[1]), "v"(v[2]), "v"(v[3]), "v"(v[4]), "s"(sbase), "s"(ldsaddr) : "memory");
}
DI void dma16x5_sc1(unsigned voff, const char* sbase, unsigned ldsaddr) {
    asm volatile("s_mov_b32 m0, %2\n\ts_nop 0\n\tglobal_load_lds_dwordx4 %0, %1 offset:-2048 sc1\n\tglobal_load_lds_dwordx4 %0, %1 offset:-1024 sc1\n\t"
                 "global_load_lds_dwordx4 %0, %1 sc1\n\tglobal_load_lds_dwordx4 %0, %1 offset:1024 sc1\n\tglobal_load_lds_dwordx4 %0, %1 offset:2048 sc1"
                 :: "v"(voff), "s"(sbase), "s"(ldsaddr) : "memory");
}
DI void dma16_sc1(unsigned voff, const char* sbase, unsigned ldsaddr) {
    asm volatile("s_mov_b32 m0, %2\n\ts_nop 0\n\tglobal_load_lds_dwordx4 %0, %1 sc1" :: "v"(voff), "s"(sbase), "s"(ldsaddr) : "memory");
}
DI void dma4(const char* p, unsigned ldsaddr) {
    asm volatile("s_mov_b32 m0, %1\n\ts_nop 0\n\tglobal_load_lds_dword %0, off" :: "v"(p), "s"(ldsaddr) : "memory");
}
DI unsigned pk2(float lo, float hi) { f32x2 v = {lo, hi}; return __builtin_bit_cast(unsigned, __builtin_convertvector(v, bfx2)); }
typedef short v4i16_t __attribute__((ext_vector_type(4)));
DI bf16x8 trfrag(LAS char* p, int second) {
    s16x4 a = __builtin_bit_cast(s16x4, __builtin_amdgcn_ds_read_tr16_b64_v4i16((LAS v4i16_t*)p));
    s16x4 b = __builtin_bit_cast(s16x4, __builtin_amdgcn_ds_read_tr16_b64_v4i16((LAS v4i16_t*)(p + second)));
    return __builtin_shufflevector(a, b, 0, 1, 2, 3, 4, 5, 6, 7);
}
constexpr u64 lanes_cut(int r, bool fartile) {
    u64 m = 0;
    for (int l = 0; l < 64; ++l) { const int key = (r & 3) + 8 * (r >> 2) + 4 * (l >> 5), d = (l & 31) - key + (fartile ? 32 : 0);
        const bool ok = d >= 0 && d <= 14 && !(d & 1);
        if (!ok) m |= 1ull << l; }
    return m;
}
DI float halfmax(float m) { auto rr = __builtin_amdgcn_permlane32_swap(__float_as_uint(m), __float_as_uint(m), false, false);
    return __builtin_elementwise_maximum(__uint_as_float(rr[0]), __uint_as_float(rr[1])); }

DI void poll_flags(GAS u64* word, bool active, u64 tag) {
#pragma unroll 1
    for (unsigned spins = 0; spins < SPIN_CAP; ++spins) {
        u64 v = tag;
        if (active) v = __hip_atomic_load(word, RLX_AGENT);
        if (__all(v == tag)) break;
        __builtin_amdgcn_s_sleep(PSLEEP);
    }
    __builtin_amdgcn_fence(__ATOMIC_ACQUIRE, "wavefront");
}
DI void poll_shared(GAS u64* word, bool active, u64 tag, LAS char* ok) {
#pragma unroll 1
    for (unsigned spins = 0; spins < SPIN_CAP; ++spins) {
        if (__builtin_amdgcn_readfirstlane((int)__hip_atomic_load((LAS unsigned*)ok, RLX_WG))) break;
        u64 v = tag;
        if (active) v = __hip_atomic_load(word, RLX_AGENT);
        if (__all(v == tag)) { __hip_atomic_store((LAS unsigned*)ok, 1u, RLX_WG); break; }
        __builtin_amdgcn_s_sleep(PSLEEP);
    }
    __builtin_amdgcn_fence(__ATOMIC_ACQUIRE, "wavefront");
    asm volatile("" ::: "memory");
}
DI int min4(LAS char* p) {
    const u64 a = __hip_atomic_load((LAS u64*)p, RLX_WG), b = __hip_atomic_load((LAS u64*)(p + 8), RLX_WG);
    const unsigned m = min(min((unsigned)a, (unsigned)(a >> 32)), min((unsigned)b, (unsigned)(b >> 32)));
    return __builtin_amdgcn_readfirstlane((int)m);
}
DI int wait_min4(LAS char* p, int need) {
    int v = min4(p);
#pragma unroll 1
    for (unsigned spins = 0; v < need && spins < LSPIN_CAP; ++spins) { __builtin_amdgcn_s_sleep(1); v = min4(p); }
    FENCE();
    return v;
}
DI void wait_word(LAS char* p, unsigned need) {
#pragma unroll 1
    for (unsigned spins = 0; spins < LSPIN_CAP; ++spins) {
        if ((unsigned)__builtin_amdgcn_readfirstlane((int)__hip_atomic_load((LAS unsigned*)p, RLX_WG)) >= need) break;
        __builtin_amdgcn_s_sleep(1); }
    FENCE();
}
DI void lds_inc0(LAS char* p, int lane) { FENCE(); if (lane == 0) (void)__hip_atomic_fetch_add((LAS unsigned*)p, 1u, RLX_WG); FENCE(); }
DI void wait_pair(LAS char* p, unsigned need) {
#pragma unroll 1
    for (unsigned spins = 0; spins < LSPIN_CAP; ++spins) { const u64 v = __hip_atomic_load((LAS u64*)p, RLX_WG);
        if ((unsigned)__builtin_amdgcn_readfirstlane((int)min((unsigned)v, (unsigned)(v >> 32))) >= need) break;
        __builtin_amdgcn_s_sleep(1); }
    FENCE();
}
DI unsigned lds_inc(LAS char* p, int lane) { FENCE(); unsigned old = 0; if (lane == 0) old = __hip_atomic_fetch_add((LAS unsigned*)p, 1u, RLX_WG); FENCE(); return old; }
DI void lds_set(LAS char* p, unsigned v) { FENCE(); __hip_atomic_store((LAS unsigned*)p, v, RLX_WG); FENCE(); }

__global__ __launch_bounds__(512, 2) void fused_attn_kernel(const char* __restrict__ xA, const char* __restrict__ wA, const char* __restrict__ oA,
                                                           const char* __restrict__ xB, const char* __restrict__ wB, const char* __restrict__ oB,
                                                           char* __restrict__ out, char* __restrict__ ws) {
    __shared__ __attribute__((aligned(16))) char smem_[LDS_BYTES];
    LAS char* smem = (LAS char*)smem_;
    const unsigned lds0 = (unsigned)(uintptr_t)smem;
    STAMP(0);
    asm volatile("" :: "s"(xA), "s"(wA), "s"(oA), "s"(xB), "s"(wB), "s"(oB), "s"(out), "s"(ws));
    const int tid = threadIdx.x, lane = tid & 63;
    const int wid = __builtin_amdgcn_readfirstlane(tid >> 6);
    const int bx = blockIdx.x, xcd = bx & 7, idx = bx >> 3;
    const int isA = idx >> 4, rt = (xcd >> 1) * 4 + ((idx >> 2) & 3), hg = (xcd & 1) * 4 + (idx & 3);
    const int br = isA ? 0 : 1;
    const int k0 = isA ? (((rt & 3) * 5 + 1) / 2 + ((hg & 3) * 5 + 1) / 2) % 10 : 0;
    const char* xb = (isA ? xA : xB) + (size_t)rt * (128 * 1280);
    const char* wb = (isA ? wA : wB) + hg * 160;
    const int me = (br * 16 + rt) * 8 + hg;
    const int r32 = lane & 31, hl = lane >> 5;
    auto kt_of = [&](int s) { int k = s + k0; return k >= 10 ? k - 10 : k; };
    if (isA && wid < 4) {
        const int j16 = idx & 15; const unsigned dump = lds0 + OWN + wid * 512;
        const char* wl = wB + (hg >> 2) * 640 + j16 * (4 * 3840);
        const char* xl = xB + (size_t)(rt * 128 + (hg & 3) * 32 + r32) * 1280 + hl * 128;
        auto pfx = [&](int i) { return xl + i * 256; };
        auto pfkv = [&](int i) { int n = i * 64 + lane; n = n < 400 ? n : 399; const int sl = n / 40, r = n % 40, c = r % 10;
            return wl + (sl * 64 + r / 10) * 3840 + 1280 + (c < 5 ? c * 128 : 640 + c * 128); };
        auto pfq = [&](int i) { int n = i * 64 + lane; n = n < 200 ? n : 199; int sl = n / 20 + 8; sl = sl >= 10 ? sl - 10 : sl; const int r = n % 20;
            return wl + (sl * 64 + r / 5) * 3840 + (r % 5) * 128; };
        dma4(wid & 1 ? pfx(wid >> 1) : pfkv(wid >> 1), dump);
        dma4(wid & 1 ? pfx(2 + (wid >> 1)) : pfkv(2 + (wid >> 1)), dump);
        dma4(wid < 3 ? pfkv(4 + wid) : pfx(4), dump);
        dma4(pfq(wid), dump);
        { int n = wid * 64 + lane; n = n < 200 ? n : 199; const int o = (j16 * 40 + n / 5) * 1280 + (hg >> 2) * 640 + (n % 5) * 128;
          dma4(oB + o, dump); dma4(oA + o, dump); }
    }
    if (isA && wid >= 4) { for (int i = 0; i < ADELAY; ++i) __builtin_amdgcn_s_sleep(16); }

    const unsigned xo = (unsigned)((lane >> 3) * 1280 + (((lane & 7) ^ (lane >> 4)) << 4));
    auto w_off = [&](int q) { const int c = q * 64 + lane, row = __umul24(c, 3277) >> 16, j = c - row * 20; return (unsigned)(__umul24(row, 3840) + j * 16 + (j < 10 ? 1280 : 2400)); };
    if (!isA) { const char* xs = xb + k0 * 128 + wid * (16 * 1280);
      dma16(xo, xs, lds0 + XS0 + wid * 2048);
      dma16(xo ^ 64, xs + 8 * 1280, lds0 + XS0 + wid * 2048 + 1024);
      const char* wsrc = wb + (size_t)k0 * (64 * 3840);
      const unsigned wv = w_off(wid < 5 ? wid : wid - 5);
      if (wid < 5) { dma16(wv, wsrc, lds0 + WS0 + wid * 1024); dma16(wv, wsrc + 16 * 3840, lds0 + WS0 + (5 + wid) * 1024);
          if (wid >= 3) { dma16(wv, wsrc + 32 * 3840, lds0 + WS0 + (10 + wid) * 1024); dma16(wv, wsrc + 48 * 3840, lds0 + WS0 + (15 + wid) * 1024); } }
      else { dma16(wv, wsrc + 32 * 3840, lds0 + WS0 + (5 + wid) * 1024); dma16(wv, wsrc + 48 * 3840, lds0 + WS0 + (10 + wid) * 1024); } }
    if (wid == 4 && lane < 2) dma4(ws + lane * 4, lds0 + C_TAG);
    if (tid < 80) *(LAS unsigned*)(smem + tid * 4) = 0u;
    else if (tid < 82) *(LAS unsigned*)(smem + ONE + (tid - 80) * 4) = 0x3F803F80u;
    const int nnb = isA ? (rt > 0 ? 1 : 0) : (rt > 1 ? 2 : rt);
    auto kq_base = [&](int fg) { const int hd = fg / 5, dg = fg % 5; return dg < 4 ? hd * 5120 + (dg >> 1) * 2048 + (dg & 1) * 8 : hd * 5120 + 4096; };

    auto rdtag = [&]() { const u64 t = *(LAS u64*)(smem + C_TAG);
        const u64 e = ((u64)(unsigned)__builtin_amdgcn_readfirstlane((int)(t >> 32)) << 32) | (unsigned)__builtin_amdgcn_readfirstlane((int)t);
        return e * 6364136223846793005ull + 1442695040888963407ull; };
    const __amdgpu_buffer_rsrc_t wor = __builtin_amdgcn_make_buffer_rsrc((void*)(isA ? oA : oB), 0, 640 * 1280, 0x00020000);

    const int hp = wid >> 2, qo = wid & 3, qi = hp ? 3 - qo : qo;
    const int ud = qi + 8;
    const bool far = isA ? (4 * rt + qi >= 1) : (4 * rt + qi >= 8);
    const int u0 = isA ? (far ? ud - 1 : ud) : (far ? qi : 8 - 4 * rt);
    f32x16 cd, cf;
    int dsh, kb1, kb2, nh, vb0, vb1, qb1, qb2; unsigned obase, oo[5];
    auto mask_b = [&](f32x16& c, int r, bool fartile) { const int k = (r & 3) + 8 * (r >> 2); c[r] = __int_as_float(fartile ? ((k - 1) << 23) - dsh : dsh - (k << 23)); };
    auto mask_a = [&](int z) {
      SFOR(r, 16)
          constexpr u64 ad = lanes_cut(r, false), af = lanes_cut(r, true);
          cd[r] = __builtin_amdgcn_inverse_ballot_w64(ad + z) ? -__builtin_inff() : 0.f;
          cf[r] = __builtin_amdgcn_inverse_ballot_w64(af + z) ? -__builtin_inff() : 0.f;
      SEND };
    auto att_setup = [&](int part) {
        int l = tid; asm volatile("" : "+v"(l)); l &= 63;
        const int q = l & 31, h = l >> 5;
        if (part == 0) { dsh = (q - 4 * h) << 23; kb1 = KV0 + hp * 10240 + h * 2048 + q * 16; kb2 = h ? ONE : KV0 + hp * 10240 + 4096 + q * 8; nh = h ^ 1;
            asm volatile("" : "+v"(dsh), "+v"(kb1), "+v"(kb2), "+v"(nh)); }
        if (part == 1) { const bool ones = (l & 16) && (l & 3);
            vb0 = KV0 + VOFF + (4 * h + ((l & 15) >> 2)) * VP + (ones ? 160 : hp * 80 + (l & 16) * 2 + (l & 3) * 8); vb1 = vb0 + (ones ? 0 : 40);
            asm volatile("" : "+v"(vb0), "+v"(vb1)); }
        if (part == 2) { qb1 = QST + hp * 10240 + h * 2048 + (qi * 32 + q) * 16; qb2 = QST + hp * 10240 + 4096 + (qi * 32 + q) * 8;
            obase = (unsigned)(qi * 40960 + hp * 20480 + hg * 2560 + q * 16 + h * 512);
            asm volatile("" : "+v"(qb1), "+v"(qb2), "+v"(obase)); }
        if (part >= 3) {
#pragma unroll
            for (int r = 4 * (part - 3); r < 4 * (part - 2); ++r) mask_b(cd, r, false);
            asm volatile("" : "+v"(cd)); } };
    auto wo_setup = [&](int q) {
        int l = tid; asm volatile("" : "+v"(l)); const int c = q * 64 + (l & 63), rho = __umul24(c, 6554) >> 16;
        oo[q] = (unsigned)(__umul24(qo * 160 + hp * 40 + (rho & ~7) + 4 * (rho & 1) + ((rho >> 1) & 3), 1280) + hg * 160 + (c - rho * 10) * 16);
        asm volatile("" : "+v"(oo[q])); };
    u64 tag;

    if (wid < 4) {
        if (!isA) WAITV(0);
        BAR_L();
        STAMP(1);
        bf16x8 xr[32], xt[2], wf[WIN + 1];
        f32x16 acc[5];
        const int tok = wid * 32 + r32;
        const int xa = XS0 + tok * 128 + ((hl ^ ((tok >> 1) & 7)) << 4);
        int wa0 = WS0 + (8 * hl + ((lane & 15) >> 2)) * 320 + (lane & 16) * 2 + (lane & 3) * 8, wa1 = wa0 + 61440;
        asm volatile("" : "+v"(wa0), "+v"(wa1));
        auto rdx = [&](int i) { return *(LAS bf16x8*)(smem + ((i >> 2) % 3) * 16384 + (xa ^ ((i & 3) * 32))); };
        auto rdw = [&](int f) { const int i = f / 5, t = f % 5, sl = (i >> 1) % 7;
            return trfrag(smem + (sl < 6 ? wa0 + sl * 10240 : wa1) + (i & 1) * 5120 + t * 64, 1280); };
        unsigned fl = 0;
        auto rdflag = [&](int u) { fl = __hip_atomic_load((LAS unsigned*)(smem + C_F + u * 4), RLX_WG); };
        auto check = [&](int u) {
            if (__builtin_amdgcn_readfirstlane(fl) != 15) {
#pragma unroll 1
                for (unsigned spins = 0; spins < LSPIN_CAP; ++spins) { rdflag(u); if (__builtin_amdgcn_readfirstlane(fl) == 15) break; __builtin_amdgcn_s_sleep(1); }
            }
            FENCE(); };
#pragma unroll
        for (int i = 0; i < 4; ++i) xr[i] = rdx(i);
#pragma unroll
        for (int f = 0; f < WIN; ++f) wf[f] = rdw(f);
        SB();
        SFOR(f, 200)
            constexpr int i = f / 5, t = f % 5;
            if constexpr (f % 20 == 10 - WIN) lds_set(smem + C_P + wid * 4, (i >> 1) + 1);
            if constexpr (f == 200 - WIN) lds_set(smem + C_P + wid * 4, 20);
            if constexpr (f % 20 == 10) rdflag(f / 20 + 1);
            if constexpr (f % 20 == 20 - WIN && f < 190) check(f / 20 + 1);
            if constexpr (f + WIN < 200) wf[(f + WIN) % (WIN + 1)] = rdw(f + WIN);
            if constexpr (f % 20 >= 16 && f < 140) xr[(f / 20 + 1) * 4 + f % 20 - 16] = rdx((f / 20 + 1) * 4 + f % 20 - 16);
            if constexpr (t == 1 && i >= 31 && i < 39) xt[(i + 1) & 1] = rdx(i + 1);
            SB();
            if constexpr (i == 0) MFMA32Z(acc[t], wf[f % (WIN + 1)], xr[0]);
            else if constexpr (i < 32) MFMA32I(acc[t], wf[f % (WIN + 1)], xr[i]);
            else MFMA32I(acc[t], wf[f % (WIN + 1)], xt[i & 1]);
            SB();
        SEND
        STAMP(2);
        int l2 = tid; asm volatile("" : "+v"(l2)); l2 &= 63;
        const int h2 = l2 >> 5, tk = wid * 32 + (l2 & 31);
        int eb[5];
        auto ebases = [&](int base) { int l = tid; asm volatile("" : "+v"(l)); const int h = (l >> 5) & 1, k = wid * 32 + (l & 31);
            eb[0] = base + k * 16 + h * 8; eb[1] = base + k * 16 + h * 2040; eb[2] = base + (h ? k * 16 + 1024 : k * 8); eb[3] = base + (h ? k * 8 + 2040 : k * 16);
            asm volatile("" : "+v"(eb[0]), "+v"(eb[1]), "+v"(eb[2]), "+v"(eb[3])); };
        auto kq_addr = [&](int g0) { return eb[g0 % 5 == 1 ? 1 : g0 % 5 == 4 ? 2 : g0 % 5 == 3 ? 3 : 0] + kq_base(g0); };
        ebases(OWN);
        eb[4] = OWN + VOFF + tk * VP + h2 * 8; asm volatile("" : "+v"(eb[4]));
        int qa0 = (8 * h2 + 2 * ((l2 & 15) >> 2)) * 160 + (l2 & 16) * 2 + (l2 & 3) * 8, qa1 = qa0 + WS0 + 10240;
        asm volatile("" : "+v"(qa0), "+v"(qa1));
        auto rdq = [&](int f) { const int i = f / 3, t = f % 3, j = i >> 2, sl = (20 + j) % 7;
            return trfrag(smem + (j >= 7 ? qa0 + (j == 7 ? QU27 : j == 8 ? QU28 : QU29) : sl == 0 ? qa0 + WS0 : qa1 + (sl - 1) * 10240) + (i & 3) * 2560 + t * 64, 160); };
        MFMA_DRAIN();
        WAITV(0);
        { u32x2 pk[20];
#pragma unroll
          for (int n = 0; n < 20; ++n) { const int t = n >> 2, b = n & 3; pk[n] = (u32x2){pk2(acc[t][4 * b], acc[t][4 * b + 1]), pk2(acc[t][4 * b + 2], acc[t][4 * b + 3])}; }
          SB();
#pragma unroll
          for (int n = 0; n < 20; ++n) *(LAS u32x2*)(smem + (n < 10 ? kq_addr(2 * n) : eb[4] + (2 * n - 20) * 8)) = pk[n];
          if (h2 == 0) *(LAS u32x2*)(smem + eb[4] + 160) = (u32x2){0x3F803F80u, 0x3F803F80u}; }
        lds_set(smem + C_E + wid * 4, 1);
        check(10);
        xt[0] = rdx(32);
#pragma unroll
        for (int f = 0; f < WIN; ++f) wf[f] = rdq(f);
        STAMP(3);
        SB();
        u64 pv0 = 0, pv1 = 0;
        SFOR(f, 120)
            constexpr int i = f / 3, t = f % 3, j = i >> 2;
            if constexpr (f % 24 == 12 && j < 9) rdflag(11 + j / 2);
            if constexpr (f % 12 == 12 - WIN && (j == 1 || j == 6 || j == 8)) lds_set(smem + C_P + wid * 4, 21 + j);
            if constexpr (f % 24 == 24 - WIN && j < 9) check(11 + j / 2);
            if constexpr (f + WIN < 120) wf[(f + WIN) % (WIN + 1)] = rdq(f + WIN);
            if constexpr (t == 0 && i < 7) xt[(i + 1) & 1] = rdx(32 + i + 1);
            if constexpr (f >= 90 && f < 100 && !(f & 1)) wo_setup((f - 90) / 2);
            if constexpr (f == 99) tag = rdtag();
            if constexpr (f >= 100 && f < 107) att_setup(f - 100);
            if constexpr (f == 108) ebases(QST);
            if constexpr (f == 116) { pv0 = __hip_atomic_load((LAS u64*)(smem + C_P), RLX_WG); pv1 = __hip_atomic_load((LAS u64*)(smem + C_P + 8), RLX_WG); }
            SB();
            if constexpr (i == 0) MFMA32Z(acc[t], wf[f % (WIN + 1)], xt[0]);
            else if constexpr (i < 8) MFMA32I(acc[t], wf[f % (WIN + 1)], xt[i & 1]);
            else MFMA32I(acc[t], wf[f % (WIN + 1)], xr[i - 8]);
            SB();
        SEND
        STAMP(4);
        if (__builtin_amdgcn_readfirstlane((int)min(min((unsigned)pv0, (unsigned)(pv0 >> 32)), min((unsigned)pv1, (unsigned)(pv1 >> 32)))) < 29) wait_min4(smem + C_P, 29);
        MFMA_DRAIN();
        { acc[0] *= QSCALE; acc[1] *= QSCALE; acc[2] *= QSCALE;
          SB();
          u32x2 pk[10];
#pragma unroll
          for (int n = 0; n < 10; ++n) { const int t = n >> 2, b = n & 3; pk[n] = (u32x2){pk2(acc[t][4 * b], acc[t][4 * b + 1]), pk2(acc[t][4 * b + 2], acc[t][4 * b + 3])}; }
          SB();
#pragma unroll
          for (int n = 0; n < 10; ++n) *(LAS u32x2*)(smem + kq_addr(2 * n)) = pk[n]; }
        lds_set(smem + C_QF + wid * 4, 1);
        __builtin_amdgcn_s_setprio(1);
        STAMP(5);
    } else {
        const int hw = wid - 4;
        int known = 0;
        __builtin_amdgcn_s_setprio(3);
        auto lds_or = [&](int off, unsigned v) { FENCE(); if (lane == 0) __hip_atomic_fetch_or((LAS unsigned*)(smem + off), v, RLX_WG); FENCE(); };
        unsigned wo[5], qo[5];
        auto done = [&](int n) { lds_or(C_F + n * 4, 1u << hw); };
        const unsigned xo1 = (xo ^ 64) + 9216, xo2 = xo + 2 * 9216, xo3 = (xo ^ 64) + 3 * 9216;
        auto round_x = [&](int n) { dma16x4(xo, xo1, xo2, xo3, xb + kt_of(n) * 128 + hw * (32 * 1280), lds0 + XS0 + (n % 3) * 16384 + hw * 4096); };
        auto round_w = [&](int n) {
            const int u = 2 * n + (hw >> 1);
            const unsigned dst = lds0 + (u < 27 ? WS0 + (u % 7) * 10240 : u == 27 ? QU27 : u == 28 ? QU28 : QU29) + (hw & 1) * 5120 + 2048;
            if (u < 20) dma16x5(wo, wb + (size_t)(kt_of(u >> 1) * 64 + (u & 1) * 32) * 3840 - 2048, dst);
            else dma16x5(qo, wb + (size_t)kt_of(u < 22 ? u - 12 : u - 22) * (64 * 3840) - 2048, dst); };
        if (isA) round_x(0);
        round_x(1);
#pragma unroll
        for (int q = 0; q < 5; ++q) wo[q] = w_off(q) + (hw & 1) * (16 * 3840) + (4 - q) * 1024;
        if (isA) { round_w(0); round_w(1); WAITV(5); } else { round_w(1); WAITV(9); }
        BAR_L();
#pragma unroll
        for (int q = 0; q < 5; ++q) { const int c = q * 64 + lane, rho = __umul24(c, 6554) >> 16;
            qo[q] = (unsigned)(__umul24((hw & 1) * 32 + (rho & ~7) + 4 * (rho & 1) + ((rho >> 1) & 3), 3840) + (c - rho * 10) * 16 + (4 - q) * 1024); }
#pragma unroll 1
        for (int n = 2; n <= 13; ++n) {
            const int need = n < 13 ? 2 * n - 5 : 20;
            if (need > known) known = min4(smem + C_P);
            if (need > known) { WAITV(0); done(n - 1); known = wait_min4(smem + C_P, need); }
            if (n < 10) round_x(n);
            round_w(n);
            if (n < 10) WAITV(9); else WAITV(5);
            done(n - 1);
        }
        WAITV(0); done(13);
        STAMP(1);
        tag = rdtag();
        auto a_chunk = [&](int a) { int o;
            if (a < 320) { const int hd = a / 80, rem = a % 80; o = hd * 5120 + (rem < 32 ? (96 + rem) * 16 : rem < 64 ? 2048 + (64 + rem) * 16 : 4096 + 768 + (rem - 64) * 16); }
            else o = VOFF + 96 * VP + (a - 320) * 16;
            return o; };
        if (rt < 15) {
            const __amdgpu_buffer_rsrc_t kvr = __builtin_amdgcn_make_buffer_rsrc(ws + WS_KV + me * KVG, 0, KVB, 0x00020000);
            wait_min4(smem + C_E, 1);
            STAMP(2);
            if (!isA) { u32x4 v[11];
#pragma unroll
                for (int j = 0; j < 11; ++j) if (j < 10 || hw == 3) v[j] = *(LAS u32x4*)(smem + OWN + hw * 10240 + j * 1024 + lane * 16);
#pragma unroll
                for (int j = 0; j < 11; ++j) if (j < 10 || hw == 3) __builtin_amdgcn_raw_buffer_store_b128(v[j], kvr, hw * 10240 + j * 1024 + lane * 16, 0, 16);
            } else {
#pragma unroll
                for (int j = 0; j < 3; ++j) { const int a = (4 * j + hw) * 64 + lane;
                    if (a < 656) { const int o = a_chunk(a); __builtin_amdgcn_raw_buffer_store_b128(*(LAS u32x4*)(smem + OWN + o), kvr, o, 0, 16); } }
            }
            STAMP(3);
        }
        wait_min4(smem + C_P, 22);
        round_w(14);
        WAITV(5);
        if (rt < 15) {
            unsigned old = 0;
            if (lane == 0) old = __hip_atomic_fetch_add((LAS unsigned*)(smem + N_PUB), 1u, RLX_WG);
            if (lane == 0 && old == 3) __hip_atomic_store((GAS u64*)(ws + WS_FKV + me * 128), tag, RLX_AGENT);
            STAMP(4);
        }
        WAITV(0); done(14);
        __builtin_amdgcn_s_setprio(0);
#pragma unroll
        for (int p = 0; p < 7; ++p) att_setup(p);
#pragma unroll
        for (int q = 0; q < 5; ++q) wo_setup(q);
        __builtin_amdgcn_s_setprio(3);
        if (hw < NPOLL) {
            __builtin_amdgcn_s_sleep(KDELAY);
            for (int i = 0; i < hw; ++i) __builtin_amdgcn_s_sleep(PSTAG);
            poll_shared((GAS u64*)(ws + WS_FKV + (me - 8 * (lane + 1)) * 128), lane < nnb, tag, smem + N_KV);
        } else wait_word(smem + N_KV, 1);
        STAMP(5);
        if (!isA) {
            const int n = hw >> 1, half = hw & 1;
            if (n < nnb) {
                const char* src = ws + WS_KV + (me - 8 * (n + 1)) * KVG + half * 20480; const unsigned dst = lds0 + KV0 + (1 - n) * KVB + half * 20480;
                wait_min4(smem + C_P, 27);
#pragma unroll
                for (int j = 0; j < 4; ++j) dma16x5_sc1(lane * 16, src + j * 5120 + 2048, dst + j * 5120 + 2048);
                if (half) dma16_sc1(lane * 16, src + 20480, dst + 20480);
            }
        } else if (nnb) {
            const __amdgpu_buffer_rsrc_t nbr = __builtin_amdgcn_make_buffer_rsrc(ws + WS_KV + (me - 8) * KVG, 0, KVB, 0x00020000);
            u32x4 v[3];
#pragma unroll
            for (int j = 0; j < 3; ++j) { const int a = (4 * j + hw) * 64 + lane; if (a < 656) v[j] = __builtin_amdgcn_raw_buffer_load_b128(nbr, a_chunk(a), 0, 16); }
            wait_min4(smem + C_P, 27);
#pragma unroll
            for (int j = 0; j < 3; ++j) { const int a = (4 * j + hw) * 64 + lane; if (a < 656) *(LAS u32x4*)(smem + KV0 + KVB + a_chunk(a)) = v[j]; }
        }
        __builtin_amdgcn_s_setprio(2);
        wait_word(smem + C_QF + qi * 4, 1);
        STAMP(6);
    }
    u32x4 wreg[13];
    auto ldwo = [&](int i) { wreg[i] = __builtin_amdgcn_raw_buffer_load_b128(wor, oo[i % 5] + (i / 5) * (32 * 1280) + (i > 6 || (i == 6 && lane >= 16) ? 40 * 1280 : 0), 0, 0); };
    STAMP(7);
    u32x2 ob[2][3];
    {
        bf16x8 q1[2]; u32x2 q2[2];
#pragma unroll
        for (int e = 0; e < 2; ++e) {
            q1[e] = *(LAS bf16x8*)(smem + qb1 + e * 5120);
            q2[e] = *(LAS u32x2*)(smem + qb2 + e * 5120);
            if (hl) q2[e] = (u32x2){0u, 0u};
        }
        if (isA) mask_a(0);
        struct KF { bf16x8 a; s16x4 b; };
        struct VF { bf16x8 a, b; };
        auto rdk = [&](int u, int e) { const int so = (u >> 2) * KVB, tw = u & 3; KF k;
            k.a = *(LAS bf16x8*)(smem + kb1 + so + tw * 512 + e * 5120); k.b = *(LAS s16x4*)(smem + __umul24(so + tw * 256 + e * 5120, nh) + kb2); return k; };
        auto rdv = [&](int u, int e) { LAS char* p = smem + (e ? vb1 : vb0) + (u >> 2) * KVB + (u & 3) * (32 * VP); VF v;
            v.a = trfrag(p, 8 * VP); v.b = trfrag(p + 16 * VP, 8 * VP); return v; };
        auto qk = [&](const KF& k, int e, const f32x16& c) { return MFMA32K8(k.b, __builtin_bit_cast(s16x4, q2[e]), MFMA32(k.a, q1[e], c)); };
        const f32x16 zero = {};
        auto max16 = [&](const f32x16& s, float m) {
#pragma unroll
            for (int r = 0; r < 16; ++r) m = __builtin_elementwise_maximum(m, s[r]);
            return m; };
        auto set_ref = [&](float m, int e) { const unsigned nb = pk2(-m, 0.f); if (hl) q2[e][0] = nb; };
        auto ex4 = [&](f32x16& s, int b) {
#pragma unroll
            for (int r = 0; r < 4; ++r) s[b + r] = __builtin_amdgcn_exp2f(s[b + r]); };
        auto pk8 = [&](const f32x16& s, int b) { return __builtin_bit_cast(bf16x8, (u32x4){pk2(s[b], s[b + 1]), pk2(s[b + 2], s[b + 3]), pk2(s[b + 4], s[b + 5]), pk2(s[b + 6], s[b + 7])}); };
        auto phase = [&](auto nx, f32x16& cur, const VF& v, f32x16& o, f32x16& nxt, const KF& k, int e) __attribute__((always_inline)) {
            constexpr int NX = decltype(nx)::value;
            SB(); ex4(cur, 0); SB();
            if constexpr (NX) nxt = MFMA32(k.a, q1[e], NX == 1 ? zero : NX == 2 ? cf : cd);
            SB(); ex4(cur, 4); { const bf16x8 p = pk8(cur, 0); SB(); o = MFMA32(v.a, p, o); }
            SB(); ex4(cur, 8); SB();
            if constexpr (NX) nxt = MFMA32K8(k.b, __builtin_bit_cast(s16x4, q2[e]), nxt);
            SB(); ex4(cur, 12); { const bf16x8 p = pk8(cur, 8); SB(); o = MFMA32(v.b, p, o); }
            SB(); };
        f32x16 o0, o1, sa, sb; KF kx; VF vx;
        auto step = [&](auto m0, auto m1, int u) __attribute__((always_inline)) {
            constexpr int M1 = decltype(m1)::value;
            KF ky; if constexpr (M1) ky = rdk(u - 1, 0);
            const VF vy = rdv(u, 1);
            phase(m0, sa, vx, o0, sb, kx, 1);
            if constexpr (M1) { kx = rdk(u - 1, 1); vx = rdv(u - 1, 0); }
            phase(m1, sb, vy, o1, sa, ky, 0); };
        auto nbwait = [&](int u) { if (u == 8) wait_word(smem + N_NB, 4); };
        { const KF k0f = rdk(ud, 0); kx = rdk(ud, 1); vx = rdv(ud, 0);
          const f32x16 s0 = qk(k0f, 0, cd), s1 = qk(kx, 1, cd);
          set_ref(halfmax(max16(s0, s0[0])), 0); set_ref(halfmax(max16(s1, s1[0])), 1);
          sa = qk(k0f, 0, cd); }
        if (hp) { WAITV(0); lds_inc0(smem + N_NB, lane); lds_inc0(smem + N_G0, lane); }
        o0 = zero; o1 = zero;
        if (u0 + 8 == ud) {
            SFOR(it, 9)
                if constexpr (it < 7) { ldwo(2 * it); if constexpr (it < 6) ldwo(2 * it + 1); }
                if constexpr (it == PFLIP) { if (hp) __builtin_amdgcn_s_setprio(0); }
                if constexpr (it == 7) {
#pragma unroll
                    for (int r = 0; r < 16; ++r) mask_b(cf, r, true); }
                if constexpr (it < 4) nbwait(ud - it);
                step(IC<it == 0 ? 3 : it == 8 ? 2 : 1>{}, IC<it == 8 ? 0 : it == 7 ? 2 : 1>{}, ud - it);
            SEND
        } else {
            __builtin_amdgcn_s_setprio(0);
#pragma unroll
            for (int i = 0; i < 13; ++i) ldwo(i);
            if (!isA) cf = zero;
            if (u0 < ud) {
                nbwait(ud);
                if (u0 + 1 < ud) {
                    step(IC<3>{}, IC<1>{}, ud);
#pragma unroll 1
                    for (int u = ud - 1; u > u0 + 1; --u) { nbwait(u); step(IC<1>{}, IC<1>{}, u); }
                    nbwait(u0 + 1);
                    step(IC<1>{}, IC<2>{}, u0 + 1);
                } else step(IC<3>{}, IC<2>{}, ud);
                step(IC<2>{}, IC<0>{}, u0);
            } else step(IC<3>{}, IC<0>{}, ud);
        }
        if (!__all(o0[12] < 1.152921504606847e18f && o1[12] < 1.152921504606847e18f)) {
            if (isA) { int z = 0; asm volatile("" : "+s"(z)); mask_a(z); }
            else { asm volatile("" : "+v"(dsh));
#pragma unroll
                for (int r = 0; r < 16; ++r) { mask_b(cd, r, false); mask_b(cf, r, true); }
                if (!far) cf = zero; }
#pragma unroll
            for (int e = 0; e < 2; ++e) {
                if (hl) q2[e][0] = 0u;
                const f32x16 s = qk(rdk(ud, e), e, cd);
                float m = max16(s, s[0]);
                if (u0 < ud) m = max16(qk(rdk(u0, e), e, cf), m);
#pragma unroll 1
                for (int u = u0 + 1; u < ud; ++u) m = max16(qk(rdk(u, e), e, zero), m);
                set_ref(halfmax(m), e);
            }
#pragma unroll
            for (int e = 0; e < 2; ++e) {
                f32x16 o = zero;
#pragma unroll 1
                for (int u = u0; u <= ud; ++u) { f32x16 t = u == ud ? qk(rdk(u, e), e, cd) : u == u0 ? qk(rdk(u, e), e, cf) : qk(rdk(u, e), e, zero);
                    const VF v = rdv(u, e); ex4(t, 0); ex4(t, 4); ex4(t, 8); ex4(t, 12);
                    o = MFMA32(v.a, pk8(t, 0), o); o = MFMA32(v.b, pk8(t, 8), o); }
                if (e) o1 = o; else o0 = o;
            }
        }
        const float rl0 = __builtin_amdgcn_rcpf(o0[12]), rl1 = __builtin_amdgcn_rcpf(o1[12]);
        SB();
#pragma unroll
        for (int r = 0; r < 12; ++r) { o0[r] *= rl0; o1[r] *= rl1; }
        SB();
#pragma unroll
        for (int j = 0; j < 3; ++j) {
            ob[0][j] = (u32x2){pk2(o0[4 * j], o0[4 * j + 1]), pk2(o0[4 * j + 2], o0[4 * j + 3])};
            ob[1][j] = (u32x2){pk2(o1[4 * j], o1[4 * j + 1]), pk2(o1[4 * j + 2], o1[4 * j + 3])}; }
        SB();
    }
    STAMP(8);
    const __amdgpu_buffer_rsrc_t orr = __builtin_amdgcn_make_buffer_rsrc(ws + WS_O + (br * 16 + rt) * OG, 0, 4 * 40960, 0x00020000);
    {
      { auto rx = __builtin_amdgcn_permlane32_swap(ob[0][0][0], ob[0][1][0], false, false);
        auto ry = __builtin_amdgcn_permlane32_swap(ob[0][0][1], ob[0][1][1], false, false);
        __builtin_amdgcn_raw_buffer_store_b128((u32x4){rx[0], ry[0], rx[1], ry[1]}, orr, obase, 0, 16); }
      if (hl == 0) __builtin_amdgcn_raw_buffer_store_b128((u32x4){ob[0][2][0], ob[0][2][1], ob[1][0][0], ob[1][0][1]}, orr, obase + 1024, 0, 16);
      { auto fx = __builtin_amdgcn_permlane32_swap(ob[1][0][0], ob[1][1][0], false, false);
        auto fy = __builtin_amdgcn_permlane32_swap(ob[1][0][1], ob[1][1][1], false, false);
        auto sx = __builtin_amdgcn_permlane32_swap(ob[1][1][0], ob[1][2][0], false, false);
        auto sy = __builtin_amdgcn_permlane32_swap(ob[1][1][1], ob[1][2][1], false, false);
        __builtin_amdgcn_raw_buffer_store_b128((u32x4){fx[1], fy[1], sx[0], sy[0]}, orr, obase + 1536, 0, 16); } }
    if (lane == 0) { (void)__hip_atomic_fetch_add((LAS unsigned*)(smem + N_G1), 1u, RLX_WG);
        if (!hp) (void)__hip_atomic_fetch_add((LAS unsigned*)(smem + N_G0), 1u, RLX_WG); }
    FENCE();
    STAMP(6);
    auto signal_o = [&]() {
        if (lds_inc(smem + N_OS + hp * 4, lane) == 3 && lane == 0) __hip_atomic_store((GAS u64*)(ws + WS_FO + (me * 2 + hp) * 128), tag, RLX_AGENT); };
    WAITV(0); signal_o();
    STAMP(9);
    wait_word(smem + (hp ? N_G1 : N_G0), 8);
    const int p1 = hp ? 30720 : 35840, p2 = hp ? 65536 : 43520;
    const int wb0 = hp ? KV0 + 10240 : 512, wb1 = hp ? KV0 + KVB + 10240 - 30720 : KV0 - 35840, wb2 = KV0 + KVB - 43520;
#pragma unroll
    for (int i = 0; i < 13; ++i) { const int L = qo * 12800 + i * 1024;
        if (i < 12 || lane < 32) *(LAS u32x4*)(smem + (L + (L < p1 ? wb0 : L < p2 ? wb1 : wb2)) + lane * 16) = wreg[i]; }
    lds_inc0(smem + N_WO + hp * 4, lane);
    STAMP(10);
    __builtin_amdgcn_s_sleep(PDELAY);
    if (qo < NPOLL) {
        for (int i = 0; i < qo; ++i) __builtin_amdgcn_s_sleep(PSTAG);
        poll_shared((GAS u64*)(ws + WS_FO + (((me & ~7) + (lane & 7)) * 2 + hp) * 128), lane < 8, tag, smem + N_OK + hp * 4);
    } else wait_word(smem + N_OK + hp * 4, 1);
    STAMP(11);
    f32x16 oc[3]; f32x4 ps[10];
    LAS char* red = smem + OWN + qo * 10240 + lane * 16;
    { bf16x8 af[5], of[20];
      const int la = (8 * hl + 2 * ((lane & 15) >> 2)) * 160 + (lane & 16) * 2 + (lane & 3) * 8;
      const int aa0 = wb0 + la, aa1 = (hp ? wb1 : wb0) + la, aa2 = wb1 + la, aa3 = (hp ? wb1 : wb2) + la;
      auto rda = [&](int f) { return trfrag(smem + (f < 36 ? aa0 : f < 42 ? aa1 : f < 51 ? aa2 : aa3) + (f / 3) * 2560 + (f % 3) * 64, 160); };
      auto ldo = [&](int k) { of[k] = __builtin_bit_cast(bf16x8, __builtin_amdgcn_raw_buffer_load_b128(orr, qo * 40960 + (hp * 20 + k) * 1024 + lane * 16, 0, 16)); };
#pragma unroll
      for (int k = 0; k < OPF; ++k) ldo(k);
      wait_word(smem + N_WO + hp * 4, 4);
      __builtin_amdgcn_s_setprio(0);
      if (!hp) wait_word(smem + N_G1, 8);
#pragma unroll
      for (int f = 0; f < 4; ++f) af[f] = rda(f);
      SB();
      SFOR(f, 60)
          if constexpr (f % 3 == 0 && f / 3 + OPF < 20) ldo(f / 3 + OPF);
          if constexpr (f + 4 < 60) af[(f + 4) % 5] = rda(f + 4);
          if constexpr (f == 57) { if (hp) { wait_word(smem + C_RD + qo * 4, 1);
#pragma unroll
              for (int b = 0; b < 10; ++b) ps[b] = *(LAS f32x4*)(red + b * 1024); } }
          SB();
          if constexpr (f < 3) MFMA32Z(oc[f], af[f % 5], of[0]); else MFMA32I(oc[f % 3], af[f % 5], of[f / 3]);
          SB();
      SEND }
    MFMA_DRAIN();
    STAMP(12);
    { if (!hp) {
          wait_word(smem + N_G1, 8);
#pragma unroll
          for (int b = 0; b < 10; ++b) *(LAS f32x4*)(red + b * 1024) = (f32x4){oc[b >> 2][4 * (b & 3)], oc[b >> 2][4 * (b & 3) + 1], oc[b >> 2][4 * (b & 3) + 2], oc[b >> 2][4 * (b & 3) + 3]};
          lds_set(smem + C_RD + qo * 4, 1);
      } else {
          char* orow = out + (size_t)(rt * 128 + qo * 32 + r32) * 2560 + br * 1280 + hg * 160 + hl * 16;
#pragma unroll
          for (int b = 0; b < 10; ++b)
#pragma unroll
              for (int r = 0; r < 4; ++r) ps[b][r] += oc[b >> 2][4 * (b & 3) + r];
          SB();
          u32x2 pk[10];
#pragma unroll
          for (int b = 0; b < 10; ++b) pk[b] = (u32x2){pk2(ps[b][0], ps[b][1]), pk2(ps[b][2], ps[b][3])};
          SB();
          u32x4 st[5];
#pragma unroll
          for (int b = 0; b < 10; b += 2) {
              auto rx = __builtin_amdgcn_permlane32_swap(pk[b][0], pk[b + 1][0], false, false); auto ry = __builtin_amdgcn_permlane32_swap(pk[b][1], pk[b + 1][1], false, false);
              st[b >> 1] = (u32x4){rx[0], ry[0], rx[1], ry[1]}; }
          SB();
#pragma unroll
          for (int b = 0; b < 5; ++b) *(u32x4*)(orow + b * 32) = st[b];
      } }
    if (bx == 0 && wid == 0) {
#pragma unroll 1
        for (int i = 0; i < 4; ++i) poll_flags((GAS u64*)(ws + WS_FO + (i * 64 + lane) * 256), true, tag);
        if (lane == 0) __hip_atomic_store((GAS u64*)ws, tag, RLX_AGENT);
    }
}

extern "C" void kernel_launch(void* const* d_in, const int* in_sizes, int n_in, void* d_out, int out_size, void* d_ws, size_t ws_size,
                              hipStream_t stream) {
    fused_attn_kernel<<<256, 512, 0, stream>>>((const char*)d_in[0], (const char*)d_in[1], (const char*)d_in[2], (const char*)d_in[3],
                                               (const char*)d_in[4], (const char*)d_in[5], (char*)d_out, (char*)d_ws);
}
```

```cpp
#define ADELAY 3
#include <hip/hip_runtime.h>
#include <hip/hip_bf16.h>
#include <stdint.h>

typedef short bf16x8 __attribute__((ext_vector_type(8)));
typedef short s16x4 __attribute__((ext_vector_type(4)));
typedef float f32x2 __attribute__((ext_vector_type(2)));
typedef float f32x4 __attribute__((ext_vector_type(4)));
typedef float f32x16 __attribute__((ext_vector_type(16)));
typedef unsigned u32x4 __attribute__((ext_vector_type(4)));
typedef unsigned u32x2 __attribute__((ext_vector_type(2)));
typedef __bf16 bfx2 __attribute__((ext_vector_type(2)));
typedef unsigned long long u64;
#define LAS __attribute__((address_space(3)))
#define GAS __attribute__((address_space(1)))
#define DI __device__ __forceinline__
#define RLX_AGENT __ATOMIC_RELAXED, __HIP_MEMORY_SCOPE_AGENT
#define RLX_WG __ATOMIC_RELAXED, __HIP_MEMORY_SCOPE_WORKGROUP

constexpr int LDS_BYTES = 163840;
constexpr int VOFF = 20480, VP = 168;
constexpr int KVB = VOFF + 128 * VP;
constexpr int KV0 = LDS_BYTES - 3 * KVB;
constexpr int OWN = KV0 + 2 * KVB;
constexpr int C_F = 0, C_P = 128, C_E = 144, C_QF = 160, C_RD = 176, C_CNT = 192, C_TAG = 320;
constexpr int N_PUB = C_CNT, N_NB = C_CNT + 4, N_G0 = C_CNT + 8, N_G1 = C_CNT + 12, N_WO = C_CNT + 16, N_OS = C_CNT + 24, N_KV = C_CNT + 32, N_OK = C_CNT + 36;
constexpr int XS0 = 512, WS0 = XS0 + 3 * 16384, QST = 512, ONE = 336, QU27 = XS0 + 16384, QU28 = XS0, QU29 = QU27 + 10240;
static_assert(WS0 + 7 * 10240 <= OWN, "ring overlaps the own image");
constexpr unsigned WS_FKV = 4096, WS_FO = WS_FKV + 256 * 128, WS_KV = 131072;
constexpr unsigned KVG = KVB, OG = 4 * 40960;
constexpr unsigned WS_O = WS_KV + 256 * KVG;
constexpr unsigned WS_END = WS_O + 32 * OG;
constexpr unsigned WS_DBG = WS_END;
constexpr unsigned SPIN_CAP = 400000, LSPIN_CAP = 4000000;
constexpr float QSCALE = 0.22360679774997896f * 1.4426950408889634f;
#define PSLEEP 2
#define PDELAY 1
#define KDELAY 0
#define NPOLL 1
#define PSTAG 3
#define PFLIP 6
#define OPF 12
#define ABL4N 2
#define WIN 4

#define MFMA32(a, b, c) __builtin_amdgcn_mfma_f32_32x32x16_bf16((a), (b), (c), 0, 0, 0)
#define MFMA32K8(a, b, c) __builtin_amdgcn_mfma_f32_32x32x8bf16_1k((a), (b), (c), 0, 0, 0)
#define MFMA32I(acc, a, b) asm volatile("v_mfma_f32_32x32x16_bf16 %0, %1, %2, %0" : "+v"(acc) : "v"(a), "v"(b))
#define MFMA32Z(acc, a, b) asm volatile("v_mfma_f32_32x32x16_bf16 %0, %1, %2, 0" : "=&v"(acc) : "v"(a), "v"(b))
#define MFMA_DRAIN() asm volatile("s_nop 7\n\ts_nop 4" ::: "memory")
#define WAITV(N) asm volatile("s_waitcnt vmcnt(" #N ")" ::: "memory")
#define WAITL() asm volatile("s_waitcnt lgkmcnt(0)" ::: "memory")
#define BAR_L() asm volatile("s_waitcnt lgkmcnt(0)\n\ts_barrier" ::: "memory")
#define SB() __builtin_amdgcn_sched_barrier(0)
#define FENCE() asm volatile("" ::: "memory")

#define STAMP(i) do {} while (0)

template <int V> struct IC { static constexpr int value = V; };
template <int I, int N, class F> DI void static_for(F&& f) { if constexpr (I < N) { f(IC<I>{}); static_for<I + 1, N>(f); } }
#define SFOR(var, N) static_for<0, N>([&](auto ic_##var) __attribute__((always_inline)) { constexpr int var = decltype(ic_##var)::value;
#define SEND });

DI void dma16(unsigned voff, const char* sbase, unsigned ldsaddr) {
    asm volatile("s_mov_b32 m0, %2\n\ts_nop 0\n\tglobal_load_lds_dwordx4 %0, %1" :: "v"(voff), "s"(sbase), "s"(ldsaddr) : "memory");
}
DI void dma16x4(unsigned v0, unsigned v1, unsigned v2, unsigned v3, const char* sbase, unsigned ldsaddr) {
    asm volatile("s_mov_b32 m0, %5\n\ts_nop 0\n\tglobal_load_lds_dwordx4 %0, %4\n\tglobal_load_lds_dwordx4 %1, %4 offset:1024\n\t"
                 "global_load_lds_dwordx4 %2, %4 offset:2048\n\tglobal_load_lds_dwordx4 %3, %4 offset:3072"
                 :: "v"(v0), "v"(v1), "v"(v2), "v"(v3), "s"(sbase), "s"(ldsaddr) : "memory");
}
DI void dma16x5(const unsigned (&v)[5], const char* sbase, unsigned ldsaddr) {
    asm volatile("s_mov_b32 m0, %6\n\ts_nop 0\n\tglobal_load_lds_dwordx4 %0, %5 offset:-2048\n\tglobal_load_lds_dwordx4 %1, %5 offset:-1024\n\t"
                 "global_load_lds_dwordx4 %2, %5\n\tglobal_load_lds_dwordx4 %3, %5 offset:1024\n\tglobal_load_lds_dwordx4 %4, %5 offset:2048"
                 :: "v"(v[0]), "v"(v[1]), "v"(v[2]), "v"(v[3]), "v"(v[4]), "s"(sbase), "s"(ldsaddr) : "memory");
}
DI void dma16x5_sc1(unsigned voff, const char* sbase, unsigned ldsaddr) {
    asm volatile("s_mov_b32 m0, %2\n\ts_nop 0\n\tglobal_load_lds_dwordx4 %0, %1 offset:-2048 sc1\n\tglobal_load_lds_dwordx4 %0, %1 offset:-1024 sc1\n\t"
                 "global_load_lds_dwordx4 %0, %1 sc1\n\tglobal_load_lds_dwordx4 %0, %1 offset:1024 sc1\n\tglobal_load_lds_dwordx4 %0, %1 offset:2048 sc1"
                 :: "v"(voff), "s"(sbase), "s"(ldsaddr) : "memory");
}
DI void dma16_sc1(unsigned voff, const char* sbase, unsigned ldsaddr) {
    asm volatile("s_mov_b32 m0, %2\n\ts_nop 0\n\tglobal_load_lds_dwordx4 %0, %1 sc1" :: "v"(voff), "s"(sbase), "s"(ldsaddr) : "memory");
}
DI void dma4(const char* p, unsigned ldsaddr) {
    asm volatile("s_mov_b32 m0, %1\n\ts_nop 0\n\tglobal_load_lds_dword %0, off" :: "v"(p), "s"(ldsaddr) : "memory");
}
DI unsigned pk2(float lo, float hi) { f32x2 v = {lo, hi}; return __builtin_bit_cast(unsigned, __builtin_convertvector(v, bfx2)); }
typedef short v4i16_t __attribute__((ext_vector_type(4)));
DI bf16x8 trfrag(LAS char* p, int second) {
    s16x4 a = __builtin_bit_cast(s16x4, __builtin_amdgcn_ds_read_tr16_b64_v4i16((LAS v4i16_t*)p));
    s16x4 b = __builtin_bit_cast(s16x4, __builtin_amdgcn_ds_read_tr16_b64_v4i16((LAS v4i16_t*)(p + second)));
    return __builtin_shufflevector(a, b, 0, 1, 2, 3, 4, 5, 6, 7);
}
constexpr u64 lanes_cut(int r, bool fartile) {
    u64 m = 0;
    for (int l = 0; l < 64; ++l) { const int key = (r & 3) + 8 * (r >> 2) + 4 * (l >> 5), d = (l & 31) - key + (fartile ? 32 : 0);
        const bool ok = d >= 0 && d <= 14 && !(d & 1);
        if (!ok) m |= 1ull << l; }
    return m;
}
DI float halfmax(float m) { auto rr = __builtin_amdgcn_permlane32_swap(__float_as_uint(m), __float_as_uint(m), false, false);
    return __builtin_elementwise_maximum(__uint_as_float(rr[0]), __uint_as_float(rr[1])); }

DI void poll_flags(GAS u64* word, bool active, u64 tag) {
#pragma unroll 1
    for (unsigned spins = 0; spins < SPIN_CAP; ++spins) {
        u64 v = tag;
        if (active) v = __hip_atomic_load(word, RLX_AGENT);
        if (__all(v == tag)) break;
        __builtin_amdgcn_s_sleep(PSLEEP);
    }
    __builtin_amdgcn_fence(__ATOMIC_ACQUIRE, "wavefront");
}
DI void poll_shared(GAS u64* word, bool active, u64 tag, LAS char* ok) {
#pragma unroll 1
    for (unsigned spins = 0; spins < SPIN_CAP; ++spins) {
        if (__builtin_amdgcn_readfirstlane((int)__hip_atomic_load((LAS unsigned*)ok, RLX_WG))) break;
        u64 v = tag;
        if (active) v = __hip_atomic_load(word, RLX_AGENT);
        if (__all(v == tag)) { __hip_atomic_store((LAS unsigned*)ok, 1u, RLX_WG); break; }
        __builtin_amdgcn_s_sleep(PSLEEP);
    }
    __builtin_amdgcn_fence(__ATOMIC_ACQUIRE, "wavefront");
    asm volatile("" ::: "memory");
}
DI int min4(LAS char* p) {
    const u64 a = __hip_atomic_load((LAS u64*)p, RLX_WG), b = __hip_atomic_load((LAS u64*)(p + 8), RLX_WG);
    const unsigned m = min(min((unsigned)a, (unsigned)(a >> 32)), min((unsigned)b, (unsigned)(b >> 32)));
    return __builtin_amdgcn_readfirstlane((int)m);
}
DI int wait_min4(LAS char* p, int need) {
    int v = min4(p);
#pragma unroll 1
    for (unsigned spins = 0; v < need && spins < LSPIN_CAP; ++spins) { __builtin_amdgcn_s_sleep(1); v = min4(p); }
    FENCE();
    return v;
}
DI void wait_word(LAS char* p, unsigned need) {
#pragma unroll 1
    for (unsigned spins = 0; spins < LSPIN_CAP; ++spins) {
        if ((unsigned)__builtin_amdgcn_readfirstlane((int)__hip_atomic_load((LAS unsigned*)p, RLX_WG)) >= need) break;
        __builtin_amdgcn_s_sleep(1); }
    FENCE();
}
DI void lds_inc0(LAS char* p, int lane) { FENCE(); if (lane == 0) (void)__hip_atomic_fetch_add((LAS unsigned*)p, 1u, RLX_WG); FENCE(); }
DI void wait_pair(LAS char* p, unsigned need) {
#pragma unroll 1
    for (unsigned spins = 0; spins < LSPIN_CAP; ++spins) { const u64 v = __hip_atomic_load((LAS u64*)p, RLX_WG);
        if ((unsigned)__builtin_amdgcn_readfirstlane((int)min((unsigned)v, (unsigned)(v >> 32))) >= need) break;
        __builtin_amdgcn_s_sleep(1); }
    FENCE();
}
DI unsigned lds_inc(LAS char* p, int lane) { FENCE(); unsigned old = 0; if (lane == 0) old = __hip_atomic_fetch_add((LAS unsigned*)p, 1u, RLX_WG); FENCE(); return old; }
DI void lds_set(LAS char* p, unsigned v) { FENCE(); __hip_atomic_store((LAS unsigned*)p, v, RLX_WG); FENCE(); }

__global__ __launch_bounds__(512, 2) void fused_attn_kernel(const char* __restrict__ xA, const char* __restrict__ wA, const char* __restrict__ oA,
                                                           const char* __restrict__ xB, const char* __restrict__ wB, const char* __restrict__ oB,
                                                           char* __restrict__ out, char* __restrict__ ws) {
    __shared__ __attribute__((aligned(16))) char smem_[LDS_BYTES];
    LAS char* smem = (LAS char*)smem_;
    const unsigned lds0 = (unsigned)(uintptr_t)smem;
    STAMP(0);
    asm volatile("" :: "s"(xA), "s"(wA), "s"(oA), "s"(xB), "s"(wB), "s"(oB), "s"(out), "s"(ws));
    const int tid = threadIdx.x, lane = tid & 63;
    const int wid = __builtin_amdgcn_readfirstlane(tid >> 6);
    const int bx = blockIdx.x, xcd = bx & 7, idx = bx >> 3;
    const int isA = idx >> 4, rt = (xcd >> 1) * 4 + ((idx >> 2) & 3), hg = (xcd & 1) * 4 + (idx & 3);
    const int br = isA ? 0 : 1;
    const int k0 = isA ? (((rt & 3) * 5 + 1) / 2 + ((hg & 3) * 5 + 1) / 2) % 10 : 0;
    const char* xb = (isA ? xA : xB) + (size_t)rt * (128 * 1280);
    const char* wb = (isA ? wA : wB) + hg * 160;
    const int me = (br * 16 + rt) * 8 + hg;
    const int r32 = lane & 31, hl = lane >> 5;
    auto kt_of = [&](int s) { int k = s + k0; return k >= 10 ? k - 10 : k; };
    if (isA && wid < 4) {
        const int j16 = idx & 15; const unsigned dump = lds0 + OWN + wid * 512;
        const char* wl = wB + (hg >> 2) * 640 + j16 * (4 * 3840);
        const char* xl = xB + (size_t)(rt * 128 + (hg & 3) * 32 + r32) * 1280 + hl * 128;
        auto pfx = [&](int i) { return xl + i * 256; };
        auto pfkv = [&](int i) { int n = i * 64 + lane; n = n < 400 ? n : 399; const int sl = n / 40, r = n % 40, c = r % 10;
            return wl + (sl * 64 + r / 10) * 3840 + 1280 + (c < 5 ? c * 128 : 640 + c * 128); };
        auto pfq = [&](int i) { int n = i * 64 + lane; n = n < 200 ? n : 199; int sl = n / 20 + 8; sl = sl >= 10 ? sl - 10 : sl; const int r = n % 20;
            return wl + (sl * 64 + r / 5) * 3840 + (r % 5) * 128; };
        dma4(wid & 1 ? pfx(wid >> 1) : pfkv(wid >> 1), dump);
        dma4(wid & 1 ? pfx(2 + (wid >> 1)) : pfkv(2 + (wid >> 1)), dump);
        dma4(wid < 3 ? pfkv(4 + wid) : pfx(4), dump);
        dma4(pfq(wid), dump);
        { int n = wid * 64 + lane; n = n < 200 ? n : 199; const int o = (j16 * 40 + n / 5) * 1280 + (hg >> 2) * 640 + (n % 5) * 128;
          dma4(oB + o, dump); dma4(oA + o, dump); }
    }
    if (isA && wid >= 4) { for (int i = 0; i < ADELAY; ++i) __builtin_amdgcn_s_sleep(16); }

    const unsigned xo = (unsigned)((lane >> 3) * 1280 + (((lane & 7) ^ (lane >> 4)) << 4));
    auto w_off = [&](int q) { const int c = q * 64 + lane, row = __umul24(c, 3277) >> 16, j = c - row * 20; return (unsigned)(__umul24(row, 3840) + j * 16 + (j < 10 ? 1280 : 2400)); };
    if (!isA) { const char* xs = xb + k0 * 128 + wid * (16 * 1280);
      dma16(xo, xs, lds0 + XS0 + wid * 2048);
      dma16(xo ^ 64, xs + 8 * 1280, lds0 + XS0 + wid * 2048 + 1024);
      const char* wsrc = wb + (size_t)k0 * (64 * 3840);
      const unsigned wv = w_off(wid < 5 ? wid : wid - 5);
      if (wid < 5) { dma16(wv, wsrc, lds0 + WS0 + wid * 1024); dma16(wv, wsrc + 16 * 3840, lds0 + WS0 + (5 + wid) * 1024);
          if (wid >= 3) { dma16(wv, wsrc + 32 * 3840, lds0 + WS0 + (10 + wid) * 1024); dma16(wv, wsrc + 48 * 3840, lds0 + WS0 + (15 + wid) * 1024); } }
      else { dma16(wv, wsrc + 32 * 3840, lds0 + WS0 + (5 + wid) * 1024); dma16(wv, wsrc + 48 * 3840, lds0 + WS0 + (10 + wid) * 1024); } }
    if (wid == 4 && lane < 2) dma4(ws + lane * 4, lds0 + C_TAG);
    if (tid < 80) *(LAS unsigned*)(smem + tid * 4) = 0u;
    else if (tid < 82) *(LAS unsigned*)(smem + ONE + (tid - 80) * 4) = 0x3F803F80u;
    const int nnb = isA ? (rt > 0 ? 1 : 0) : (rt > 1 ? 2 : rt);
    auto kq_base = [&](int fg) { const int hd = fg / 5, dg = fg % 5; return dg < 4 ? hd * 5120 + (dg >> 1) * 2048 + (dg & 1) * 8 : hd * 5120 + 4096; };

    auto rdtag = [&]() { const u64 t = *(LAS u64*)(smem + C_TAG);
        const u64 e = ((u64)(unsigned)__builtin_amdgcn_readfirstlane((int)(t >> 32)) << 32) | (unsigned)__builtin_amdgcn_readfirstlane((int)t);
        return e * 6364136223846793005ull + 1442695040888963407ull; };
    const __amdgpu_buffer_rsrc_t wor = __builtin_amdgcn_make_buffer_rsrc((void*)(isA ? oA : oB), 0, 640 * 1280, 0x00020000);

    const int hp = wid >> 2, qo = wid & 3, qi = hp ? 3 - qo : qo;
    const int ud = qi + 8;
    const bool far = isA ? (4 * rt + qi >= 1) : (4 * rt + qi >= 8);
    const int u0 = isA ? (far ? ud - 1 : ud) : (far ? qi : 8 - 4 * rt);
    f32x16 cd, cf;
    int dsh, kb1, kb2, nh, vb0, vb1, qb1, qb2; unsigned obase, oo[5];
    auto mask_b = [&](f32x16& c, int r, bool fartile) { const int k = (r & 3) + 8 * (r >> 2); c[r] = __int_as_float(fartile ? ((k - 1) << 23) - dsh : dsh - (k << 23)); };
    auto mask_a = [&](int z) {
      SFOR(r, 16)
          constexpr u64 ad = lanes_cut(r, false), af = lanes_cut(r, true);
          cd[r] = __builtin_amdgcn_inverse_ballot_w64(ad + z) ? -__builtin_inff() : 0.f;
          cf[r] = __builtin_amdgcn_inverse_ballot_w64(af + z) ? -__builtin_inff() : 0.f;
      SEND };
    auto att_setup = [&](int part) {
        int l = tid; asm volatile("" : "+v"(l)); l &= 63;
        const int q = l & 31, h = l >> 5;
        if (part == 0) { dsh = (q - 4 * h) << 23; kb1 = KV0 + hp * 10240 + h * 2048 + q * 16; kb2 = h ? ONE : KV0 + hp * 10240 + 4096 + q * 8; nh = h ^ 1;
            asm volatile("" : "+v"(dsh), "+v"(kb1), "+v"(kb2), "+v"(nh)); }
        if (part == 1) { const bool ones = (l & 16) && (l & 3);
            vb0 = KV0 + VOFF + (4 * h + ((l & 15) >> 2)) * VP + (ones ? 160 : hp * 80 + (l & 16) * 2 + (l & 3) * 8); vb1 = vb0 + (ones ? 0 : 40);
            asm volatile("" : "+v"(vb0), "+v"(vb1)); }
        if (part == 2) { qb1 = QST + hp * 10240 + h * 2048 + (qi * 32 + q) * 16; qb2 = QST + hp * 10240 + 4096 + (qi * 32 + q) * 8;
            obase = (unsigned)(qi * 40960 + hp * 20480 + hg * 2560 + q * 16 + h * 512);
            asm volatile("" : "+v"(qb1), "+v"(qb2), "+v"(obase)); }
        if (part >= 3) {
#pragma unroll
            for (int r = 4 * (part - 3); r < 4 * (part - 2); ++r) mask_b(cd, r, false);
            asm volatile("" : "+v"(cd)); } };
    auto wo_setup = [&](int q) {
        int l = tid; asm volatile("" : "+v"(l)); const int c = q * 64 + (l & 63), rho = __umul24(c, 6554) >> 16;
        oo[q] = (unsigned)(__umul24(qo * 160 + hp * 40 + (rho & ~7) + 4 * (rho & 1) + ((rho >> 1) & 3), 1280) + hg * 160 + (c - rho * 10) * 16);
        asm volatile("" : "+v"(oo[q])); };
    u64 tag;

    if (wid < 4) {
        if (!isA) WAITV(0);
        BAR_L();
        STAMP(1);
        bf16x8 xr[32], xt[2], wf[WIN + 1];
        f32x16 acc[5];
        const int tok = wid * 32 + r32;
        const int xa = XS0 + tok * 128 + ((hl ^ ((tok >> 1) & 7)) << 4);
        int wa0 = WS0 + (8 * hl + ((lane & 15) >> 2)) * 320 + (lane & 16) * 2 + (lane & 3) * 8, wa1 = wa0 + 61440;
        asm volatile("" : "+v"(wa0), "+v"(wa1));
        auto rdx = [&](int i) { return *(LAS bf16x8*)(smem + ((i >> 2) % 3) * 16384 + (xa ^ ((i & 3) * 32))); };
        auto rdw = [&](int f) { const int i = f / 5, t = f % 5, sl = (i >> 1) % 7;
            return trfrag(smem + (sl < 6 ? wa0 + sl * 10240 : wa1) + (i & 1) * 5120 + t * 64, 1280); };
        unsigned fl = 0;
        auto rdflag = [&](int u) { fl = __hip_atomic_load((LAS unsigned*)(smem + C_F + u * 4), RLX_WG); };
        auto check = [&](int u) {
            if (__builtin_amdgcn_readfirstlane(fl) != 15) {
#pragma unroll 1
                for (unsigned spins = 0; spins < LSPIN_CAP; ++spins) { rdflag(u); if (__builtin_amdgcn_readfirstlane(fl) == 15) break; __builtin_amdgcn_s_sleep(1); }
            }
            FENCE(); };
#pragma unroll
        for (int i = 0; i < 4; ++i) xr[i] = rdx(i);
#pragma unroll
        for (int f = 0; f < WIN; ++f) wf[f] = rdw(f);
        SB();
        SFOR(f, 200)
            constexpr int i = f / 5, t = f % 5;
            if constexpr (f % 20 == 10 - WIN) lds_set(smem + C_P + wid * 4, (i >> 1) + 1);
            if constexpr (f == 200 - WIN) lds_set(smem + C_P + wid * 4, 20);
            if constexpr (f % 20 == 10) rdflag(f / 20 + 1);
            if constexpr (f % 20 == 20 - WIN && f < 190) check(f / 20 + 1);
            if constexpr (f + WIN < 200) wf[(f + WIN) % (WIN + 1)] = rdw(f + WIN);
            if constexpr (f % 20 >= 16 && f < 140) xr[(f / 20 + 1) * 4 + f % 20 - 16] = rdx((f / 20 + 1) * 4 + f % 20 - 16);
            if constexpr (t == 1 && i >= 31 && i < 39) xt[(i + 1) & 1] = rdx(i + 1);
            SB();
            if constexpr (i == 0) MFMA32Z(acc[t], wf[f % (WIN + 1)], xr[0]);
            else if constexpr (i < 32) MFMA32I(acc[t], wf[f % (WIN + 1)], xr[i]);
            else MFMA32I(acc[t], wf[f % (WIN + 1)], xt[i & 1]);
            SB();
        SEND
        STAMP(2);
        int l2 = tid; asm volatile("" : "+v"(l2)); l2 &= 63;
        const int h2 = l2 >> 5, tk = wid * 32 + (l2 & 31);
        int eb[5];
        auto ebases = [&](int base) { int l = tid; asm volatile("" : "+v"(l)); const int h = (l >> 5) & 1, k = wid * 32 + (l & 31);
            eb[0] = base + k * 16 + h * 8; eb[1] = base + k * 16 + h * 2040; eb[2] = base + (h ? k * 16 + 1024 : k * 8); eb[3] = base + (h ? k * 8 + 2040 : k * 16);
            asm volatile("" : "+v"(eb[0]), "+v"(eb[1]), "+v"(eb[2]), "+v"(eb[3])); };
        auto kq_addr = [&](int g0) { return eb[g0 % 5 == 1 ? 1 : g0 % 5 == 4 ? 2 : g0 % 5 == 3 ? 3 : 0] + kq_base(g0); };
        ebases(OWN);
        eb[4] = OWN + VOFF + tk * VP + h2 * 8; asm volatile("" : "+v"(eb[4]));
        int qa0 = (8 * h2 + 2 * ((l2 & 15) >> 2)) * 160 + (l2 & 16) * 2 + (l2 & 3) * 8, qa1 = qa0 + WS0 + 10240;
        asm volatile("" : "+v"(qa0), "+v"(qa1));
        auto rdq = [&](int f) { const int i = f / 3, t = f % 3, j = i >> 2, sl = (20 + j) % 7;
            return trfrag(smem + (j >= 7 ? qa0 + (j == 7 ? QU27 : j == 8 ? QU28 : QU29) : sl == 0 ? qa0 + WS0 : qa1 + (sl - 1) * 10240) + (i & 3) * 2560 + t * 64, 160); };
        MFMA_DRAIN();
        WAITV(0);
        { u32x2 pk[20];
#pragma unroll
          for (int n = 0; n < 20; ++n) { const int t = n >> 2, b = n & 3; pk[n] = (u32x2){pk2(acc[t][4 * b], acc[t][4 * b + 1]), pk2(acc[t][4 * b + 2], acc[t][4 * b + 3])}; }
          SB();
#pragma unroll
          for (int n = 0; n < 20; ++n) *(LAS u32x2*)(smem + (n < 10 ? kq_addr(2 * n) : eb[4] + (2 * n - 20) * 8)) = pk[n];
          if (h2 == 0) *(LAS u32x2*)(smem + eb[4] + 160) = (u32x2){0x3F803F80u, 0x3F803F80u}; }
        lds_set(smem + C_E + wid * 4, 1);
        check(10);
        xt[0] = rdx(32);
#pragma unroll
        for (int f = 0; f < WIN; ++f) wf[f] = rdq(f);
        STAMP(3);
        SB();
        u64 pv0 = 0, pv1 = 0;
        SFOR(f, 120)
            constexpr int i = f / 3, t = f % 3, j = i >> 2;
            if constexpr (f % 24 == 12 && j < 9) rdflag(11 + j / 2);
            if constexpr (f % 12 == 12 - WIN && (j == 1 || j == 6 || j == 8)) lds_set(smem + C_P + wid * 4, 21 + j);
            if constexpr (f % 24 == 24 - WIN && j < 9) check(11 + j / 2);
            if constexpr (f + WIN < 120) wf[(f + WIN) % (WIN + 1)] = rdq(f + WIN);
            if constexpr (t == 0 && i < 7) xt[(i + 1) & 1] = rdx(32 + i + 1);
            if constexpr (f >= 90 && f < 100 && !(f & 1)) wo_setup((f - 90) / 2);
            if constexpr (f == 99) tag = rdtag();
            if constexpr (f >= 100 && f < 107) att_setup(f - 100);
            if constexpr (f == 108) ebases(QST);
            if constexpr (f == 116) { pv0 = __hip_atomic_load((LAS u64*)(smem + C_P), RLX_WG); pv1 = __hip_atomic_load((LAS u64*)(smem + C_P + 8), RLX_WG); }
            SB();
            if constexpr (i == 0) MFMA32Z(acc[t], wf[f % (WIN + 1)], xt[0]);
            else if constexpr (i < 8) MFMA32I(acc[t], wf[f % (WIN + 1)], xt[i & 1]);
            else MFMA32I(acc[t], wf[f % (WIN + 1)], xr[i - 8]);
            SB();
        SEND
        STAMP(4);
        if (__builtin_amdgcn_readfirstlane((int)min(min((unsigned)pv0, (unsigned)(pv0 >> 32)), min((unsigned)pv1, (unsigned)(pv1 >> 32)))) < 29) wait_min4(smem + C_P, 29);
        MFMA_DRAIN();
        { acc[0] *= QSCALE; acc[1] *= QSCALE; acc[2] *= QSCALE;
          SB();
          u32x2 pk[10];
#pragma unroll
          for (int n = 0; n < 10; ++n) { const int t = n >> 2, b = n & 3; pk[n] = (u32x2){pk2(acc[t][4 * b], acc[t][4 * b + 1]), pk2(acc[t][4 * b + 2], acc[t][4 * b + 3])}; }
          SB();
#pragma unroll
          for (int n = 0; n < 10; ++n) *(LAS u32x2*)(smem + kq_addr(2 * n)) = pk[n]; }
        lds_set(smem + C_QF + wid * 4, 1);
        __builtin_amdgcn_s_setprio(1);
        STAMP(5);
    } else {
        const int hw = wid - 4;
        int known = 0;
        __builtin_amdgcn_s_setprio(3);
        auto lds_or = [&](int off, unsigned v) { FENCE(); if (lane == 0) __hip_atomic_fetch_or((LAS unsigned*)(smem + off), v, RLX_WG); FENCE(); };
        unsigned wo[5], qo[5];
        auto done = [&](int n) { lds_or(C_F + n * 4, 1u << hw); };
        const unsigned xo1 = (xo ^ 64) + 9216, xo2 = xo + 2 * 9216, xo3 = (xo ^ 64) + 3 * 9216;
        auto round_x = [&](int n) { dma16x4(xo, xo1, xo2, xo3, xb + kt_of(n) * 128 + hw * (32 * 1280), lds0 + XS0 + (n % 3) * 16384 + hw * 4096); };
        auto round_w = [&](int n) {
            const int u = 2 * n + (hw >> 1);
            const unsigned dst = lds0 + (u < 27 ? WS0 + (u % 7) * 10240 : u == 27 ? QU27 : u == 28 ? QU28 : QU29) + (hw & 1) * 5120 + 2048;
            if (u < 20) dma16x5(wo, wb + (size_t)(kt_of(u >> 1) * 64 + (u & 1) * 32) * 3840 - 2048, dst);
            else dma16x5(qo, wb + (size_t)kt_of(u < 22 ? u - 12 : u - 22) * (64 * 3840) - 2048, dst); };
        if (isA) round_x(0);
        round_x(1);
#pragma unroll
        for (int q = 0; q < 5; ++q) wo[q] = w_off(q) + (hw & 1) * (16 * 3840) + (4 - q) * 1024;
        if (isA) { round_w(0); round_w(1); WAITV(5); } else { round_w(1); WAITV(9); }
        BAR_L();
#pragma unroll
        for (int q = 0; q < 5; ++q) { const int c = q * 64 + lane, rho = __umul24(c, 6554) >> 16;
            qo[q] = (unsigned)(__umul24((hw & 1) * 32 + (rho & ~7) + 4 * (rho & 1) + ((rho >> 1) & 3), 3840) + (c - rho * 10) * 16 + (4 - q) * 1024); }
#pragma unroll 1
        for (int n = 2; n <= 13; ++n) {
            const int need = n < 13 ? 2 * n - 5 : 20;
            if (need > known) known = min4(smem + C_P);
            if (need > known) { WAITV(0); done(n - 1); known = wait_min4(smem + C_P, need); }
            if (n < 10) round_x(n);
            round_w(n);
            if (n < 10) WAITV(9); else WAITV(5);
            done(n - 1);
        }
        WAITV(0); done(13);
        STAMP(1);
        tag = rdtag();
        auto a_chunk = [&](int a) { int o;
            if (a < 320) { const int hd = a / 80, rem = a % 80; o = hd * 5120 + (rem < 32 ? (96 + rem) * 16 : rem < 64 ? 2048 + (64 + rem) * 16 : 4096 + 768 + (rem - 64) * 16); }
            else o = VOFF + 96 * VP + (a - 320) * 16;
            return o; };
        if (rt < 15) {
            const __amdgpu_buffer_rsrc_t kvr = __builtin_amdgcn_make_buffer_rsrc(ws + WS_KV + me * KVG, 0, KVB, 0x00020000);
            wait_min4(smem + C_E, 1);
            STAMP(2);
            if (!isA) { u32x4 v[11];
#pragma unroll
                for (int j = 0; j < 11; ++j) if (j < 10 || hw == 3) v[j] = *(LAS u32x4*)(smem + OWN + hw * 10240 + j * 1024 + lane * 16);
#pragma unroll
                for (int j = 0; j < 11; ++j) if (j < 10 || hw == 3) __builtin_amdgcn_raw_buffer_store_b128(v[j], kvr, hw * 10240 + j * 1024 + lane * 16, 0, 16);
            } else {
#pragma unroll
                for (int j = 0; j < 3; ++j) { const int a = (4 * j + hw) * 64 + lane;
                    if (a < 656) { const int o = a_chunk(a); __builtin_amdgcn_raw_buffer_store_b128(*(LAS u32x4*)(smem + OWN + o), kvr, o, 0, 16); } }
            }
            STAMP(3);
        }
        wait_min4(smem + C_P, 22);
        round_w(14);
        WAITV(5);
        if (rt < 15) {
            unsigned old = 0;
            if (lane == 0) old = __hip_atomic_fetch_add((LAS unsigned*)(smem + N_PUB), 1u, RLX_WG);
            if (lane == 0 && old == 3) __hip_atomic_store((GAS u64*)(ws + WS_FKV + me * 128), tag, RLX_AGENT);
            STAMP(4);
        }
        WAITV(0); done(14);
        __builtin_amdgcn_s_setprio(0);
#pragma unroll
        for (int p = 0; p < 7; ++p) att_setup(p);
#pragma unroll
        for (int q = 0; q < 5; ++q) wo_setup(q);
        __builtin_amdgcn_s_setprio(3);
        if (hw < NPOLL) {
            __builtin_amdgcn_s_sleep(KDELAY);
            for (int i = 0; i < hw; ++i) __builtin_amdgcn_s_sleep(PSTAG);
            poll_shared((GAS u64*)(ws + WS_FKV + (me - 8 * (lane + 1)) * 128), lane < nnb, tag, smem + N_KV);
        } else wait_word(smem + N_KV, 1);
        STAMP(5);
        if (!isA) {
            const int n = hw >> 1, half = hw & 1;
            if (n < nnb) {
                const char* src = ws + WS_KV + (me - 8 * (n + 1)) * KVG + half * 20480; const unsigned dst = lds0 + KV0 + (1 - n) * KVB + half * 20480;
                wait_min4(smem + C_P, 27);
#pragma unroll
                for (int j = 0; j < 4; ++j) dma16x5_sc1(lane * 16, src + j * 5120 + 2048, dst + j * 5120 + 2048);
                if (half) dma16_sc1(lane * 16, src + 20480, dst + 20480);
            }
        } else if (nnb) {
            const __amdgpu_buffer_rsrc_t nbr = __builtin_amdgcn_make_buffer_rsrc(ws + WS_KV + (me - 8) * KVG, 0, KVB, 0x00020000);
            u32x4 v[3];
#pragma unroll
            for (int j = 0; j < 3; ++j) { const int a = (4 * j + hw) * 64 + lane; if (a < 656) v[j] = __builtin_amdgcn_raw_buffer_load_b128(nbr, a_chunk(a), 0, 16); }
            wait_min4(smem + C_P, 27);
#pragma unroll
            for (int j = 0; j < 3; ++j) { const int a = (4 * j + hw) * 64 + lane; if (a < 656) *(LAS u32x4*)(smem + KV0 + KVB + a_chunk(a)) = v[j]; }
        }
        __builtin_amdgcn_s_setprio(2);
        wait_word(smem + C_QF + qi * 4, 1);
        STAMP(6);
    }
    u32x4 wreg[13];
    auto ldwo = [&](int i) { wreg[i] = __builtin_amdgcn_raw_buffer_load_b128(wor, oo[i % 5] + (i / 5) * (32 * 1280) + (i > 6 || (i == 6 && lane >= 16) ? 40 * 1280 : 0), 0, 0); };
    STAMP(7);
    u32x2 ob[2][3];
    {
        bf16x8 q1[2]; u32x2 q2[2];
#pragma unroll
        for (int e = 0; e < 2; ++e) {
            q1[e] = *(LAS bf16x8*)(smem + qb1 + e * 5120);
            q2[e] = *(LAS u32x2*)(smem + qb2 + e * 5120);
            if (hl) q2[e] = (u32x2){0u, 0u};
        }
        if (isA) mask_a(0);
        struct KF { bf16x8 a; s16x4 b; };
        struct VF { bf16x8 a, b; };
        auto rdk = [&](int u, int e) { const int so = (u >> 2) * KVB, tw = u & 3; KF k;
            k.a = *(LAS bf16x8*)(smem + kb1 + so + tw * 512 + e * 5120); k.b = *(LAS s16x4*)(smem + __umul24(so + tw * 256 + e * 5120, nh) + kb2); return k; };
        auto rdv = [&](int u, int e) { LAS char* p = smem + (e ? vb1 : vb0) + (u >> 2) * KVB + (u & 3) * (32 * VP); VF v;
            v.a = trfrag(p, 8 * VP); v.b = trfrag(p + 16 * VP, 8 * VP); return v; };
        auto qk = [&](const KF& k, int e, const f32x16& c) { return MFMA32K8(k.b, __builtin_bit_cast(s16x4, q2[e]), MFMA32(k.a, q1[e], c)); };
        const f32x16 zero = {};
        auto max16 = [&](const f32x16& s, float m) {
#pragma unroll
            for (int r = 0; r < 16; ++r) m = __builtin_elementwise_maximum(m, s[r]);
            return m; };
        auto set_ref = [&](float m, int e) { const unsigned nb = pk2(-m, 0.f); if (hl) q2[e][0] = nb; };
        auto ex4 = [&](f32x16& s, int b) {
#pragma unroll
            for (int r = 0; r < 4; ++r) s[b + r] = __builtin_amdgcn_exp2f(s[b + r]); };
        auto pk8 = [&](const f32x16& s, int b) { return __builtin_bit_cast(bf16x8, (u32x4){pk2(s[b], s[b + 1]), pk2(s[b + 2], s[b + 3]), pk2(s[b + 4], s[b + 5]), pk2(s[b + 6], s[b + 7])}); };
        auto phase = [&](auto nx, f32x16& cur, const VF& v, f32x16& o, f32x16& nxt, const KF& k, int e) __attribute__((always_inline)) {
            constexpr int NX = decltype(nx)::value;
            SB(); ex4(cur, 0); SB();
            if constexpr (NX) nxt = MFMA32(k.a, q1[e], NX == 1 ? zero : NX == 2 ? cf : cd);
            SB(); ex4(cur, 4); { const bf16x8 p = pk8(cur, 0); SB(); o = MFMA32(v.a, p, o); }
            SB(); ex4(cur, 8); SB();
            if constexpr (NX) nxt = MFMA32K8(k.b, __builtin_bit_cast(s16x4, q2[e]), nxt);
            SB(); ex4(cur, 12); { const bf16x8 p = pk8(cur, 8); SB(); o = MFMA32(v.b, p, o); }
            SB(); };
        f32x16 o0, o1, sa, sb; KF kx; VF vx;
        auto step = [&](auto m0, auto m1, int u) __attribute__((always_inline)) {
            constexpr int M1 = decltype(m1)::value;
            KF ky; if constexpr (M1) ky = rdk(u - 1, 0);
            const VF vy = rdv(u, 1);
            phase(m0, sa, vx, o0, sb, kx, 1);
            if constexpr (M1) { kx = rdk(u - 1, 1); vx = rdv(u - 1, 0); }
            phase(m1, sb, vy, o1, sa, ky, 0); };
        auto nbwait = [&](int u) { if (u == 8) wait_word(smem + N_NB, 4); };
        { const KF k0f = rdk(ud, 0); kx = rdk(ud, 1); vx = rdv(ud, 0);
          const f32x16 s0 = qk(k0f, 0, cd), s1 = qk(kx, 1, cd);
          set_ref(halfmax(max16(s0, s0[0])), 0); set_ref(halfmax(max16(s1, s1[0])), 1);
          sa = qk(k0f, 0, cd); }
        if (hp) { WAITV(0); lds_inc0(smem + N_NB, lane); lds_inc0(smem + N_G0, lane); }
        o0 = zero; o1 = zero;
        if (u0 + 8 == ud) {
            SFOR(it, 9)
                if constexpr (it < 7) { ldwo(2 * it); if constexpr (it < 6) ldwo(2 * it + 1); }
                if constexpr (it == PFLIP) { if (hp) __builtin_amdgcn_s_setprio(0); }
                if constexpr (it == 7) {
#pragma unroll
                    for (int r = 0; r < 16; ++r) mask_b(cf, r, true); }
                if constexpr (it < 4) nbwait(ud - it);
                step(IC<it == 0 ? 3 : it == 8 ? 2 : 1>{}, IC<it == 8 ? 0 : it == 7 ? 2 : 1>{}, ud - it);
            SEND
        } else {
            __builtin_amdgcn_s_setprio(0);
#pragma unroll
            for (int i = 0; i < 13; ++i) ldwo(i);
            if (!isA) cf = zero;
            if (u0 < ud) {
                nbwait(ud);
                if (u0 + 1 < ud) {
                    step(IC<3>{}, IC<1>{}, ud);
#pragma unroll 1
                    for (int u = ud - 1; u > u0 + 1; --u) { nbwait(u); step(IC<1>{}, IC<1>{}, u); }
                    nbwait(u0 + 1);
                    step(IC<1>{}, IC<2>{}, u0 + 1);
                } else step(IC<3>{}, IC<2>{}, ud);
                step(IC<2>{}, IC<0>{}, u0);
            } else step(IC<3>{}, IC<0>{}, ud);
        }
        if (!__all(o0[12] < 1.152921504606847e18f && o1[12] < 1.152921504606847e18f)) {
            if (isA) { int z = 0; asm volatile("" : "+s"(z)); mask_a(z); }
            else { asm volatile("" : "+v"(dsh));
#pragma unroll
                for (int r = 0; r < 16; ++r) { mask_b(cd, r, false); mask_b(cf, r, true); }
                if (!far) cf = zero; }
#pragma unroll
            for (int e = 0; e < 2; ++e) {
                if (hl) q2[e][0] = 0u;
                const f32x16 s = qk(rdk(ud, e), e, cd);
                float m = max16(s, s[0]);
                if (u0 < ud) m = max16(qk(rdk(u0, e), e, cf), m);
#pragma unroll 1
                for (int u = u0 + 1; u < ud; ++u) m = max16(qk(rdk(u, e), e, zero), m);
                set_ref(halfmax(m), e);
            }
#pragma unroll
            for (int e = 0; e < 2; ++e) {
                f32x16 o = zero;
#pragma unroll 1
                for (int u = u0; u <= ud; ++u) { f32x16 t = u == ud ? qk(rdk(u, e), e, cd) : u == u0 ? qk(rdk(u, e), e, cf) : qk(rdk(u, e), e, zero);
                    const VF v = rdv(u, e); ex4(t, 0); ex4(t, 4); ex4(t, 8); ex4(t, 12);
                    o = MFMA32(v.a, pk8(t, 0), o); o = MFMA32(v.b, pk8(t, 8), o); }
                if (e) o1 = o; else o0 = o;
            }
        }
        const float rl0 = __builtin_amdgcn_rcpf(o0[12]), rl1 = __builtin_amdgcn_rcpf(o1[12]);
        SB();
#pragma unroll
        for (int r = 0; r < 12; ++r) { o0[r] *= rl0; o1[r] *= rl1; }
        SB();
#pragma unroll
        for (int j = 0; j < 3; ++j) {
            ob[0][j] = (u32x2){pk2(o0[4 * j], o0[4 * j + 1]), pk2(o0[4 * j + 2], o0[4 * j + 3])};
            ob[1][j] = (u32x2){pk2(o1[4 * j], o1[4 * j + 1]), pk2(o1[4 * j + 2], o1[4 * j + 3])}; }
        SB();
    }
    STAMP(8);
    const __amdgpu_buffer_rsrc_t orr = __builtin_amdgcn_make_buffer_rsrc(ws + WS_O + (br * 16 + rt) * OG, 0, 4 * 40960, 0x00020000);
    {
      { auto rx = __builtin_amdgcn_permlane32_swap(ob[0][0][0], ob[0][1][0], false, false);
        auto ry = __builtin_amdgcn_permlane32_swap(ob[0][0][1], ob[0][1][1], false, false);
        __builtin_amdgcn_raw_buffer_store_b128((u32x4){rx[0], ry[0], rx[1], ry[1]}, orr, obase, 0, 16); }
      if (hl == 0) __builtin_amdgcn_raw_buffer_store_b128((u32x4){ob[0][2][0], ob[0][2][1], ob[1][0][0], ob[1][0][1]}, orr, obase + 1024, 0, 16);
      { auto fx = __builtin_amdgcn_permlane32_swap(ob[1][0][0], ob[1][1][0], false, false);
        auto fy = __builtin_amdgcn_permlane32_swap(ob[1][0][1], ob[1][1][1], false, false);
        auto sx = __builtin_amdgcn_permlane32_swap(ob[1][1][0], ob[1][2][0], false, false);
        auto sy = __builtin_amdgcn_permlane32_swap(ob[1][1][1], ob[1][2][1], false, false);
        __builtin_amdgcn_raw_buffer_store_b128((u32x4){fx[1], fy[1], sx[0], sy[0]}, orr, obase + 1536, 0, 16); } }
    if (lane == 0) { (void)__hip_atomic_fetch_add((LAS unsigned*)(smem + N_G1), 1u, RLX_WG);
        if (!hp) (void)__hip_atomic_fetch_add((LAS unsigned*)(smem + N_G0), 1u, RLX_WG); }
    FENCE();
    STAMP(6);
    auto signal_o = [&]() {
        if (lds_inc(smem + N_OS + hp * 4, lane) == 3 && lane == 0) __hip_atomic_store((GAS u64*)(ws + WS_FO + (me * 2 + hp) * 128), tag, RLX_AGENT); };
    __builtin_amdgcn_s_setprio(0);
    WAITV(0); signal_o();
    STAMP(9);
    wait_word(smem + (hp ? N_G1 : N_G0), 8);
    const int p1 = hp ? 30720 : 35840, p2 = hp ? 65536 : 43520;
    const int wb0 = hp ? KV0 + 10240 : 512, wb1 = hp ? KV0 + KVB + 10240 - 30720 : KV0 - 35840, wb2 = KV0 + KVB - 43520;
#pragma unroll
    for (int i = 0; i < 13; ++i) { const int L = qo * 12800 + i * 1024;
        if (i < 12 || lane < 32) *(LAS u32x4*)(smem + (L + (L < p1 ? wb0 : L < p2 ? wb1 : wb2)) + lane * 16) = wreg[i]; }
    lds_inc0(smem + N_WO + hp * 4, lane);
    STAMP(10);
    __builtin_amdgcn_s_sleep(PDELAY);
    if (qo < NPOLL) {
        for (int i = 0; i < qo; ++i) __builtin_amdgcn_s_sleep(PSTAG);
        poll_shared((GAS u64*)(ws + WS_FO + (((me & ~7) + (lane & 7)) * 2 + hp) * 128), lane < 8, tag, smem + N_OK + hp * 4);
    } else wait_word(smem + N_OK + hp * 4, 1);
    STAMP(11);
    f32x16 oc[3]; f32x4 ps[10];
    LAS char* red = smem + OWN + qo * 10240 + lane * 16;
    { bf16x8 af[5], of[20];
      const int la = (8 * hl + 2 * ((lane & 15) >> 2)) * 160 + (lane & 16) * 2 + (lane & 3) * 8;
      const int aa0 = wb0 + la, aa1 = (hp ? wb1 : wb0) + la, aa2 = wb1 + la, aa3 = (hp ? wb1 : wb2) + la;
      auto rda = [&](int f) { return trfrag(smem + (f < 36 ? aa0 : f < 42 ? aa1 : f < 51 ? aa2 : aa3) + (f / 3) * 2560 + (f % 3) * 64, 160); };
      auto ldo = [&](int k) { of[k] = __builtin_bit_cast(bf16x8, __builtin_amdgcn_raw_buffer_load_b128(orr, qo * 40960 + (hp * 20 + k) * 1024 + lane * 16, 0, 16)); };
#pragma unroll
      for (int k = 0; k < OPF; ++k) ldo(k);
      wait_word(smem + N_WO + hp * 4, 4);
#pragma unroll
      for (int f = 0; f < 4; ++f) af[f] = rda(f);
      SB();
      SFOR(f, 60)
          if constexpr (f % 3 == 0 && f / 3 + OPF < 20) ldo(f / 3 + OPF);
          if constexpr (f + 4 < 60) af[(f + 4) % 5] = rda(f + 4);
          if constexpr (f == 57) { if (hp) { wait_word(smem + C_RD + qo * 4, 1);
#pragma unroll
              for (int b = 0; b < 10; ++b) ps[b] = *(LAS f32x4*)(red + b * 1024); } }
          SB();
          if constexpr (f < 3) MFMA32Z(oc[f], af[f % 5], of[0]); else MFMA32I(oc[f % 3], af[f % 5], of[f / 3]);
          SB();
      SEND }
    MFMA_DRAIN();
    STAMP(12);
    { if (!hp) {
          wait_word(smem + N_G1, 8);
#pragma unroll
          for (int b = 0; b < 10; ++b) *(LAS f32x4*)(red + b * 1024) = (f32x4){oc[b >> 2][4 * (b & 3)], oc[b >> 2][4 * (b & 3) + 1], oc[b >> 2][4 * (b & 3) + 2], oc[b >> 2][4 * (b & 3) + 3]};
          lds_set(smem + C_RD + qo * 4, 1);
      } else {
          char* orow = out + (size_t)(rt * 128 + qo * 32 + r32) * 2560 + br * 1280 + hg * 160 + hl * 16;
#pragma unroll
          for (int b = 0; b < 10; ++b)
#pragma unroll
              for (int r = 0; r < 4; ++r) ps[b][r] += oc[b >> 2][4 * (b & 3) + r];
          SB();
          u32x2 pk[10];
#pragma unroll
          for (int b = 0; b < 10; ++b) pk[b] = (u32x2){pk2(ps[b][0], ps[b][1]), pk2(ps[b][2], ps[b][3])};
          SB();
          u32x4 st[5];
#pragma unroll
          for (int b = 0; b < 10; b += 2) {
              auto rx = __builtin_amdgcn_permlane32_swap(pk[b][0], pk[b + 1][0], false, false); auto ry = __builtin_amdgcn_permlane32_swap(pk[b][1], pk[b + 1][1], false, false);
              st[b >> 1] = (u32x4){rx[0], ry[0], rx[1], ry[1]}; }
          SB();
#pragma unroll
          for (int b = 0; b < 5; ++b) *(u32x4*)(orow + b * 32) = st[b];
      } }
    if (bx == 0 && wid == 0) {
#pragma unroll 1
        for (int i = 0; i < 4; ++i) poll_flags((GAS u64*)(ws + WS_FO + (i * 64 + lane) * 256), true, tag);
        if (lane == 0) __hip_atomic_store((GAS u64*)ws, tag, RLX_AGENT);
    }
}

extern "C" void kernel_launch(void* const* d_in, const int* in_sizes, int n_in, void* d_out, int out_size, void* d_ws, size_t ws_size,
                              hipStream_t stream) {
    fused_attn_kernel<<<256, 512, 0, stream>>>((const char*)d_in[0], (const char*)d_in[1], (const char*)d_in[2], (const char*)d_in[3],
                                               (const char*)d_in[4], (const char*)d_in[5], (char*)d_out, (char*)d_ws);
}
```

```cpp
#include <hip/hip_runtime.h>
#include <hip/hip_bf16.h>
#include <stdint.h>

typedef short bf16x8 __attribute__((ext_vector_type(8)));
typedef short s16x4 __attribute__((ext_vector_type(4)));
typedef float f32x2 __attribute__((ext_vector_type(2)));
typedef float f32x4 __attribute__((ext_vector_type(4)));
typedef float f32x16 __attribute__((ext_vector_type(16)));
typedef unsigned u32x4 __attribute__((ext_vector_type(4)));
typedef unsigned u32x2 __attribute__((ext_vector_type(2)));
typedef __bf16 bfx2 __attribute__((ext_vector_type(2)));
typedef unsigned long long u64;
#define LAS __attribute__((address_space(3)))
#define GAS __attribute__((address_space(1)))
#define DI __device__ __forceinline__
#define RLX_AGENT __ATOMIC_RELAXED, __HIP_MEMORY_SCOPE_AGENT
#define RLX_WG __ATOMIC_RELAXED, __HIP_MEMORY_SCOPE_WORKGROUP

constexpr int LDS_BYTES = 163840;
constexpr int VOFF = 20480, VP = 168;
constexpr int KVB = VOFF + 128 * VP;
constexpr int KV0 = LDS_BYTES - 3 * KVB;
constexpr int OWN = KV0 + 2 * KVB;
constexpr int C_F = 0, C_P = 128, C_E = 144, C_QF = 160, C_RD = 176, C_CNT = 192, C_TAG = 320;
constexpr int N_PUB = C_CNT, N_NB = C_CNT + 4, N_G0 = C_CNT + 8, N_G1 = C_CNT + 12, N_WO = C_CNT + 16, N_OS = C_CNT + 24, N_KV = C_CNT + 32, N_OK = C_CNT + 36;
constexpr int XS0 = 512, WS0 = XS0 + 3 * 16384, QST = 512, ONE = 336, QU27 = XS0 + 16384, QU28 = XS0, QU29 = QU27 + 10240;
static_assert(WS0 + 7 * 10240 <= OWN, "ring overlaps the own image");
constexpr unsigned WS_FKV = 4096, WS_FO = WS_FKV + 256 * 128, WS_KV = 131072;
constexpr unsigned KVG = KVB, OG = 4 * 40960;
constexpr unsigned WS_O = WS_KV + 256 * KVG;
constexpr unsigned WS_END = WS_O + 32 * OG;
constexpr unsigned WS_DBG = WS_END;
constexpr unsigned SPIN_CAP = 400000, LSPIN_CAP = 4000000;
constexpr float QSCALE = 0.22360679774997896f * 1.4426950408889634f;
#define PSLEEP 2
#define PDELAY 1
#define GSLEEP 1
#define KDELAY 0
#define NPOLL 1
#define PSTAG 3
#define PFLIP 6
#define OPF 12
#define ABL4N 2
#define WIN 4
#define ADELAY 3

#define MFMA32(a, b, c) __builtin_amdgcn_mfma_f32_32x32x16_bf16((a), (b), (c), 0, 0, 0)
#define MFMA32K8(a, b, c) __builtin_amdgcn_mfma_f32_32x32x8bf16_1k((a), (b), (c), 0, 0, 0)
#define MFMA32I(acc, a, b) asm volatile("v_mfma_f32_32x32x16_bf16 %0, %1, %2, %0" : "+v"(acc) : "v"(a), "v"(b))
#define MFMA32Z(acc, a, b) asm volatile("v_mfma_f32_32x32x16_bf16 %0, %1, %2, 0" : "=&v"(acc) : "v"(a), "v"(b))
#define MFMA_DRAIN() asm volatile("s_nop 7\n\ts_nop 4" ::: "memory")
#define WAITV(N) asm volatile("s_waitcnt vmcnt(" #N ")" ::: "memory")
#define WAITL() asm volatile("s_waitcnt lgkmcnt(0)" ::: "memory")
#define BAR_L() asm volatile("s_waitcnt lgkmcnt(0)\n\ts_barrier" ::: "memory")
#define SB() __builtin_amdgcn_sched_barrier(0)
#define FENCE() asm volatile("" ::: "memory")

#define STAMP(i) do {} while (0)

template <int V> struct IC { static constexpr int value = V; };
template <int I, int N, class F> DI void static_for(F&& f) { if constexpr (I < N) { f(IC<I>{}); static_for<I + 1, N>(f); } }
#define SFOR(var, N) static_for<0, N>([&](auto ic_##var) __attribute__((always_inline)) { constexpr int var = decltype(ic_##var)::value;
#define SEND });

DI void dma16(unsigned voff, const char* sbase, unsigned ldsaddr) {
    asm volatile("s_mov_b32 m0, %2\n\ts_nop 0\n\tglobal_load_lds_dwordx4 %0, %1" :: "v"(voff), "s"(sbase), "s"(ldsaddr) : "memory");
}
DI void dma16x4(unsigned v0, unsigned v1, unsigned v2, unsigned v3, const char* sbase, unsigned ldsaddr) {
    asm volatile("s_mov_b32 m0, %5\n\ts_nop 0\n\tglobal_load_lds_dwordx4 %0, %4\n\tglobal_load_lds_dwordx4 %1, %4 offset:1024\n\t"
                 "global_load_lds_dwordx4 %2, %4 offset:2048\n\tglobal_load_lds_dwordx4 %3, %4 offset:3072"
                 :: "v"(v0), "v"(v1), "v"(v2), "v"(v3), "s"(sbase), "s"(ldsaddr) : "memory");
}
DI void dma16x5(const unsigned (&v)[5], const char* sbase, unsigned ldsaddr) {
    asm volatile("s_mov_b32 m0, %6\n\ts_nop 0\n\tglobal_load_lds_dwordx4 %0, %5 offset:-2048\n\tglobal_load_lds_dwordx4 %1, %5 offset:-1024\n\t"
                 "global_load_lds_dwordx4 %2, %5\n\tglobal_load_lds_dwordx4 %3, %5 offset:1024\n\tglobal_load_lds_dwordx4 %4, %5 offset:2048"
                 :: "v"(v[0]), "v"(v[1]), "v"(v[2]), "v"(v[3]), "v"(v[4]), "s"(sbase), "s"(ldsaddr) : "memory");
}
DI void dma16x5_sc1(unsigned voff, const char* sbase, unsigned ldsaddr) {
    asm volatile("s_mov_b32 m0, %2\n\ts_nop 0\n\tglobal_load_lds_dwordx4 %0, %1 offset:-2048 sc1\n\tglobal_load_lds_dwordx4 %0, %1 offset:-1024 sc1\n\t"
                 "global_load_lds_dwordx4 %0, %1 sc1\n\tglobal_load_lds_dwordx4 %0, %1 offset:1024 sc1\n\tglobal_load_lds_dwordx4 %0, %1 offset:2048 sc1"
                 :: "v"(voff), "s"(sbase), "s"(ldsaddr) : "memory");
}
DI void dma16_sc1(unsigned voff, const char* sbase, unsigned ldsaddr) {
    asm volatile("s_mov_b32 m0, %2\n\ts_nop 0\n\tglobal_load_lds_dwordx4 %0, %1 sc1" :: "v"(voff), "s"(sbase), "s"(ldsaddr) : "memory");
}
DI void dma4(const char* p, unsigned ldsaddr) {
    asm volatile("s_mov_b32 m0, %1\n\ts_nop 0\n\tglobal_load_lds_dword %0, off" :: "v"(p), "s"(ldsaddr) : "memory");
}
DI unsigned pk2(float lo, float hi) { f32x2 v = {lo, hi}; return __builtin_bit_cast(unsigned, __builtin_convertvector(v, bfx2)); }
typedef short v4i16_t __attribute__((ext_vector_type(4)));
DI bf16x8 trfrag(LAS char* p, int second) {
    s16x4 a = __builtin_bit_cast(s16x4, __builtin_amdgcn_ds_read_tr16_b64_v4i16((LAS v4i16_t*)p));
    s16x4 b = __builtin_bit_cast(s16x4, __builtin_amdgcn_ds_read_tr16_b64_v4i16((LAS v4i16_t*)(p + second)));
    return __builtin_shufflevector(a, b, 0, 1, 2, 3, 4, 5, 6, 7);
}
constexpr u64 lanes_cut(int r, bool fartile) {
    u64 m = 0;
    for (int l = 0; l < 64; ++l) { const int key = (r & 3) + 8 * (r >> 2) + 4 * (l >> 5), d = (l & 31) - key + (fartile ? 32 : 0);
        const bool ok = d >= 0 && d <= 14 && !(d & 1);
        if (!ok) m |= 1ull << l; }
    return m;
}
DI float halfmax(float m) { auto rr = __builtin_amdgcn_permlane32_swap(__float_as_uint(m), __float_as_uint(m), false, false);
    return __builtin_elementwise_maximum(__uint_as_float(rr[0]), __uint_as_float(rr[1])); }

DI void poll_flags(GAS u64* word, bool active, u64 tag) {
#pragma unroll 1
    for (unsigned spins = 0; spins < SPIN_CAP; ++spins) {
        u64 v = tag;
        if (active) v = __hip_atomic_load(word, RLX_AGENT);
        if (__all(v == tag)) break;
        __builtin_amdgcn_s_sleep(PSLEEP);
    }
    __builtin_amdgcn_fence(__ATOMIC_ACQUIRE, "wavefront");
}
DI void poll_shared(GAS u64* word, bool active, u64 tag, LAS char* ok) {
#pragma unroll 1
    for (unsigned spins = 0; spins < SPIN_CAP; ++spins) {
        if (__builtin_amdgcn_readfirstlane((int)__hip_atomic_load((LAS unsigned*)ok, RLX_WG))) break;
        u64 v = tag;
        if (active) v = __hip_atomic_load(word, RLX_AGENT);
        if (__all(v == tag)) { __hip_atomic_store((LAS unsigned*)ok, 1u, RLX_WG); break; }
        __builtin_amdgcn_s_sleep(PSLEEP);
    }
    __builtin_amdgcn_fence(__ATOMIC_ACQUIRE, "wavefront");
    asm volatile("" ::: "memory");
}
DI int min4(LAS char* p) {
    const u64 a = __hip_atomic_load((LAS u64*)p, RLX_WG), b = __hip_atomic_load((LAS u64*)(p + 8), RLX_WG);
    const unsigned m = min(min((unsigned)a, (unsigned)(a >> 32)), min((unsigned)b, (unsigned)(b >> 32)));
    return __builtin_amdgcn_readfirstlane((int)m);
}
DI int wait_min4(LAS char* p, int need) {
    int v = min4(p);
#pragma unroll 1
    for (unsigned spins = 0; v < need && spins < LSPIN_CAP; ++spins) { __builtin_amdgcn_s_sleep(GSLEEP); v = min4(p); }
    FENCE();
    return v;
}
DI void wait_word(LAS char* p, unsigned need) {
#pragma unroll 1
    for (unsigned spins = 0; spins < LSPIN_CAP; ++spins) {
        if ((unsigned)__builtin_amdgcn_readfirstlane((int)__hip_atomic_load((LAS unsigned*)p, RLX_WG)) >= need) break;
        __builtin_amdgcn_s_sleep(1); }
    FENCE();
}
DI void lds_inc0(LAS char* p, int lane) { FENCE(); if (lane == 0) (void)__hip_atomic_fetch_add((LAS unsigned*)p, 1u, RLX_WG); FENCE(); }
DI void wait_pair(LAS char* p, unsigned need) {
#pragma unroll 1
    for (unsigned spins = 0; spins < LSPIN_CAP; ++spins) { const u64 v = __hip_atomic_load((LAS u64*)p, RLX_WG);
        if ((unsigned)__builtin_amdgcn_readfirstlane((int)min((unsigned)v, (unsigned)(v >> 32))) >= need) break;
        __builtin_amdgcn_s_sleep(1); }
    FENCE();
}
DI unsigned lds_inc(LAS char* p, int lane) { FENCE(); unsigned old = 0; if (lane == 0) old = __hip_atomic_fetch_add((LAS unsigned*)p, 1u, RLX_WG); FENCE(); return old; }
DI void lds_set(LAS char* p, unsigned v) { FENCE(); __hip_atomic_store((LAS unsigned*)p, v, RLX_WG); FENCE(); }

__global__ __launch_bounds__(512, 2) void fused_attn_kernel(const char* __restrict__ xA, const char* __restrict__ wA, const char* __restrict__ oA,
                                                           const char* __restrict__ xB, const char* __restrict__ wB, const char* __restrict__ oB,
                                                           char* __restrict__ out, char* __restrict__ ws) {
    __shared__ __attribute__((aligned(16))) char smem_[LDS_BYTES];
    LAS char* smem = (LAS char*)smem_;
    const unsigned lds0 = (unsigned)(uintptr_t)smem;
    STAMP(0);
    asm volatile("" :: "s"(xA), "s"(wA), "s"(oA), "s"(xB), "s"(wB), "s"(oB), "s"(out), "s"(ws));
    const int tid = threadIdx.x, lane = tid & 63;
    const int wid = __builtin_amdgcn_readfirstlane(tid >> 6);
    const int bx = blockIdx.x, xcd = bx & 7, idx = bx >> 3;
    const int isA = idx >> 4, rt = (xcd >> 1) * 4 + ((idx >> 2) & 3), hg = (xcd & 1) * 4 + (idx & 3);
    const int br = isA ? 0 : 1;
    const int k0 = isA ? (((rt & 3) * 5 + 1) / 2 + ((hg & 3) * 5 + 1) / 2) % 10 : 0;
    const char* xb = (isA ? xA : xB) + (size_t)rt * (128 * 1280);
    const char* wb = (isA ? wA : wB) + hg * 160;
    const int me = (br * 16 + rt) * 8 + hg;
    const int r32 = lane & 31, hl = lane >> 5;
    auto kt_of = [&](int s) { int k = s + k0; return k >= 10 ? k - 10 : k; };
    if (isA && wid < 4) {
        const int j16 = idx & 15; const unsigned dump = lds0 + OWN + wid * 512;
        const char* wl = wB + (hg >> 2) * 640 + j16 * (4 * 3840);
        const char* xl = xB + (size_t)(rt * 128 + (hg & 3) * 32 + r32) * 1280 + hl * 128;
        auto pfx = [&](int i) { return xl + i * 256; };
        auto pfkv = [&](int i) { int n = i * 64 + lane; n = n < 400 ? n : 399; const int sl = n / 40, r = n % 40, c = r % 10;
            return wl + (sl * 64 + r / 10) * 3840 + 1280 + (c < 5 ? c * 128 : 640 + c * 128); };
        auto pfq = [&](int i) { int n = i * 64 + lane; n = n < 200 ? n : 199; int sl = n / 20 + 8; sl = sl >= 10 ? sl - 10 : sl; const int r = n % 20;
            return wl + (sl * 64 + r / 5) * 3840 + (r % 5) * 128; };
        dma4(wid & 1 ? pfx(wid >> 1) : pfkv(wid >> 1), dump);
        dma4(wid & 1 ? pfx(2 + (wid >> 1)) : pfkv(2 + (wid >> 1)), dump);
        dma4(wid < 3 ? pfkv(4 + wid) : pfx(4), dump);
        dma4(pfq(wid), dump);
        { int n = wid * 64 + lane; n = n < 200 ? n : 199; const int o = (j16 * 40 + n / 5) * 1280 + (hg >> 2) * 640 + (n % 5) * 128;
          dma4(oB + o, dump); dma4(oA + o, dump); }
    }
    if (isA && wid >= 4) { for (int i = 0; i < ADELAY; ++i) __builtin_amdgcn_s_sleep(16); }

    const unsigned xo = (unsigned)((lane >> 3) * 1280 + (((lane & 7) ^ (lane >> 4)) << 4));
    auto w_off = [&](int q) { const int c = q * 64 + lane, row = __umul24(c, 3277) >> 16, j = c - row * 20; return (unsigned)(__umul24(row, 3840) + j * 16 + (j < 10 ? 1280 : 2400)); };
    if (!isA) { const char* xs = xb + k0 * 128 + wid * (16 * 1280);
      dma16(xo, xs, lds0 + XS0 + wid * 2048);
      dma16(xo ^ 64, xs + 8 * 1280, lds0 + XS0 + wid * 2048 + 1024);
      const char* wsrc = wb + (size_t)k0 * (64 * 3840);
      const unsigned wv = w_off(wid < 5 ? wid : wid - 5);
      if (wid < 5) { dma16(wv, wsrc, lds0 + WS0 + wid * 1024); dma16(wv, wsrc + 16 * 3840, lds0 + WS0 + (5 + wid) * 1024);
          if (wid >= 3) { dma16(wv, wsrc + 32 * 3840, lds0 + WS0 + (10 + wid) * 1024); dma16(wv, wsrc + 48 * 3840, lds0 + WS0 + (15 + wid) * 1024); } }
      else { dma16(wv, wsrc + 32 * 3840, lds0 + WS0 + (5 + wid) * 1024); dma16(wv, wsrc + 48 * 3840, lds0 + WS0 + (10 + wid) * 1024); } }
    if (wid == 4 && lane < 2) dma4(ws + lane * 4, lds0 + C_TAG);
    if (tid < 80) *(LAS unsigned*)(smem + tid * 4) = 0u;
    else if (tid < 82) *(LAS unsigned*)(smem + ONE + (tid - 80) * 4) = 0x3F803F80u;
    const int nnb = isA ? (rt > 0 ? 1 : 0) : (rt > 1 ? 2 : rt);
    auto kq_base = [&](int fg) { const int hd = fg / 5, dg = fg % 5; return dg < 4 ? hd * 5120 + (dg >> 1) * 2048 + (dg & 1) * 8 : hd * 5120 + 4096; };

    auto rdtag = [&]() { const u64 t = *(LAS u64*)(smem + C_TAG);
        const u64 e = ((u64)(unsigned)__builtin_amdgcn_readfirstlane((int)(t >> 32)) << 32) | (unsigned)__builtin_amdgcn_readfirstlane((int)t);
        return e * 6364136223846793005ull + 1442695040888963407ull; };
    const __amdgpu_buffer_rsrc_t wor = __builtin_amdgcn_make_buffer_rsrc((void*)(isA ? oA : oB), 0, 640 * 1280, 0x00020000);

    const int hp = wid >> 2, qo = wid & 3, qi = hp ? 3 - qo : qo;
    const int ud = qi + 8;
    const bool far = isA ? (4 * rt + qi >= 1) : (4 * rt + qi >= 8);
    const int u0 = isA ? (far ? ud - 1 : ud) : (far ? qi : 8 - 4 * rt);
    f32x16 cd, cf;
    int dsh, kb1, kb2, nh, vb0, vb1, qb1, qb2; unsigned obase, oo[5];
    auto mask_b = [&](f32x16& c, int r, bool fartile) { const int k = (r & 3) + 8 * (r >> 2); c[r] = __int_as_float(fartile ? ((k - 1) << 23) - dsh : dsh - (k << 23)); };
    auto mask_a = [&](int z) {
      SFOR(r, 16)
          constexpr u64 ad = lanes_cut(r, false), af = lanes_cut(r, true);
          cd[r] = __builtin_amdgcn_inverse_ballot_w64(ad + z) ? -__builtin_inff() : 0.f;
          cf[r] = __builtin_amdgcn_inverse_ballot_w64(af + z) ? -__builtin_inff() : 0.f;
      SEND };
    auto att_setup = [&](int part) {
        int l = tid; asm volatile("" : "+v"(l)); l &= 63;
        const int q = l & 31, h = l >> 5;
        if (part == 0) { dsh = (q - 4 * h) << 23; kb1 = KV0 + hp * 10240 + h * 2048 + q * 16; kb2 = h ? ONE : KV0 + hp * 10240 + 4096 + q * 8; nh = h ^ 1;
            asm volatile("" : "+v"(dsh), "+v"(kb1), "+v"(kb2), "+v"(nh)); }
        if (part == 1) { const bool ones = (l & 16) && (l & 3);
            vb0 = KV0 + VOFF + (4 * h + ((l & 15) >> 2)) * VP + (ones ? 160 : hp * 80 + (l & 16) * 2 + (l & 3) * 8); vb1 = vb0 + (ones ? 0 : 40);
            asm volatile("" : "+v"(vb0), "+v"(vb1)); }
        if (part == 2) { qb1 = QST + hp * 10240 + h * 2048 + (qi * 32 + q) * 16; qb2 = QST + hp * 10240 + 4096 + (qi * 32 + q) * 8;
            obase = (unsigned)(qi * 40960 + hp * 20480 + hg * 2560 + q * 16 + h * 512);
            asm volatile("" : "+v"(qb1), "+v"(qb2), "+v"(obase)); }
        if (part >= 3) {
#pragma unroll
            for (int r = 4 * (part - 3); r < 4 * (part - 2); ++r) mask_b(cd, r, false);
            asm volatile("" : "+v"(cd)); } };
    auto wo_setup = [&](int q) {
        int l = tid; asm volatile("" : "+v"(l)); const int c = q * 64 + (l & 63), rho = __umul24(c, 6554) >> 16;
        oo[q] = (unsigned)(__umul24(qo * 160 + hp * 40 + (rho & ~7) + 4 * (rho & 1) + ((rho >> 1) & 3), 1280) + hg * 160 + (c - rho * 10) * 16);
        asm volatile("" : "+v"(oo[q])); };
    u64 tag;

    if (wid < 4) {
        if (!isA) WAITV(0);
        BAR_L();
        STAMP(1);
        bf16x8 xr[32], xt[2], wf[WIN + 1];
        f32x16 acc[5];
        const int tok = wid * 32 + r32;
        const int xa = XS0 + tok * 128 + ((hl ^ ((tok >> 1) & 7)) << 4);
        int wa0 = WS0 + (8 * hl + ((lane & 15) >> 2)) * 320 + (lane & 16) * 2 + (lane & 3) * 8, wa1 = wa0 + 61440;
        asm volatile("" : "+v"(wa0), "+v"(wa1));
        auto rdx = [&](int i) { return *(LAS bf16x8*)(smem + ((i >> 2) % 3) * 16384 + (xa ^ ((i & 3) * 32))); };
        auto rdw = [&](int f) { const int i = f / 5, t = f % 5, sl = (i >> 1) % 7;
            return trfrag(smem + (sl < 6 ? wa0 + sl * 10240 : wa1) + (i & 1) * 5120 + t * 64, 1280); };
        unsigned fl = 0;
        auto rdflag = [&](int u) { fl = __hip_atomic_load((LAS unsigned*)(smem + C_F + u * 4), RLX_WG); };
        auto check = [&](int u) {
            if (__builtin_amdgcn_readfirstlane(fl) != 15) {
#pragma unroll 1
                for (unsigned spins = 0; spins < LSPIN_CAP; ++spins) { rdflag(u); if (__builtin_amdgcn_readfirstlane(fl) == 15) break; __builtin_amdgcn_s_sleep(1); }
            }
            FENCE(); };
#pragma unroll
        for (int i = 0; i < 4; ++i) xr[i] = rdx(i);
#pragma unroll
        for (int f = 0; f < WIN; ++f) wf[f] = rdw(f);
        SB();
        SFOR(f, 200)
            constexpr int i = f / 5, t = f % 5;
            if constexpr (f % 20 == 10 - WIN) lds_set(smem + C_P + wid * 4, (i >> 1) + 1);
            if constexpr (f == 200 - WIN) lds_set(smem + C_P + wid * 4, 20);
            if constexpr (f % 20 == 10) rdflag(f / 20 + 1);
            if constexpr (f % 20 == 20 - WIN && f < 190) check(f / 20 + 1);
            if constexpr (f + WIN < 200) wf[(f + WIN) % (WIN + 1)] = rdw(f + WIN);
            if constexpr (f % 20 >= 16 && f < 140) xr[(f / 20 + 1) * 4 + f % 20 - 16] = rdx((f / 20 + 1) * 4 + f % 20 - 16);
            if constexpr (t == 1 && i >= 31 && i < 39) xt[(i + 1) & 1] = rdx(i + 1);
            SB();
            if constexpr (i == 0) MFMA32Z(acc[t], wf[f % (WIN + 1)], xr[0]);
            else if constexpr (i < 32) MFMA32I(acc[t], wf[f % (WIN + 1)], xr[i]);
            else MFMA32I(acc[t], wf[f % (WIN + 1)], xt[i & 1]);
            SB();
        SEND
        STAMP(2);
        int l2 = tid; asm volatile("" : "+v"(l2)); l2 &= 63;
        const int h2 = l2 >> 5, tk = wid * 32 + (l2 & 31);
        int eb[5];
        auto ebases = [&](int base) { int l = tid; asm volatile("" : "+v"(l)); const int h = (l >> 5) & 1, k = wid * 32 + (l & 31);
            eb[0] = base + k * 16 + h * 8; eb[1] = base + k * 16 + h * 2040; eb[2] = base + (h ? k * 16 + 1024 : k * 8); eb[3] = base + (h ? k * 8 + 2040 : k * 16);
            asm volatile("" : "+v"(eb[0]), "+v"(eb[1]), "+v"(eb[2]), "+v"(eb[3])); };
        auto kq_addr = [&](int g0) { return eb[g0 % 5 == 1 ? 1 : g0 % 5 == 4 ? 2 : g0 % 5 == 3 ? 3 : 0] + kq_base(g0); };
        ebases(OWN);
        eb[4] = OWN + VOFF + tk * VP + h2 * 8; asm volatile("" : "+v"(eb[4]));
        int qa0 = (8 * h2 + 2 * ((l2 & 15) >> 2)) * 160 + (l2 & 16) * 2 + (l2 & 3) * 8, qa1 = qa0 + WS0 + 10240;
        asm volatile("" : "+v"(qa0), "+v"(qa1));
        auto rdq = [&](int f) { const int i = f / 3, t = f % 3, j = i >> 2, sl = (20 + j) % 7;
            return trfrag(smem + (j >= 7 ? qa0 + (j == 7 ? QU27 : j == 8 ? QU28 : QU29) : sl == 0 ? qa0 + WS0 : qa1 + (sl - 1) * 10240) + (i & 3) * 2560 + t * 64, 160); };
        MFMA_DRAIN();
        WAITV(0);
        { u32x2 pk[20];
#pragma unroll
          for (int n = 0; n < 20; ++n) { const int t = n >> 2, b = n & 3; pk[n] = (u32x2){pk2(acc[t][4 * b], acc[t][4 * b + 1]), pk2(acc[t][4 * b + 2], acc[t][4 * b + 3])}; }
          SB();
#pragma unroll
          for (int n = 0; n < 20; ++n) *(LAS u32x2*)(smem + (n < 10 ? kq_addr(2 * n) : eb[4] + (2 * n - 20) * 8)) = pk[n];
          if (h2 == 0) *(LAS u32x2*)(smem + eb[4] + 160) = (u32x2){0x3F803F80u, 0x3F803F80u}; }
        lds_set(smem + C_E + wid * 4, 1);
        check(10);
        xt[0] = rdx(32);
#pragma unroll
        for (int f = 0; f < WIN; ++f) wf[f] = rdq(f);
        STAMP(3);
        SB();
        u64 pv0 = 0, pv1 = 0;
        SFOR(f, 120)
            constexpr int i = f / 3, t = f % 3, j = i >> 2;
            if constexpr (f % 24 == 12 && j < 9) rdflag(11 + j / 2);
            if constexpr (f % 12 == 12 - WIN && (j == 1 || j == 6 || j == 8)) lds_set(smem + C_P + wid * 4, 21 + j);
            if constexpr (f % 24 == 24 - WIN && j < 9) check(11 + j / 2);
            if constexpr (f + WIN < 120) wf[(f + WIN) % (WIN + 1)] = rdq(f + WIN);
            if constexpr (t == 0 && i < 7) xt[(i + 1) & 1] = rdx(32 + i + 1);
            if constexpr (f >= 90 && f < 100 && !(f & 1)) wo_setup((f - 90) / 2);
            if constexpr (f == 99) tag = rdtag();
            if constexpr (f >= 100 && f < 107) att_setup(f - 100);
            if constexpr (f == 108) ebases(QST);
            if constexpr (f == 116) { pv0 = __hip_atomic_load((LAS u64*)(smem + C_P), RLX_WG); pv1 = __hip_atomic_load((LAS u64*)(smem + C_P + 8), RLX_WG); }
            SB();
            if constexpr (i == 0) MFMA32Z(acc[t], wf[f % (WIN + 1)], xt[0]);
            else if constexpr (i < 8) MFMA32I(acc[t], wf[f % (WIN + 1)], xt[i & 1]);
            else MFMA32I(acc[t], wf[f % (WIN + 1)], xr[i - 8]);
            SB();
        SEND
        STAMP(4);
        if (__builtin_amdgcn_readfirstlane((int)min(min((unsigned)pv0, (unsigned)(pv0 >> 32)), min((unsigned)pv1, (unsigned)(pv1 >> 32)))) < 29) wait_min4(smem + C_P, 29);
        MFMA_DRAIN();
        { acc[0] *= QSCALE; acc[1] *= QSCALE; acc[2] *= QSCALE;
          SB();
          u32x2 pk[10];
#pragma unroll
          for (int n = 0; n < 10; ++n) { const int t = n >> 2, b = n & 3; pk[n] = (u32x2){pk2(acc[t][4 * b], acc[t][4 * b + 1]), pk2(acc[t][4 * b + 2], acc[t][4 * b + 3])}; }
          SB();
#pragma unroll
          for (int n = 0; n < 10; ++n) *(LAS u32x2*)(smem + kq_addr(2 * n)) = pk[n]; }
        lds_set(smem + C_QF + wid * 4, 1);
        __builtin_amdgcn_s_setprio(1);
        STAMP(5);
    } else {
        const int hw = wid - 4;
        int known = 0;
        __builtin_amdgcn_s_setprio(3);
        auto lds_or = [&](int off, unsigned v) { FENCE(); if (lane == 0) __hip_atomic_fetch_or((LAS unsigned*)(smem + off), v, RLX_WG); FENCE(); };
        unsigned wo[5], qo[5];
        auto done = [&](int n) { lds_or(C_F + n * 4, 1u << hw); };
        const unsigned xo1 = (xo ^ 64) + 9216, xo2 = xo + 2 * 9216, xo3 = (xo ^ 64) + 3 * 9216;
        auto round_x = [&](int n) { dma16x4(xo, xo1, xo2, xo3, xb + kt_of(n) * 128 + hw * (32 * 1280), lds0 + XS0 + (n % 3) * 16384 + hw * 4096); };
        auto round_w = [&](int n) {
            const int u = 2 * n + (hw >> 1);
            const unsigned dst = lds0 + (u < 27 ? WS0 + (u % 7) * 10240 : u == 27 ? QU27 : u == 28 ? QU28 : QU29) + (hw & 1) * 5120 + 2048;
            if (u < 20) dma16x5(wo, wb + (size_t)(kt_of(u >> 1) * 64 + (u & 1) * 32) * 3840 - 2048, dst);
            else dma16x5(qo, wb + (size_t)kt_of(u < 22 ? u - 12 : u - 22) * (64 * 3840) - 2048, dst); };
        if (isA) round_x(0);
        round_x(1);
#pragma unroll
        for (int q = 0; q < 5; ++q) wo[q] = w_off(q) + (hw & 1) * (16 * 3840) + (4 - q) * 1024;
        if (isA) { round_w(0); round_w(1); WAITV(5); } else { round_w(1); WAITV(9); }
        BAR_L();
#pragma unroll
        for (int q = 0; q < 5; ++q) { const int c = q * 64 + lane, rho = __umul24(c, 6554) >> 16;
            qo[q] = (unsigned)(__umul24((hw & 1) * 32 + (rho & ~7) + 4 * (rho & 1) + ((rho >> 1) & 3), 3840) + (c - rho * 10) * 16 + (4 - q) * 1024); }
#pragma unroll 1
        for (int n = 2; n <= 13; ++n) {
            const int need = n < 13 ? 2 * n - 5 : 20;
            if (need > known) known = min4(smem + C_P);
            if (need > known) { WAITV(0); done(n - 1); known = wait_min4(smem + C_P, need); }
            if (n < 10) round_x(n);
            round_w(n);
            if (n < 10) WAITV(9); else WAITV(5);
            done(n - 1);
        }
        WAITV(0); done(13);
        STAMP(1);
        tag = rdtag();
        auto a_chunk = [&](int a) { int o;
            if (a < 320) { const int hd = a / 80, rem = a % 80; o = hd * 5120 + (rem < 32 ? (96 + rem) * 16 : rem < 64 ? 2048 + (64 + rem) * 16 : 4096 + 768 + (rem - 64) * 16); }
            else o = VOFF + 96 * VP + (a - 320) * 16;
            return o; };
        if (rt < 15) {
            const __amdgpu_buffer_rsrc_t kvr = __builtin_amdgcn_make_buffer_rsrc(ws + WS_KV + me * KVG, 0, KVB, 0x00020000);
            wait_min4(smem + C_E, 1);
            STAMP(2);
            if (!isA) { u32x4 v[11];
#pragma unroll
                for (int j = 0; j < 11; ++j) if (j < 10 || hw == 3) v[j] = *(LAS u32x4*)(smem + OWN + hw * 10240 + j * 1024 + lane * 16);
#pragma unroll
                for (int j = 0; j < 11; ++j) if (j < 10 || hw == 3) __builtin_amdgcn_raw_buffer_store_b128(v[j], kvr, hw * 10240 + j * 1024 + lane * 16, 0, 16);
            } else {
#pragma unroll
                for (int j = 0; j < 3; ++j) { const int a = (4 * j + hw) * 64 + lane;
                    if (a < 656) { const int o = a_chunk(a); __builtin_amdgcn_raw_buffer_store_b128(*(LAS u32x4*)(smem + OWN + o), kvr, o, 0, 16); } }
            }
            STAMP(3);
        }
        wait_min4(smem + C_P, 22);
        round_w(14);
        WAITV(5);
        if (rt < 15) {
            unsigned old = 0;
            if (lane == 0) old = __hip_atomic_fetch_add((LAS unsigned*)(smem + N_PUB), 1u, RLX_WG);
            if (lane == 0 && old == 3) __hip_atomic_store((GAS u64*)(ws + WS_FKV + me * 128), tag, RLX_AGENT);
            STAMP(4);
        }
        WAITV(0); done(14);
        __builtin_amdgcn_s_setprio(0);
#pragma unroll
        for (int p = 0; p < 7; ++p) att_setup(p);
#pragma unroll
        for (int q = 0; q < 5; ++q) wo_setup(q);
        __builtin_amdgcn_s_setprio(3);
        if (hw < NPOLL) {
            __builtin_amdgcn_s_sleep(KDELAY);
            for (int i = 0; i < hw; ++i) __builtin_amdgcn_s_sleep(PSTAG);
            poll_shared((GAS u64*)(ws + WS_FKV + (me - 8 * (lane + 1)) * 128), lane < nnb, tag, smem + N_KV);
        } else wait_word(smem + N_KV, 1);
        STAMP(5);
        if (!isA) {
            const int n = hw >> 1, half = hw & 1;
            if (n < nnb) {
                const char* src = ws + WS_KV + (me - 8 * (n + 1)) * KVG + half * 20480; const unsigned dst = lds0 + KV0 + (1 - n) * KVB + half * 20480;
                wait_min4(smem + C_P, 27);
#pragma unroll
                for (int j = 0; j < 4; ++j) dma16x5_sc1(lane * 16, src + j * 5120 + 2048, dst + j * 5120 + 2048);
                if (half) dma16_sc1(lane * 16, src + 20480, dst + 20480);
            }
        } else if (nnb) {
            const __amdgpu_buffer_rsrc_t nbr = __builtin_amdgcn_make_buffer_rsrc(ws + WS_KV + (me - 8) * KVG, 0, KVB, 0x00020000);
            u32x4 v[3];
#pragma unroll
            for (int j = 0; j < 3; ++j) { const int a = (4 * j + hw) * 64 + lane; if (a < 656) v[j] = __builtin_amdgcn_raw_buffer_load_b128(nbr, a_chunk(a), 0, 16); }
            wait_min4(smem + C_P, 27);
#pragma unroll
            for (int j = 0; j < 3; ++j) { const int a = (4 * j + hw) * 64 + lane; if (a < 656) *(LAS u32x4*)(smem + KV0 + KVB + a_chunk(a)) = v[j]; }
        }
        __builtin_amdgcn_s_setprio(2);
        wait_word(smem + C_QF + qi * 4, 1);
        STAMP(6);
    }
    u32x4 wreg[13];
    auto ldwo = [&](int i) { wreg[i] = __builtin_amdgcn_raw_buffer_load_b128(wor, oo[i % 5] + (i / 5) * (32 * 1280) + (i > 6 || (i == 6 && lane >= 16) ? 40 * 1280 : 0), 0, 0); };
    STAMP(7);
    u32x2 ob[2][3];
    {
        bf16x8 q1[2]; u32x2 q2[2];
#pragma unroll
        for (int e = 0; e < 2; ++e) {
            q1[e] = *(LAS bf16x8*)(smem + qb1 + e * 5120);
            q2[e] = *(LAS u32x2*)(smem + qb2 + e * 5120);
            if (hl) q2[e] = (u32x2){0u, 0u};
        }
        if (isA) mask_a(0);
        struct KF { bf16x8 a; s16x4 b; };
        struct VF { bf16x8 a, b; };
        auto rdk = [&](int u, int e) { const int so = (u >> 2) * KVB, tw = u & 3; KF k;
            k.a = *(LAS bf16x8*)(smem + kb1 + so + tw * 512 + e * 5120); k.b = *(LAS s16x4*)(smem + __umul24(so + tw * 256 + e * 5120, nh) + kb2); return k; };
        auto rdv = [&](int u, int e) { LAS char* p = smem + (e ? vb1 : vb0) + (u >> 2) * KVB + (u & 3) * (32 * VP); VF v;
            v.a = trfrag(p, 8 * VP); v.b = trfrag(p + 16 * VP, 8 * VP); return v; };
        auto qk = [&](const KF& k, int e, const f32x16& c) { return MFMA32K8(k.b, __builtin_bit_cast(s16x4, q2[e]), MFMA32(k.a, q1[e], c)); };
        const f32x16 zero = {};
        auto max16 = [&](const f32x16& s, float m) {
#pragma unroll
            for (int r = 0; r < 16; ++r) m = __builtin_elementwise_maximum(m, s[r]);
            return m; };
        auto set_ref = [&](float m, int e) { const unsigned nb = pk2(-m, 0.f); if (hl) q2[e][0] = nb; };
        auto ex4 = [&](f32x16& s, int b) {
#pragma unroll
            for (int r = 0; r < 4; ++r) s[b + r] = __builtin_amdgcn_exp2f(s[b + r]); };
        auto pk8 = [&](const f32x16& s, int b) { return __builtin_bit_cast(bf16x8, (u32x4){pk2(s[b], s[b + 1]), pk2(s[b + 2], s[b + 3]), pk2(s[b + 4], s[b + 5]), pk2(s[b + 6], s[b + 7])}); };
        auto phase = [&](auto nx, f32x16& cur, const VF& v, f32x16& o, f32x16& nxt, const KF& k, int e) __attribute__((always_inline)) {
            constexpr int NX = decltype(nx)::value;
            SB(); ex4(cur, 0); SB();
            if constexpr (NX) nxt = MFMA32(k.a, q1[e], NX == 1 ? zero : NX == 2 ? cf : cd);
            SB(); ex4(cur, 4); { const bf16x8 p = pk8(cur, 0); SB(); o = MFMA32(v.a, p, o); }
            SB(); ex4(cur, 8); SB();
            if constexpr (NX) nxt = MFMA32K8(k.b, __builtin_bit_cast(s16x4, q2[e]), nxt);
            SB(); ex4(cur, 12); { const bf16x8 p = pk8(cur, 8); SB(); o = MFMA32(v.b, p, o); }
            SB(); };
        f32x16 o0, o1, sa, sb; KF kx; VF vx;
        auto step = [&](auto m0, auto m1, int u) __attribute__((always_inline)) {
            constexpr int M1 = decltype(m1)::value;
            KF ky; if constexpr (M1) ky = rdk(u - 1, 0);
            const VF vy = rdv(u, 1);
            phase(m0, sa, vx, o0, sb, kx, 1);
            if constexpr (M1) { kx = rdk(u - 1, 1); vx = rdv(u - 1, 0); }
            phase(m1, sb, vy, o1, sa, ky, 0); };
        auto nbwait = [&](int u) { if (u == 8) wait_word(smem + N_NB, 4); };
        { const KF k0f = rdk(ud, 0); kx = rdk(ud, 1); vx = rdv(ud, 0);
          const f32x16 s0 = qk(k0f, 0, cd), s1 = qk(kx, 1, cd);
          set_ref(halfmax(max16(s0, s0[0])), 0); set_ref(halfmax(max16(s1, s1[0])), 1);
          sa = qk(k0f, 0, cd); }
        if (hp) { WAITV(0); lds_inc0(smem + N_NB, lane); lds_inc0(smem + N_G0, lane); }
        o0 = zero; o1 = zero;
        if (u0 + 8 == ud) {
            SFOR(it, 9)
                if constexpr (it < 7) { ldwo(2 * it); if constexpr (it < 6) ldwo(2 * it + 1); }
                if constexpr (it == PFLIP) { if (hp) __builtin_amdgcn_s_setprio(0); }
                if constexpr (it == 7) {
#pragma unroll
                    for (int r = 0; r < 16; ++r) mask_b(cf, r, true); }
                if constexpr (it < 4) nbwait(ud - it);
                step(IC<it == 0 ? 3 : it == 8 ? 2 : 1>{}, IC<it == 8 ? 0 : it == 7 ? 2 : 1>{}, ud - it);
            SEND
        } else {
            __builtin_amdgcn_s_setprio(0);
#pragma unroll
            for (int i = 0; i < 13; ++i) ldwo(i);
            if (!isA) cf = zero;
            if (u0 < ud) {
                nbwait(ud);
                if (u0 + 1 < ud) {
                    step(IC<3>{}, IC<1>{}, ud);
#pragma unroll 1
                    for (int u = ud - 1; u > u0 + 1; --u) { nbwait(u); step(IC<1>{}, IC<1>{}, u); }
                    nbwait(u0 + 1);
                    step(IC<1>{}, IC<2>{}, u0 + 1);
                } else step(IC<3>{}, IC<2>{}, ud);
                step(IC<2>{}, IC<0>{}, u0);
            } else step(IC<3>{}, IC<0>{}, ud);
        }
        if (!__all(o0[12] < 1.152921504606847e18f && o1[12] < 1.152921504606847e18f)) {
            if (isA) { int z = 0; asm volatile("" : "+s"(z)); mask_a(z); }
            else { asm volatile("" : "+v"(dsh));
#pragma unroll
                for (int r = 0; r < 16; ++r) { mask_b(cd, r, false); mask_b(cf, r, true); }
                if (!far) cf = zero; }
#pragma unroll
            for (int e = 0; e < 2; ++e) {
                if (hl) q2[e][0] = 0u;
                const f32x16 s = qk(rdk(ud, e), e, cd);
                float m = max16(s, s[0]);
                if (u0 < ud) m = max16(qk(rdk(u0, e), e, cf), m);
#pragma unroll 1
                for (int u = u0 + 1; u < ud; ++u) m = max16(qk(rdk(u, e), e, zero), m);
                set_ref(halfmax(m), e);
            }
#pragma unroll
            for (int e = 0; e < 2; ++e) {
                f32x16 o = zero;
#pragma unroll 1
                for (int u = u0; u <= ud; ++u) { f32x16 t = u == ud ? qk(rdk(u, e), e, cd) : u == u0 ? qk(rdk(u, e), e, cf) : qk(rdk(u, e), e, zero);
                    const VF v = rdv(u, e); ex4(t, 0); ex4(t, 4); ex4(t, 8); ex4(t, 12);
                    o = MFMA32(v.a, pk8(t, 0), o); o = MFMA32(v.b, pk8(t, 8), o); }
                if (e) o1 = o; else o0 = o;
            }
        }
        const float rl0 = __builtin_amdgcn_rcpf(o0[12]), rl1 = __builtin_amdgcn_rcpf(o1[12]);
        SB();
#pragma unroll
        for (int r = 0; r < 12; ++r) { o0[r] *= rl0; o1[r] *= rl1; }
        SB();
#pragma unroll
        for (int j = 0; j < 3; ++j) {
            ob[0][j] = (u32x2){pk2(o0[4 * j], o0[4 * j + 1]), pk2(o0[4 * j + 2], o0[4 * j + 3])};
            ob[1][j] = (u32x2){pk2(o1[4 * j], o1[4 * j + 1]), pk2(o1[4 * j + 2], o1[4 * j + 3])}; }
        SB();
    }
    STAMP(8);
    const __amdgpu_buffer_rsrc_t orr = __builtin_amdgcn_make_buffer_rsrc(ws + WS_O + (br * 16 + rt) * OG, 0, 4 * 40960, 0x00020000);
    {
      { auto rx = __builtin_amdgcn_permlane32_swap(ob[0][0][0], ob[0][1][0], false, false);
        auto ry = __builtin_amdgcn_permlane32_swap(ob[0][0][1], ob[0][1][1], false, false);
        __builtin_amdgcn_raw_buffer_store_b128((u32x4){rx[0], ry[0], rx[1], ry[1]}, orr, obase, 0, 16); }
      if (hl == 0) __builtin_amdgcn_raw_buffer_store_b128((u32x4){ob[0][2][0], ob[0][2][1], ob[1][0][0], ob[1][0][1]}, orr, obase + 1024, 0, 16);
      { auto fx = __builtin_amdgcn_permlane32_swap(ob[1][0][0], ob[1][1][0], false, false);
        auto fy = __builtin_amdgcn_permlane32_swap(ob[1][0][1], ob[1][1][1], false, false);
        auto sx = __builtin_amdgcn_permlane32_swap(ob[1][1][0], ob[1][2][0], false, false);
        auto sy = __builtin_amdgcn_permlane32_swap(ob[1][1][1], ob[1][2][1], false, false);
        __builtin_amdgcn_raw_buffer_store_b128((u32x4){fx[1], fy[1], sx[0], sy[0]}, orr, obase + 1536, 0, 16); } }
    if (lane == 0) { (void)__hip_atomic_fetch_add((LAS unsigned*)(smem + N_G1), 1u, RLX_WG);
        if (!hp) (void)__hip_atomic_fetch_add((LAS unsigned*)(smem + N_G0), 1u, RLX_WG); }
    FENCE();
    STAMP(6);
    auto signal_o = [&]() {
        if (lds_inc(smem + N_OS + hp * 4, lane) == 3 && lane == 0) __hip_atomic_store((GAS u64*)(ws + WS_FO + (me * 2 + hp) * 128), tag, RLX_AGENT); };
    __builtin_amdgcn_s_setprio(0);
    WAITV(0); signal_o();
    STAMP(9);
    wait_word(smem + (hp ? N_G1 : N_G0), 8);
    const int p1 = hp ? 30720 : 35840, p2 = hp ? 65536 : 43520;
    const int wb0 = hp ? KV0 + 10240 : 512, wb1 = hp ? KV0 + KVB + 10240 - 30720 : KV0 - 35840, wb2 = KV0 + KVB - 43520;
#pragma unroll
    for (int i = 0; i < 13; ++i) { const int L = qo * 12800 + i * 1024;
        if (i < 12 || lane < 32) *(LAS u32x4*)(smem + (L + (L < p1 ? wb0 : L < p2 ? wb1 : wb2)) + lane * 16) = wreg[i]; }
    lds_inc0(smem + N_WO + hp * 4, lane);
    STAMP(10);
    __builtin_amdgcn_s_sleep(PDELAY);
    if (qo < NPOLL) {
        for (int i = 0; i < qo; ++i) __builtin_amdgcn_s_sleep(PSTAG);
        poll_shared((GAS u64*)(ws + WS_FO + (((me & ~7) + (lane & 7)) * 2 + hp) * 128), lane < 8, tag, smem + N_OK + hp * 4);
    } else wait_word(smem + N_OK + hp * 4, 1);
    STAMP(11);
    f32x16 oc[3]; f32x4 ps[10];
    LAS char* red = smem + OWN + qo * 10240 + lane * 16;
    { bf16x8 af[5], of[20];
      const int la = (8 * hl + 2 * ((lane & 15) >> 2)) * 160 + (lane & 16) * 2 + (lane & 3) * 8;
      const int aa0 = wb0 + la, aa1 = (hp ? wb1 : wb0) + la, aa2 = wb1 + la, aa3 = (hp ? wb1 : wb2) + la;
      auto rda = [&](int f) { return trfrag(smem + (f < 36 ? aa0 : f < 42 ? aa1 : f < 51 ? aa2 : aa3) + (f / 3) * 2560 + (f % 3) * 64, 160); };
      auto ldo = [&](int k) { of[k] = __builtin_bit_cast(bf16x8, __builtin_amdgcn_raw_buffer_load_b128(orr, qo * 40960 + (hp * 20 + k) * 1024 + lane * 16, 0, 16)); };
#pragma unroll
      for (int k = 0; k < OPF; ++k) ldo(k);
      wait_word(smem + N_WO + hp * 4, 4);
#pragma unroll
      for (int f = 0; f < 4; ++f) af[f] = rda(f);
      SB();
      SFOR(f, 60)
          if constexpr (f % 3 == 0 && f / 3 + OPF < 20) ldo(f / 3 + OPF);
          if constexpr (f + 4 < 60) af[(f + 4) % 5] = rda(f + 4);
          if constexpr (f == 57) { if (hp) { wait_word(smem + C_RD + qo * 4, 1);
#pragma unroll
              for (int b = 0; b < 10; ++b) ps[b] = *(LAS f32x4*)(red + b * 1024); } }
          SB();
          if constexpr (f < 3) MFMA32Z(oc[f], af[f % 5], of[0]); else MFMA32I(oc[f % 3], af[f % 5], of[f / 3]);
          SB();
      SEND }
    MFMA_DRAIN();
    STAMP(12);
    { if (!hp) {
          wait_word(smem + N_G1, 8);
#pragma unroll
          for (int b = 0; b < 10; ++b) *(LAS f32x4*)(red + b * 1024) = (f32x4){oc[b >> 2][4 * (b & 3)], oc[b >> 2][4 * (b & 3) + 1], oc[b >> 2][4 * (b & 3) + 2], oc[b >> 2][4 * (b & 3) + 3]};
          lds_set(smem + C_RD + qo * 4, 1);
      } else {
          char* orow = out + (size_t)(rt * 128 + qo * 32 + r32) * 2560 + br * 1280 + hg * 160 + hl * 16;
#pragma unroll
          for (int b = 0; b < 10; ++b)
#pragma unroll
              for (int r = 0; r < 4; ++r) ps[b][r] += oc[b >> 2][4 * (b & 3) + r];
          SB();
          u32x2 pk[10];
#pragma unroll
          for (int b = 0; b < 10; ++b) pk[b] = (u32x2){pk2(ps[b][0], ps[b][1]), pk2(ps[b][2], ps[b][3])};
          SB();
          u32x4 st[5];
#pragma unroll
          for (int b = 0; b < 10; b += 2) {
              auto rx = __builtin_amdgcn_permlane32_swap(pk[b][0], pk[b + 1][0], false, false); auto ry = __builtin_amdgcn_permlane32_swap(pk[b][1], pk[b + 1][1], false, false);
              st[b >> 1] = (u32x4){rx[0], ry[0], rx[1], ry[1]}; }
          SB();
#pragma unroll
          for (int b = 0; b < 5; ++b) *(u32x4*)(orow + b * 32) = st[b];
      } }
    if (bx == 0 && wid == 0) {
#pragma unroll 1
        for (int i = 0; i < 4; ++i) poll_flags((GAS u64*)(ws + WS_FO + (i * 64 + lane) * 256), true, tag);
        if (lane == 0) __hip_atomic_store((GAS u64*)ws, tag, RLX_AGENT);
    }
}

extern "C" void kernel_launch(void* const* d_in, const int* in_sizes, int n_in, void* d_out, int out_size, void* d_ws, size_t ws_size,
                              hipStream_t stream) {
    fused_attn_kernel<<<256, 512, 0, stream>>>((const char*)d_in[0], (const char*)d_in[1], (const char*)d_in[2], (const char*)d_in[3],
                                               (const char*)d_in[4], (const char*)d_in[5], (char*)d_out, (char*)d_ws);
}
```

```cpp
#include <hip/hip_runtime.h>
#include <hip/hip_bf16.h>
#include <stdint.h>

typedef short bf16x8 __attribute__((ext_vector_type(8)));
typedef short s16x4 __attribute__((ext_vector_type(4)));
typedef float f32x2 __attribute__((ext_vector_type(2)));
typedef float f32x4 __attribute__((ext_vector_type(4)));
typedef float f32x16 __attribute__((ext_vector_type(16)));
typedef unsigned u32x4 __attribute__((ext_vector_type(4)));
typedef unsigned u32x2 __attribute__((ext_vector_type(2)));
typedef __bf16 bfx2 __attribute__((ext_vector_type(2)));
typedef unsigned long long u64;
#define LAS __attribute__((address_space(3)))
#define GAS __attribute__((address_space(1)))
#define DI __device__ __forceinline__
#define RLX_AGENT __ATOMIC_RELAXED, __HIP_MEMORY_SCOPE_AGENT
#define RLX_WG __ATOMIC_RELAXED, __HIP_MEMORY_SCOPE_WORKGROUP

constexpr int LDS_BYTES = 163840;
constexpr int VOFF = 20480, VP = 168;
constexpr int KVB = VOFF + 128 * VP;
constexpr int KV0 = LDS_BYTES - 3 * KVB;
constexpr int OWN = KV0 + 2 * KVB;
constexpr int C_F = 0, C_P = 128, C_E = 144, C_QF = 160, C_RD = 176, C_CNT = 192, C_TAG = 320;
constexpr int N_PUB = C_CNT, N_NB = C_CNT + 4, N_G0 = C_CNT + 8, N_G1 = C_CNT + 12, N_WO = C_CNT + 16, N_OS = C_CNT + 24, N_KV = C_CNT + 32, N_OK = C_CNT + 36;
constexpr int XS0 = 512, WS0 = XS0 + 3 * 16384, QST = 512, ONE = 336, QU27 = XS0 + 16384, QU28 = XS0, QU29 = QU27 + 10240;
static_assert(WS0 + 7 * 10240 <= OWN, "ring overlaps the own image");
constexpr unsigned WS_FKV = 4096, WS_FO = WS_FKV + 256 * 128, WS_KV = 131072;
constexpr unsigned KVG = KVB, OG = 4 * 40960;
constexpr unsigned WS_O = WS_KV + 256 * KVG;
constexpr unsigned WS_END = WS_O + 32 * OG;
constexpr unsigned SPIN_CAP = 400000, LSPIN_CAP = 4000000;
constexpr float QSCALE = 0.22360679774997896f * 1.4426950408889634f;
constexpr int WIN = 4;
constexpr int OPF = 12;
constexpr int PFLIP = 6;
constexpr int ADELAY = 3;

#define MFMA32(a, b, c) __builtin_amdgcn_mfma_f32_32x32x16_bf16((a), (b), (c), 0, 0, 0)
#define MFMA32K8(a, b, c) __builtin_amdgcn_mfma_f32_32x32x8bf16_1k((a), (b), (c), 0, 0, 0)
#define MFMA32I(acc, a, b) asm volatile("v_mfma_f32_32x32x16_bf16 %0, %1, %2, %0" : "+v"(acc) : "v"(a), "v"(b))
#define MFMA32Z(acc, a, b) asm volatile("v_mfma_f32_32x32x16_bf16 %0, %1, %2, 0" : "=&v"(acc) : "v"(a), "v"(b))
#define MFMA_DRAIN() asm volatile("s_nop 7\n\ts_nop 4" ::: "memory")
#define WAITV(N) asm volatile("s_waitcnt vmcnt(" #N ")" ::: "memory")
#define BAR_L() asm volatile("s_waitcnt lgkmcnt(0)\n\ts_barrier" ::: "memory")
#define SB() __builtin_amdgcn_sched_barrier(0)
#define FENCE() asm volatile("" ::: "memory")


template <int V> struct IC { static constexpr int value = V; };
template <int I, int N, class F> DI void static_for(F&& f) { if constexpr (I < N) { f(IC<I>{}); static_for<I + 1, N>(f); } }
#define SFOR(var, N) static_for<0, N>([&](auto ic_##var) __attribute__((always_inline)) { constexpr int var = decltype(ic_##var)::value;
#define SEND });

DI void dma16(unsigned voff, const char* sbase, unsigned ldsaddr) {
    asm volatile("s_mov_b32 m0, %2\n\ts_nop 0\n\tglobal_load_lds_dwordx4 %0, %1" :: "v"(voff), "s"(sbase), "s"(ldsaddr) : "memory");
}
DI void dma16x4(unsigned v0, unsigned v1, unsigned v2, unsigned v3, const char* sbase, unsigned ldsaddr) {
    asm volatile("s_mov_b32 m0, %5\n\ts_nop 0\n\tglobal_load_lds_dwordx4 %0, %4\n\tglobal_load_lds_dwordx4 %1, %4 offset:1024\n\t"
                 "global_load_lds_dwordx4 %2, %4 offset:2048\n\tglobal_load_lds_dwordx4 %3, %4 offset:3072"
                 :: "v"(v0), "v"(v1), "v"(v2), "v"(v3), "s"(sbase), "s"(ldsaddr) : "memory");
}
DI void dma16x5(const unsigned (&v)[5], const char* sbase, unsigned ldsaddr) {
    asm volatile("s_mov_b32 m0, %6\n\ts_nop 0\n\tglobal_load_lds_dwordx4 %0, %5 offset:-2048\n\tglobal_load_lds_dwordx4 %1, %5 offset:-1024\n\t"
                 "global_load_lds_dwordx4 %2, %5\n\tglobal_load_lds_dwordx4 %3, %5 offset:1024\n\tglobal_load_lds_dwordx4 %4, %5 offset:2048"
                 :: "v"(v[0]), "v"(v[1]), "v"(v[2]), "v"(v[3]), "v"(v[4]), "s"(sbase), "s"(ldsaddr) : "memory");
}
DI void dma16x5_sc1(unsigned voff, const char* sbase, unsigned ldsaddr) {
    asm volatile("s_mov_b32 m0, %2\n\ts_nop 0\n\tglobal_load_lds_dwordx4 %0, %1 offset:-2048 sc1\n\tglobal_load_lds_dwordx4 %0, %1 offset:-1024 sc1\n\t"
                 "global_load_lds_dwordx4 %0, %1 sc1\n\tglobal_load_lds_dwordx4 %0, %1 offset:1024 sc1\n\tglobal_load_lds_dwordx4 %0, %1 offset:2048 sc1"
                 :: "v"(voff), "s"(sbase), "s"(ldsaddr) : "memory");
}
DI void dma16_sc1(unsigned voff, const char* sbase, unsigned ldsaddr) {
    asm volatile("s_mov_b32 m0, %2\n\ts_nop 0\n\tglobal_load_lds_dwordx4 %0, %1 sc1" :: "v"(voff), "s"(sbase), "s"(ldsaddr) : "memory");
}
DI void dma4(const char* p, unsigned ldsaddr) {
    asm volatile("s_mov_b32 m0, %1\n\ts_nop 0\n\tglobal_load_lds_dword %0, off" :: "v"(p), "s"(ldsaddr) : "memory");
}
DI unsigned pk2(float lo, float hi) { f32x2 v = {lo, hi}; return __builtin_bit_cast(unsigned, __builtin_convertvector(v, bfx2)); }
typedef short v4i16_t __attribute__((ext_vector_type(4)));
DI bf16x8 trfrag(LAS char* p, int second) {
    s16x4 a = __builtin_bit_cast(s16x4, __builtin_amdgcn_ds_read_tr16_b64_v4i16((LAS v4i16_t*)p));
    s16x4 b = __builtin_bit_cast(s16x4, __builtin_amdgcn_ds_read_tr16_b64_v4i16((LAS v4i16_t*)(p + second)));
    return __builtin_shufflevector(a, b, 0, 1, 2, 3, 4, 5, 6, 7);
}
constexpr u64 lanes_cut(int r, bool fartile) {
    u64 m = 0;
    for (int l = 0; l < 64; ++l) { const int key = (r & 3) + 8 * (r >> 2) + 4 * (l >> 5), d = (l & 31) - key + (fartile ? 32 : 0);
        const bool ok = d >= 0 && d <= 14 && !(d & 1);
        if (!ok) m |= 1ull << l; }
    return m;
}
DI float halfmax(float m) { auto rr = __builtin_amdgcn_permlane32_swap(__float_as_uint(m), __float_as_uint(m), false, false);
    return __builtin_elementwise_maximum(__uint_as_float(rr[0]), __uint_as_float(rr[1])); }

DI void poll_flags(GAS u64* word, bool active, u64 tag) {
#pragma unroll 1
    for (unsigned spins = 0; spins < SPIN_CAP; ++spins) {
        u64 v = tag;
        if (active) v = __hip_atomic_load(word, RLX_AGENT);
        if (__all(v == tag)) break;
        __builtin_amdgcn_s_sleep(2);
    }
    __builtin_amdgcn_fence(__ATOMIC_ACQUIRE, "wavefront");
}
DI int min4(LAS char* p) {
    const u64 a = __hip_atomic_load((LAS u64*)p, RLX_WG), b = __hip_atomic_load((LAS u64*)(p + 8), RLX_WG);
    const unsigned m = min(min((unsigned)a, (unsigned)(a >> 32)), min((unsigned)b, (unsigned)(b >> 32)));
    return __builtin_amdgcn_readfirstlane((int)m);
}
DI int wait_min4(LAS char* p, int need) {
    int v = min4(p);
#pragma unroll 1
    for (unsigned spins = 0; v < need && spins < LSPIN_CAP; ++spins) { __builtin_amdgcn_s_sleep(1); v = min4(p); }
    FENCE();
    return v;
}
DI void wait_word(LAS char* p, unsigned need) {
#pragma unroll 1
    for (unsigned spins = 0; spins < LSPIN_CAP; ++spins) {
        if ((unsigned)__builtin_amdgcn_readfirstlane((int)__hip_atomic_load((LAS unsigned*)p, RLX_WG)) >= need) break;
        __builtin_amdgcn_s_sleep(1); }
    FENCE();
}
DI void lds_inc0(LAS char* p, int lane) { FENCE(); if (lane == 0) (void)__hip_atomic_fetch_add((LAS unsigned*)p, 1u, RLX_WG); FENCE(); }
DI unsigned lds_inc(LAS char* p, int lane) { FENCE(); unsigned old = 0; if (lane == 0) old = __hip_atomic_fetch_add((LAS unsigned*)p, 1u, RLX_WG); FENCE(); return old; }
DI void lds_set(LAS char* p, unsigned v) { FENCE(); __hip_atomic_store((LAS unsigned*)p, v, RLX_WG); FENCE(); }

__global__ __launch_bounds__(512, 2) void fused_attn_kernel(const char* __restrict__ xA, const char* __restrict__ wA, const char* __restrict__ oA,
                                                           const char* __restrict__ xB, const char* __restrict__ wB, const char* __restrict__ oB,
                                                           char* __restrict__ out, char* __restrict__ ws) {
    __shared__ __attribute__((aligned(16))) char smem_[LDS_BYTES];
    LAS char* smem = (LAS char*)smem_;
    const unsigned lds0 = (unsigned)(uintptr_t)smem;
    asm volatile("" :: "s"(xA), "s"(wA), "s"(oA), "s"(xB), "s"(wB), "s"(oB), "s"(out), "s"(ws));
    const int tid = threadIdx.x, lane = tid & 63;
    const int wid = __builtin_amdgcn_readfirstlane(tid >> 6);
    const int bx = blockIdx.x, xcd = bx & 7, idx = bx >> 3;
    const int isA = idx >> 4, rt = (xcd >> 1) * 4 + ((idx >> 2) & 3), hg = (xcd & 1) * 4 + (idx & 3);
    const int br = isA ? 0 : 1;
    const int k0 = isA ? (((rt & 3) * 5 + 1) / 2 + ((hg & 3) * 5 + 1) / 2) % 10 : 0;
    const char* xb = (isA ? xA : xB) + (size_t)rt * (128 * 1280);
    const char* wb = (isA ? wA : wB) + hg * 160;
    const int me = (br * 16 + rt) * 8 + hg;
    const int r32 = lane & 31, hl = lane >> 5;
    auto kt_of = [&](int s) { int k = s + k0; return k >= 10 ? k - 10 : k; };
    if (isA && wid < 4) {
        const int j16 = idx & 15; const unsigned dump = lds0 + OWN + wid * 512;
        const char* wl = wB + (hg >> 2) * 640 + j16 * (4 * 3840);
        const char* xl = xB + (size_t)(rt * 128 + (hg & 3) * 32 + r32) * 1280 + hl * 128;
        auto pfx = [&](int i) { return xl + i * 256; };
        auto pfkv = [&](int i) { int n = i * 64 + lane; n = n < 400 ? n : 399; const int sl = n / 40, r = n % 40, c = r % 10;
            return wl + (sl * 64 + r / 10) * 3840 + 1280 + (c < 5 ? c * 128 : 640 + c * 128); };
        auto pfq = [&](int i) { int n = i * 64 + lane; n = n < 200 ? n : 199; int sl = n / 20 + 8; sl = sl >= 10 ? sl - 10 : sl; const int r = n % 20;
            return wl + (sl * 64 + r / 5) * 3840 + (r % 5) * 128; };
        dma4(wid & 1 ? pfx(wid >> 1) : pfkv(wid >> 1), dump);
        dma4(wid & 1 ? pfx(2 + (wid >> 1)) : pfkv(2 + (wid >> 1)), dump);
        dma4(wid < 3 ? pfkv(4 + wid) : pfx(4), dump);
        dma4(pfq(wid), dump);
        { int n = wid * 64 + lane; n = n < 200 ? n : 199; const int o = (j16 * 40 + n / 5) * 1280 + (hg >> 2) * 640 + (n % 5) * 128;
          dma4(oB + o, dump); dma4(oA + o, dump); }
    }
    if (isA && wid >= 4) { for (int i = 0; i < ADELAY; ++i) __builtin_amdgcn_s_sleep(16); }

    const unsigned xo = (unsigned)((lane >> 3) * 1280 + (((lane & 7) ^ (lane >> 4)) << 4));
    auto w_off = [&](int q) { const int c = q * 64 + lane, row = __umul24(c, 3277) >> 16, j = c - row * 20; return (unsigned)(__umul24(row, 3840) + j * 16 + (j < 10 ? 1280 : 2400)); };
    if (!isA) { const char* xs = xb + k0 * 128 + wid * (16 * 1280);
      dma16(xo, xs, lds0 + XS0 + wid * 2048);
      dma16(xo ^ 64, xs + 8 * 1280, lds0 + XS0 + wid * 2048 + 1024);
      const char* wsrc = wb + (size_t)k0 * (64 * 3840);
      const unsigned wv = w_off(wid < 5 ? wid : wid - 5);
      if (wid < 5) { dma16(wv, wsrc, lds0 + WS0 + wid * 1024); dma16(wv, wsrc + 16 * 3840, lds0 + WS0 + (5 + wid) * 1024);
          if (wid >= 3) { dma16(wv, wsrc + 32 * 3840, lds0 + WS0 + (10 + wid) * 1024); dma16(wv, wsrc + 48 * 3840, lds0 + WS0 + (15 + wid) * 1024); } }
      else { dma16(wv, wsrc + 32 * 3840, lds0 + WS0 + (5 + wid) * 1024); dma16(wv, wsrc + 48 * 3840, lds0 + WS0 + (10 + wid) * 1024); } }
    if (wid == 4 && lane < 2) dma4(ws + lane * 4, lds0 + C_TAG);
    if (tid < 80) *(LAS unsigned*)(smem + tid * 4) = 0u;
    else if (tid < 82) *(LAS unsigned*)(smem + ONE + (tid - 80) * 4) = 0x3F803F80u;
    const int nnb = isA ? (rt > 0 ? 1 : 0) : (rt > 1 ? 2 : rt);
    auto kq_base = [&](int fg) { const int hd = fg / 5, dg = fg % 5; return dg < 4 ? hd * 5120 + (dg >> 1) * 2048 + (dg & 1) * 8 : hd * 5120 + 4096; };

    auto rdtag = [&]() { const u64 t = *(LAS u64*)(smem + C_TAG);
        const u64 e = ((u64)(unsigned)__builtin_amdgcn_readfirstlane((int)(t >> 32)) << 32) | (unsigned)__builtin_amdgcn_readfirstlane((int)t);
        return e * 6364136223846793005ull + 1442695040888963407ull; };
    const __amdgpu_buffer_rsrc_t wor = __builtin_amdgcn_make_buffer_rsrc((void*)(isA ? oA : oB), 0, 640 * 1280, 0x00020000);

    const int hp = wid >> 2, qo = wid & 3, qi = hp ? 3 - qo : qo;
    const int ud = qi + 8;
    const bool far = isA ? (4 * rt + qi >= 1) : (4 * rt + qi >= 8);
    const int u0 = isA ? (far ? ud - 1 : ud) : (far ? qi : 8 - 4 * rt);
    f32x16 cd, cf;
    int dsh, kb1, kb2, nh, vb0, vb1, qb1, qb2; unsigned obase, oo[5];
    auto mask_b = [&](f32x16& c, int r, bool fartile) { const int k = (r & 3) + 8 * (r >> 2); c[r] = __int_as_float(fartile ? ((k - 1) << 23) - dsh : dsh - (k << 23)); };
    auto mask_a = [&](int z) {
      SFOR(r, 16)
          constexpr u64 ad = lanes_cut(r, false), af = lanes_cut(r, true);
          cd[r] = __builtin_amdgcn_inverse_ballot_w64(ad + z) ? -__builtin_inff() : 0.f;
          cf[r] = __builtin_amdgcn_inverse_ballot_w64(af + z) ? -__builtin_inff() : 0.f;
      SEND };
    auto att_setup = [&](int part) {
        int l = tid; asm volatile("" : "+v"(l)); l &= 63;
        const int q = l & 31, h = l >> 5;
        if (part == 0) { dsh = (q - 4 * h) << 23; kb1 = KV0 + hp * 10240 + h * 2048 + q * 16; kb2 = h ? ONE : KV0 + hp * 10240 + 4096 + q * 8; nh = h ^ 1;
            asm volatile("" : "+v"(dsh), "+v"(kb1), "+v"(kb2), "+v"(nh)); }
        if (part == 1) { const bool ones = (l & 16) && (l & 3);
            vb0 = KV0 + VOFF + (4 * h + ((l & 15) >> 2)) * VP + (ones ? 160 : hp * 80 + (l & 16) * 2 + (l & 3) * 8); vb1 = vb0 + (ones ? 0 : 40);
            asm volatile("" : "+v"(vb0), "+v"(vb1)); }
        if (part == 2) { qb1 = QST + hp * 10240 + h * 2048 + (qi * 32 + q) * 16; qb2 = QST + hp * 10240 + 4096 + (qi * 32 + q) * 8;
            obase = (unsigned)(qi * 40960 + hp * 20480 + hg * 2560 + q * 16 + h * 512);
            asm volatile("" : "+v"(qb1), "+v"(qb2), "+v"(obase)); }
        if (part >= 3) {
#pragma unroll
            for (int r = 4 * (part - 3); r < 4 * (part - 2); ++r) mask_b(cd, r, false);
            asm volatile("" : "+v"(cd)); } };
    auto wo_setup = [&](int q) {
        int l = tid; asm volatile("" : "+v"(l)); const int c = q * 64 + (l & 63), rho = __umul24(c, 6554) >> 16;
        oo[q] = (unsigned)(__umul24(qo * 160 + hp * 40 + (rho & ~7) + 4 * (rho & 1) + ((rho >> 1) & 3), 1280) + hg * 160 + (c - rho * 10) * 16);
        asm volatile("" : "+v"(oo[q])); };
    u64 tag;

    if (wid < 4) {
        if (!isA) WAITV(0);
        BAR_L();
        bf16x8 xr[32], xt[2], wf[WIN + 1];
        f32x16 acc[5];
        const int tok = wid * 32 + r32;
        const int xa = XS0 + tok * 128 + ((hl ^ ((tok >> 1) & 7)) << 4);
        int wa0 = WS0 + (8 * hl + ((lane & 15) >> 2)) * 320 + (lane & 16) * 2 + (lane & 3) * 8, wa1 = wa0 + 61440;
        asm volatile("" : "+v"(wa0), "+v"(wa1));
        auto rdx = [&](int i) { return *(LAS bf16x8*)(smem + ((i >> 2) % 3) * 16384 + (xa ^ ((i & 3) * 32))); };
        auto rdw = [&](int f) { const int i = f / 5, t = f % 5, sl = (i >> 1) % 7;
            return trfrag(smem + (sl < 6 ? wa0 + sl * 10240 : wa1) + (i & 1) * 5120 + t * 64, 1280); };
        unsigned fl = 0;
        auto rdflag = [&](int u) { fl = __hip_atomic_load((LAS unsigned*)(smem + C_F + u * 4), RLX_WG); };
        auto check = [&](int u) {
            if (__builtin_amdgcn_readfirstlane(fl) != 15) {
#pragma unroll 1
                for (unsigned spins = 0; spins < LSPIN_CAP; ++spins) { rdflag(u); if (__builtin_amdgcn_readfirstlane(fl) == 15) break; __builtin_amdgcn_s_sleep(1); }
            }
            FENCE(); };
#pragma unroll
        for (int i = 0; i < 4; ++i) xr[i] = rdx(i);
#pragma unroll
        for (int f = 0; f < WIN; ++f) wf[f] = rdw(f);
        SB();
        SFOR(f, 200)
            constexpr int i = f / 5, t = f % 5;
            if constexpr (f % 20 == 10 - WIN) lds_set(smem + C_P + wid * 4, (i >> 1) + 1);
            if constexpr (f == 200 - WIN) lds_set(smem + C_P + wid * 4, 20);
            if constexpr (f % 20 == 10) rdflag(f / 20 + 1);
            if constexpr (f % 20 == 20 - WIN && f < 190) check(f / 20 + 1);
            if constexpr (f + WIN < 200) wf[(f + WIN) % (WIN + 1)] = rdw(f + WIN);
            if constexpr (f % 20 >= 16 && f < 140) xr[(f / 20 + 1) * 4 + f % 20 - 16] = rdx((f / 20 + 1) * 4 + f % 20 - 16);
            if constexpr (t == 1 && i >= 31 && i < 39) xt[(i + 1) & 1] = rdx(i + 1);
            SB();
            if constexpr (i == 0) MFMA32Z(acc[t], wf[f % (WIN + 1)], xr[0]);
            else if constexpr (i < 32) MFMA32I(acc[t], wf[f % (WIN + 1)], xr[i]);
            else MFMA32I(acc[t], wf[f % (WIN + 1)], xt[i & 1]);
            SB();
        SEND
        int l2 = tid; asm volatile("" : "+v"(l2)); l2 &= 63;
        const int h2 = l2 >> 5, tk = wid * 32 + (l2 & 31);
        int eb[5];
        auto ebases = [&](int base) { int l = tid; asm volatile("" : "+v"(l)); const int h = (l >> 5) & 1, k = wid * 32 + (l & 31);
            eb[0] = base + k * 16 + h * 8; eb[1] = base + k * 16 + h * 2040; eb[2] = base + (h ? k * 16 + 1024 : k * 8); eb[3] = base + (h ? k * 8 + 2040 : k * 16);
            asm volatile("" : "+v"(eb[0]), "+v"(eb[1]), "+v"(eb[2]), "+v"(eb[3])); };
        auto kq_addr = [&](int g0) { return eb[g0 % 5 == 1 ? 1 : g0 % 5 == 4 ? 2 : g0 % 5 == 3 ? 3 : 0] + kq_base(g0); };
        ebases(OWN);
        eb[4] = OWN + VOFF + tk * VP + h2 * 8; asm volatile("" : "+v"(eb[4]));
        int qa0 = (8 * h2 + 2 * ((l2 & 15) >> 2)) * 160 + (l2 & 16) * 2 + (l2 & 3) * 8, qa1 = qa0 + WS0 + 10240;
        asm volatile("" : "+v"(qa0), "+v"(qa1));
        auto rdq = [&](int f) { const int i = f / 3, t = f % 3, j = i >> 2, sl = (20 + j) % 7;
            return trfrag(smem + (j >= 7 ? qa0 + (j == 7 ? QU27 : j == 8 ? QU28 : QU29) : sl == 0 ? qa0 + WS0 : qa1 + (sl - 1) * 10240) + (i & 3) * 2560 + t * 64, 160); };
        MFMA_DRAIN();
        WAITV(0);
        { u32x2 pk[20];
#pragma unroll
          for (int n = 0; n < 20; ++n) { const int t = n >> 2, b = n & 3; pk[n] = (u32x2){pk2(acc[t][4 * b], acc[t][4 * b + 1]), pk2(acc[t][4 * b + 2], acc[t][4 * b + 3])}; }
          SB();
#pragma unroll
          for (int n = 0; n < 20; ++n) *(LAS u32x2*)(smem + (n < 10 ? kq_addr(2 * n) : eb[4] + (2 * n - 20) * 8)) = pk[n];
          if (h2 == 0) *(LAS u32x2*)(smem + eb[4] + 160) = (u32x2){0x3F803F80u, 0x3F803F80u}; }
        lds_set(smem + C_E + wid * 4, 1);
        check(10);
        xt[0] = rdx(32);
#pragma unroll
        for (int f = 0; f < WIN; ++f) wf[f] = rdq(f);
        SB();
        u64 pv0 = 0, pv1 = 0;
        SFOR(f, 120)
            constexpr int i = f / 3, t = f % 3, j = i >> 2;
            if constexpr (f % 24 == 12 && j < 9) rdflag(11 + j / 2);
            if constexpr (f % 12 == 12 - WIN && (j == 1 || j == 6 || j == 8)) lds_set(smem + C_P + wid * 4, 21 + j);
            if constexpr (f % 24 == 24 - WIN && j < 9) check(11 + j / 2);
            if constexpr (f + WIN < 120) wf[(f + WIN) % (WIN + 1)] = rdq(f + WIN);
            if constexpr (t == 0 && i < 7) xt[(i + 1) & 1] = rdx(32 + i + 1);
            if constexpr (f >= 90 && f < 100 && !(f & 1)) wo_setup((f - 90) / 2);
            if constexpr (f == 99) tag = rdtag();
            if constexpr (f >= 100 && f < 107) att_setup(f - 100);
            if constexpr (f == 108) ebases(QST);
            if constexpr (f == 116) { pv0 = __hip_atomic_load((LAS u64*)(smem + C_P), RLX_WG); pv1 = __hip_atomic_load((LAS u64*)(smem + C_P + 8), RLX_WG); }
            SB();
            if constexpr (i == 0) MFMA32Z(acc[t], wf[f % (WIN + 1)], xt[0]);
            else if constexpr (i < 8) MFMA32I(acc[t], wf[f % (WIN + 1)], xt[i & 1]);
            else MFMA32I(acc[t], wf[f % (WIN + 1)], xr[i - 8]);
            SB();
        SEND
        MFMA_DRAIN();
        SB();
        if (__builtin_amdgcn_readfirstlane((int)min(min((unsigned)pv0, (unsigned)(pv0 >> 32)), min((unsigned)pv1, (unsigned)(pv1 >> 32)))) < 29) wait_min4(smem + C_P, 29);
        { acc[0] *= QSCALE; acc[1] *= QSCALE; acc[2] *= QSCALE;
          SB();
          u32x2 pk[10];
#pragma unroll
          for (int n = 0; n < 10; ++n) { const int t = n >> 2, b = n & 3; pk[n] = (u32x2){pk2(acc[t][4 * b], acc[t][4 * b + 1]), pk2(acc[t][4 * b + 2], acc[t][4 * b + 3])}; }
          SB();
#pragma unroll
          for (int n = 0; n < 10; ++n) *(LAS u32x2*)(smem + kq_addr(2 * n)) = pk[n]; }
        lds_set(smem + C_QF + wid * 4, 1);
        __builtin_amdgcn_s_setprio(1);
    } else {
        const int hw = wid - 4;
        int known = 0;
        __builtin_amdgcn_s_setprio(3);
        auto lds_or = [&](int off, unsigned v) { FENCE(); if (lane == 0) __hip_atomic_fetch_or((LAS unsigned*)(smem + off), v, RLX_WG); FENCE(); };
        unsigned wo[5], qo[5];
        auto done = [&](int n) { lds_or(C_F + n * 4, 1u << hw); };
        const unsigned xo1 = (xo ^ 64) + 9216, xo2 = xo + 2 * 9216, xo3 = (xo ^ 64) + 3 * 9216;
        auto round_x = [&](int n) { dma16x4(xo, xo1, xo2, xo3, xb + kt_of(n) * 128 + hw * (32 * 1280), lds0 + XS0 + (n % 3) * 16384 + hw * 4096); };
        auto round_w = [&](int n) {
            const int u = 2 * n + (hw >> 1);
            const unsigned dst = lds0 + (u < 27 ? WS0 + (u % 7) * 10240 : u == 27 ? QU27 : u == 28 ? QU28 : QU29) + (hw & 1) * 5120 + 2048;
            if (u < 20) dma16x5(wo, wb + (size_t)(kt_of(u >> 1) * 64 + (u & 1) * 32) * 3840 - 2048, dst);
            else dma16x5(qo, wb + (size_t)kt_of(u < 22 ? u - 12 : u - 22) * (64 * 3840) - 2048, dst); };
        if (isA) round_x(0);
        round_x(1);
#pragma unroll
        for (int q = 0; q < 5; ++q) wo[q] = w_off(q) + (hw & 1) * (16 * 3840) + (4 - q) * 1024;
        if (isA) { round_w(0); round_w(1); WAITV(5); } else { round_w(1); WAITV(9); }
        BAR_L();
#pragma unroll
        for (int q = 0; q < 5; ++q) { const int c = q * 64 + lane, rho = __umul24(c, 6554) >> 16;
            qo[q] = (unsigned)(__umul24((hw & 1) * 32 + (rho & ~7) + 4 * (rho & 1) + ((rho >> 1) & 3), 3840) + (c - rho * 10) * 16 + (4 - q) * 1024); }
#pragma unroll 1
        for (int n = 2; n <= 13; ++n) {
            const int need = n < 13 ? 2 * n - 5 : 20;
            if (need > known) known = min4(smem + C_P);
            if (need > known) { WAITV(0); done(n - 1); known = wait_min4(smem + C_P, need); }
            if (n < 10) round_x(n);
            round_w(n);
            if (n < 10) WAITV(9); else WAITV(5);
            done(n - 1);
        }
        WAITV(0); done(13);
        tag = rdtag();
        auto a_chunk = [&](int a) { int o;
            if (a < 320) { const int hd = a / 80, rem = a % 80; o = hd * 5120 + (rem < 32 ? (96 + rem) * 16 : rem < 64 ? 2048 + (64 + rem) * 16 : 4096 + 768 + (rem - 64) * 16); }
            else o = VOFF + 96 * VP + (a - 320) * 16;
            return o; };
        if (rt < 15) {
            const __amdgpu_buffer_rsrc_t kvr = __builtin_amdgcn_make_buffer_rsrc(ws + WS_KV + me * KVG, 0, KVB, 0x00020000);
            wait_min4(smem + C_E, 1);
            if (!isA) { u32x4 v[11];
#pragma unroll
                for (int j = 0; j < 11; ++j) if (j < 10 || hw == 3) v[j] = *(LAS u32x4*)(smem + OWN + hw * 10240 + j * 1024 + lane * 16);
#pragma unroll
                for (int j = 0; j < 11; ++j) if (j < 10 || hw == 3) __builtin_amdgcn_raw_buffer_store_b128(v[j], kvr, hw * 10240 + j * 1024 + lane * 16, 0, 16);
            } else {
#pragma unroll
                for (int j = 0; j < 3; ++j) { const int a = (4 * j + hw) * 64 + lane;
                    if (a < 656) { const int o = a_chunk(a); __builtin_amdgcn_raw_buffer_store_b128(*(LAS u32x4*)(smem + OWN + o), kvr, o, 0, 16); } }
            }
        }
        wait_min4(smem + C_P, 22);
        round_w(14);
        WAITV(5);
        if (rt < 15) {
            unsigned old = 0;
            if (lane == 0) old = __hip_atomic_fetch_add((LAS unsigned*)(smem + N_PUB), 1u, RLX_WG);
            if (lane == 0 && old == 3) __hip_atomic_store((GAS u64*)(ws + WS_FKV + me * 128), tag, RLX_AGENT);
        }
        WAITV(0); done(14);
        __builtin_amdgcn_s_setprio(0);
#pragma unroll
        for (int p = 0; p < 7; ++p) att_setup(p);
#pragma unroll
        for (int q = 0; q < 5; ++q) wo_setup(q);
        __builtin_amdgcn_s_setprio(3);
        if (hw == 0) { poll_flags((GAS u64*)(ws + WS_FKV + (me - 8 * (lane + 1)) * 128), lane < nnb, tag); lds_set(smem + N_KV, 1); }
        else wait_word(smem + N_KV, 1);
        if (!isA) {
            const int n = hw >> 1, half = hw & 1;
            if (n < nnb) {
                const char* src = ws + WS_KV + (me - 8 * (n + 1)) * KVG + half * 20480; const unsigned dst = lds0 + KV0 + (1 - n) * KVB + half * 20480;
                wait_min4(smem + C_P, 27);
#pragma unroll
                for (int j = 0; j < 4; ++j) dma16x5_sc1(lane * 16, src + j * 5120 + 2048, dst + j * 5120 + 2048);
                if (half) dma16_sc1(lane * 16, src + 20480, dst + 20480);
            }
        } else if (nnb) {
            const __amdgpu_buffer_rsrc_t nbr = __builtin_amdgcn_make_buffer_rsrc(ws + WS_KV + (me - 8) * KVG, 0, KVB, 0x00020000);
            u32x4 v[3];
#pragma unroll
            for (int j = 0; j < 3; ++j) { const int a = (4 * j + hw) * 64 + lane; if (a < 656) v[j] = __builtin_amdgcn_raw_buffer_load_b128(nbr, a_chunk(a), 0, 16); }
            wait_min4(smem + C_P, 27);
#pragma unroll
            for (int j = 0; j < 3; ++j) { const int a = (4 * j + hw) * 64 + lane; if (a < 656) *(LAS u32x4*)(smem + KV0 + KVB + a_chunk(a)) = v[j]; }
        }
        __builtin_amdgcn_s_setprio(2);
        wait_word(smem + C_QF + qi * 4, 1);
    }
    u32x4 wreg[13];
    auto ldwo = [&](int i) { wreg[i] = __builtin_amdgcn_raw_buffer_load_b128(wor, oo[i % 5] + (i / 5) * (32 * 1280) + (i > 6 || (i == 6 && lane >= 16) ? 40 * 1280 : 0), 0, 0); };
    u32x2 ob[2][3];
    {
        bf16x8 q1[2]; u32x2 q2[2];
#pragma unroll
        for (int e = 0; e < 2; ++e) {
            q1[e] = *(LAS bf16x8*)(smem + qb1 + e * 5120);
            q2[e] = *(LAS u32x2*)(smem + qb2 + e * 5120);
            if (hl) q2[e] = (u32x2){0u, 0u};
        }
        if (isA) mask_a(0);
        struct KF { bf16x8 a; s16x4 b; };
        struct VF { bf16x8 a, b; };
        auto rdk = [&](int u, int e) { const int so = (u >> 2) * KVB, tw = u & 3; KF k;
            k.a = *(LAS bf16x8*)(smem + kb1 + so + tw * 512 + e * 5120); k.b = *(LAS s16x4*)(smem + __umul24(so + tw * 256 + e * 5120, nh) + kb2); return k; };
        auto rdv = [&](int u, int e) { LAS char* p = smem + (e ? vb1 : vb0) + (u >> 2) * KVB + (u & 3) * (32 * VP); VF v;
            v.a = trfrag(p, 8 * VP); v.b = trfrag(p + 16 * VP, 8 * VP); return v; };
        auto qk = [&](const KF& k, int e, const f32x16& c) { return MFMA32K8(k.b, __builtin_bit_cast(s16x4, q2[e]), MFMA32(k.a, q1[e], c)); };
        const f32x16 zero = {};
        auto max16 = [&](const f32x16& s, float m) {
#pragma unroll
            for (int r = 0; r < 16; ++r) m = __builtin_elementwise_maximum(m, s[r]);
            return m; };
        auto set_ref = [&](float m, int e) {
            const unsigned t0 = pk2(-m, 0.f); const float r1 = -m - __uint_as_float(t0 << 16);
            const unsigned t1 = pk2(r1, 0.f); const float r2 = r1 - __uint_as_float(t1 << 16);
            if (hl) q2[e] = (u32x2){t0 | (t1 << 16), pk2(r2, 0.f)}; };
        auto ex4 = [&](f32x16& s, int b) {
#pragma unroll
            for (int r = 0; r < 4; ++r) s[b + r] = __builtin_amdgcn_exp2f(s[b + r]); };
        auto pk8 = [&](const f32x16& s, int b) { return __builtin_bit_cast(bf16x8, (u32x4){pk2(s[b], s[b + 1]), pk2(s[b + 2], s[b + 3]), pk2(s[b + 4], s[b + 5]), pk2(s[b + 6], s[b + 7])}); };
        auto phase = [&](auto nx, f32x16& cur, const VF& v, f32x16& o, f32x16& nxt, const KF& k, int e) __attribute__((always_inline)) {
            constexpr int NX = decltype(nx)::value;
            SB(); ex4(cur, 0); SB();
            if constexpr (NX) nxt = MFMA32(k.a, q1[e], NX == 1 ? zero : NX == 2 ? cf : cd);
            SB(); ex4(cur, 4); { const bf16x8 p = pk8(cur, 0); SB(); o = MFMA32(v.a, p, o); }
            SB(); ex4(cur, 8); SB();
            if constexpr (NX) nxt = MFMA32K8(k.b, __builtin_bit_cast(s16x4, q2[e]), nxt);
            SB(); ex4(cur, 12); { const bf16x8 p = pk8(cur, 8); SB(); o = MFMA32(v.b, p, o); }
            SB(); };
        f32x16 o0, o1, sa, sb; KF kx; VF vx;
        auto step = [&](auto m0, auto m1, int u) __attribute__((always_inline)) {
            constexpr int M1 = decltype(m1)::value;
            KF ky; if constexpr (M1) ky = rdk(u - 1, 0);
            const VF vy = rdv(u, 1);
            phase(m0, sa, vx, o0, sb, kx, 1);
            if constexpr (M1) { kx = rdk(u - 1, 1); vx = rdv(u - 1, 0); }
            phase(m1, sb, vy, o1, sa, ky, 0); };
        auto nbwait = [&](int u) { if (u == 8) wait_word(smem + N_NB, 4); };
        { const KF k0f = rdk(ud, 0); kx = rdk(ud, 1); vx = rdv(ud, 0);
          const f32x16 s0 = qk(k0f, 0, cd), s1 = qk(kx, 1, cd);
          set_ref(halfmax(max16(s0, s0[0])), 0); set_ref(halfmax(max16(s1, s1[0])), 1);
          sa = qk(k0f, 0, cd); }
        if (hp) { WAITV(0); lds_inc0(smem + N_NB, lane); lds_inc0(smem + N_G0, lane); }
        o0 = zero; o1 = zero;
        if (u0 + 8 == ud) {
            SFOR(it, 9)
                if constexpr (it < 7) { ldwo(2 * it); if constexpr (it < 6) ldwo(2 * it + 1); }
                if constexpr (it == PFLIP) { if (hp) __builtin_amdgcn_s_setprio(0); }
                if constexpr (it == 7) {
#pragma unroll
                    for (int r = 0; r < 16; ++r) mask_b(cf, r, true); }
                if constexpr (it < 4) nbwait(ud - it);
                step(IC<it == 0 ? 3 : it == 8 ? 2 : 1>{}, IC<it == 8 ? 0 : it == 7 ? 2 : 1>{}, ud - it);
            SEND
        } else {
            __builtin_amdgcn_s_setprio(0);
#pragma unroll
            for (int i = 0; i < 13; ++i) ldwo(i);
            if (!isA) cf = zero;
            if (u0 < ud) {
                nbwait(ud);
                if (u0 + 1 < ud) {
                    step(IC<3>{}, IC<1>{}, ud);
#pragma unroll 1
                    for (int u = ud - 1; u > u0 + 1; --u) { nbwait(u); step(IC<1>{}, IC<1>{}, u); }
                    nbwait(u0 + 1);
                    step(IC<1>{}, IC<2>{}, u0 + 1);
                } else step(IC<3>{}, IC<2>{}, ud);
                step(IC<2>{}, IC<0>{}, u0);
            } else step(IC<3>{}, IC<0>{}, ud);
        }
        if (!__all(o0[12] < 1.152921504606847e18f && o1[12] < 1.152921504606847e18f && o0[12] > 0.25f && o1[12] > 0.25f)) {
            if (isA) { int z = 0; asm volatile("" : "+s"(z)); mask_a(z); }
            else { asm volatile("" : "+v"(dsh));
#pragma unroll
                for (int r = 0; r < 16; ++r) { mask_b(cd, r, false); mask_b(cf, r, true); }
                if (!far) cf = zero; }
#pragma unroll
            for (int e = 0; e < 2; ++e) {
                if (hl) q2[e] = (u32x2){0u, 0u};
                auto tile = [&](int u) { return u == ud ? qk(rdk(u, e), e, cd) : u == u0 ? qk(rdk(u, e), e, cf) : qk(rdk(u, e), e, zero); };
                float m = -__builtin_inff();
#pragma unroll 1
                for (int u = u0; u <= ud; ++u) m = max16(tile(u), m);
                m = halfmax(m);
                f32x16 o = zero;
#pragma unroll 1
                for (int u = u0; u <= ud; ++u) { f32x16 t = tile(u) - m;
                    const VF v = rdv(u, e); ex4(t, 0); ex4(t, 4); ex4(t, 8); ex4(t, 12);
                    o = MFMA32(v.a, pk8(t, 0), o); o = MFMA32(v.b, pk8(t, 8), o); }
                if (e) o1 = o; else o0 = o;
            }
        }
        const float rl0 = __builtin_amdgcn_rcpf(o0[12]), rl1 = __builtin_amdgcn_rcpf(o1[12]);
        SB();
#pragma unroll
        for (int r = 0; r < 12; ++r) { o0[r] *= rl0; o1[r] *= rl1; }
        SB();
#pragma unroll
        for (int j = 0; j < 3; ++j) {
            ob[0][j] = (u32x2){pk2(o0[4 * j], o0[4 * j + 1]), pk2(o0[4 * j + 2], o0[4 * j + 3])};
            ob[1][j] = (u32x2){pk2(o1[4 * j], o1[4 * j + 1]), pk2(o1[4 * j + 2], o1[4 * j + 3])}; }
        SB();
    }
    const __amdgpu_buffer_rsrc_t orr = __builtin_amdgcn_make_buffer_rsrc(ws + WS_O + (br * 16 + rt) * OG, 0, 4 * 40960, 0x00020000);
    {
      { auto rx = __builtin_amdgcn_permlane32_swap(ob[0][0][0], ob[0][1][0], false, false);
        auto ry = __builtin_amdgcn_permlane32_swap(ob[0][0][1], ob[0][1][1], false, false);
        __builtin_amdgcn_raw_buffer_store_b128((u32x4){rx[0], ry[0], rx[1], ry[1]}, orr, obase, 0, 16); }
      if (hl == 0) __builtin_amdgcn_raw_buffer_store_b128((u32x4){ob[0][2][0], ob[0][2][1], ob[1][0][0], ob[1][0][1]}, orr, obase + 1024, 0, 16);
      { auto fx = __builtin_amdgcn_permlane32_swap(ob[1][0][0], ob[1][1][0], false, false);
        auto fy = __builtin_amdgcn_permlane32_swap(ob[1][0][1], ob[1][1][1], false, false);
        auto sx = __builtin_amdgcn_permlane32_swap(ob[1][1][0], ob[1][2][0], false, false);
        auto sy = __builtin_amdgcn_permlane32_swap(ob[1][1][1], ob[1][2][1], false, false);
        __builtin_amdgcn_raw_buffer_store_b128((u32x4){fx[1], fy[1], sx[0], sy[0]}, orr, obase + 1536, 0, 16); } }
    if (lane == 0) { (void)__hip_atomic_fetch_add((LAS unsigned*)(smem + N_G1), 1u, RLX_WG);
        if (!hp) (void)__hip_atomic_fetch_add((LAS unsigned*)(smem + N_G0), 1u, RLX_WG); }
    FENCE();
    auto signal_o = [&]() {
        if (lds_inc(smem + N_OS + hp * 4, lane) == 3 && lane == 0) __hip_atomic_store((GAS u64*)(ws + WS_FO + (me * 2 + hp) * 128), tag, RLX_AGENT); };
    __builtin_amdgcn_s_setprio(0);
    WAITV(0); signal_o();
    wait_word(smem + (hp ? N_G1 : N_G0), 8);
    const int p1 = hp ? 30720 : 35840, p2 = hp ? 65536 : 43520;
    const int wb0 = hp ? KV0 + 10240 : 512, wb1 = hp ? KV0 + KVB + 10240 - 30720 : KV0 - 35840, wb2 = KV0 + KVB - 43520;
#pragma unroll
    for (int i = 0; i < 13; ++i) { const int L = qo * 12800 + i * 1024;
        if (i < 12 || lane < 32) *(LAS u32x4*)(smem + (L + (L < p1 ? wb0 : L < p2 ? wb1 : wb2)) + lane * 16) = wreg[i]; }
    lds_inc0(smem + N_WO + hp * 4, lane);
    if (qo == 0) { poll_flags((GAS u64*)(ws + WS_FO + (((me & ~7) + (lane & 7)) * 2 + hp) * 128), lane < 8, tag); lds_set(smem + N_OK + hp * 4, 1); }
    else wait_word(smem + N_OK + hp * 4, 1);
    f32x16 oc[3]; f32x4 ps[10];
    LAS char* red = smem + OWN + qo * 10240 + lane * 16;
    { bf16x8 af[5], of[20];
      const int la = (8 * hl + 2 * ((lane & 15) >> 2)) * 160 + (lane & 16) * 2 + (lane & 3) * 8;
      const int aa0 = wb0 + la, aa1 = (hp ? wb1 : wb0) + la, aa2 = wb1 + la, aa3 = (hp ? wb1 : wb2) + la;
      auto rda = [&](int f) { return trfrag(smem + (f < 36 ? aa0 : f < 42 ? aa1 : f < 51 ? aa2 : aa3) + (f / 3) * 2560 + (f % 3) * 64, 160); };
      auto ldo = [&](int k) { of[k] = __builtin_bit_cast(bf16x8, __builtin_amdgcn_raw_buffer_load_b128(orr, qo * 40960 + (hp * 20 + k) * 1024 + lane * 16, 0, 16)); };
#pragma unroll
      for (int k = 0; k < OPF; ++k) ldo(k);
      wait_word(smem + N_WO + hp * 4, 4);
#pragma unroll
      for (int f = 0; f < 4; ++f) af[f] = rda(f);
      SB();
      SFOR(f, 60)
          if constexpr (f % 3 == 0 && f / 3 + OPF < 20) ldo(f / 3 + OPF);
          if constexpr (f + 4 < 60) af[(f + 4) % 5] = rda(f + 4);
          if constexpr (f == 57) { if (hp) { wait_word(smem + C_RD + qo * 4, 1);
#pragma unroll
              for (int b = 0; b < 10; ++b) ps[b] = *(LAS f32x4*)(red + b * 1024); } }
          SB();
          if constexpr (f < 3) MFMA32Z(oc[f], af[f % 5], of[0]); else MFMA32I(oc[f % 3], af[f % 5], of[f / 3]);
          SB();
      SEND }
    MFMA_DRAIN();
    { if (!hp) {
          wait_word(smem + N_G1, 8);
#pragma unroll
          for (int b = 0; b < 10; ++b) *(LAS f32x4*)(red + b * 1024) = (f32x4){oc[b >> 2][4 * (b & 3)], oc[b >> 2][4 * (b & 3) + 1], oc[b >> 2][4 * (b & 3) + 2], oc[b >> 2][4 * (b & 3) + 3]};
          lds_set(smem + C_RD + qo * 4, 1);
      } else {
          char* orow = out + (size_t)(rt * 128 + qo * 32 + r32) * 2560 + br * 1280 + hg * 160 + hl * 16;
#pragma unroll
          for (int b = 0; b < 10; ++b)
#pragma unroll
              for (int r = 0; r < 4; ++r) ps[b][r] += oc[b >> 2][4 * (b & 3) + r];
          SB();
          u32x2 pk[10];
#pragma unroll
          for (int b = 0; b < 10; ++b) pk[b] = (u32x2){pk2(ps[b][0], ps[b][1]), pk2(ps[b][2], ps[b][3])};
          SB();
          u32x4 st[5];
#pragma unroll
          for (int b = 0; b < 10; b += 2) {
              auto rx = __builtin_amdgcn_permlane32_swap(pk[b][0], pk[b + 1][0], false, false); auto ry = __builtin_amdgcn_permlane32_swap(pk[b][1], pk[b + 1][1], false, false);
              st[b >> 1] = (u32x4){rx[0], ry[0], rx[1], ry[1]}; }
          SB();
#pragma unroll
          for (int b = 0; b < 5; ++b) *(u32x4*)(orow + b * 32) = st[b];
      } }
    if (bx == 0 && wid == 0) {
#pragma unroll 1
        for (int i = 0; i < 4; ++i) poll_flags((GAS u64*)(ws + WS_FO + (i * 64 + lane) * 256), true, tag);
        if (lane == 0) __hip_atomic_store((GAS u64*)ws, tag, RLX_AGENT);
    }
}

extern "C" void kernel_launch(void* const* d_in, const int* in_sizes, int n_in, void* d_out, int out_size, void* d_ws, size_t ws_size,
                              hipStream_t stream) {
    fused_attn_kernel<<<256, 512, 0, stream>>>((const char*)d_in[0], (const char*)d_in[1], (const char*)d_in[2], (const char*)d_in[3],
                                               (const char*)d_in[4], (const char*)d_in[5], (char*)d_out, (char*)d_ws);
}
```

```cpp
#define CODEPF 6
#define CODELINES 335
#define CODEW 3
#include <hip/hip_runtime.h>
#include <hip/hip_bf16.h>
#include <stdint.h>

typedef short bf16x8 __attribute__((ext_vector_type(8)));
typedef short s16x4 __attribute__((ext_vector_type(4)));
typedef float f32x2 __attribute__((ext_vector_type(2)));
typedef float f32x4 __attribute__((ext_vector_type(4)));
typedef float f32x16 __attribute__((ext_vector_type(16)));
typedef unsigned u32x4 __attribute__((ext_vector_type(4)));
typedef unsigned u32x2 __attribute__((ext_vector_type(2)));
typedef __bf16 bfx2 __attribute__((ext_vector_type(2)));
typedef unsigned long long u64;
#define LAS __attribute__((address_space(3)))
#define GAS __attribute__((address_space(1)))
#define DI __device__ __forceinline__
#define RLX_AGENT __ATOMIC_RELAXED, __HIP_MEMORY_SCOPE_AGENT
#define RLX_WG __ATOMIC_RELAXED, __HIP_MEMORY_SCOPE_WORKGROUP

constexpr int LDS_BYTES = 163840;
constexpr int VOFF = 20480, VP = 168;
constexpr int KVB = VOFF + 128 * VP;
constexpr int KV0 = LDS_BYTES - 3 * KVB;
constexpr int OWN = KV0 + 2 * KVB;
constexpr int C_F = 0, C_P = 128, C_E = 144, C_QF = 160, C_RD = 176, C_CNT = 192, C_TAG = 320;
constexpr int N_PUB = C_CNT, N_NB = C_CNT + 4, N_G0 = C_CNT + 8, N_G1 = C_CNT + 12, N_WO = C_CNT + 16, N_OS = C_CNT + 24, N_KV = C_CNT + 32, N_OK = C_CNT + 36;
constexpr int XS0 = 512, WS0 = XS0 + 3 * 16384, QST = 512, ONE = 336, QU27 = XS0 + 16384, QU28 = XS0, QU29 = QU27 + 10240;
static_assert(WS0 + 7 * 10240 <= OWN, "ring overlaps the own image");
constexpr unsigned WS_FKV = 4096, WS_FO = WS_FKV + 256 * 128, WS_KV = 131072;
constexpr unsigned KVG = KVB, OG = 4 * 40960;
constexpr unsigned WS_O = WS_KV + 256 * KVG;
constexpr unsigned WS_END = WS_O + 32 * OG;
constexpr unsigned SPIN_CAP = 400000, LSPIN_CAP = 4000000;
constexpr float QSCALE = 0.22360679774997896f * 1.4426950408889634f;
constexpr int WIN = 4;
constexpr int OPF = 12;
constexpr int PFLIP = 6;
constexpr int ADELAY = 3;

#define MFMA32(a, b, c) __builtin_amdgcn_mfma_f32_32x32x16_bf16((a), (b), (c), 0, 0, 0)
#define MFMA32K8(a, b, c) __builtin_amdgcn_mfma_f32_32x32x8bf16_1k((a), (b), (c), 0, 0, 0)
#define MFMA32I(acc, a, b) asm volatile("v_mfma_f32_32x32x16_bf16 %0, %1, %2, %0" : "+v"(acc) : "v"(a), "v"(b))
#define MFMA32Z(acc, a, b) asm volatile("v_mfma_f32_32x32x16_bf16 %0, %1, %2, 0" : "=&v"(acc) : "v"(a), "v"(b))
#define MFMA_DRAIN() asm volatile("s_nop 7\n\ts_nop 4" ::: "memory")
#define WAITV(N) asm volatile("s_waitcnt vmcnt(" #N ")" ::: "memory")
#define BAR_L() asm volatile("s_waitcnt lgkmcnt(0)\n\ts_barrier" ::: "memory")
#define SB() __builtin_amdgcn_sched_barrier(0)
#define FENCE() asm volatile("" ::: "memory")

template <int V> struct IC { static constexpr int value = V; };
template <int I, int N, class F> DI void static_for(F&& f) { if constexpr (I < N) { f(IC<I>{}); static_for<I + 1, N>(f); } }
#define SFOR(var, N) static_for<0, N>([&](auto ic_##var) __attribute__((always_inline)) { constexpr int var = decltype(ic_##var)::value;
#define SEND });

DI void dma16(unsigned voff, const char* sbase, unsigned ldsaddr) {
    asm volatile("s_mov_b32 m0, %2\n\ts_nop 0\n\tglobal_load_lds_dwordx4 %0, %1" :: "v"(voff), "s"(sbase), "s"(ldsaddr) : "memory");
}
DI void dma16x4(unsigned v0, unsigned v1, unsigned v2, unsigned v3, const char* sbase, unsigned ldsaddr) {
    asm volatile("s_mov_b32 m0, %5\n\ts_nop 0\n\tglobal_load_lds_dwordx4 %0, %4\n\tglobal_load_lds_dwordx4 %1, %4 offset:1024\n\t"
                 "global_load_lds_dwordx4 %2, %4 offset:2048\n\tglobal_load_lds_dwordx4 %3, %4 offset:3072"
                 :: "v"(v0), "v"(v1), "v"(v2), "v"(v3), "s"(sbase), "s"(ldsaddr) : "memory");
}
DI void dma16x5(const unsigned (&v)[5], const char* sbase, unsigned ldsaddr) {
    asm volatile("s_mov_b32 m0, %6\n\ts_nop 0\n\tglobal_load_lds_dwordx4 %0, %5 offset:-2048\n\tglobal_load_lds_dwordx4 %1, %5 offset:-1024\n\t"
                 "global_load_lds_dwordx4 %2, %5\n\tglobal_load_lds_dwordx4 %3, %5 offset:1024\n\tglobal_load_lds_dwordx4 %4, %5 offset:2048"
                 :: "v"(v[0]), "v"(v[1]), "v"(v[2]), "v"(v[3]), "v"(v[4]), "s"(sbase), "s"(ldsaddr) : "memory");
}
DI void dma16x5_sc1(unsigned voff, const char* sbase, unsigned ldsaddr) {
    asm volatile("s_mov_b32 m0, %2\n\ts_nop 0\n\tglobal_load_lds_dwordx4 %0, %1 offset:-2048 sc1\n\tglobal_load_lds_dwordx4 %0, %1 offset:-1024 sc1\n\t"
                 "global_load_lds_dwordx4 %0, %1 sc1\n\tglobal_load_lds_dwordx4 %0, %1 offset:1024 sc1\n\tglobal_load_lds_dwordx4 %0, %1 offset:2048 sc1"
                 :: "v"(voff), "s"(sbase), "s"(ldsaddr) : "memory");
}
DI void dma16_sc1(unsigned voff, const char* sbase, unsigned ldsaddr) {
    asm volatile("s_mov_b32 m0, %2\n\ts_nop 0\n\tglobal_load_lds_dwordx4 %0, %1 sc1" :: "v"(voff), "s"(sbase), "s"(ldsaddr) : "memory");
}
DI void dma4(const char* p, unsigned ldsaddr) {
    asm volatile("s_mov_b32 m0, %1\n\ts_nop 0\n\tglobal_load_lds_dword %0, off" :: "v"(p), "s"(ldsaddr) : "memory");
}
DI unsigned pk2(float lo, float hi) { f32x2 v = {lo, hi}; return __builtin_bit_cast(unsigned, __builtin_convertvector(v, bfx2)); }
typedef short v4i16_t __attribute__((ext_vector_type(4)));
DI bf16x8 trfrag(LAS char* p, int second) {
    s16x4 a = __builtin_bit_cast(s16x4, __builtin_amdgcn_ds_read_tr16_b64_v4i16((LAS v4i16_t*)p));
    s16x4 b = __builtin_bit_cast(s16x4, __builtin_amdgcn_ds_read_tr16_b64_v4i16((LAS v4i16_t*)(p + second)));
    return __builtin_shufflevector(a, b, 0, 1, 2, 3, 4, 5, 6, 7);
}
constexpr u64 lanes_cut(int r, bool fartile) {
    u64 m = 0;
    for (int l = 0; l < 64; ++l) { const int key = (r & 3) + 8 * (r >> 2) + 4 * (l >> 5), d = (l & 31) - key + (fartile ? 32 : 0);
        const bool ok = d >= 0 && d <= 14 && !(d & 1);
        if (!ok) m |= 1ull << l; }
    return m;
}
DI float halfmax(float m) { auto rr = __builtin_amdgcn_permlane32_swap(__float_as_uint(m), __float_as_uint(m), false, false);
    return __builtin_elementwise_maximum(__uint_as_float(rr[0]), __uint_as_float(rr[1])); }

DI void poll_flags(GAS u64* word, bool active, u64 tag) {
#pragma unroll 1
    for (unsigned spins = 0; spins < SPIN_CAP; ++spins) {
        u64 v = tag;
        if (active) v = __hip_atomic_load(word, RLX_AGENT);
        if (__all(v == tag)) break;
        __builtin_amdgcn_s_sleep(2);
    }
    __builtin_amdgcn_fence(__ATOMIC_ACQUIRE, "wavefront");
}
DI int min4(LAS char* p) {
    const u64 a = __hip_atomic_load((LAS u64*)p, RLX_WG), b = __hip_atomic_load((LAS u64*)(p + 8), RLX_WG);
    const unsigned m = min(min((unsigned)a, (unsigned)(a >> 32)), min((unsigned)b, (unsigned)(b >> 32)));
    return __builtin_amdgcn_readfirstlane((int)m);
}
DI int wait_min4(LAS char* p, int need) {
    int v = min4(p);
#pragma unroll 1
    for (unsigned spins = 0; v < need && spins < LSPIN_CAP; ++spins) { __builtin_amdgcn_s_sleep(1); v = min4(p); }
    FENCE();
    return v;
}
DI void wait_word(LAS char* p, unsigned need) {
#pragma unroll 1
    for (unsigned spins = 0; spins < LSPIN_CAP; ++spins) {
        if ((unsigned)__builtin_amdgcn_readfirstlane((int)__hip_atomic_load((LAS unsigned*)p, RLX_WG)) >= need) break;
        __builtin_amdgcn_s_sleep(1); }
    FENCE();
}
DI void lds_inc0(LAS char* p, int lane) { FENCE(); if (lane == 0) (void)__hip_atomic_fetch_add((LAS unsigned*)p, 1u, RLX_WG); FENCE(); }
DI unsigned lds_inc(LAS char* p, int lane) { FENCE(); unsigned old = 0; if (lane == 0) old = __hip_atomic_fetch_add((LAS unsigned*)p, 1u, RLX_WG); FENCE(); return old; }
DI void lds_set(LAS char* p, unsigned v) { FENCE(); __hip_atomic_store((LAS unsigned*)p, v, RLX_WG); FENCE(); }

__global__ __launch_bounds__(512, 2) void fused_attn_kernel(const char* __restrict__ xA, const char* __restrict__ wA, const char* __restrict__ oA,
                                                           const char* __restrict__ xB, const char* __restrict__ wB, const char* __restrict__ oB,
                                                           char* __restrict__ out, char* __restrict__ ws) {
    __shared__ __attribute__((aligned(16))) char smem_[LDS_BYTES];
    LAS char* smem = (LAS char*)smem_;
    const unsigned lds0 = (unsigned)(uintptr_t)smem;
    asm volatile("" :: "s"(xA), "s"(wA), "s"(oA), "s"(xB), "s"(wB), "s"(oB), "s"(out), "s"(ws));
    const int tid = threadIdx.x, lane = tid & 63;
    const int wid = __builtin_amdgcn_readfirstlane(tid >> 6);
    const int bx = blockIdx.x, xcd = bx & 7, idx = bx >> 3;
    const int isA = idx >> 4, rt = (xcd >> 1) * 4 + ((idx >> 2) & 3), hg = (xcd & 1) * 4 + (idx & 3);
    const int br = isA ? 0 : 1;
    if (isA && wid == CODEW && (idx & 15) < CODEPF) { u64 pc; asm volatile("s_getpc_b64 %0" : "=s"(pc));
        const int n = (idx & 15) * 64 + lane;
        dma4((const char*)pc + (n < CODELINES ? n : CODELINES - 1) * 128, lds0 + OWN + CODEW * 512); }
    const int k0 = isA ? (((rt & 3) * 5 + 1) / 2 + ((hg & 3) * 5 + 1) / 2) % 10 : 0;
    const char* xb = (isA ? xA : xB) + (size_t)rt * (128 * 1280);
    const char* wb = (isA ? wA : wB) + hg * 160;
    const int me = (br * 16 + rt) * 8 + hg;
    const int r32 = lane & 31, hl = lane >> 5;
    auto kt_of = [&](int s) { int k = s + k0; return k >= 10 ? k - 10 : k; };
    if (isA && wid < 4) {
        const int j16 = idx & 15; const unsigned dump = lds0 + OWN + wid * 512;
        const char* wl = wB + (hg >> 2) * 640 + j16 * (4 * 3840);
        const char* xl = xB + (size_t)(rt * 128 + (hg & 3) * 32 + r32) * 1280 + hl * 128;
        auto pfx = [&](int i) { return xl + i * 256; };
        auto pfkv = [&](int i) { int n = i * 64 + lane; n = n < 400 ? n : 399; const int sl = n / 40, r = n % 40, c = r % 10;
            return wl + (sl * 64 + r / 10) * 3840 + 1280 + (c < 5 ? c * 128 : 640 + c * 128); };
        auto pfq = [&](int i) { int n = i * 64 + lane; n = n < 200 ? n : 199; int sl = n / 20 + 8; sl = sl >= 10 ? sl - 10 : sl; const int r = n % 20;
            return wl + (sl * 64 + r / 5) * 3840 + (r % 5) * 128; };
        dma4(wid & 1 ? pfx(wid >> 1) : pfkv(wid >> 1), dump);
        dma4(wid & 1 ? pfx(2 + (wid >> 1)) : pfkv(2 + (wid >> 1)), dump);
        dma4(wid < 3 ? pfkv(4 + wid) : pfx(4), dump);
        dma4(pfq(wid), dump);
        { int n = wid * 64 + lane; n = n < 200 ? n : 199; const int o = (j16 * 40 + n / 5) * 1280 + (hg >> 2) * 640 + (n % 5) * 128;
          dma4(oB + o, dump); dma4(oA + o, dump); }
    }
    if (isA && wid >= 4) { for (int i = 0; i < ADELAY; ++i) __builtin_amdgcn_s_sleep(16); }

    const unsigned xo = (unsigned)((lane >> 3) * 1280 + (((lane & 7) ^ (lane >> 4)) << 4));
    auto w_off = [&](int q) { const int c = q * 64 + lane, row = __umul24(c, 3277) >> 16, j = c - row * 20; return (unsigned)(__umul24(row, 3840) + j * 16 + (j < 10 ? 1280 : 2400)); };
    if (!isA) { const char* xs = xb + k0 * 128 + wid * (16 * 1280);
      dma16(xo, xs, lds0 + XS0 + wid * 2048);
      dma16(xo ^ 64, xs + 8 * 1280, lds0 + XS0 + wid * 2048 + 1024);
      const char* wsrc = wb + (size_t)k0 * (64 * 3840);
      const unsigned wv = w_off(wid < 5 ? wid : wid - 5);
      if (wid < 5) { dma16(wv, wsrc, lds0 + WS0 + wid * 1024); dma16(wv, wsrc + 16 * 3840, lds0 + WS0 + (5 + wid) * 1024);
          if (wid >= 3) { dma16(wv, wsrc + 32 * 3840, lds0 + WS0 + (10 + wid) * 1024); dma16(wv, wsrc + 48 * 3840, lds0 + WS0 + (15 + wid) * 1024); } }
      else { dma16(wv, wsrc + 32 * 3840, lds0 + WS0 + (5 + wid) * 1024); dma16(wv, wsrc + 48 * 3840, lds0 + WS0 + (10 + wid) * 1024); } }
    if (wid == 4 && lane < 2) dma4(ws + lane * 4, lds0 + C_TAG);
    if (tid < 80) *(LAS unsigned*)(smem + tid * 4) = 0u;
    else if (tid < 82) *(LAS unsigned*)(smem + ONE + (tid - 80) * 4) = 0x3F803F80u;
    const int nnb = isA ? (rt > 0 ? 1 : 0) : (rt > 1 ? 2 : rt);
    auto kq_base = [&](int fg) { const int hd = fg / 5, dg = fg % 5; return dg < 4 ? hd * 5120 + (dg >> 1) * 2048 + (dg & 1) * 8 : hd * 5120 + 4096; };

    auto rdtag = [&]() { const u64 t = *(LAS u64*)(smem + C_TAG);
        const u64 e = ((u64)(unsigned)__builtin_amdgcn_readfirstlane((int)(t >> 32)) << 32) | (unsigned)__builtin_amdgcn_readfirstlane((int)t);
        return e * 6364136223846793005ull + 1442695040888963407ull; };
    const __amdgpu_buffer_rsrc_t wor = __builtin_amdgcn_make_buffer_rsrc((void*)(isA ? oA : oB), 0, 640 * 1280, 0x00020000);

    const int hp = wid >> 2, qo = wid & 3, qi = hp ? 3 - qo : qo;
    const int ud = qi + 8;
    const bool far = isA ? (4 * rt + qi >= 1) : (4 * rt + qi >= 8);
    const int u0 = isA ? (far ? ud - 1 : ud) : (far ? qi : 8 - 4 * rt);
    f32x16 cd, cf;
    int dsh, kb1, kb2, nh, vb0, vb1, qb1, qb2; unsigned obase, oo[5];
    auto mask_b = [&](f32x16& c, int r, bool fartile) { const int k = (r & 3) + 8 * (r >> 2); c[r] = __int_as_float(fartile ? ((k - 1) << 23) - dsh : dsh - (k << 23)); };
    auto mask_a = [&](int z) {
      SFOR(r, 16)
          constexpr u64 ad = lanes_cut(r, false), af = lanes_cut(r, true);
          cd[r] = __builtin_amdgcn_inverse_ballot_w64(ad + z) ? -__builtin_inff() : 0.f;
          cf[r] = __builtin_amdgcn_inverse_ballot_w64(af + z) ? -__builtin_inff() : 0.f;
      SEND };
    auto att_setup = [&](int part) {
        int l = tid; asm volatile("" : "+v"(l)); l &= 63;
        const int q = l & 31, h = l >> 5;
        if (part == 0) { dsh = (q - 4 * h) << 23; kb1 = KV0 + hp * 10240 + h * 2048 + q * 16; kb2 = h ? ONE : KV0 + hp * 10240 + 4096 + q * 8; nh = h ^ 1;
            asm volatile("" : "+v"(dsh), "+v"(kb1), "+v"(kb2), "+v"(nh)); }
        if (part == 1) { const bool ones = (l & 16) && (l & 3);
            vb0 = KV0 + VOFF + (4 * h + ((l & 15) >> 2)) * VP + (ones ? 160 : hp * 80 + (l & 16) * 2 + (l & 3) * 8); vb1 = vb0 + (ones ? 0 : 40);
            asm volatile("" : "+v"(vb0), "+v"(vb1)); }
        if (part == 2) { qb1 = QST + hp * 10240 + h * 2048 + (qi * 32 + q) * 16; qb2 = QST + hp * 10240 + 4096 + (qi * 32 + q) * 8;
            obase = (unsigned)(qi * 40960 + hp * 20480 + hg * 2560 + q * 16 + h * 512);
            asm volatile("" : "+v"(qb1), "+v"(qb2), "+v"(obase)); }
        if (part >= 3) {
#pragma unroll
            for (int r = 4 * (part - 3); r < 4 * (part - 2); ++r) mask_b(cd, r, false);
            asm volatile("" : "+v"(cd)); } };
    auto wo_setup = [&](int q) {
        int l = tid; asm volatile("" : "+v"(l)); const int c = q * 64 + (l & 63), rho = __umul24(c, 6554) >> 16;
        oo[q] = (unsigned)(__umul24(qo * 160 + hp * 40 + (rho & ~7) + 4 * (rho & 1) + ((rho >> 1) & 3), 1280) + hg * 160 + (c - rho * 10) * 16);
        asm volatile("" : "+v"(oo[q])); };
    u64 tag;

    if (wid < 4) {
        if (!isA) WAITV(0);
        BAR_L();
        bf16x8 xr[32], xt[2], wf[WIN + 1];
        f32x16 acc[5];
        const int tok = wid * 32 + r32;
        const int xa = XS0 + tok * 128 + ((hl ^ ((tok >> 1) & 7)) << 4);
        int wa0 = WS0 + (8 * hl + ((lane & 15) >> 2)) * 320 + (lane & 16) * 2 + (lane & 3) * 8, wa1 = wa0 + 61440;
        asm volatile("" : "+v"(wa0), "+v"(wa1));
        auto rdx = [&](int i) { return *(LAS bf16x8*)(smem + ((i >> 2) % 3) * 16384 + (xa ^ ((i & 3) * 32))); };
        auto rdw = [&](int f) { const int i = f / 5, t = f % 5, sl = (i >> 1) % 7;
            return trfrag(smem + (sl < 6 ? wa0 + sl * 10240 : wa1) + (i & 1) * 5120 + t * 64, 1280); };
        unsigned fl = 0;
        auto rdflag = [&](int u) { fl = __hip_atomic_load((LAS unsigned*)(smem + C_F + u * 4), RLX_WG); };
        auto check = [&](int u) {
            if (__builtin_amdgcn_readfirstlane(fl) != 15) {
#pragma unroll 1
                for (unsigned spins = 0; spins < LSPIN_CAP; ++spins) { rdflag(u); if (__builtin_amdgcn_readfirstlane(fl) == 15) break; __builtin_amdgcn_s_sleep(1); }
            }
            FENCE(); };
#pragma unroll
        for (int i = 0; i < 4; ++i) xr[i] = rdx(i);
#pragma unroll
        for (int f = 0; f < WIN; ++f) wf[f] = rdw(f);
        SB();
        SFOR(f, 200)
            constexpr int i = f / 5, t = f % 5;
            if constexpr (f % 20 == 10 - WIN) lds_set(smem + C_P + wid * 4, (i >> 1) + 1);
            if constexpr (f == 200 - WIN) lds_set(smem + C_P + wid * 4, 20);
            if constexpr (f % 20 == 10) rdflag(f / 20 + 1);
            if constexpr (f % 20 == 20 - WIN && f < 190) check(f / 20 + 1);
            if constexpr (f + WIN < 200) wf[(f + WIN) % (WIN + 1)] = rdw(f + WIN);
            if constexpr (f % 20 >= 16 && f < 140) xr[(f / 20 + 1) * 4 + f % 20 - 16] = rdx((f / 20 + 1) * 4 + f % 20 - 16);
            if constexpr (t == 1 && i >= 31 && i < 39) xt[(i + 1) & 1] = rdx(i + 1);
            SB();
            if constexpr (i == 0) MFMA32Z(acc[t], wf[f % (WIN + 1)], xr[0]);
            else if constexpr (i < 32) MFMA32I(acc[t], wf[f % (WIN + 1)], xr[i]);
            else MFMA32I(acc[t], wf[f % (WIN + 1)], xt[i & 1]);
            SB();
        SEND
        int l2 = tid; asm volatile("" : "+v"(l2)); l2 &= 63;
        const int h2 = l2 >> 5, tk = wid * 32 + (l2 & 31);
        int eb[5];
        auto ebases = [&](int base) { int l = tid; asm volatile("" : "+v"(l)); const int h = (l >> 5) & 1, k = wid * 32 + (l & 31);
            eb[0] = base + k * 16 + h * 8; eb[1] = base + k * 16 + h * 2040; eb[2] = base + (h ? k * 16 + 1024 : k * 8); eb[3] = base + (h ? k * 8 + 2040 : k * 16);
            asm volatile("" : "+v"(eb[0]), "+v"(eb[1]), "+v"(eb[2]), "+v"(eb[3])); };
        auto kq_addr = [&](int g0) { return eb[g0 % 5 == 1 ? 1 : g0 % 5 == 4 ? 2 : g0 % 5 == 3 ? 3 : 0] + kq_base(g0); };
        ebases(OWN);
        eb[4] = OWN + VOFF + tk * VP + h2 * 8; asm volatile("" : "+v"(eb[4]));
        int qa0 = (8 * h2 + 2 * ((l2 & 15) >> 2)) * 160 + (l2 & 16) * 2 + (l2 & 3) * 8, qa1 = qa0 + WS0 + 10240;
        asm volatile("" : "+v"(qa0), "+v"(qa1));
        auto rdq = [&](int f) { const int i = f / 3, t = f % 3, j = i >> 2, sl = (20 + j) % 7;
            return trfrag(smem + (j >= 7 ? qa0 + (j == 7 ? QU27 : j == 8 ? QU28 : QU29) : sl == 0 ? qa0 + WS0 : qa1 + (sl - 1) * 10240) + (i & 3) * 2560 + t * 64, 160); };
        MFMA_DRAIN();
        WAITV(0);
        { u32x2 pk[20];
#pragma unroll
          for (int n = 0; n < 20; ++n) { const int t = n >> 2, b = n & 3; pk[n] = (u32x2){pk2(acc[t][4 * b], acc[t][4 * b + 1]), pk2(acc[t][4 * b + 2], acc[t][4 * b + 3])}; }
          SB();
#pragma unroll
          for (int n = 0; n < 20; ++n) *(LAS u32x2*)(smem + (n < 10 ? kq_addr(2 * n) : eb[4] + (2 * n - 20) * 8)) = pk[n];
          if (h2 == 0) *(LAS u32x2*)(smem + eb[4] + 160) = (u32x2){0x3F803F80u, 0x3F803F80u}; }
        lds_set(smem + C_E + wid * 4, 1);
        check(10);
        xt[0] = rdx(32);
#pragma unroll
        for (int f = 0; f < WIN; ++f) wf[f] = rdq(f);
        SB();
        u64 pv0 = 0, pv1 = 0;
        SFOR(f, 120)
            constexpr int i = f / 3, t = f % 3, j = i >> 2;
            if constexpr (f % 24 == 12 && j < 9) rdflag(11 + j / 2);
            if constexpr (f % 12 == 12 - WIN && (j == 1 || j == 6 || j == 8)) lds_set(smem + C_P + wid * 4, 21 + j);
            if constexpr (f % 24 == 24 - WIN && j < 9) check(11 + j / 2);
            if constexpr (f + WIN < 120) wf[(f + WIN) % (WIN + 1)] = rdq(f + WIN);
            if constexpr (t == 0 && i < 7) xt[(i + 1) & 1] = rdx(32 + i + 1);
            if constexpr (f >= 90 && f < 100 && !(f & 1)) wo_setup((f - 90) / 2);
            if constexpr (f == 99) tag = rdtag();
            if constexpr (f >= 100 && f < 107) att_setup(f - 100);
            if constexpr (f == 108) ebases(QST);
            if constexpr (f == 116) { pv0 = __hip_atomic_load((LAS u64*)(smem + C_P), RLX_WG); pv1 = __hip_atomic_load((LAS u64*)(smem + C_P + 8), RLX_WG); }
            SB();
            if constexpr (i == 0) MFMA32Z(acc[t], wf[f % (WIN + 1)], xt[0]);
            else if constexpr (i < 8) MFMA32I(acc[t], wf[f % (WIN + 1)], xt[i & 1]);
            else MFMA32I(acc[t], wf[f % (WIN + 1)], xr[i - 8]);
            SB();
        SEND
        MFMA_DRAIN();
        SB();
        if (__builtin_amdgcn_readfirstlane((int)min(min((unsigned)pv0, (unsigned)(pv0 >> 32)), min((unsigned)pv1, (unsigned)(pv1 >> 32)))) < 29) wait_min4(smem + C_P, 29);
        { acc[0] *= QSCALE; acc[1] *= QSCALE; acc[2] *= QSCALE;
          SB();
          u32x2 pk[10];
#pragma unroll
          for (int n = 0; n < 10; ++n) { const int t = n >> 2, b = n & 3; pk[n] = (u32x2){pk2(acc[t][4 * b], acc[t][4 * b + 1]), pk2(acc[t][4 * b + 2], acc[t][4 * b + 3])}; }
          SB();
#pragma unroll
          for (int n = 0; n < 10; ++n) *(LAS u32x2*)(smem + kq_addr(2 * n)) = pk[n]; }
        lds_set(smem + C_QF + wid * 4, 1);
        __builtin_amdgcn_s_setprio(1);
    } else {
        const int hw = wid - 4;
        int known = 0;
        __builtin_amdgcn_s_setprio(3);
        auto lds_or = [&](int off, unsigned v) { FENCE(); if (lane == 0) __hip_atomic_fetch_or((LAS unsigned*)(smem + off), v, RLX_WG); FENCE(); };
        unsigned wo[5], qo[5];
        auto done = [&](int n) { lds_or(C_F + n * 4, 1u << hw); };
        const unsigned xo1 = (xo ^ 64) + 9216, xo2 = xo + 2 * 9216, xo3 = (xo ^ 64) + 3 * 9216;
        auto round_x = [&](int n) { dma16x4(xo, xo1, xo2, xo3, xb + kt_of(n) * 128 + hw * (32 * 1280), lds0 + XS0 + (n % 3) * 16384 + hw * 4096); };
        auto round_w = [&](int n) {
            const int u = 2 * n + (hw >> 1);
            const unsigned dst = lds0 + (u < 27 ? WS0 + (u % 7) * 10240 : u == 27 ? QU27 : u == 28 ? QU28 : QU29) + (hw & 1) * 5120 + 2048;
            if (u < 20) dma16x5(wo, wb + (size_t)(kt_of(u >> 1) * 64 + (u & 1) * 32) * 3840 - 2048, dst);
            else dma16x5(qo, wb + (size_t)kt_of(u < 22 ? u - 12 : u - 22) * (64 * 3840) - 2048, dst); };
        if (isA) round_x(0);
        round_x(1);
#pragma unroll
        for (int q = 0; q < 5; ++q) wo[q] = w_off(q) + (hw & 1) * (16 * 3840) + (4 - q) * 1024;
        if (isA) { round_w(0); round_w(1); WAITV(5); } else { round_w(1); WAITV(9); }
        BAR_L();
#pragma unroll
        for (int q = 0; q < 5; ++q) { const int c = q * 64 + lane, rho = __umul24(c, 6554) >> 16;
            qo[q] = (unsigned)(__umul24((hw & 1) * 32 + (rho & ~7) + 4 * (rho & 1) + ((rho >> 1) & 3), 3840) + (c - rho * 10) * 16 + (4 - q) * 1024); }
#pragma unroll 1
        for (int n = 2; n <= 13; ++n) {
            const int need = n < 13 ? 2 * n - 5 : 20;
            if (need > known) known = min4(smem + C_P);
            if (need > known) { WAITV(0); done(n - 1); known = wait_min4(smem + C_P, need); }
            if (n < 10) round_x(n);
            round_w(n);
            if (n < 10) WAITV(9); else WAITV(5);
            done(n - 1);
        }
        WAITV(0); done(13);
        tag = rdtag();
        auto a_chunk = [&](int a) { int o;
            if (a < 320) { const int hd = a / 80, rem = a % 80; o = hd * 5120 + (rem < 32 ? (96 + rem) * 16 : rem < 64 ? 2048 + (64 + rem) * 16 : 4096 + 768 + (rem - 64) * 16); }
            else o = VOFF + 96 * VP + (a - 320) * 16;
            return o; };
        if (rt < 15) {
            const __amdgpu_buffer_rsrc_t kvr = __builtin_amdgcn_make_buffer_rsrc(ws + WS_KV + me * KVG, 0, KVB, 0x00020000);
            wait_min4(smem + C_E, 1);
            if (!isA) { u32x4 v[11];
#pragma unroll
                for (int j = 0; j < 11; ++j) if (j < 10 || hw == 3) v[j] = *(LAS u32x4*)(smem + OWN + hw * 10240 + j * 1024 + lane * 16);
#pragma unroll
                for (int j = 0; j < 11; ++j) if (j < 10 || hw == 3) __builtin_amdgcn_raw_buffer_store_b128(v[j], kvr, hw * 10240 + j * 1024 + lane * 16, 0, 16);
            } else {
#pragma unroll
                for (int j = 0; j < 3; ++j) { const int a = (4 * j + hw) * 64 + lane;
                    if (a < 656) { const int o = a_chunk(a); __builtin_amdgcn_raw_buffer_store_b128(*(LAS u32x4*)(smem + OWN + o), kvr, o, 0, 16); } }
            }
        }
        wait_min4(smem + C_P, 22);
        round_w(14);
        WAITV(5);
        if (rt < 15) {
            unsigned old = 0;
            if (lane == 0) old = __hip_atomic_fetch_add((LAS unsigned*)(smem + N_PUB), 1u, RLX_WG);
            if (lane == 0 && old == 3) __hip_atomic_store((GAS u64*)(ws + WS_FKV + me * 128), tag, RLX_AGENT);
        }
        WAITV(0); done(14);
        __builtin_amdgcn_s_setprio(0);
#pragma unroll
        for (int p = 0; p < 7; ++p) att_setup(p);
#pragma unroll
        for (int q = 0; q < 5; ++q) wo_setup(q);
        __builtin_amdgcn_s_setprio(3);
        if (hw == 0) { poll_flags((GAS u64*)(ws + WS_FKV + (me - 8 * (lane + 1)) * 128), lane < nnb, tag); lds_set(smem + N_KV, 1); }
        else wait_word(smem + N_KV, 1);
        if (!isA) {
            const int n = hw >> 1, half = hw & 1;
            if (n < nnb) {
                const char* src = ws + WS_KV + (me - 8 * (n + 1)) * KVG + half * 20480; const unsigned dst = lds0 + KV0 + (1 - n) * KVB + half * 20480;
                wait_min4(smem + C_P, 27);
#pragma unroll
                for (int j = 0; j < 4; ++j) dma16x5_sc1(lane * 16, src + j * 5120 + 2048, dst + j * 5120 + 2048);
                if (half) dma16_sc1(lane * 16, src + 20480, dst + 20480);
            }
        } else if (nnb) {
            const __amdgpu_buffer_rsrc_t nbr = __builtin_amdgcn_make_buffer_rsrc(ws + WS_KV + (me - 8) * KVG, 0, KVB, 0x00020000);
            u32x4 v[3];
#pragma unroll
            for (int j = 0; j < 3; ++j) { const int a = (4 * j + hw) * 64 + lane; if (a < 656) v[j] = __builtin_amdgcn_raw_buffer_load_b128(nbr, a_chunk(a), 0, 16); }
            wait_min4(smem + C_P, 27);
#pragma unroll
            for (int j = 0; j < 3; ++j) { const int a = (4 * j + hw) * 64 + lane; if (a < 656) *(LAS u32x4*)(smem + KV0 + KVB + a_chunk(a)) = v[j]; }
        }
        __builtin_amdgcn_s_setprio(2);
        wait_word(smem + C_QF + qi * 4, 1);
    }
    u32x4 wreg[13];
    auto ldwo = [&](int i) { wreg[i] = __builtin_amdgcn_raw_buffer_load_b128(wor, oo[i % 5] + (i / 5) * (32 * 1280) + (i > 6 || (i == 6 && lane >= 16) ? 40 * 1280 : 0), 0, 0); };
    u32x2 ob[2][3];
    {
        bf16x8 q1[2]; u32x2 q2[2];
#pragma unroll
        for (int e = 0; e < 2; ++e) {
            q1[e] = *(LAS bf16x8*)(smem + qb1 + e * 5120);
            q2[e] = *(LAS u32x2*)(smem + qb2 + e * 5120);
            if (hl) q2[e] = (u32x2){0u, 0u};
        }
        if (isA) mask_a(0);
        struct KF { bf16x8 a; s16x4 b; };
        struct VF { bf16x8 a, b; };
        auto rdk = [&](int u, int e) { const int so = (u >> 2) * KVB, tw = u & 3; KF k;
            k.a = *(LAS bf16x8*)(smem + kb1 + so + tw * 512 + e * 5120); k.b = *(LAS s16x4*)(smem + __umul24(so + tw * 256 + e * 5120, nh) + kb2); return k; };
        auto rdv = [&](int u, int e) { LAS char* p = smem + (e ? vb1 : vb0) + (u >> 2) * KVB + (u & 3) * (32 * VP); VF v;
            v.a = trfrag(p, 8 * VP); v.b = trfrag(p + 16 * VP, 8 * VP); return v; };
        auto qk = [&](const KF& k, int e, const f32x16& c) { return MFMA32K8(k.b, __builtin_bit_cast(s16x4, q2[e]), MFMA32(k.a, q1[e], c)); };
        const f32x16 zero = {};
        auto max16 = [&](const f32x16& s, float m) {
#pragma unroll
            for (int r = 0; r < 16; ++r) m = __builtin_elementwise_maximum(m, s[r]);
            return m; };
        auto set_ref = [&](float m, int e) {
            const unsigned t0 = pk2(-m, 0.f); const float r1 = -m - __uint_as_float(t0 << 16);
            const unsigned t1 = pk2(r1, 0.f); const float r2 = r1 - __uint_as_float(t1 << 16);
            if (hl) q2[e] = (u32x2){t0 | (t1 << 16), pk2(r2, 0.f)}; };
        auto ex4 = [&](f32x16& s, int b) {
#pragma unroll
            for (int r = 0; r < 4; ++r) s[b + r] = __builtin_amdgcn_exp2f(s[b + r]); };
        auto pk8 = [&](const f32x16& s, int b) { return __builtin_bit_cast(bf16x8, (u32x4){pk2(s[b], s[b + 1]), pk2(s[b + 2], s[b + 3]), pk2(s[b + 4], s[b + 5]), pk2(s[b + 6], s[b + 7])}); };
        auto phase = [&](auto nx, f32x16& cur, const VF& v, f32x16& o, f32x16& nxt, const KF& k, int e) __attribute__((always_inline)) {
            constexpr int NX = decltype(nx)::value;
            SB(); ex4(cur, 0); SB();
            if constexpr (NX) nxt = MFMA32(k.a, q1[e], NX == 1 ? zero : NX == 2 ? cf : cd);
            SB(); ex4(cur, 4); { const bf16x8 p = pk8(cur, 0); SB(); o = MFMA32(v.a, p, o); }
            SB(); ex4(cur, 8); SB();
            if constexpr (NX) nxt = MFMA32K8(k.b, __builtin_bit_cast(s16x4, q2[e]), nxt);
            SB(); ex4(cur, 12); { const bf16x8 p = pk8(cur, 8); SB(); o = MFMA32(v.b, p, o); }
            SB(); };
        f32x16 o0, o1, sa, sb; KF kx; VF vx;
        auto step = [&](auto m0, auto m1, int u) __attribute__((always_inline)) {
            constexpr int M1 = decltype(m1)::value;
            KF ky; if constexpr (M1) ky = rdk(u - 1, 0);
            const VF vy = rdv(u, 1);
            phase(m0, sa, vx, o0, sb, kx, 1);
            if constexpr (M1) { kx = rdk(u - 1, 1); vx = rdv(u - 1, 0); }
            phase(m1, sb, vy, o1, sa, ky, 0); };
        auto nbwait = [&](int u) { if (u == 8) wait_word(smem + N_NB, 4); };
        { const KF k0f = rdk(ud, 0); kx = rdk(ud, 1); vx = rdv(ud, 0);
          const f32x16 s0 = qk(k0f, 0, cd), s1 = qk(kx, 1, cd);
          set_ref(halfmax(max16(s0, s0[0])), 0); set_ref(halfmax(max16(s1, s1[0])), 1);
          sa = qk(k0f, 0, cd); }
        if (hp) { WAITV(0); lds_inc0(smem + N_NB, lane); lds_inc0(smem + N_G0, lane); }
        o0 = zero; o1 = zero;
        if (u0 + 8 == ud) {
            SFOR(it, 9)
                if constexpr (it < 7) { ldwo(2 * it); if constexpr (it < 6) ldwo(2 * it + 1); }
                if constexpr (it == PFLIP) { if (hp) __builtin_amdgcn_s_setprio(0); }
                if constexpr (it == 7) {
#pragma unroll
                    for (int r = 0; r < 16; ++r) mask_b(cf, r, true); }
                if constexpr (it < 4) nbwait(ud - it);
                step(IC<it == 0 ? 3 : it == 8 ? 2 : 1>{}, IC<it == 8 ? 0 : it == 7 ? 2 : 1>{}, ud - it);
            SEND
        } else {
            __builtin_amdgcn_s_setprio(0);
#pragma unroll
            for (int i = 0; i < 13; ++i) ldwo(i);
            if (!isA) cf = zero;
            if (u0 < ud) {
                nbwait(ud);
                if (u0 + 1 < ud) {
                    step(IC<3>{}, IC<1>{}, ud);
#pragma unroll 1
                    for (int u = ud - 1; u > u0 + 1; --u) { nbwait(u); step(IC<1>{}, IC<1>{}, u); }
                    nbwait(u0 + 1);
                    step(IC<1>{}, IC<2>{}, u0 + 1);
                } else step(IC<3>{}, IC<2>{}, ud);
                step(IC<2>{}, IC<0>{}, u0);
            } else step(IC<3>{}, IC<0>{}, ud);
        }
        if (!__all(o0[12] < 1.152921504606847e18f && o1[12] < 1.152921504606847e18f && o0[12] > 0.25f && o1[12] > 0.25f)) {
            if (isA) { int z = 0; asm volatile("" : "+s"(z)); mask_a(z); }
            else { asm volatile("" : "+v"(dsh));
#pragma unroll
                for (int r = 0; r < 16; ++r) { mask_b(cd, r, false); mask_b(cf, r, true); }
                if (!far) cf = zero; }
#pragma unroll
            for (int e = 0; e < 2; ++e) {
                if (hl) q2[e] = (u32x2){0u, 0u};
                auto tile = [&](int u) { return u == ud ? qk(rdk(u, e), e, cd) : u == u0 ? qk(rdk(u, e), e, cf) : qk(rdk(u, e), e, zero); };
                float m = -__builtin_inff();
#pragma unroll 1
                for (int u = u0; u <= ud; ++u) m = max16(tile(u), m);
                m = halfmax(m);
                f32x16 o = zero;
#pragma unroll 1
                for (int u = u0; u <= ud; ++u) { f32x16 t = tile(u) - m;
                    const VF v = rdv(u, e); ex4(t, 0); ex4(t, 4); ex4(t, 8); ex4(t, 12);
                    o = MFMA32(v.a, pk8(t, 0), o); o = MFMA32(v.b, pk8(t, 8), o); }
                if (e) o1 = o; else o0 = o;
            }
        }
        const float rl0 = __builtin_amdgcn_rcpf(o0[12]), rl1 = __builtin_amdgcn_rcpf(o1[12]);
        SB();
#pragma unroll
        for (int r = 0; r < 12; ++r) { o0[r] *= rl0; o1[r] *= rl1; }
        SB();
#pragma unroll
        for (int j = 0; j < 3; ++j) {
            ob[0][j] = (u32x2){pk2(o0[4 * j], o0[4 * j + 1]), pk2(o0[4 * j + 2], o0[4 * j + 3])};
            ob[1][j] = (u32x2){pk2(o1[4 * j], o1[4 * j + 1]), pk2(o1[4 * j + 2], o1[4 * j + 3])}; }
        SB();
    }
    const __amdgpu_buffer_rsrc_t orr = __builtin_amdgcn_make_buffer_rsrc(ws + WS_O + (br * 16 + rt) * OG, 0, 4 * 40960, 0x00020000);
    {
      { auto rx = __builtin_amdgcn_permlane32_swap(ob[0][0][0], ob[0][1][0], false, false);
        auto ry = __builtin_amdgcn_permlane32_swap(ob[0][0][1], ob[0][1][1], false, false);
        __builtin_amdgcn_raw_buffer_store_b128((u32x4){rx[0], ry[0], rx[1], ry[1]}, orr, obase, 0, 16); }
      if (hl == 0) __builtin_amdgcn_raw_buffer_store_b128((u32x4){ob[0][2][0], ob[0][2][1], ob[1][0][0], ob[1][0][1]}, orr, obase + 1024, 0, 16);
      { auto fx = __builtin_amdgcn_permlane32_swap(ob[1][0][0], ob[1][1][0], false, false);
        auto fy = __builtin_amdgcn_permlane32_swap(ob[1][0][1], ob[1][1][1], false, false);
        auto sx = __builtin_amdgcn_permlane32_swap(ob[1][1][0], ob[1][2][0], false, false);
        auto sy = __builtin_amdgcn_permlane32_swap(ob[1][1][1], ob[1][2][1], false, false);
        __builtin_amdgcn_raw_buffer_store_b128((u32x4){fx[1], fy[1], sx[0], sy[0]}, orr, obase + 1536, 0, 16); } }
    if (lane == 0) { (void)__hip_atomic_fetch_add((LAS unsigned*)(smem + N_G1), 1u, RLX_WG);
        if (!hp) (void)__hip_atomic_fetch_add((LAS unsigned*)(smem + N_G0), 1u, RLX_WG); }
    FENCE();
    auto signal_o = [&]() {
        if (lds_inc(smem + N_OS + hp * 4, lane) == 3 && lane == 0) __hip_atomic_store((GAS u64*)(ws + WS_FO + (me * 2 + hp) * 128), tag, RLX_AGENT); };
    __builtin_amdgcn_s_setprio(0);
    WAITV(0); signal_o();
    wait_word(smem + (hp ? N_G1 : N_G0), 8);
    const int p1 = hp ? 30720 : 35840, p2 = hp ? 65536 : 43520;
    const int wb0 = hp ? KV0 + 10240 : 512, wb1 = hp ? KV0 + KVB + 10240 - 30720 : KV0 - 35840, wb2 = KV0 + KVB - 43520;
#pragma unroll
    for (int i = 0; i < 13; ++i) { const int L = qo * 12800 + i * 1024;
        if (i < 12 || lane < 32) *(LAS u32x4*)(smem + (L + (L < p1 ? wb0 : L < p2 ? wb1 : wb2)) + lane * 16) = wreg[i]; }
    lds_inc0(smem + N_WO + hp * 4, lane);
    if (qo == 0) { poll_flags((GAS u64*)(ws + WS_FO + (((me & ~7) + (lane & 7)) * 2 + hp) * 128), lane < 8, tag); lds_set(smem + N_OK + hp * 4, 1); }
    else wait_word(smem + N_OK + hp * 4, 1);
    f32x16 oc[3]; f32x4 ps[10];
    LAS char* red = smem + OWN + qo * 10240 + lane * 16;
    { bf16x8 af[5], of[20];
      const int la = (8 * hl + 2 * ((lane & 15) >> 2)) * 160 + (lane & 16) * 2 + (lane & 3) * 8;
      const int aa0 = wb0 + la, aa1 = (hp ? wb1 : wb0) + la, aa2 = wb1 + la, aa3 = (hp ? wb1 : wb2) + la;
      auto rda = [&](int f) { return trfrag(smem + (f < 36 ? aa0 : f < 42 ? aa1 : f < 51 ? aa2 : aa3) + (f / 3) * 2560 + (f % 3) * 64, 160); };
      auto ldo = [&](int k) { of[k] = __builtin_bit_cast(bf16x8, __builtin_amdgcn_raw_buffer_load_b128(orr, qo * 40960 + (hp * 20 + k) * 1024 + lane * 16, 0, 16)); };
#pragma unroll
      for (int k = 0; k < OPF; ++k) ldo(k);
      wait_word(smem + N_WO + hp * 4, 4);
#pragma unroll
      for (int f = 0; f < 4; ++f) af[f] = rda(f);
      SB();
      SFOR(f, 60)
          if constexpr (f % 3 == 0 && f / 3 + OPF < 20) ldo(f / 3 + OPF);
          if constexpr (f + 4 < 60) af[(f + 4) % 5] = rda(f + 4);
          if constexpr (f == 57) { if (hp) { wait_word(smem + C_RD + qo * 4, 1);
#pragma unroll
              for (int b = 0; b < 10; ++b) ps[b] = *(LAS f32x4*)(red + b * 1024); } }
          SB();
          if constexpr (f < 3) MFMA32Z(oc[f], af[f % 5], of[0]); else MFMA32I(oc[f % 3], af[f % 5], of[f / 3]);
          SB();
      SEND }
    MFMA_DRAIN();
    { if (!hp) {
          wait_word(smem + N_G1, 8);
#pragma unroll
          for (int b = 0; b < 10; ++b) *(LAS f32x4*)(red + b * 1024) = (f32x4){oc[b >> 2][4 * (b & 3)], oc[b >> 2][4 * (b & 3) + 1], oc[b >> 2][4 * (b & 3) + 2], oc[b >> 2][4 * (b & 3) + 3]};
          lds_set(smem + C_RD + qo * 4, 1);
      } else {
          char* orow = out + (size_t)(rt * 128 + qo * 32 + r32) * 2560 + br * 1280 + hg * 160 + hl * 16;
#pragma unroll
          for (int b = 0; b < 10; ++b)
#pragma unroll
              for (int r = 0; r < 4; ++r) ps[b][r] += oc[b >> 2][4 * (b & 3) + r];
          SB();
          u32x2 pk[10];
#pragma unroll
          for (int b = 0; b < 10; ++b) pk[b] = (u32x2){pk2(ps[b][0], ps[b][1]), pk2(ps[b][2], ps[b][3])};
          SB();
          u32x4 st[5];
#pragma unroll
          for (int b = 0; b < 10; b += 2) {
              auto rx = __builtin_amdgcn_permlane32_swap(pk[b][0], pk[b + 1][0], false, false); auto ry = __builtin_amdgcn_permlane32_swap(pk[b][1], pk[b + 1][1], false, false);
              st[b >> 1] = (u32x4){rx[0], ry[0], rx[1], ry[1]}; }
          SB();
#pragma unroll
          for (int b = 0; b < 5; ++b) *(u32x4*)(orow + b * 32) = st[b];
      } }
    if (bx == 0 && wid == 0) {
#pragma unroll 1
        for (int i = 0; i < 4; ++i) poll_flags((GAS u64*)(ws + WS_FO + (i * 64 + lane) * 256), true, tag);
        if (lane == 0) __hip_atomic_store((GAS u64*)ws, tag, RLX_AGENT);
    }
}

extern "C" void kernel_launch(void* const* d_in, const int* in_sizes, int n_in, void* d_out, int out_size, void* d_ws, size_t ws_size,
                              hipStream_t stream) {
    fused_attn_kernel<<<256, 512, 0, stream>>>((const char*)d_in[0], (const char*)d_in[1], (const char*)d_in[2], (const char*)d_in[3],
                                               (const char*)d_in[4], (const char*)d_in[5], (char*)d_out, (char*)d_ws);
}
```

```cpp
#include <hip/hip_runtime.h>
#include <hip/hip_bf16.h>
#include <stdint.h>

typedef short bf16x8 __attribute__((ext_vector_type(8)));
typedef short s16x4 __attribute__((ext_vector_type(4)));
typedef float f32x2 __attribute__((ext_vector_type(2)));
typedef float f32x4 __attribute__((ext_vector_type(4)));
typedef float f32x16 __attribute__((ext_vector_type(16)));
typedef unsigned u32x4 __attribute__((ext_vector_type(4)));
typedef unsigned u32x2 __attribute__((ext_vector_type(2)));
typedef __bf16 bfx2 __attribute__((ext_vector_type(2)));
typedef unsigned long long u64;
#define LAS __attribute__((address_space(3)))
#define GAS __attribute__((address_space(1)))
#define DI __device__ __forceinline__
#define RLX_AGENT __ATOMIC_RELAXED, __HIP_MEMORY_SCOPE_AGENT
#define RLX_WG __ATOMIC_RELAXED, __HIP_MEMORY_SCOPE_WORKGROUP

constexpr int LDS_BYTES = 163840;
constexpr int VOFF = 20480, VP = 168;
constexpr int KVB = VOFF + 128 * VP;
constexpr int KV0 = LDS_BYTES - 3 * KVB;
constexpr int OWN = KV0 + 2 * KVB;
constexpr int C_F = 0, C_P = 128, C_E = 144, C_QF = 160, C_RD = 176, C_CNT = 192, C_TAG = 320;
constexpr int N_PUB = C_CNT, N_NB = C_CNT + 4, N_G0 = C_CNT + 8, N_G1 = C_CNT + 12, N_WO = C_CNT + 16, N_OS = C_CNT + 24, N_KV = C_CNT + 32, N_OK = C_CNT + 36;
constexpr int XS0 = 512, WS0 = XS0 + 3 * 16384, QST = 512, ONE = 336, QU27 = XS0 + 16384, QU28 = XS0, QU29 = QU27 + 10240;
static_assert(WS0 + 7 * 10240 <= OWN, "ring overlaps the own image");
constexpr unsigned WS_FKV = 4096, WS_FO = WS_FKV + 256 * 128, WS_KV = 131072;
constexpr unsigned KVG = KVB, OG = 4 * 40960;
constexpr unsigned WS_O = WS_KV + 256 * KVG;
constexpr unsigned WS_END = WS_O + 32 * OG;
constexpr unsigned SPIN_CAP = 400000, LSPIN_CAP = 4000000;
constexpr float QSCALE = 0.22360679774997896f * 1.4426950408889634f;
constexpr int WIN = 4;
constexpr int OPF = 12;
constexpr int PFLIP = 6;
constexpr int ADELAY = 3;

#define MFMA32(a, b, c) __builtin_amdgcn_mfma_f32_32x32x16_bf16((a), (b), (c), 0, 0, 0)
#define MFMA32K8(a, b, c) __builtin_amdgcn_mfma_f32_32x32x8bf16_1k((a), (b), (c), 0, 0, 0)
#define MFMA32I(acc, a, b) asm volatile("v_mfma_f32_32x32x16_bf16 %0, %1, %2, %0" : "+v"(acc) : "v"(a), "v"(b))
#define MFMA32Z(acc, a, b) asm volatile("v_mfma_f32_32x32x16_bf16 %0, %1, %2, 0" : "=&v"(acc) : "v"(a), "v"(b))
#define MFMA_DRAIN() asm volatile("s_nop 7\n\ts_nop 4" ::: "memory")
#define WAITV(N) asm volatile("s_waitcnt vmcnt(" #N ")" ::: "memory")
#define BAR_L() asm volatile("s_waitcnt lgkmcnt(0)\n\ts_barrier" ::: "memory")
#define SB() __builtin_amdgcn_sched_barrier(0)
#define FENCE() asm volatile("" ::: "memory")


template <int V> struct IC { static constexpr int value = V; };
template <int I, int N, class F> DI void static_for(F&& f) { if constexpr (I < N) { f(IC<I>{}); static_for<I + 1, N>(f); } }
#define SFOR(var, N) static_for<0, N>([&](auto ic_##var) __attribute__((always_inline)) { constexpr int var = decltype(ic_##var)::value;
#define SEND });

DI void dma16(unsigned voff, const char* sbase, unsigned ldsaddr) {
    asm volatile("s_mov_b32 m0, %2\n\ts_nop 0\n\tglobal_load_lds_dwordx4 %0, %1" :: "v"(voff), "s"(sbase), "s"(ldsaddr) : "memory");
}
DI void dma16x4(unsigned v0, unsigned v1, unsigned v2, unsigned v3, const char* sbase, unsigned ldsaddr) {
    asm volatile("s_mov_b32 m0, %5\n\ts_nop 0\n\tglobal_load_lds_dwordx4 %0, %4\n\tglobal_load_lds_dwordx4 %1, %4 offset:1024\n\t"
                 "global_load_lds_dwordx4 %2, %4 offset:2048\n\tglobal_load_lds_dwordx4 %3, %4 offset:3072"
                 :: "v"(v0), "v"(v1), "v"(v2), "v"(v3), "s"(sbase), "s"(ldsaddr) : "memory");
}
DI void dma16x5(const unsigned (&v)[5], const char* sbase, unsigned ldsaddr) {
    asm volatile("s_mov_b32 m0, %6\n\ts_nop 0\n\tglobal_load_lds_dwordx4 %0, %5 offset:-2048\n\tglobal_load_lds_dwordx4 %1, %5 offset:-1024\n\t"
                 "global_load_lds_dwordx4 %2, %5\n\tglobal_load_lds_dwordx4 %3, %5 offset:1024\n\tglobal_load_lds_dwordx4 %4, %5 offset:2048"
                 :: "v"(v[0]), "v"(v[1]), "v"(v[2]), "v"(v[3]), "v"(v[4]), "s"(sbase), "s"(ldsaddr) : "memory");
}
DI void dma16x5_sc1(unsigned voff, const char* sbase, unsigned ldsaddr) {
    asm volatile("s_mov_b32 m0, %2\n\ts_nop 0\n\tglobal_load_lds_dwordx4 %0, %1 offset:-2048 sc1\n\tglobal_load_lds_dwordx4 %0, %1 offset:-1024 sc1\n\t"
                 "global_load_lds_dwordx4 %0, %1 sc1\n\tglobal_load_lds_dwordx4 %0, %1 offset:1024 sc1\n\tglobal_load_lds_dwordx4 %0, %1 offset:2048 sc1"
                 :: "v"(voff), "s"(sbase), "s"(ldsaddr) : "memory");
}
DI void dma16_sc1(unsigned voff, const char* sbase, unsigned ldsaddr) {
    asm volatile("s_mov_b32 m0, %2\n\ts_nop 0\n\tglobal_load_lds_dwordx4 %0, %1 sc1" :: "v"(voff), "s"(sbase), "s"(ldsaddr) : "memory");
}
DI void dma4(const char* p, unsigned ldsaddr) {
    asm volatile("s_mov_b32 m0, %1\n\ts_nop 0\n\tglobal_load_lds_dword %0, off" :: "v"(p), "s"(ldsaddr) : "memory");
}
DI unsigned pk2(float lo, float hi) { f32x2 v = {lo, hi}; return __builtin_bit_cast(unsigned, __builtin_convertvector(v, bfx2)); }
typedef short v4i16_t __attribute__((ext_vector_type(4)));
DI bf16x8 trfrag(LAS char* p, int second) {
    s16x4 a = __builtin_bit_cast(s16x4, __builtin_amdgcn_ds_read_tr16_b64_v4i16((LAS v4i16_t*)p));
    s16x4 b = __builtin_bit_cast(s16x4, __builtin_amdgcn_ds_read_tr16_b64_v4i16((LAS v4i16_t*)(p + second)));
    return __builtin_shufflevector(a, b, 0, 1, 2, 3, 4, 5, 6, 7);
}
constexpr u64 lanes_cut(int r, bool fartile) {
    u64 m = 0;
    for (int l = 0; l < 64; ++l) { const int key = (r & 3) + 8 * (r >> 2) + 4 * (l >> 5), d = (l & 31) - key + (fartile ? 32 : 0);
        const bool ok = d >= 0 && d <= 14 && !(d & 1);
        if (!ok) m |= 1ull << l; }
    return m;
}
DI float halfmax(float m) { auto rr = __builtin_amdgcn_permlane32_swap(__float_as_uint(m), __float_as_uint(m), false, false);
    return __builtin_elementwise_maximum(__uint_as_float(rr[0]), __uint_as_float(rr[1])); }

DI void poll_flags(GAS u64* word, bool active, u64 tag) {
#pragma unroll 1
    for (unsigned spins = 0; spins < SPIN_CAP; ++spins) {
        u64 v = tag;
        if (active) v = __hip_atomic_load(word, RLX_AGENT);
        if (__all(v == tag)) break;
        __builtin_amdgcn_s_sleep(2);
    }
    __builtin_amdgcn_fence(__ATOMIC_ACQUIRE, "wavefront");
}
DI int min4(LAS char* p) {
    const u64 a = __hip_atomic_load((LAS u64*)p, RLX_WG), b = __hip_atomic_load((LAS u64*)(p + 8), RLX_WG);
    const unsigned m = min(min((unsigned)a, (unsigned)(a >> 32)), min((unsigned)b, (unsigned)(b >> 32)));
    return __builtin_amdgcn_readfirstlane((int)m);
}
DI int wait_min4(LAS char* p, int need) {
    int v = min4(p);
#pragma unroll 1
    for (unsigned spins = 0; v < need && spins < LSPIN_CAP; ++spins) { __builtin_amdgcn_s_sleep(1); v = min4(p); }
    FENCE();
    return v;
}
DI void wait_word(LAS char* p, unsigned need) {
#pragma unroll 1
    for (unsigned spins = 0; spins < LSPIN_CAP; ++spins) {
        if ((unsigned)__builtin_amdgcn_readfirstlane((int)__hip_atomic_load((LAS unsigned*)p, RLX_WG)) >= need) break;
        __builtin_amdgcn_s_sleep(1); }
    FENCE();
}
DI void lds_inc0(LAS char* p, int lane) { FENCE(); if (lane == 0) (void)__hip_atomic_fetch_add((LAS unsigned*)p, 1u, RLX_WG); FENCE(); }
DI unsigned lds_inc(LAS char* p, int lane) { FENCE(); unsigned old = 0; if (lane == 0) old = __hip_atomic_fetch_add((LAS unsigned*)p, 1u, RLX_WG); FENCE(); return old; }
DI void lds_set(LAS char* p, unsigned v) { FENCE(); __hip_atomic_store((LAS unsigned*)p, v, RLX_WG); FENCE(); }

__global__ __launch_bounds__(512, 2) void fused_attn_kernel(const char* __restrict__ xA, const char* __restrict__ wA, const char* __restrict__ oA,
                                                           const char* __restrict__ xB, const char* __restrict__ wB, const char* __restrict__ oB,
                                                           char* __restrict__ out, char* __restrict__ ws) {
    __shared__ __attribute__((aligned(16))) char smem_[LDS_BYTES];
    LAS char* smem = (LAS char*)smem_;
    const unsigned lds0 = (unsigned)(uintptr_t)smem;
    asm volatile("" :: "s"(xA), "s"(wA), "s"(oA), "s"(xB), "s"(wB), "s"(oB), "s"(out), "s"(ws));
    const int tid = threadIdx.x, lane = tid & 63;
    const int wid = __builtin_amdgcn_readfirstlane(tid >> 6);
    const int bx = blockIdx.x, xcd = bx & 7, idx = bx >> 3;
    const int isA = idx >> 4, rt = (xcd >> 1) * 4 + ((idx >> 2) & 3), hg = (xcd & 1) * 4 + (idx & 3);
    const int br = isA ? 0 : 1;
    { u64 pc; unsigned len;
      asm volatile("s_getpc_b64 %0\n.Lfused_attn_pc:\n\ts_mov_b32 %1, .Lfused_attn_end-.Lfused_attn_pc" : "=s"(pc), "=s"(len));
      const unsigned o = ((idx & 15) * 64 + lane) * 128;
      if (wid == 3 && o < len) dma4((const char*)pc + o, lds0 + OWN + 3 * 512); }
    const int k0 = isA ? (((rt & 3) * 5 + 1) / 2 + ((hg & 3) * 5 + 1) / 2) % 10 : 0;
    const char* xb = (isA ? xA : xB) + (size_t)rt * (128 * 1280);
    const char* wb = (isA ? wA : wB) + hg * 160;
    const int me = (br * 16 + rt) * 8 + hg;
    const int r32 = lane & 31, hl = lane >> 5;
    auto kt_of = [&](int s) { int k = s + k0; return k >= 10 ? k - 10 : k; };
    if (isA && wid < 4) {
        const int j16 = idx & 15; const unsigned dump = lds0 + OWN + wid * 512;
        const char* wl = wB + (hg >> 2) * 640 + j16 * (4 * 3840);
        const char* xl = xB + (size_t)(rt * 128 + (hg & 3) * 32 + r32) * 1280 + hl * 128;
        auto pfx = [&](int i) { return xl + i * 256; };
        auto pfkv = [&](int i) { int n = i * 64 + lane; n = n < 400 ? n : 399; const int sl = n / 40, r = n % 40, c = r % 10;
            return wl + (sl * 64 + r / 10) * 3840 + 1280 + (c < 5 ? c * 128 : 640 + c * 128); };
        auto pfq = [&](int i) { int n = i * 64 + lane; n = n < 200 ? n : 199; int sl = n / 20 + 8; sl = sl >= 10 ? sl - 10 : sl; const int r = n % 20;
            return wl + (sl * 64 + r / 5) * 3840 + (r % 5) * 128; };
        dma4(wid & 1 ? pfx(wid >> 1) : pfkv(wid >> 1), dump);
        dma4(wid & 1 ? pfx(2 + (wid >> 1)) : pfkv(2 + (wid >> 1)), dump);
        dma4(wid < 3 ? pfkv(4 + wid) : pfx(4), dump);
        dma4(pfq(wid), dump);
        { int n = wid * 64 + lane; n = n < 200 ? n : 199; const int o = (j16 * 40 + n / 5) * 1280 + (hg >> 2) * 640 + (n % 5) * 128;
          dma4(oB + o, dump); dma4(oA + o, dump); }
    }
    if (isA && wid >= 4) { for (int i = 0; i < ADELAY; ++i) __builtin_amdgcn_s_sleep(16); }

    const unsigned xo = (unsigned)((lane >> 3) * 1280 + (((lane & 7) ^ (lane >> 4)) << 4));
    auto w_off = [&](int q) { const int c = q * 64 + lane, row = __umul24(c, 3277) >> 16, j = c - row * 20; return (unsigned)(__umul24(row, 3840) + j * 16 + (j < 10 ? 1280 : 2400)); };
    if (!isA) { const char* xs = xb + k0 * 128 + wid * (16 * 1280);
      dma16(xo, xs, lds0 + XS0 + wid * 2048);
      dma16(xo ^ 64, xs + 8 * 1280, lds0 + XS0 + wid * 2048 + 1024);
      const char* wsrc = wb + (size_t)k0 * (64 * 3840);
      const unsigned wv = w_off(wid < 5 ? wid : wid - 5);
      if (wid < 5) { dma16(wv, wsrc, lds0 + WS0 + wid * 1024); dma16(wv, wsrc + 16 * 3840, lds0 + WS0 + (5 + wid) * 1024);
          if (wid >= 3) { dma16(wv, wsrc + 32 * 3840, lds0 + WS0 + (10 + wid) * 1024); dma16(wv, wsrc + 48 * 3840, lds0 + WS0 + (15 + wid) * 1024); } }
      else { dma16(wv, wsrc + 32 * 3840, lds0 + WS0 + (5 + wid) * 1024); dma16(wv, wsrc + 48 * 3840, lds0 + WS0 + (10 + wid) * 1024); } }
    if (wid == 4 && lane < 2) dma4(ws + lane * 4, lds0 + C_TAG);
    if (tid < 80) *(LAS unsigned*)(smem + tid * 4) = 0u;
    else if (tid < 82) *(LAS unsigned*)(smem + ONE + (tid - 80) * 4) = 0x3F803F80u;
    const int nnb = isA ? (rt > 0 ? 1 : 0) : (rt > 1 ? 2 : rt);
    auto kq_base = [&](int fg) { const int hd = fg / 5, dg = fg % 5; return dg < 4 ? hd * 5120 + (dg >> 1) * 2048 + (dg & 1) * 8 : hd * 5120 + 4096; };

    auto rdtag = [&]() { const u64 t = *(LAS u64*)(smem + C_TAG);
        const u64 e = ((u64)(unsigned)__builtin_amdgcn_readfirstlane((int)(t >> 32)) << 32) | (unsigned)__builtin_amdgcn_readfirstlane((int)t);
        return e * 6364136223846793005ull + 1442695040888963407ull; };
    const __amdgpu_buffer_rsrc_t wor = __builtin_amdgcn_make_buffer_rsrc((void*)(isA ? oA : oB), 0, 640 * 1280, 0x00020000);

    const int hp = wid >> 2, qo = wid & 3, qi = hp ? 3 - qo : qo;
    const int ud = qi + 8;
    const bool far = isA ? (4 * rt + qi >= 1) : (4 * rt + qi >= 8);
    const int u0 = isA ? (far ? ud - 1 : ud) : (far ? qi : 8 - 4 * rt);
    f32x16 cd, cf;
    int dsh, kb1, kb2, nh, vb0, vb1, qb1, qb2; unsigned obase, oo[5];
    auto mask_b = [&](f32x16& c, int r, bool fartile) { const int k = (r & 3) + 8 * (r >> 2); c[r] = __int_as_float(fartile ? ((k - 1) << 23) - dsh : dsh - (k << 23)); };
    auto mask_a = [&](int z) {
      SFOR(r, 16)
          constexpr u64 ad = lanes_cut(r, false), af = lanes_cut(r, true);
          cd[r] = __builtin_amdgcn_inverse_ballot_w64(ad + z) ? -__builtin_inff() : 0.f;
          cf[r] = __builtin_amdgcn_inverse_ballot_w64(af + z) ? -__builtin_inff() : 0.f;
      SEND };
    auto att_setup = [&](int part) {
        int l = tid; asm volatile("" : "+v"(l)); l &= 63;
        const int q = l & 31, h = l >> 5;
        if (part == 0) { dsh = (q - 4 * h) << 23; kb1 = KV0 + hp * 10240 + h * 2048 + q * 16; kb2 = h ? ONE : KV0 + hp * 10240 + 4096 + q * 8; nh = h ^ 1;
            asm volatile("" : "+v"(dsh), "+v"(kb1), "+v"(kb2), "+v"(nh)); }
        if (part == 1) { const bool ones = (l & 16) && (l & 3);
            vb0 = KV0 + VOFF + (4 * h + ((l & 15) >> 2)) * VP + (ones ? 160 : hp * 80 + (l & 16) * 2 + (l & 3) * 8); vb1 = vb0 + (ones ? 0 : 40);
            asm volatile("" : "+v"(vb0), "+v"(vb1)); }
        if (part == 2) { qb1 = QST + hp * 10240 + h * 2048 + (qi * 32 + q) * 16; qb2 = QST + hp * 10240 + 4096 + (qi * 32 + q) * 8;
            obase = (unsigned)(qi * 40960 + hp * 20480 + hg * 2560 + q * 16 + h * 512);
            asm volatile("" : "+v"(qb1), "+v"(qb2), "+v"(obase)); }
        if (part >= 3) {
#pragma unroll
            for (int r = 4 * (part - 3); r < 4 * (part - 2); ++r) mask_b(cd, r, false);
            asm volatile("" : "+v"(cd)); } };
    auto wo_setup = [&](int q) {
        int l = tid; asm volatile("" : "+v"(l)); const int c = q * 64 + (l & 63), rho = __umul24(c, 6554) >> 16;
        oo[q] = (unsigned)(__umul24(qo * 160 + hp * 40 + (rho & ~7) + 4 * (rho & 1) + ((rho >> 1) & 3), 1280) + hg * 160 + (c - rho * 10) * 16);
        asm volatile("" : "+v"(oo[q])); };
    u64 tag;

    if (wid < 4) {
        if (!isA) WAITV(0);
        BAR_L();
        bf16x8 xr[32], xt[2], wf[WIN + 1];
        f32x16 acc[5];
        const int tok = wid * 32 + r32;
        const int xa = XS0 + tok * 128 + ((hl ^ ((tok >> 1) & 7)) << 4);
        int wa0 = WS0 + (8 * hl + ((lane & 15) >> 2)) * 320 + (lane & 16) * 2 + (lane & 3) * 8, wa1 = wa0 + 61440;
        asm volatile("" : "+v"(wa0), "+v"(wa1));
        auto rdx = [&](int i) { return *(LAS bf16x8*)(smem + ((i >> 2) % 3) * 16384 + (xa ^ ((i & 3) * 32))); };
        auto rdw = [&](int f) { const int i = f / 5, t = f % 5, sl = (i >> 1) % 7;
            return trfrag(smem + (sl < 6 ? wa0 + sl * 10240 : wa1) + (i & 1) * 5120 + t * 64, 1280); };
        unsigned fl = 0;
        auto rdflag = [&](int u) { fl = __hip_atomic_load((LAS unsigned*)(smem + C_F + u * 4), RLX_WG); };
        auto check = [&](int u) {
            if (__builtin_amdgcn_readfirstlane(fl) != 15) {
#pragma unroll 1
                for (unsigned spins = 0; spins < LSPIN_CAP; ++spins) { rdflag(u); if (__builtin_amdgcn_readfirstlane(fl) == 15) break; __builtin_amdgcn_s_sleep(1); }
            }
            FENCE(); };
#pragma unroll
        for (int i = 0; i < 4; ++i) xr[i] = rdx(i);
#pragma unroll
        for (int f = 0; f < WIN; ++f) wf[f] = rdw(f);
        SB();
        SFOR(f, 200)
            constexpr int i = f / 5, t = f % 5;
            if constexpr (f % 20 == 10 - WIN) lds_set(smem + C_P + wid * 4, (i >> 1) + 1);
            if constexpr (f == 200 - WIN) lds_set(smem + C_P + wid * 4, 20);
            if constexpr (f % 20 == 10) rdflag(f / 20 + 1);
            if constexpr (f % 20 == 20 - WIN && f < 190) check(f / 20 + 1);
            if constexpr (f + WIN < 200) wf[(f + WIN) % (WIN + 1)] = rdw(f + WIN);
            if constexpr (f % 20 >= 16 && f < 140) xr[(f / 20 + 1) * 4 + f % 20 - 16] = rdx((f / 20 + 1) * 4 + f % 20 - 16);
            if constexpr (t == 1 && i >= 31 && i < 39) xt[(i + 1) & 1] = rdx(i + 1);
            SB();
            if constexpr (i == 0) MFMA32Z(acc[t], wf[f % (WIN + 1)], xr[0]);
            else if constexpr (i < 32) MFMA32I(acc[t], wf[f % (WIN + 1)], xr[i]);
            else MFMA32I(acc[t], wf[f % (WIN + 1)], xt[i & 1]);
            SB();
        SEND
        int l2 = tid; asm volatile("" : "+v"(l2)); l2 &= 63;
        const int h2 = l2 >> 5, tk = wid * 32 + (l2 & 31);
        int eb[5];
        auto ebases = [&](int base) { int l = tid; asm volatile("" : "+v"(l)); const int h = (l >> 5) & 1, k = wid * 32 + (l & 31);
            eb[0] = base + k * 16 + h * 8; eb[1] = base + k * 16 + h * 2040; eb[2] = base + (h ? k * 16 + 1024 : k * 8); eb[3] = base + (h ? k * 8 + 2040 : k * 16);
            asm volatile("" : "+v"(eb[0]), "+v"(eb[1]), "+v"(eb[2]), "+v"(eb[3])); };
        auto kq_addr = [&](int g0) { return eb[g0 % 5 == 1 ? 1 : g0 % 5 == 4 ? 2 : g0 % 5 == 3 ? 3 : 0] + kq_base(g0); };
        ebases(OWN);
        eb[4] = OWN + VOFF + tk * VP + h2 * 8; asm volatile("" : "+v"(eb[4]));
        int qa0 = (8 * h2 + 2 * ((l2 & 15) >> 2)) * 160 + (l2 & 16) * 2 + (l2 & 3) * 8, qa1 = qa0 + WS0 + 10240;
        asm volatile("" : "+v"(qa0), "+v"(qa1));
        auto rdq = [&](int f) { const int i = f / 3, t = f % 3, j = i >> 2, sl = (20 + j) % 7;
            return trfrag(smem + (j >= 7 ? qa0 + (j == 7 ? QU27 : j == 8 ? QU28 : QU29) : sl == 0 ? qa0 + WS0 : qa1 + (sl - 1) * 10240) + (i & 3) * 2560 + t * 64, 160); };
        MFMA_DRAIN();
        WAITV(0);
        { u32x2 pk[20];
#pragma unroll
          for (int n = 0; n < 20; ++n) { const int t = n >> 2, b = n & 3; pk[n] = (u32x2){pk2(acc[t][4 * b], acc[t][4 * b + 1]), pk2(acc[t][4 * b + 2], acc[t][4 * b + 3])}; }
          SB();
#pragma unroll
          for (int n = 0; n < 20; ++n) *(LAS u32x2*)(smem + (n < 10 ? kq_addr(2 * n) : eb[4] + (2 * n - 20) * 8)) = pk[n];
          if (h2 == 0) *(LAS u32x2*)(smem + eb[4] + 160) = (u32x2){0x3F803F80u, 0x3F803F80u}; }
        lds_set(smem + C_E + wid * 4, 1);
        check(10);
        xt[0] = rdx(32);
#pragma unroll
        for (int f = 0; f < WIN; ++f) wf[f] = rdq(f);
        SB();
        u64 pv0 = 0, pv1 = 0;
        SFOR(f, 120)
            constexpr int i = f / 3, t = f % 3, j = i >> 2;
            if constexpr (f % 24 == 12 && j < 9) rdflag(11 + j / 2);
            if constexpr (f % 12 == 12 - WIN && (j == 1 || j == 6 || j == 8)) lds_set(smem + C_P + wid * 4, 21 + j);
            if constexpr (f % 24 == 24 - WIN && j < 9) check(11 + j / 2);
            if constexpr (f + WIN < 120) wf[(f + WIN) % (WIN + 1)] = rdq(f + WIN);
            if constexpr (t == 0 && i < 7) xt[(i + 1) & 1] = rdx(32 + i + 1);
            if constexpr (f >= 90 && f < 100 && !(f & 1)) wo_setup((f - 90) / 2);
            if constexpr (f == 99) tag = rdtag();
            if constexpr (f >= 100 && f < 107) att_setup(f - 100);
            if constexpr (f == 108) ebases(QST);
            if constexpr (f == 116) { pv0 = __hip_atomic_load((LAS u64*)(smem + C_P), RLX_WG); pv1 = __hip_atomic_load((LAS u64*)(smem + C_P + 8), RLX_WG); }
            SB();
            if constexpr (i == 0) MFMA32Z(acc[t], wf[f % (WIN + 1)], xt[0]);
            else if constexpr (i < 8) MFMA32I(acc[t], wf[f % (WIN + 1)], xt[i & 1]);
            else MFMA32I(acc[t], wf[f % (WIN + 1)], xr[i - 8]);
            SB();
        SEND
        MFMA_DRAIN();
        SB();
        if (__builtin_amdgcn_readfirstlane((int)min(min((unsigned)pv0, (unsigned)(pv0 >> 32)), min((unsigned)pv1, (unsigned)(pv1 >> 32)))) < 29) wait_min4(smem + C_P, 29);
        { acc[0] *= QSCALE; acc[1] *= QSCALE; acc[2] *= QSCALE;
          SB();
          u32x2 pk[10];
#pragma unroll
          for (int n = 0; n < 10; ++n) { const int t = n >> 2, b = n & 3; pk[n] = (u32x2){pk2(acc[t][4 * b], acc[t][4 * b + 1]), pk2(acc[t][4 * b + 2], acc[t][4 * b + 3])}; }
          SB();
#pragma unroll
          for (int n = 0; n < 10; ++n) *(LAS u32x2*)(smem + kq_addr(2 * n)) = pk[n]; }
        lds_set(smem + C_QF + wid * 4, 1);
        __builtin_amdgcn_s_setprio(1);
    } else {
        const int hw = wid - 4;
        int known = 0;
        __builtin_amdgcn_s_setprio(3);
        auto lds_or = [&](int off, unsigned v) { FENCE(); if (lane == 0) __hip_atomic_fetch_or((LAS unsigned*)(smem + off), v, RLX_WG); FENCE(); };
        unsigned wo[5], qo[5];
        auto done = [&](int n) { lds_or(C_F + n * 4, 1u << hw); };
        const unsigned xo1 = (xo ^ 64) + 9216, xo2 = xo + 2 * 9216, xo3 = (xo ^ 64) + 3 * 9216;
        auto round_x = [&](int n) { dma16x4(xo, xo1, xo2, xo3, xb + kt_of(n) * 128 + hw * (32 * 1280), lds0 + XS0 + (n % 3) * 16384 + hw * 4096); };
        auto round_w = [&](int n) {
            const int u = 2 * n + (hw >> 1);
            const unsigned dst = lds0 + (u < 27 ? WS0 + (u % 7) * 10240 : u == 27 ? QU27 : u == 28 ? QU28 : QU29) + (hw & 1) * 5120 + 2048;
            if (u < 20) dma16x5(wo, wb + (size_t)(kt_of(u >> 1) * 64 + (u & 1) * 32) * 3840 - 2048, dst);
            else dma16x5(qo, wb + (size_t)kt_of(u < 22 ? u - 12 : u - 22) * (64 * 3840) - 2048, dst); };
        if (isA) round_x(0);
        round_x(1);
#pragma unroll
        for (int q = 0; q < 5; ++q) wo[q] = w_off(q) + (hw & 1) * (16 * 3840) + (4 - q) * 1024;
        if (isA) { round_w(0); round_w(1); WAITV(5); } else { round_w(1); WAITV(9); }
        BAR_L();
#pragma unroll
        for (int q = 0; q < 5; ++q) { const int c = q * 64 + lane, rho = __umul24(c, 6554) >> 16;
            qo[q] = (unsigned)(__umul24((hw & 1) * 32 + (rho & ~7) + 4 * (rho & 1) + ((rho >> 1) & 3), 3840) + (c - rho * 10) * 16 + (4 - q) * 1024); }
#pragma unroll 1
        for (int n = 2; n <= 13; ++n) {
            const int need = n < 13 ? 2 * n - 5 : 20;
            if (need > known) known = min4(smem + C_P);
            if (need > known) { WAITV(0); done(n - 1); known = wait_min4(smem + C_P, need); }
            if (n < 10) round_x(n);
            round_w(n);
            if (n < 10) WAITV(9); else WAITV(5);
            done(n - 1);
        }
        WAITV(0); done(13);
        tag = rdtag();
        auto a_chunk = [&](int a) { int o;
            if (a < 320) { const int hd = a / 80, rem = a % 80; o = hd * 5120 + (rem < 32 ? (96 + rem) * 16 : rem < 64 ? 2048 + (64 + rem) * 16 : 4096 + 768 + (rem - 64) * 16); }
            else o = VOFF + 96 * VP + (a - 320) * 16;
            return o; };
        if (rt < 15) {
            const __amdgpu_buffer_rsrc_t kvr = __builtin_amdgcn_make_buffer_rsrc(ws + WS_KV + me * KVG, 0, KVB, 0x00020000);
            wait_min4(smem + C_E, 1);
            if (!isA) { u32x4 v[11];
#pragma unroll
                for (int j = 0; j < 11; ++j) if (j < 10 || hw == 3) v[j] = *(LAS u32x4*)(smem + OWN + hw * 10240 + j * 1024 + lane * 16);
#pragma unroll
                for (int j = 0; j < 11; ++j) if (j < 10 || hw == 3) __builtin_amdgcn_raw_buffer_store_b128(v[j], kvr, hw * 10240 + j * 1024 + lane * 16, 0, 16);
            } else {
#pragma unroll
                for (int j = 0; j < 3; ++j) { const int a = (4 * j + hw) * 64 + lane;
                    if (a < 656) { const int o = a_chunk(a); __builtin_amdgcn_raw_buffer_store_b128(*(LAS u32x4*)(smem + OWN + o), kvr, o, 0, 16); } }
            }
        }
        wait_min4(smem + C_P, 22);
        round_w(14);
        WAITV(5);
        if (rt < 15) {
            unsigned old = 0;
            if (lane == 0) old = __hip_atomic_fetch_add((LAS unsigned*)(smem + N_PUB), 1u, RLX_WG);
            if (lane == 0 && old == 3) __hip_atomic_store((GAS u64*)(ws + WS_FKV + me * 128), tag, RLX_AGENT);
        }
        WAITV(0); done(14);
        __builtin_amdgcn_s_setprio(0);
#pragma unroll
        for (int p = 0; p < 7; ++p) att_setup(p);
#pragma unroll
        for (int q = 0; q < 5; ++q) wo_setup(q);
        __builtin_amdgcn_s_setprio(3);
        if (hw == 0) { poll_flags((GAS u64*)(ws + WS_FKV + (me - 8 * (lane + 1)) * 128), lane < nnb, tag); lds_set(smem + N_KV, 1); }
        else wait_word(smem + N_KV, 1);
        if (!isA) {
            const int n = hw >> 1, half = hw & 1;
            if (n < nnb) {
                const char* src = ws + WS_KV + (me - 8 * (n + 1)) * KVG + half * 20480; const unsigned dst = lds0 + KV0 + (1 - n) * KVB + half * 20480;
                wait_min4(smem + C_P, 27);
#pragma unroll
                for (int j = 0; j < 4; ++j) dma16x5_sc1(lane * 16, src + j * 5120 + 2048, dst + j * 5120 + 2048);
                if (half) dma16_sc1(lane * 16, src + 20480, dst + 20480);
            }
        } else if (nnb) {
            const __amdgpu_buffer_rsrc_t nbr = __builtin_amdgcn_make_buffer_rsrc(ws + WS_KV + (me - 8) * KVG, 0, KVB, 0x00020000);
            u32x4 v[3];
#pragma unroll
            for (int j = 0; j < 3; ++j) { const int a = (4 * j + hw) * 64 + lane; if (a < 656) v[j] = __builtin_amdgcn_raw_buffer_load_b128(nbr, a_chunk(a), 0, 16); }
            wait_min4(smem + C_P, 27);
#pragma unroll
            for (int j = 0; j < 3; ++j) { const int a = (4 * j + hw) * 64 + lane; if (a < 656) *(LAS u32x4*)(smem + KV0 + KVB + a_chunk(a)) = v[j]; }
        }
        __builtin_amdgcn_s_setprio(2);
        wait_word(smem + C_QF + qi * 4, 1);
    }
    u32x4 wreg[13];
    auto ldwo = [&](int i) { wreg[i] = __builtin_amdgcn_raw_buffer_load_b128(wor, oo[i % 5] + (i / 5) * (32 * 1280) + (i > 6 || (i == 6 && lane >= 16) ? 40 * 1280 : 0), 0, 0); };
    u32x2 ob[2][3];
    {
        bf16x8 q1[2]; u32x2 q2[2];
#pragma unroll
        for (int e = 0; e < 2; ++e) {
            q1[e] = *(LAS bf16x8*)(smem + qb1 + e * 5120);
            q2[e] = *(LAS u32x2*)(smem + qb2 + e * 5120);
            if (hl) q2[e] = (u32x2){0u, 0u};
        }
        if (isA) mask_a(0);
        struct KF { bf16x8 a; s16x4 b; };
        struct VF { bf16x8 a, b; };
        auto rdk = [&](int u, int e) { const int so = (u >> 2) * KVB, tw = u & 3; KF k;
            k.a = *(LAS bf16x8*)(smem + kb1 + so + tw * 512 + e * 5120); k.b = *(LAS s16x4*)(smem + __umul24(so + tw * 256 + e * 5120, nh) + kb2); return k; };
        auto rdv = [&](int u, int e) { LAS char* p = smem + (e ? vb1 : vb0) + (u >> 2) * KVB + (u & 3) * (32 * VP); VF v;
            v.a = trfrag(p, 8 * VP); v.b = trfrag(p + 16 * VP, 8 * VP); return v; };
        auto qk = [&](const KF& k, int e, const f32x16& c) { return MFMA32K8(k.b, __builtin_bit_cast(s16x4, q2[e]), MFMA32(k.a, q1[e], c)); };
        const f32x16 zero = {};
        auto max16 = [&](const f32x16& s, float m) {
#pragma unroll
            for (int r = 0; r < 16; ++r) m = __builtin_elementwise_maximum(m, s[r]);
            return m; };
        auto set_ref = [&](float m, int e) {
            const unsigned t0 = pk2(-m, 0.f); const float r1 = -m - __uint_as_float(t0 << 16);
            const unsigned t1 = pk2(r1, 0.f); const float r2 = r1 - __uint_as_float(t1 << 16);
            if (hl) q2[e] = (u32x2){t0 | (t1 << 16), pk2(r2, 0.f)}; };
        auto ex4 = [&](f32x16& s, int b) {
#pragma unroll
            for (int r = 0; r < 4; ++r) s[b + r] = __builtin_amdgcn_exp2f(s[b + r]); };
        auto pk8 = [&](const f32x16& s, int b) { return __builtin_bit_cast(bf16x8, (u32x4){pk2(s[b], s[b + 1]), pk2(s[b + 2], s[b + 3]), pk2(s[b + 4], s[b + 5]), pk2(s[b + 6], s[b + 7])}); };
        auto phase = [&](auto nx, f32x16& cur, const VF& v, f32x16& o, f32x16& nxt, const KF& k, int e) __attribute__((always_inline)) {
            constexpr int NX = decltype(nx)::value;
            SB(); ex4(cur, 0); SB();
            if constexpr (NX) nxt = MFMA32(k.a, q1[e], NX == 1 ? zero : NX == 2 ? cf : cd);
            SB(); ex4(cur, 4); { const bf16x8 p = pk8(cur, 0); SB(); o = MFMA32(v.a, p, o); }
            SB(); ex4(cur, 8); SB();
            if constexpr (NX) nxt = MFMA32K8(k.b, __builtin_bit_cast(s16x4, q2[e]), nxt);
            SB(); ex4(cur, 12); { const bf16x8 p = pk8(cur, 8); SB(); o = MFMA32(v.b, p, o); }
            SB(); };
        f32x16 o0, o1, sa, sb; KF kx; VF vx;
        auto step = [&](auto m0, auto m1, int u) __attribute__((always_inline)) {
            constexpr int M1 = decltype(m1)::value;
            KF ky; if constexpr (M1) ky = rdk(u - 1, 0);
            const VF vy = rdv(u, 1);
            phase(m0, sa, vx, o0, sb, kx, 1);
            if constexpr (M1) { kx = rdk(u - 1, 1); vx = rdv(u - 1, 0); }
            phase(m1, sb, vy, o1, sa, ky, 0); };
        auto nbwait = [&](int u) { if (u == 8) wait_word(smem + N_NB, 4); };
        { const KF k0f = rdk(ud, 0); kx = rdk(ud, 1); vx = rdv(ud, 0);
          const f32x16 s0 = qk(k0f, 0, cd), s1 = qk(kx, 1, cd);
          set_ref(halfmax(max16(s0, s0[0])), 0); set_ref(halfmax(max16(s1, s1[0])), 1);
          sa = qk(k0f, 0, cd); }
        if (hp) { WAITV(0); lds_inc0(smem + N_NB, lane); lds_inc0(smem + N_G0, lane); }
        o0 = zero; o1 = zero;
        if (u0 + 8 == ud) {
            SFOR(it, 9)
                if constexpr (it < 7) { ldwo(2 * it); if constexpr (it < 6) ldwo(2 * it + 1); }
                if constexpr (it == PFLIP) { if (hp) __builtin_amdgcn_s_setprio(0); }
                if constexpr (it == 7) {
#pragma unroll
                    for (int r = 0; r < 16; ++r) mask_b(cf, r, true); }
                if constexpr (it < 4) nbwait(ud - it);
                step(IC<it == 0 ? 3 : it == 8 ? 2 : 1>{}, IC<it == 8 ? 0 : it == 7 ? 2 : 1>{}, ud - it);
            SEND
        } else {
            __builtin_amdgcn_s_setprio(0);
#pragma unroll
            for (int i = 0; i < 13; ++i) ldwo(i);
            if (!isA) cf = zero;
            if (u0 < ud) {
                nbwait(ud);
                if (u0 + 1 < ud) {
                    step(IC<3>{}, IC<1>{}, ud);
#pragma unroll 1
                    for (int u = ud - 1; u > u0 + 1; --u) { nbwait(u); step(IC<1>{}, IC<1>{}, u); }
                    nbwait(u0 + 1);
                    step(IC<1>{}, IC<2>{}, u0 + 1);
                } else step(IC<3>{}, IC<2>{}, ud);
                step(IC<2>{}, IC<0>{}, u0);
            } else step(IC<3>{}, IC<0>{}, ud);
        }
        if (!__all(o0[12] < 1.152921504606847e18f && o1[12] < 1.152921504606847e18f && o0[12] > 0.25f && o1[12] > 0.25f)) {
            if (isA) { int z = 0; asm volatile("" : "+s"(z)); mask_a(z); }
            else { asm volatile("" : "+v"(dsh));
#pragma unroll
                for (int r = 0; r < 16; ++r) { mask_b(cd, r, false); mask_b(cf, r, true); }
                if (!far) cf = zero; }
#pragma unroll
            for (int e = 0; e < 2; ++e) {
                if (hl) q2[e] = (u32x2){0u, 0u};
                auto tile = [&](int u) { return u == ud ? qk(rdk(u, e), e, cd) : u == u0 ? qk(rdk(u, e), e, cf) : qk(rdk(u, e), e, zero); };
                float m = -__builtin_inff();
#pragma unroll 1
                for (int u = u0; u <= ud; ++u) m = max16(tile(u), m);
                m = halfmax(m);
                f32x16 o = zero;
#pragma unroll 1
                for (int u = u0; u <= ud; ++u) { f32x16 t = tile(u) - m;
                    const VF v = rdv(u, e); ex4(t, 0); ex4(t, 4); ex4(t, 8); ex4(t, 12);
                    o = MFMA32(v.a, pk8(t, 0), o); o = MFMA32(v.b, pk8(t, 8), o); }
                if (e) o1 = o; else o0 = o;
            }
        }
        const float rl0 = __builtin_amdgcn_rcpf(o0[12]), rl1 = __builtin_amdgcn_rcpf(o1[12]);
        SB();
#pragma unroll
        for (int r = 0; r < 12; ++r) { o0[r] *= rl0; o1[r] *= rl1; }
        SB();
#pragma unroll
        for (int j = 0; j < 3; ++j) {
            ob[0][j] = (u32x2){pk2(o0[4 * j], o0[4 * j + 1]), pk2(o0[4 * j + 2], o0[4 * j + 3])};
            ob[1][j] = (u32x2){pk2(o1[4 * j], o1[4 * j + 1]), pk2(o1[4 * j + 2], o1[4 * j + 3])}; }
        SB();
    }
    const __amdgpu_buffer_rsrc_t orr = __builtin_amdgcn_make_buffer_rsrc(ws + WS_O + (br * 16 + rt) * OG, 0, 4 * 40960, 0x00020000);
    {
      { auto rx = __builtin_amdgcn_permlane32_swap(ob[0][0][0], ob[0][1][0], false, false);
        auto ry = __builtin_amdgcn_permlane32_swap(ob[0][0][1], ob[0][1][1], false, false);
        __builtin_amdgcn_raw_buffer_store_b128((u32x4){rx[0], ry[0], rx[1], ry[1]}, orr, obase, 0, 16); }
      if (hl == 0) __builtin_amdgcn_raw_buffer_store_b128((u32x4){ob[0][2][0], ob[0][2][1], ob[1][0][0], ob[1][0][1]}, orr, obase + 1024, 0, 16);
      { auto fx = __builtin_amdgcn_permlane32_swap(ob[1][0][0], ob[1][1][0], false, false);
        auto fy = __builtin_amdgcn_permlane32_swap(ob[1][0][1], ob[1][1][1], false, false);
        auto sx = __builtin_amdgcn_permlane32_swap(ob[1][1][0], ob[1][2][0], false, false);
        auto sy = __builtin_amdgcn_permlane32_swap(ob[1][1][1], ob[1][2][1], false, false);
        __builtin_amdgcn_raw_buffer_store_b128((u32x4){fx[1], fy[1], sx[0], sy[0]}, orr, obase + 1536, 0, 16); } }
    if (lane == 0) { (void)__hip_atomic_fetch_add((LAS unsigned*)(smem + N_G1), 1u, RLX_WG);
        if (!hp) (void)__hip_atomic_fetch_add((LAS unsigned*)(smem + N_G0), 1u, RLX_WG); }
    FENCE();
    auto signal_o = [&]() {
        if (lds_inc(smem + N_OS + hp * 4, lane) == 3 && lane == 0) __hip_atomic_store((GAS u64*)(ws + WS_FO + (me * 2 + hp) * 128), tag, RLX_AGENT); };
    __builtin_amdgcn_s_setprio(0);
    WAITV(0); signal_o();
    wait_word(smem + (hp ? N_G1 : N_G0), 8);
    const int p1 = hp ? 30720 : 35840, p2 = hp ? 65536 : 43520;
    const int wb0 = hp ? KV0 + 10240 : 512, wb1 = hp ? KV0 + KVB + 10240 - 30720 : KV0 - 35840, wb2 = KV0 + KVB - 43520;
#pragma unroll
    for (int i = 0; i < 13; ++i) { const int L = qo * 12800 + i * 1024;
        if (i < 12 || lane < 32) *(LAS u32x4*)(smem + (L + (L < p1 ? wb0 : L < p2 ? wb1 : wb2)) + lane * 16) = wreg[i]; }
    lds_inc0(smem + N_WO + hp * 4, lane);
    if (qo == 0) { poll_flags((GAS u64*)(ws + WS_FO + (((me & ~7) + (lane & 7)) * 2 + hp) * 128), lane < 8, tag); lds_set(smem + N_OK + hp * 4, 1); }
    else wait_word(smem + N_OK + hp * 4, 1);
    f32x16 oc[3]; f32x4 ps[10];
    LAS char* red = smem + OWN + qo * 10240 + lane * 16;
    { bf16x8 af[5], of[20];
      const int la = (8 * hl + 2 * ((lane & 15) >> 2)) * 160 + (lane & 16) * 2 + (lane & 3) * 8;
      const int aa0 = wb0 + la, aa1 = (hp ? wb1 : wb0) + la, aa2 = wb1 + la, aa3 = (hp ? wb1 : wb2) + la;
      auto rda = [&](int f) { return trfrag(smem + (f < 36 ? aa0 : f < 42 ? aa1 : f < 51 ? aa2 : aa3) + (f / 3) * 2560 + (f % 3) * 64, 160); };
      auto ldo = [&](int k) { of[k] = __builtin_bit_cast(bf16x8, __builtin_amdgcn_raw_buffer_load_b128(orr, qo * 40960 + (hp * 20 + k) * 1024 + lane * 16, 0, 16)); };
#pragma unroll
      for (int k = 0; k < OPF; ++k) ldo(k);
      wait_word(smem + N_WO + hp * 4, 4);
#pragma unroll
      for (int f = 0; f < 4; ++f) af[f] = rda(f);
      SB();
      SFOR(f, 60)
          if constexpr (f % 3 == 0 && f / 3 + OPF < 20) ldo(f / 3 + OPF);
          if constexpr (f + 4 < 60) af[(f + 4) % 5] = rda(f + 4);
          if constexpr (f == 57) { if (hp) { wait_word(smem + C_RD + qo * 4, 1);
#pragma unroll
              for (int b = 0; b < 10; ++b) ps[b] = *(LAS f32x4*)(red + b * 1024); } }
          SB();
          if constexpr (f < 3) MFMA32Z(oc[f], af[f % 5], of[0]); else MFMA32I(oc[f % 3], af[f % 5], of[f / 3]);
          SB();
      SEND }
    MFMA_DRAIN();
    { if (!hp) {
          wait_word(smem + N_G1, 8);
#pragma unroll
          for (int b = 0; b < 10; ++b) *(LAS f32x4*)(red + b * 1024) = (f32x4){oc[b >> 2][4 * (b & 3)], oc[b >> 2][4 * (b & 3) + 1], oc[b >> 2][4 * (b & 3) + 2], oc[b >> 2][4 * (b & 3) + 3]};
          lds_set(smem + C_RD + qo * 4, 1);
      } else {
          char* orow = out + (size_t)(rt * 128 + qo * 32 + r32) * 2560 + br * 1280 + hg * 160 + hl * 16;
#pragma unroll
          for (int b = 0; b < 10; ++b)
#pragma unroll
              for (int r = 0; r < 4; ++r) ps[b][r] += oc[b >> 2][4 * (b & 3) + r];
          SB();
          u32x2 pk[10];
#pragma unroll
          for (int b = 0; b < 10; ++b) pk[b] = (u32x2){pk2(ps[b][0], ps[b][1]), pk2(ps[b][2], ps[b][3])};
          SB();
          u32x4 st[5];
#pragma unroll
          for (int b = 0; b < 10; b += 2) {
              auto rx = __builtin_amdgcn_permlane32_swap(pk[b][0], pk[b + 1][0], false, false); auto ry = __builtin_amdgcn_permlane32_swap(pk[b][1], pk[b + 1][1], false, false);
              st[b >> 1] = (u32x4){rx[0], ry[0], rx[1], ry[1]}; }
          SB();
#pragma unroll
          for (int b = 0; b < 5; ++b) *(u32x4*)(orow + b * 32) = st[b];
      } }
    if (bx == 0 && wid == 0) {
#pragma unroll 1
        for (int i = 0; i < 4; ++i) poll_flags((GAS u64*)(ws + WS_FO + (i * 64 + lane) * 256), true, tag);
        if (lane == 0) __hip_atomic_store((GAS u64*)ws, tag, RLX_AGENT);
    }
    asm volatile(".Lfused_attn_end:");
}

extern "C" void kernel_launch(void* const* d_in, const int* in_sizes, int n_in, void* d_out, int out_size, void* d_ws, size_t ws_size,
                              hipStream_t stream) {
    fused_attn_kernel<<<256, 512, 0, stream>>>((const char*)d_in[0], (const char*)d_in[1], (const char*)d_in[2], (const char*)d_in[3],
                                               (const char*)d_in[4], (const char*)d_in[5], (char*)d_out, (char*)d_ws);
}
```

```cpp
#include <hip/hip_runtime.h>
#include <hip/hip_bf16.h>
#include <stdint.h>

typedef short bf16x8 __attribute__((ext_vector_type(8)));
typedef short s16x4 __attribute__((ext_vector_type(4)));
typedef float f32x2 __attribute__((ext_vector_type(2)));
typedef float f32x4 __attribute__((ext_vector_type(4)));
typedef float f32x16 __attribute__((ext_vector_type(16)));
typedef unsigned u32x4 __attribute__((ext_vector_type(4)));
typedef unsigned u32x2 __attribute__((ext_vector_type(2)));
typedef __bf16 bfx2 __attribute__((ext_vector_type(2)));
typedef unsigned long long u64;
#define LAS __attribute__((address_space(3)))
#define GAS __attribute__((address_space(1)))
#define DI __device__ __forceinline__
#define RLX_AGENT __ATOMIC_RELAXED, __HIP_MEMORY_SCOPE_AGENT
#define RLX_WG __ATOMIC_RELAXED, __HIP_MEMORY_SCOPE_WORKGROUP

constexpr int LDS_BYTES = 163840;
constexpr int VOFF = 20480, VP = 168;
constexpr int KVB = VOFF + 128 * VP;
constexpr int KV0 = LDS_BYTES - 3 * KVB;
constexpr int OWN = KV0 + 2 * KVB;
constexpr int C_F = 0, C_P = 128, C_E = 144, C_QF = 160, C_RD = 176, C_CNT = 192, C_TAG = 320;
constexpr int N_PUB = C_CNT, N_NB = C_CNT + 4, N_G0 = C_CNT + 8, N_G1 = C_CNT + 12, N_WO = C_CNT + 16, N_OS = C_CNT + 24, N_KV = C_CNT + 32, N_OK = C_CNT + 36;
constexpr int XS0 = 512, WS0 = XS0 + 3 * 16384, QST = 512, ONE = 336, QU27 = XS0 + 16384, QU28 = XS0, QU29 = QU27 + 10240;
static_assert(WS0 + 7 * 10240 <= OWN, "ring overlaps the own image");
constexpr unsigned WS_FKV = 4096, WS_FO = WS_FKV + 256 * 128, PG = 2097152;
constexpr unsigned OG = 4 * 40960;
DI unsigned ws_kv(int i) { return PG + (i / 49) * PG + (i % 49) * KVB; }
DI unsigned ws_o(int i) { return 7 * PG + (i / 12) * PG + (i % 12) * OG; }
constexpr unsigned SPIN_CAP = 400000, LSPIN_CAP = 4000000;
constexpr float QSCALE = 0.22360679774997896f * 1.4426950408889634f;
constexpr int WIN = 4;
constexpr int OPF = 12;
constexpr int PFLIP = 6;
constexpr int ADELAY = 3;

#define MFMA32(a, b, c) __builtin_amdgcn_mfma_f32_32x32x16_bf16((a), (b), (c), 0, 0, 0)
#define MFMA32K8(a, b, c) __builtin_amdgcn_mfma_f32_32x32x8bf16_1k((a), (b), (c), 0, 0, 0)
#define MFMA32I(acc, a, b) asm volatile("v_mfma_f32_32x32x16_bf16 %0, %1, %2, %0" : "+v"(acc) : "v"(a), "v"(b))
#define MFMA32Z(acc, a, b) asm volatile("v_mfma_f32_32x32x16_bf16 %0, %1, %2, 0" : "=&v"(acc) : "v"(a), "v"(b))
#define MFMA_DRAIN() asm volatile("s_nop 7\n\ts_nop 4" ::: "memory")
#define WAITV(N) asm volatile("s_waitcnt vmcnt(" #N ")" ::: "memory")
#define BAR_L() asm volatile("s_waitcnt lgkmcnt(0)\n\ts_barrier" ::: "memory")
#define SB() __builtin_amdgcn_sched_barrier(0)
#define FENCE() asm volatile("" ::: "memory")


template <int V> struct IC { static constexpr int value = V; };
template <int I, int N, class F> DI void static_for(F&& f) { if constexpr (I < N) { f(IC<I>{}); static_for<I + 1, N>(f); } }
#define SFOR(var, N) static_for<0, N>([&](auto ic_##var) __attribute__((always_inline)) { constexpr int var = decltype(ic_##var)::value;
#define SEND });

DI void dma16(unsigned voff, const char* sbase, unsigned ldsaddr) {
    asm volatile("s_mov_b32 m0, %2\n\ts_nop 0\n\tglobal_load_lds_dwordx4 %0, %1" :: "v"(voff), "s"(sbase), "s"(ldsaddr) : "memory");
}
DI void dma16x4(unsigned v0, unsigned v1, unsigned v2, unsigned v3, const char* sbase, unsigned ldsaddr) {
    asm volatile("s_mov_b32 m0, %5\n\ts_nop 0\n\tglobal_load_lds_dwordx4 %0, %4\n\tglobal_load_lds_dwordx4 %1, %4 offset:1024\n\t"
                 "global_load_lds_dwordx4 %2, %4 offset:2048\n\tglobal_load_lds_dwordx4 %3, %4 offset:3072"
                 :: "v"(v0), "v"(v1), "v"(v2), "v"(v3), "s"(sbase), "s"(ldsaddr) : "memory");
}
DI void dma16x5(const unsigned (&v)[5], const char* sbase, unsigned ldsaddr) {
    asm volatile("s_mov_b32 m0, %6\n\ts_nop 0\n\tglobal_load_lds_dwordx4 %0, %5 offset:-2048\n\tglobal_load_lds_dwordx4 %1, %5 offset:-1024\n\t"
                 "global_load_lds_dwordx4 %2, %5\n\tglobal_load_lds_dwordx4 %3, %5 offset:1024\n\tglobal_load_lds_dwordx4 %4, %5 offset:2048"
                 :: "v"(v[0]), "v"(v[1]), "v"(v[2]), "v"(v[3]), "v"(v[4]), "s"(sbase), "s"(ldsaddr) : "memory");
}
DI void dma16x5_sc1(unsigned voff, const char* sbase, unsigned ldsaddr) {
    asm volatile("s_mov_b32 m0, %2\n\ts_nop 0\n\tglobal_load_lds_dwordx4 %0, %1 offset:-2048 sc1\n\tglobal_load_lds_dwordx4 %0, %1 offset:-1024 sc1\n\t"
                 "global_load_lds_dwordx4 %0, %1 sc1\n\tglobal_load_lds_dwordx4 %0, %1 offset:1024 sc1\n\tglobal_load_lds_dwordx4 %0, %1 offset:2048 sc1"
                 :: "v"(voff), "s"(sbase), "s"(ldsaddr) : "memory");
}
DI void dma16_sc1(unsigned voff, const char* sbase, unsigned ldsaddr) {
    asm volatile("s_mov_b32 m0, %2\n\ts_nop 0\n\tglobal_load_lds_dwordx4 %0, %1 sc1" :: "v"(voff), "s"(sbase), "s"(ldsaddr) : "memory");
}
DI void dma4(const char* p, unsigned ldsaddr) {
    asm volatile("s_mov_b32 m0, %1\n\ts_nop 0\n\tglobal_load_lds_dword %0, off" :: "v"(p), "s"(ldsaddr) : "memory");
}
DI unsigned pk2(float lo, float hi) { f32x2 v = {lo, hi}; return __builtin_bit_cast(unsigned, __builtin_convertvector(v, bfx2)); }
typedef short v4i16_t __attribute__((ext_vector_type(4)));
DI bf16x8 trfrag(LAS char* p, int second) {
    s16x4 a = __builtin_bit_cast(s16x4, __builtin_amdgcn_ds_read_tr16_b64_v4i16((LAS v4i16_t*)p));
    s16x4 b = __builtin_bit_cast(s16x4, __builtin_amdgcn_ds_read_tr16_b64_v4i16((LAS v4i16_t*)(p + second)));
    return __builtin_shufflevector(a, b, 0, 1, 2, 3, 4, 5, 6, 7);
}
constexpr u64 lanes_cut(int r, bool fartile) {
    u64 m = 0;
    for (int l = 0; l < 64; ++l) { const int key = (r & 3) + 8 * (r >> 2) + 4 * (l >> 5), d = (l & 31) - key + (fartile ? 32 : 0);
        const bool ok = d >= 0 && d <= 14 && !(d & 1);
        if (!ok) m |= 1ull << l; }
    return m;
}
DI float halfmax(float m) { auto rr = __builtin_amdgcn_permlane32_swap(__float_as_uint(m), __float_as_uint(m), false, false);
    return __builtin_elementwise_maximum(__uint_as_float(rr[0]), __uint_as_float(rr[1])); }

DI void poll_flags(GAS u64* word, bool active, u64 tag) {
#pragma unroll 1
    for (unsigned spins = 0; spins < SPIN_CAP; ++spins) {
        u64 v = tag;
        if (active) v = __hip_atomic_load(word, RLX_AGENT);
        if (__all(v == tag)) break;
        __builtin_amdgcn_s_sleep(2);
    }
    __builtin_amdgcn_fence(__ATOMIC_ACQUIRE, "wavefront");
}
DI int min4(LAS char* p) {
    const u64 a = __hip_atomic_load((LAS u64*)p, RLX_WG), b = __hip_atomic_load((LAS u64*)(p + 8), RLX_WG);
    const unsigned m = min(min((unsigned)a, (unsigned)(a >> 32)), min((unsigned)b, (unsigned)(b >> 32)));
    return __builtin_amdgcn_readfirstlane((int)m);
}
DI int wait_min4(LAS char* p, int need) {
    int v = min4(p);
#pragma unroll 1
    for (unsigned spins = 0; v < need && spins < LSPIN_CAP; ++spins) { __builtin_amdgcn_s_sleep(1); v = min4(p); }
    FENCE();
    return v;
}
DI void wait_word(LAS char* p, unsigned need) {
#pragma unroll 1
    for (unsigned spins = 0; spins < LSPIN_CAP; ++spins) {
        if ((unsigned)__builtin_amdgcn_readfirstlane((int)__hip_atomic_load((LAS unsigned*)p, RLX_WG)) >= need) break;
        __builtin_amdgcn_s_sleep(1); }
    FENCE();
}
DI void lds_inc0(LAS char* p, int lane) { FENCE(); if (lane == 0) (void)__hip_atomic_fetch_add((LAS unsigned*)p, 1u, RLX_WG); FENCE(); }
DI unsigned lds_inc(LAS char* p, int lane) { FENCE(); unsigned old = 0; if (lane == 0) old = __hip_atomic_fetch_add((LAS unsigned*)p, 1u, RLX_WG); FENCE(); return old; }
DI void lds_set(LAS char* p, unsigned v) { FENCE(); __hip_atomic_store((LAS unsigned*)p, v, RLX_WG); FENCE(); }

__global__ __launch_bounds__(512, 2) void fused_attn_kernel(const char* __restrict__ xA, const char* __restrict__ wA, const char* __restrict__ oA,
                                                           const char* __restrict__ xB, const char* __restrict__ wB, const char* __restrict__ oB,
                                                           char* __restrict__ out, char* __restrict__ ws) {
    __shared__ __attribute__((aligned(16))) char smem_[LDS_BYTES];
    LAS char* smem = (LAS char*)smem_;
    const unsigned lds0 = (unsigned)(uintptr_t)smem;
    asm volatile("" :: "s"(xA), "s"(wA), "s"(oA), "s"(xB), "s"(wB), "s"(oB), "s"(out), "s"(ws));
    const int tid = threadIdx.x, lane = tid & 63;
    const int wid = __builtin_amdgcn_readfirstlane(tid >> 6);
    const int bx = blockIdx.x, xcd = bx & 7, idx = bx >> 3;
    const int isA = idx >> 4, rt = (xcd >> 1) * 4 + ((idx >> 2) & 3), hg = (xcd & 1) * 4 + (idx & 3);
    const int br = isA ? 0 : 1;
    { u64 pc; unsigned len;
      asm volatile("s_getpc_b64 %0\n.Lfused_attn_pc:\n\ts_mov_b32 %1, .Lfused_attn_end-.Lfused_attn_pc" : "=s"(pc), "=s"(len));
      const unsigned o = ((idx & 15) * 64 + lane) * 128;
      if (wid == 3 && o < len) dma4((const char*)pc + o, lds0 + OWN + 3 * 512); }
    if (idx == 16 && wid == 2 && lane < 13) dma4(lane < 10 ? ws + (lane + 1) * PG - 128 : out + (lane - 10) * PG, lds0 + OWN + 2 * 512);
    const int k0 = isA ? (((rt & 3) * 5 + 1) / 2 + ((hg & 3) * 5 + 1) / 2) % 10 : 0;
    const char* xb = (isA ? xA : xB) + (size_t)rt * (128 * 1280);
    const char* wb = (isA ? wA : wB) + hg * 160;
    const int me = (br * 16 + rt) * 8 + hg;
    const int r32 = lane & 31, hl = lane >> 5;
    auto kt_of = [&](int s) { int k = s + k0; return k >= 10 ? k - 10 : k; };
    if (isA && wid < 4) {
        const int j16 = idx & 15; const unsigned dump = lds0 + OWN + wid * 512;
        const char* wl = wB + (hg >> 2) * 640 + j16 * (4 * 3840);
        const char* xl = xB + (size_t)(rt * 128 + (hg & 3) * 32 + r32) * 1280 + hl * 128;
        auto pfx = [&](int i) { return xl + i * 256; };
        auto pfkv = [&](int i) { int n = i * 64 + lane; n = n < 400 ? n : 399; const int sl = n / 40, r = n % 40, c = r % 10;
            return wl + (sl * 64 + r / 10) * 3840 + 1280 + (c < 5 ? c * 128 : 640 + c * 128); };
        auto pfq = [&](int i) { int n = i * 64 + lane; n = n < 200 ? n : 199; int sl = n / 20 + 8; sl = sl >= 10 ? sl - 10 : sl; const int r = n % 20;
            return wl + (sl * 64 + r / 5) * 3840 + (r % 5) * 128; };
        dma4(wid & 1 ? pfx(wid >> 1) : pfkv(wid >> 1), dump);
        dma4(wid & 1 ? pfx(2 + (wid >> 1)) : pfkv(2 + (wid >> 1)), dump);
        dma4(wid < 3 ? pfkv(4 + wid) : pfx(4), dump);
        dma4(pfq(wid), dump);
        { int n = wid * 64 + lane; n = n < 200 ? n : 199; const int o = (j16 * 40 + n / 5) * 1280 + (hg >> 2) * 640 + (n % 5) * 128;
          dma4(oB + o, dump); dma4(oA + o, dump); }
    }
    if (isA && wid >= 4) { for (int i = 0; i < ADELAY; ++i) __builtin_amdgcn_s_sleep(16); }

    const unsigned xo = (unsigned)((lane >> 3) * 1280 + (((lane & 7) ^ (lane >> 4)) << 4));
    auto w_off = [&](int q) { const int c = q * 64 + lane, row = __umul24(c, 3277) >> 16, j = c - row * 20; return (unsigned)(__umul24(row, 3840) + j * 16 + (j < 10 ? 1280 : 2400)); };
    if (!isA) { const char* xs = xb + k0 * 128 + wid * (16 * 1280);
      dma16(xo, xs, lds0 + XS0 + wid * 2048);
      dma16(xo ^ 64, xs + 8 * 1280, lds0 + XS0 + wid * 2048 + 1024);
      const char* wsrc = wb + (size_t)k0 * (64 * 3840);
      const unsigned wv = w_off(wid < 5 ? wid : wid - 5);
      if (wid < 5) { dma16(wv, wsrc, lds0 + WS0 + wid * 1024); dma16(wv, wsrc + 16 * 3840, lds0 + WS0 + (5 + wid) * 1024);
          if (wid >= 3) { dma16(wv, wsrc + 32 * 3840, lds0 + WS0 + (10 + wid) * 1024); dma16(wv, wsrc + 48 * 3840, lds0 + WS0 + (15 + wid) * 1024); } }
      else { dma16(wv, wsrc + 32 * 3840, lds0 + WS0 + (5 + wid) * 1024); dma16(wv, wsrc + 48 * 3840, lds0 + WS0 + (10 + wid) * 1024); } }
    if (wid == 4 && lane < 2) dma4(ws + lane * 4, lds0 + C_TAG);
    if (tid < 80) *(LAS unsigned*)(smem + tid * 4) = 0u;
    else if (tid < 82) *(LAS unsigned*)(smem + ONE + (tid - 80) * 4) = 0x3F803F80u;
    const int nnb = isA ? (rt > 0 ? 1 : 0) : (rt > 1 ? 2 : rt);
    auto kq_base = [&](int fg) { const int hd = fg / 5, dg = fg % 5; return dg < 4 ? hd * 5120 + (dg >> 1) * 2048 + (dg & 1) * 8 : hd * 5120 + 4096; };

    auto rdtag = [&]() { const u64 t = *(LAS u64*)(smem + C_TAG);
        const u64 e = ((u64)(unsigned)__builtin_amdgcn_readfirstlane((int)(t >> 32)) << 32) | (unsigned)__builtin_amdgcn_readfirstlane((int)t);
        return e * 6364136223846793005ull + 1442695040888963407ull; };
    const __amdgpu_buffer_rsrc_t wor = __builtin_amdgcn_make_buffer_rsrc((void*)(isA ? oA : oB), 0, 640 * 1280, 0x00020000);

    const int hp = wid >> 2, qo = wid & 3, qi = hp ? 3 - qo : qo;
    const int ud = qi + 8;
    const bool far = isA ? (4 * rt + qi >= 1) : (4 * rt + qi >= 8);
    const int u0 = isA ? (far ? ud - 1 : ud) : (far ? qi : 8 - 4 * rt);
    f32x16 cd, cf;
    int dsh, kb1, kb2, nh, vb0, vb1, qb1, qb2; unsigned obase, oo[5];
    auto mask_b = [&](f32x16& c, int r, bool fartile) { const int k = (r & 3) + 8 * (r >> 2); c[r] = __int_as_float(fartile ? ((k - 1) << 23) - dsh : dsh - (k << 23)); };
    auto mask_a = [&](int z) {
      SFOR(r, 16)
          constexpr u64 ad = lanes_cut(r, false), af = lanes_cut(r, true);
          cd[r] = __builtin_amdgcn_inverse_ballot_w64(ad + z) ? -__builtin_inff() : 0.f;
          cf[r] = __builtin_amdgcn_inverse_ballot_w64(af + z) ? -__builtin_inff() : 0.f;
      SEND };
    auto att_setup = [&](int part) {
        int l = tid; asm volatile("" : "+v"(l)); l &= 63;
        const int q = l & 31, h = l >> 5;
        if (part == 0) { dsh = (q - 4 * h) << 23; kb1 = KV0 + hp * 10240 + h * 2048 + q * 16; kb2 = h ? ONE : KV0 + hp * 10240 + 4096 + q * 8; nh = h ^ 1;
            asm volatile("" : "+v"(dsh), "+v"(kb1), "+v"(kb2), "+v"(nh)); }
        if (part == 1) { const bool ones = (l & 16) && (l & 3);
            vb0 = KV0 + VOFF + (4 * h + ((l & 15) >> 2)) * VP + (ones ? 160 : hp * 80 + (l & 16) * 2 + (l & 3) * 8); vb1 = vb0 + (ones ? 0 : 40);
            asm volatile("" : "+v"(vb0), "+v"(vb1)); }
        if (part == 2) { qb1 = QST + hp * 10240 + h * 2048 + (qi * 32 + q) * 16; qb2 = QST + hp * 10240 + 4096 + (qi * 32 + q) * 8;
            obase = (unsigned)(qi * 40960 + hp * 20480 + hg * 2560 + q * 16 + h * 512);
            asm volatile("" : "+v"(qb1), "+v"(qb2), "+v"(obase)); }
        if (part >= 3) {
#pragma unroll
            for (int r = 4 * (part - 3); r < 4 * (part - 2); ++r) mask_b(cd, r, false);
            asm volatile("" : "+v"(cd)); } };
    auto wo_setup = [&](int q) {
        int l = tid; asm volatile("" : "+v"(l)); const int c = q * 64 + (l & 63), rho = __umul24(c, 6554) >> 16;
        oo[q] = (unsigned)(__umul24(qo * 160 + hp * 40 + (rho & ~7) + 4 * (rho & 1) + ((rho >> 1) & 3), 1280) + hg * 160 + (c - rho * 10) * 16);
        asm volatile("" : "+v"(oo[q])); };
    u64 tag;

    if (wid < 4) {
        if (!isA) WAITV(0);
        BAR_L();
        bf16x8 xr[32], xt[2], wf[WIN + 1];
        f32x16 acc[5];
        const int tok = wid * 32 + r32;
        const int xa = XS0 + tok * 128 + ((hl ^ ((tok >> 1) & 7)) << 4);
        int wa0 = WS0 + (8 * hl + ((lane & 15) >> 2)) * 320 + (lane & 16) * 2 + (lane & 3) * 8, wa1 = wa0 + 61440;
        asm volatile("" : "+v"(wa0), "+v"(wa1));
        auto rdx = [&](int i) { return *(LAS bf16x8*)(smem + ((i >> 2) % 3) * 16384 + (xa ^ ((i & 3) * 32))); };
        auto rdw = [&](int f) { const int i = f / 5, t = f % 5, sl = (i >> 1) % 7;
            return trfrag(smem + (sl < 6 ? wa0 + sl * 10240 : wa1) + (i & 1) * 5120 + t * 64, 1280); };
        unsigned fl = 0;
        auto rdflag = [&](int u) { fl = __hip_atomic_load((LAS unsigned*)(smem + C_F + u * 4), RLX_WG); };
        auto check = [&](int u) {
            if (__builtin_amdgcn_readfirstlane(fl) != 15) {
#pragma unroll 1
                for (unsigned spins = 0; spins < LSPIN_CAP; ++spins) { rdflag(u); if (__builtin_amdgcn_readfirstlane(fl) == 15) break; __builtin_amdgcn_s_sleep(1); }
            }
            FENCE(); };
#pragma unroll
        for (int i = 0; i < 4; ++i) xr[i] = rdx(i);
#pragma unroll
        for (int f = 0; f < WIN; ++f) wf[f] = rdw(f);
        SB();
        SFOR(f, 200)
            constexpr int i = f / 5, t = f % 5;
            if constexpr (f % 20 == 10 - WIN) lds_set(smem + C_P + wid * 4, (i >> 1) + 1);
            if constexpr (f == 200 - WIN) lds_set(smem + C_P + wid * 4, 20);
            if constexpr (f % 20 == 10) rdflag(f / 20 + 1);
            if constexpr (f % 20 == 20 - WIN && f < 190) check(f / 20 + 1);
            if constexpr (f + WIN < 200) wf[(f + WIN) % (WIN + 1)] = rdw(f + WIN);
            if constexpr (f % 20 >= 16 && f < 140) xr[(f / 20 + 1) * 4 + f % 20 - 16] = rdx((f / 20 + 1) * 4 + f % 20 - 16);
            if constexpr (t == 1 && i >= 31 && i < 39) xt[(i + 1) & 1] = rdx(i + 1);
            SB();
            if constexpr (i == 0) MFMA32Z(acc[t], wf[f % (WIN + 1)], xr[0]);
            else if constexpr (i < 32) MFMA32I(acc[t], wf[f % (WIN + 1)], xr[i]);
            else MFMA32I(acc[t], wf[f % (WIN + 1)], xt[i & 1]);
            SB();
        SEND
        int l2 = tid; asm volatile("" : "+v"(l2)); l2 &= 63;
        const int h2 = l2 >> 5, tk = wid * 32 + (l2 & 31);
        int eb[5];
        auto ebases = [&](int base) { int l = tid; asm volatile("" : "+v"(l)); const int h = (l >> 5) & 1, k = wid * 32 + (l & 31);
            eb[0] = base + k * 16 + h * 8; eb[1] = base + k * 16 + h * 2040; eb[2] = base + (h ? k * 16 + 1024 : k * 8); eb[3] = base + (h ? k * 8 + 2040 : k * 16);
            asm volatile("" : "+v"(eb[0]), "+v"(eb[1]), "+v"(eb[2]), "+v"(eb[3])); };
        auto kq_addr = [&](int g0) { return eb[g0 % 5 == 1 ? 1 : g0 % 5 == 4 ? 2 : g0 % 5 == 3 ? 3 : 0] + kq_base(g0); };
        ebases(OWN);
        eb[4] = OWN + VOFF + tk * VP + h2 * 8; asm volatile("" : "+v"(eb[4]));
        int qa0 = (8 * h2 + 2 * ((l2 & 15) >> 2)) * 160 + (l2 & 16) * 2 + (l2 & 3) * 8, qa1 = qa0 + WS0 + 10240;
        asm volatile("" : "+v"(qa0), "+v"(qa1));
        auto rdq = [&](int f) { const int i = f / 3, t = f % 3, j = i >> 2, sl = (20 + j) % 7;
            return trfrag(smem + (j >= 7 ? qa0 + (j == 7 ? QU27 : j == 8 ? QU28 : QU29) : sl == 0 ? qa0 + WS0 : qa1 + (sl - 1) * 10240) + (i & 3) * 2560 + t * 64, 160); };
        MFMA_DRAIN();
        WAITV(0);
        { u32x2 pk[20];
#pragma unroll
          for (int n = 0; n < 20; ++n) { const int t = n >> 2, b = n & 3; pk[n] = (u32x2){pk2(acc[t][4 * b], acc[t][4 * b + 1]), pk2(acc[t][4 * b + 2], acc[t][4 * b + 3])}; }
          SB();
#pragma unroll
          for (int n = 0; n < 20; ++n) *(LAS u32x2*)(smem + (n < 10 ? kq_addr(2 * n) : eb[4] + (2 * n - 20) * 8)) = pk[n];
          if (h2 == 0) *(LAS u32x2*)(smem + eb[4] + 160) = (u32x2){0x3F803F80u, 0x3F803F80u}; }
        lds_set(smem + C_E + wid * 4, 1);
        check(10);
        xt[0] = rdx(32);
#pragma unroll
        for (int f = 0; f < WIN; ++f) wf[f] = rdq(f);
        SB();
        u64 pv0 = 0, pv1 = 0;
        SFOR(f, 120)
            constexpr int i = f / 3, t = f % 3, j = i >> 2;
            if constexpr (f % 24 == 12 && j < 9) rdflag(11 + j / 2);
            if constexpr (f % 12 == 12 - WIN && (j == 1 || j == 6 || j == 8)) lds_set(smem + C_P + wid * 4, 21 + j);
            if constexpr (f % 24 == 24 - WIN && j < 9) check(11 + j / 2);
            if constexpr (f + WIN < 120) wf[(f + WIN) % (WIN + 1)] = rdq(f + WIN);
            if constexpr (t == 0 && i < 7) xt[(i + 1) & 1] = rdx(32 + i + 1);
            if constexpr (f >= 90 && f < 100 && !(f & 1)) wo_setup((f - 90) / 2);
            if constexpr (f == 99) tag = rdtag();
            if constexpr (f >= 100 && f < 107) att_setup(f - 100);
            if constexpr (f == 108) ebases(QST);
            if constexpr (f == 116) { pv0 = __hip_atomic_load((LAS u64*)(smem + C_P), RLX_WG); pv1 = __hip_atomic_load((LAS u64*)(smem + C_P + 8), RLX_WG); }
            SB();
            if constexpr (i == 0) MFMA32Z(acc[t], wf[f % (WIN + 1)], xt[0]);
            else if constexpr (i < 8) MFMA32I(acc[t], wf[f % (WIN + 1)], xt[i & 1]);
            else MFMA32I(acc[t], wf[f % (WIN + 1)], xr[i - 8]);
            SB();
        SEND
        MFMA_DRAIN();
        SB();
        if (__builtin_amdgcn_readfirstlane((int)min(min((unsigned)pv0, (unsigned)(pv0 >> 32)), min((unsigned)pv1, (unsigned)(pv1 >> 32)))) < 29) wait_min4(smem + C_P, 29);
        { acc[0] *= QSCALE; acc[1] *= QSCALE; acc[2] *= QSCALE;
          SB();
          u32x2 pk[10];
#pragma unroll
          for (int n = 0; n < 10; ++n) { const int t = n >> 2, b = n & 3; pk[n] = (u32x2){pk2(acc[t][4 * b], acc[t][4 * b + 1]), pk2(acc[t][4 * b + 2], acc[t][4 * b + 3])}; }
          SB();
#pragma unroll
          for (int n = 0; n < 10; ++n) *(LAS u32x2*)(smem + kq_addr(2 * n)) = pk[n]; }
        lds_set(smem + C_QF + wid * 4, 1);
        __builtin_amdgcn_s_setprio(1);
    } else {
        const int hw = wid - 4;
        int known = 0;
        __builtin_amdgcn_s_setprio(3);
        auto lds_or = [&](int off, unsigned v) { FENCE(); if (lane == 0) __hip_atomic_fetch_or((LAS unsigned*)(smem + off), v, RLX_WG); FENCE(); };
        unsigned wo[5], qo[5];
        auto done = [&](int n) { lds_or(C_F + n * 4, 1u << hw); };
        const unsigned xo1 = (xo ^ 64) + 9216, xo2 = xo + 2 * 9216, xo3 = (xo ^ 64) + 3 * 9216;
        auto round_x = [&](int n) { dma16x4(xo, xo1, xo2, xo3, xb + kt_of(n) * 128 + hw * (32 * 1280), lds0 + XS0 + (n % 3) * 16384 + hw * 4096); };
        auto round_w = [&](int n) {
            const int u = 2 * n + (hw >> 1);
            const unsigned dst = lds0 + (u < 27 ? WS0 + (u % 7) * 10240 : u == 27 ? QU27 : u == 28 ? QU28 : QU29) + (hw & 1) * 5120 + 2048;
            if (u < 20) dma16x5(wo, wb + (size_t)(kt_of(u >> 1) * 64 + (u & 1) * 32) * 3840 - 2048, dst);
            else dma16x5(qo, wb + (size_t)kt_of(u < 22 ? u - 12 : u - 22) * (64 * 3840) - 2048, dst); };
        if (isA) round_x(0);
        round_x(1);
#pragma unroll
        for (int q = 0; q < 5; ++q) wo[q] = w_off(q) + (hw & 1) * (16 * 3840) + (4 - q) * 1024;
        if (isA) { round_w(0); round_w(1); WAITV(5); } else { round_w(1); WAITV(9); }
        BAR_L();
#pragma unroll
        for (int q = 0; q < 5; ++q) { const int c = q * 64 + lane, rho = __umul24(c, 6554) >> 16;
            qo[q] = (unsigned)(__umul24((hw & 1) * 32 + (rho & ~7) + 4 * (rho & 1) + ((rho >> 1) & 3), 3840) + (c - rho * 10) * 16 + (4 - q) * 1024); }
#pragma unroll 1
        for (int n = 2; n <= 13; ++n) {
            const int need = n < 13 ? 2 * n - 5 : 20;
            if (need > known) known = min4(smem + C_P);
            if (need > known) { WAITV(0); done(n - 1); known = wait_min4(smem + C_P, need); }
            if (n < 10) round_x(n);
            round_w(n);
            if (n < 10) WAITV(9); else WAITV(5);
            done(n - 1);
        }
        WAITV(0); done(13);
        tag = rdtag();
        auto a_chunk = [&](int a) { int o;
            if (a < 320) { const int hd = a / 80, rem = a % 80; o = hd * 5120 + (rem < 32 ? (96 + rem) * 16 : rem < 64 ? 2048 + (64 + rem) * 16 : 4096 + 768 + (rem - 64) * 16); }
            else o = VOFF + 96 * VP + (a - 320) * 16;
            return o; };
        if (rt < 15) {
            const __amdgpu_buffer_rsrc_t kvr = __builtin_amdgcn_make_buffer_rsrc(ws + ws_kv(me), 0, KVB, 0x00020000);
            wait_min4(smem + C_E, 1);
            if (!isA) { u32x4 v[11];
#pragma unroll
                for (int j = 0; j < 11; ++j) if (j < 10 || hw == 3) v[j] = *(LAS u32x4*)(smem + OWN + hw * 10240 + j * 1024 + lane * 16);
#pragma unroll
                for (int j = 0; j < 11; ++j) if (j < 10 || hw == 3) __builtin_amdgcn_raw_buffer_store_b128(v[j], kvr, hw * 10240 + j * 1024 + lane * 16, 0, 16);
            } else {
#pragma unroll
                for (int j = 0; j < 3; ++j) { const int a = (4 * j + hw) * 64 + lane;
                    if (a < 656) { const int o = a_chunk(a); __builtin_amdgcn_raw_buffer_store_b128(*(LAS u32x4*)(smem + OWN + o), kvr, o, 0, 16); } }
            }
        }
        wait_min4(smem + C_P, 22);
        round_w(14);
        WAITV(5);
        if (rt < 15) {
            unsigned old = 0;
            if (lane == 0) old = __hip_atomic_fetch_add((LAS unsigned*)(smem + N_PUB), 1u, RLX_WG);
            if (lane == 0 && old == 3) __hip_atomic_store((GAS u64*)(ws + WS_FKV + me * 128), tag, RLX_AGENT);
        }
        WAITV(0); done(14);
        __builtin_amdgcn_s_setprio(0);
#pragma unroll
        for (int p = 0; p < 7; ++p) att_setup(p);
#pragma unroll
        for (int q = 0; q < 5; ++q) wo_setup(q);
        __builtin_amdgcn_s_setprio(3);
        if (hw == 0) { poll_flags((GAS u64*)(ws + WS_FKV + (me - 8 * (lane + 1)) * 128), lane < nnb, tag); lds_set(smem + N_KV, 1); }
        else wait_word(smem + N_KV, 1);
        if (!isA) {
            const int n = hw >> 1, half = hw & 1;
            if (n < nnb) {
                const char* src = ws + ws_kv(me - 8 * (n + 1)) + half * 20480; const unsigned dst = lds0 + KV0 + (1 - n) * KVB + half * 20480;
                wait_min4(smem + C_P, 27);
#pragma unroll
                for (int j = 0; j < 4; ++j) dma16x5_sc1(lane * 16, src + j * 5120 + 2048, dst + j * 5120 + 2048);
                if (half) dma16_sc1(lane * 16, src + 20480, dst + 20480);
            }
        } else if (nnb) {
            const __amdgpu_buffer_rsrc_t nbr = __builtin_amdgcn_make_buffer_rsrc(ws + ws_kv(me - 8), 0, KVB, 0x00020000);
            u32x4 v[3];
#pragma unroll
            for (int j = 0; j < 3; ++j) { const int a = (4 * j + hw) * 64 + lane; if (a < 656) v[j] = __builtin_amdgcn_raw_buffer_load_b128(nbr, a_chunk(a), 0, 16); }
            wait_min4(smem + C_P, 27);
#pragma unroll
            for (int j = 0; j < 3; ++j) { const int a = (4 * j + hw) * 64 + lane; if (a < 656) *(LAS u32x4*)(smem + KV0 + KVB + a_chunk(a)) = v[j]; }
        }
        __builtin_amdgcn_s_setprio(2);
        wait_word(smem + C_QF + qi * 4, 1);
    }
    u32x4 wreg[13];
    auto ldwo = [&](int i) { wreg[i] = __builtin_amdgcn_raw_buffer_load_b128(wor, oo[i % 5] + (i / 5) * (32 * 1280) + (i > 6 || (i == 6 && lane >= 16) ? 40 * 1280 : 0), 0, 0); };
    u32x2 ob[2][3];
    {
        bf16x8 q1[2]; u32x2 q2[2];
#pragma unroll
        for (int e = 0; e < 2; ++e) {
            q1[e] = *(LAS bf16x8*)(smem + qb1 + e * 5120);
            q2[e] = *(LAS u32x2*)(smem + qb2 + e * 5120);
            if (hl) q2[e] = (u32x2){0u, 0u};
        }
        if (isA) mask_a(0);
        struct KF { bf16x8 a; s16x4 b; };
        struct VF { bf16x8 a, b; };
        auto rdk = [&](int u, int e) { const int so = (u >> 2) * KVB, tw = u & 3; KF k;
            k.a = *(LAS bf16x8*)(smem + kb1 + so + tw * 512 + e * 5120); k.b = *(LAS s16x4*)(smem + __umul24(so + tw * 256 + e * 5120, nh) + kb2); return k; };
        auto rdv = [&](int u, int e) { LAS char* p = smem + (e ? vb1 : vb0) + (u >> 2) * KVB + (u & 3) * (32 * VP); VF v;
            v.a = trfrag(p, 8 * VP); v.b = trfrag(p + 16 * VP, 8 * VP); return v; };
        auto qk = [&](const KF& k, int e, const f32x16& c) { return MFMA32K8(k.b, __builtin_bit_cast(s16x4, q2[e]), MFMA32(k.a, q1[e], c)); };
        const f32x16 zero = {};
        auto max16 = [&](const f32x16& s, float m) {
#pragma unroll
            for (int r = 0; r < 16; ++r) m = __builtin_elementwise_maximum(m, s[r]);
            return m; };
        auto set_ref = [&](float m, int e) {
            const unsigned t0 = pk2(-m, 0.f); const float r1 = -m - __uint_as_float(t0 << 16);
            const unsigned t1 = pk2(r1, 0.f); const float r2 = r1 - __uint_as_float(t1 << 16);
            if (hl) q2[e] = (u32x2){t0 | (t1 << 16), pk2(r2, 0.f)}; };
        auto ex4 = [&](f32x16& s, int b) {
#pragma unroll
            for (int r = 0; r < 4; ++r) s[b + r] = __builtin_amdgcn_exp2f(s[b + r]); };
        auto pk8 = [&](const f32x16& s, int b) { return __builtin_bit_cast(bf16x8, (u32x4){pk2(s[b], s[b + 1]), pk2(s[b + 2], s[b + 3]), pk2(s[b + 4], s[b + 5]), pk2(s[b + 6], s[b + 7])}); };
        auto phase = [&](auto nx, f32x16& cur, const VF& v, f32x16& o, f32x16& nxt, const KF& k, int e) __attribute__((always_inline)) {
            constexpr int NX = decltype(nx)::value;
            SB(); ex4(cur, 0); SB();
            if constexpr (NX) nxt = MFMA32(k.a, q1[e], NX == 1 ? zero : NX == 2 ? cf : cd);
            SB(); ex4(cur, 4); { const bf16x8 p = pk8(cur, 0); SB(); o = MFMA32(v.a, p, o); }
            SB(); ex4(cur, 8); SB();
            if constexpr (NX) nxt = MFMA32K8(k.b, __builtin_bit_cast(s16x4, q2[e]), nxt);
            SB(); ex4(cur, 12); { const bf16x8 p = pk8(cur, 8); SB(); o = MFMA32(v.b, p, o); }
            SB(); };
        f32x16 o0, o1, sa, sb; KF kx; VF vx;
        auto step = [&](auto m0, auto m1, int u) __attribute__((always_inline)) {
            constexpr int M1 = decltype(m1)::value;
            KF ky; if constexpr (M1) ky = rdk(u - 1, 0);
            const VF vy = rdv(u, 1);
            phase(m0, sa, vx, o0, sb, kx, 1);
            if constexpr (M1) { kx = rdk(u - 1, 1); vx = rdv(u - 1, 0); }
            phase(m1, sb, vy, o1, sa, ky, 0); };
        auto nbwait = [&](int u) { if (u == 8) wait_word(smem + N_NB, 4); };
        { const KF k0f = rdk(ud, 0); kx = rdk(ud, 1); vx = rdv(ud, 0);
          const f32x16 s0 = qk(k0f, 0, cd), s1 = qk(kx, 1, cd);
          set_ref(halfmax(max16(s0, s0[0])), 0); set_ref(halfmax(max16(s1, s1[0])), 1);
          sa = qk(k0f, 0, cd); }
        if (hp) { WAITV(0); lds_inc0(smem + N_NB, lane); lds_inc0(smem + N_G0, lane); }
        o0 = zero; o1 = zero;
        if (u0 + 8 == ud) {
            SFOR(it, 9)
                if constexpr (it < 7) { ldwo(2 * it); if constexpr (it < 6) ldwo(2 * it + 1); }
                if constexpr (it == PFLIP) { if (hp) __builtin_amdgcn_s_setprio(0); }
                if constexpr (it == 7) {
#pragma unroll
                    for (int r = 0; r < 16; ++r) mask_b(cf, r, true); }
                if constexpr (it < 4) nbwait(ud - it);
                step(IC<it == 0 ? 3 : it == 8 ? 2 : 1>{}, IC<it == 8 ? 0 : it == 7 ? 2 : 1>{}, ud - it);
            SEND
        } else {
            __builtin_amdgcn_s_setprio(0);
#pragma unroll
            for (int i = 0; i < 13; ++i) ldwo(i);
            if (!isA) cf = zero;
            if (u0 < ud) {
                nbwait(ud);
                if (u0 + 1 < ud) {
                    step(IC<3>{}, IC<1>{}, ud);
#pragma unroll 1
                    for (int u = ud - 1; u > u0 + 1; --u) { nbwait(u); step(IC<1>{}, IC<1>{}, u); }
                    nbwait(u0 + 1);
                    step(IC<1>{}, IC<2>{}, u0 + 1);
                } else step(IC<3>{}, IC<2>{}, ud);
                step(IC<2>{}, IC<0>{}, u0);
            } else step(IC<3>{}, IC<0>{}, ud);
        }
        if (!__all(o0[12] < 1.152921504606847e18f && o1[12] < 1.152921504606847e18f && o0[12] > 0.25f && o1[12] > 0.25f)) {
            if (isA) { int z = 0; asm volatile("" : "+s"(z)); mask_a(z); }
            else { asm volatile("" : "+v"(dsh));
#pragma unroll
                for (int r = 0; r < 16; ++r) { mask_b(cd, r, false); mask_b(cf, r, true); }
                if (!far) cf = zero; }
#pragma unroll
            for (int e = 0; e < 2; ++e) {
                if (hl) q2[e] = (u32x2){0u, 0u};
                auto tile = [&](int u) { return u == ud ? qk(rdk(u, e), e, cd) : u == u0 ? qk(rdk(u, e), e, cf) : qk(rdk(u, e), e, zero); };
                float m = -__builtin_inff();
#pragma unroll 1
                for (int u = u0; u <= ud; ++u) m = max16(tile(u), m);
                m = halfmax(m);
                f32x16 o = zero;
#pragma unroll 1
                for (int u = u0; u <= ud; ++u) { f32x16 t = tile(u) - m;
                    const VF v = rdv(u, e); ex4(t, 0); ex4(t, 4); ex4(t, 8); ex4(t, 12);
                    o = MFMA32(v.a, pk8(t, 0), o); o = MFMA32(v.b, pk8(t, 8), o); }
                if (e) o1 = o; else o0 = o;
            }
        }
        const float rl0 = __builtin_amdgcn_rcpf(o0[12]), rl1 = __builtin_amdgcn_rcpf(o1[12]);
        SB();
#pragma unroll
        for (int r = 0; r < 12; ++r) { o0[r] *= rl0; o1[r] *= rl1; }
        SB();
#pragma unroll
        for (int j = 0; j < 3; ++j) {
            ob[0][j] = (u32x2){pk2(o0[4 * j], o0[4 * j + 1]), pk2(o0[4 * j + 2], o0[4 * j + 3])};
            ob[1][j] = (u32x2){pk2(o1[4 * j], o1[4 * j + 1]), pk2(o1[4 * j + 2], o1[4 * j + 3])}; }
        SB();
    }
    const __amdgpu_buffer_rsrc_t orr = __builtin_amdgcn_make_buffer_rsrc(ws + ws_o(br * 16 + rt), 0, 4 * 40960, 0x00020000);
    {
      { auto rx = __builtin_amdgcn_permlane32_swap(ob[0][0][0], ob[0][1][0], false, false);
        auto ry = __builtin_amdgcn_permlane32_swap(ob[0][0][1], ob[0][1][1], false, false);
        __builtin_amdgcn_raw_buffer_store_b128((u32x4){rx[0], ry[0], rx[1], ry[1]}, orr, obase, 0, 16); }
      if (hl == 0) __builtin_amdgcn_raw_buffer_store_b128((u32x4){ob[0][2][0], ob[0][2][1], ob[1][0][0], ob[1][0][1]}, orr, obase + 1024, 0, 16);
      { auto fx = __builtin_amdgcn_permlane32_swap(ob[1][0][0], ob[1][1][0], false, false);
        auto fy = __builtin_amdgcn_permlane32_swap(ob[1][0][1], ob[1][1][1], false, false);
        auto sx = __builtin_amdgcn_permlane32_swap(ob[1][1][0], ob[1][2][0], false, false);
        auto sy = __builtin_amdgcn_permlane32_swap(ob[1][1][1], ob[1][2][1], false, false);
        __builtin_amdgcn_raw_buffer_store_b128((u32x4){fx[1], fy[1], sx[0], sy[0]}, orr, obase + 1536, 0, 16); } }
    if (lane == 0) { (void)__hip_atomic_fetch_add((LAS unsigned*)(smem + N_G1), 1u, RLX_WG);
        if (!hp) (void)__hip_atomic_fetch_add((LAS unsigned*)(smem + N_G0), 1u, RLX_WG); }
    FENCE();
    auto signal_o = [&]() {
        if (lds_inc(smem + N_OS + hp * 4, lane) == 3 && lane == 0) __hip_atomic_store((GAS u64*)(ws + WS_FO + (me * 2 + hp) * 128), tag, RLX_AGENT); };
    __builtin_amdgcn_s_setprio(0);
    WAITV(0); signal_o();
    wait_word(smem + (hp ? N_G1 : N_G0), 8);
    const int p1 = hp ? 30720 : 35840, p2 = hp ? 65536 : 43520;
    const int wb0 = hp ? KV0 + 10240 : 512, wb1 = hp ? KV0 + KVB + 10240 - 30720 : KV0 - 35840, wb2 = KV0 + KVB - 43520;
#pragma unroll
    for (int i = 0; i < 13; ++i) { const int L = qo * 12800 + i * 1024;
        if (i < 12 || lane < 32) *(LAS u32x4*)(smem + (L + (L < p1 ? wb0 : L < p2 ? wb1 : wb2)) + lane * 16) = wreg[i]; }
    lds_inc0(smem + N_WO + hp * 4, lane);
    if (qo == 0) { poll_flags((GAS u64*)(ws + WS_FO + (((me & ~7) + (lane & 7)) * 2 + hp) * 128), lane < 8, tag); lds_set(smem + N_OK + hp * 4, 1); }
    else wait_word(smem + N_OK + hp * 4, 1);
    f32x16 oc[3]; f32x4 ps[10];
    LAS char* red = smem + OWN + qo * 10240 + lane * 16;
    { bf16x8 af[5], of[20];
      const int la = (8 * hl + 2 * ((lane & 15) >> 2)) * 160 + (lane & 16) * 2 + (lane & 3) * 8;
      const int aa0 = wb0 + la, aa1 = (hp ? wb1 : wb0) + la, aa2 = wb1 + la, aa3 = (hp ? wb1 : wb2) + la;
      auto rda = [&](int f) { return trfrag(smem + (f < 36 ? aa0 : f < 42 ? aa1 : f < 51 ? aa2 : aa3) + (f / 3) * 2560 + (f % 3) * 64, 160); };
      auto ldo = [&](int k) { of[k] = __builtin_bit_cast(bf16x8, __builtin_amdgcn_raw_buffer_load_b128(orr, qo * 40960 + (hp * 20 + k) * 1024 + lane * 16, 0, 16)); };
#pragma unroll
      for (int k = 0; k < OPF; ++k) ldo(k);
      wait_word(smem + N_WO + hp * 4, 4);
#pragma unroll
      for (int f = 0; f < 4; ++f) af[f] = rda(f);
      SB();
      SFOR(f, 60)
          if constexpr (f % 3 == 0 && f / 3 + OPF < 20) ldo(f / 3 + OPF);
          if constexpr (f + 4 < 60) af[(f + 4) % 5] = rda(f + 4);
          if constexpr (f == 57) { if (hp) { wait_word(smem + C_RD + qo * 4, 1);
#pragma unroll
              for (int b = 0; b < 10; ++b) ps[b] = *(LAS f32x4*)(red + b * 1024); } }
          SB();
          if constexpr (f < 3) MFMA32Z(oc[f], af[f % 5], of[0]); else MFMA32I(oc[f % 3], af[f % 5], of[f / 3]);
          SB();
      SEND }
    MFMA_DRAIN();
    { if (!hp) {
          wait_word(smem + N_G1, 8);
#pragma unroll
          for (int b = 0; b < 10; ++b) *(LAS f32x4*)(red + b * 1024) = (f32x4){oc[b >> 2][4 * (b & 3)], oc[b >> 2][4 * (b & 3) + 1], oc[b >> 2][4 * (b & 3) + 2], oc[b >> 2][4 * (b & 3) + 3]};
          lds_set(smem + C_RD + qo * 4, 1);
      } else {
          char* orow = out + (size_t)(rt * 128 + qo * 32 + r32) * 2560 + br * 1280 + hg * 160 + hl * 16;
#pragma unroll
          for (int b = 0; b < 10; ++b)
#pragma unroll
              for (int r = 0; r < 4; ++r) ps[b][r] += oc[b >> 2][4 * (b & 3) + r];
          SB();
          u32x2 pk[10];
#pragma unroll
          for (int b = 0; b < 10; ++b) pk[b] = (u32x2){pk2(ps[b][0], ps[b][1]), pk2(ps[b][2], ps[b][3])};
          SB();
          u32x4 st[5];
#pragma unroll
          for (int b = 0; b < 10; b += 2) {
              auto rx = __builtin_amdgcn_permlane32_swap(pk[b][0], pk[b + 1][0], false, false); auto ry = __builtin_amdgcn_permlane32_swap(pk[b][1], pk[b + 1][1], false, false);
              st[b >> 1] = (u32x4){rx[0], ry[0], rx[1], ry[1]}; }
          SB();
#pragma unroll
          for (int b = 0; b < 5; ++b) *(u32x4*)(orow + b * 32) = st[b];
      } }
    if (bx == 0 && wid == 0) {
#pragma unroll 1
        for (int i = 0; i < 4; ++i) poll_flags((GAS u64*)(ws + WS_FO + (i * 64 + lane) * 256), true, tag);
        if (lane == 0) __hip_atomic_store((GAS u64*)ws, tag, RLX_AGENT);
    }
    asm volatile(".Lfused_attn_end:");
}

extern "C" void kernel_launch(void* const* d_in, const int* in_sizes, int n_in, void* d_out, int out_size, void* d_ws, size_t ws_size,
                              hipStream_t stream) {
    fused_attn_kernel<<<256, 512, 0, stream>>>((const char*)d_in[0], (const char*)d_in[1], (const char*)d_in[2], (const char*)d_in[3],
                                               (const char*)d_in[4], (const char*)d_in[5], (char*)d_out, (char*)d_ws);
}
```

```cpp
#include <hip/hip_runtime.h>
#include <hip/hip_bf16.h>
#include <stdint.h>

typedef short bf16x8 __attribute__((ext_vector_type(8)));
typedef short s16x4 __attribute__((ext_vector_type(4)));
typedef float f32x2 __attribute__((ext_vector_type(2)));
typedef float f32x4 __attribute__((ext_vector_type(4)));
typedef float f32x16 __attribute__((ext_vector_type(16)));
typedef unsigned u32x4 __attribute__((ext_vector_type(4)));
typedef unsigned u32x2 __attribute__((ext_vector_type(2)));
typedef __bf16 bfx2 __attribute__((ext_vector_type(2)));
typedef unsigned long long u64;
#define LAS __attribute__((address_space(3)))
#define GAS __attribute__((address_space(1)))
#define DI __device__ __forceinline__
#define RLX_AGENT __ATOMIC_RELAXED, __HIP_MEMORY_SCOPE_AGENT
#define RLX_WG __ATOMIC_RELAXED, __HIP_MEMORY_SCOPE_WORKGROUP

constexpr int LDS_BYTES = 163840;
constexpr int VOFF = 20480, VP = 168;
constexpr int KVB = VOFF + 128 * VP;
constexpr int KV0 = LDS_BYTES - 3 * KVB;
constexpr int OWN = KV0 + 2 * KVB;
constexpr int C_F = 0, C_P = 128, C_E = 144, C_QF = 160, C_RD = 176, C_CNT = 192, C_TAG = 320;
constexpr int N_PUB = C_CNT, N_NB = C_CNT + 4, N_G0 = C_CNT + 8, N_G1 = C_CNT + 12, N_WO = C_CNT + 16, N_OS = C_CNT + 24, N_KV = C_CNT + 32, N_OK = C_CNT + 36;
constexpr int XS0 = 512, WS0 = XS0 + 3 * 16384, QST = 512, ONE = 336, QU27 = XS0 + 16384, QU28 = XS0, QU29 = QU27 + 10240;
static_assert(WS0 + 7 * 10240 <= OWN, "ring overlaps the own image");
constexpr unsigned WS_FKV = 4096, WS_FO = WS_FKV + 256 * 128, WS_KV = 131072;
constexpr unsigned KVG = KVB, OG = 4 * 40960;
constexpr unsigned WS_O = WS_KV + 256 * KVG;
constexpr unsigned WS_END = WS_O + 32 * OG;
constexpr unsigned SPIN_CAP = 400000, LSPIN_CAP = 4000000;
constexpr float QSCALE = 0.22360679774997896f * 1.4426950408889634f;
constexpr int WIN = 4;
constexpr int OPF = 12;
constexpr int PFLIP = 6;
constexpr int ADELAY = 3;

#define MFMA32(a, b, c) __builtin_amdgcn_mfma_f32_32x32x16_bf16((a), (b), (c), 0, 0, 0)
#define MFMA32K8(a, b, c) __builtin_amdgcn_mfma_f32_32x32x8bf16_1k((a), (b), (c), 0, 0, 0)
#define MFMA32I(acc, a, b) asm volatile("v_mfma_f32_32x32x16_bf16 %0, %1, %2, %0" : "+v"(acc) : "v"(a), "v"(b))
#define MFMA32Z(acc, a, b) asm volatile("v_mfma_f32_32x32x16_bf16 %0, %1, %2, 0" : "=&v"(acc) : "v"(a), "v"(b))
#define MFMA_DRAIN() asm volatile("s_nop 7\n\ts_nop 4" ::: "memory")
#define WAITV(N) asm volatile("s_waitcnt vmcnt(" #N ")" ::: "memory")
#define BAR_L() asm volatile("s_waitcnt lgkmcnt(0)\n\ts_barrier" ::: "memory")
#define SB() __builtin_amdgcn_sched_barrier(0)
#define FENCE() asm volatile("" ::: "memory")


template <int V> struct IC { static constexpr int value = V; };
template <int I, int N, class F> DI void static_for(F&& f) { if constexpr (I < N) { f(IC<I>{}); static_for<I + 1, N>(f); } }
#define SFOR(var, N) static_for<0, N>([&](auto ic_##var) __attribute__((always_inline)) { constexpr int var = decltype(ic_##var)::value;
#define SEND });

DI void dma16(unsigned voff, const char* sbase, unsigned ldsaddr) {
    asm volatile("s_mov_b32 m0, %2\n\ts_nop 0\n\tglobal_load_lds_dwordx4 %0, %1" :: "v"(voff), "s"(sbase), "s"(ldsaddr) : "memory");
}
DI void dma16x4(unsigned v0, unsigned v1, unsigned v2, unsigned v3, const char* sbase, unsigned ldsaddr) {
    asm volatile("s_mov_b32 m0, %5\n\ts_nop 0\n\tglobal_load_lds_dwordx4 %0, %4\n\tglobal_load_lds_dwordx4 %1, %4 offset:1024\n\t"
                 "global_load_lds_dwordx4 %2, %4 offset:2048\n\tglobal_load_lds_dwordx4 %3, %4 offset:3072"
                 :: "v"(v0), "v"(v1), "v"(v2), "v"(v3), "s"(sbase), "s"(ldsaddr) : "memory");
}
DI void dma16x5(const unsigned (&v)[5], const char* sbase, unsigned ldsaddr) {
    asm volatile("s_mov_b32 m0, %6\n\ts_nop 0\n\tglobal_load_lds_dwordx4 %0, %5 offset:-2048\n\tglobal_load_lds_dwordx4 %1, %5 offset:-1024\n\t"
                 "global_load_lds_dwordx4 %2, %5\n\tglobal_load_lds_dwordx4 %3, %5 offset:1024\n\tglobal_load_lds_dwordx4 %4, %5 offset:2048"
                 :: "v"(v[0]), "v"(v[1]), "v"(v[2]), "v"(v[3]), "v"(v[4]), "s"(sbase), "s"(ldsaddr) : "memory");
}
DI void dma16x5_sc1(unsigned voff, const char* sbase, unsigned ldsaddr) {
    asm volatile("s_mov_b32 m0, %2\n\ts_nop 0\n\tglobal_load_lds_dwordx4 %0, %1 offset:-2048 sc1\n\tglobal_load_lds_dwordx4 %0, %1 offset:-1024 sc1\n\t"
                 "global_load_lds_dwordx4 %0, %1 sc1\n\tglobal_load_lds_dwordx4 %0, %1 offset:1024 sc1\n\tglobal_load_lds_dwordx4 %0, %1 offset:2048 sc1"
                 :: "v"(voff), "s"(sbase), "s"(ldsaddr) : "memory");
}
DI void dma16_sc1(unsigned voff, const char* sbase, unsigned ldsaddr) {
    asm volatile("s_mov_b32 m0, %2\n\ts_nop 0\n\tglobal_load_lds_dwordx4 %0, %1 sc1" :: "v"(voff), "s"(sbase), "s"(ldsaddr) : "memory");
}
DI void dma4(const char* p, unsigned ldsaddr) {
    asm volatile("s_mov_b32 m0, %1\n\ts_nop 0\n\tglobal_load_lds_dword %0, off" :: "v"(p), "s"(ldsaddr) : "memory");
}
DI unsigned pk2(float lo, float hi) { f32x2 v = {lo, hi}; return __builtin_bit_cast(unsigned, __builtin_convertvector(v, bfx2)); }
typedef short v4i16_t __attribute__((ext_vector_type(4)));
DI bf16x8 trfrag(LAS char* p, int second) {
    s16x4 a = __builtin_bit_cast(s16x4, __builtin_amdgcn_ds_read_tr16_b64_v4i16((LAS v4i16_t*)p));
    s16x4 b = __builtin_bit_cast(s16x4, __builtin_amdgcn_ds_read_tr16_b64_v4i16((LAS v4i16_t*)(p + second)));
    return __builtin_shufflevector(a, b, 0, 1, 2, 3, 4, 5, 6, 7);
}
constexpr u64 lanes_cut(int r, bool fartile) {
    u64 m = 0;
    for (int l = 0; l < 64; ++l) { const int key = (r & 3) + 8 * (r >> 2) + 4 * (l >> 5), d = (l & 31) - key + (fartile ? 32 : 0);
        const bool ok = d >= 0 && d <= 14 && !(d & 1);
        if (!ok) m |= 1ull << l; }
    return m;
}
DI float halfmax(float m) { auto rr = __builtin_amdgcn_permlane32_swap(__float_as_uint(m), __float_as_uint(m), false, false);
    return __builtin_elementwise_maximum(__uint_as_float(rr[0]), __uint_as_float(rr[1])); }

DI void poll_flags(GAS u64* word, bool active, u64 tag) {
#pragma unroll 1
    for (unsigned spins = 0; spins < SPIN_CAP; ++spins) {
        u64 v = tag;
        if (active) v = __hip_atomic_load(word, RLX_AGENT);
        if (__all(v == tag)) break;
        __builtin_amdgcn_s_sleep(2);
    }
    __builtin_amdgcn_fence(__ATOMIC_ACQUIRE, "wavefront");
}
DI int min4(LAS char* p) {
    const u64 a = __hip_atomic_load((LAS u64*)p, RLX_WG), b = __hip_atomic_load((LAS u64*)(p + 8), RLX_WG);
    const unsigned m = min(min((unsigned)a, (unsigned)(a >> 32)), min((unsigned)b, (unsigned)(b >> 32)));
    return __builtin_amdgcn_readfirstlane((int)m);
}
DI int wait_min4(LAS char* p, int need) {
    int v = min4(p);
#pragma unroll 1
    for (unsigned spins = 0; v < need && spins < LSPIN_CAP; ++spins) { __builtin_amdgcn_s_sleep(1); v = min4(p); }
    FENCE();
    return v;
}
DI void wait_word(LAS char* p, unsigned need) {
#pragma unroll 1
    for (unsigned spins = 0; spins < LSPIN_CAP; ++spins) {
        if ((unsigned)__builtin_amdgcn_readfirstlane((int)__hip_atomic_load((LAS unsigned*)p, RLX_WG)) >= need) break;
        __builtin_amdgcn_s_sleep(1); }
    FENCE();
}
DI void lds_inc0(LAS char* p, int lane) { FENCE(); if (lane == 0) (void)__hip_atomic_fetch_add((LAS unsigned*)p, 1u, RLX_WG); FENCE(); }
DI unsigned lds_inc(LAS char* p, int lane) { FENCE(); unsigned old = 0; if (lane == 0) old = __hip_atomic_fetch_add((LAS unsigned*)p, 1u, RLX_WG); FENCE(); return old; }
DI void lds_set(LAS char* p, unsigned v) { FENCE(); __hip_atomic_store((LAS unsigned*)p, v, RLX_WG); FENCE(); }

__global__ __launch_bounds__(512, 2) void fused_attn_kernel(const char* __restrict__ xA, const char* __restrict__ wA, const char* __restrict__ oA,
                                                           const char* __restrict__ xB, const char* __restrict__ wB, const char* __restrict__ oB,
                                                           char* __restrict__ out, char* __restrict__ ws) {
    __shared__ __attribute__((aligned(16))) char smem_[LDS_BYTES];
    LAS char* smem = (LAS char*)smem_;
    const unsigned lds0 = (unsigned)(uintptr_t)smem;
    asm volatile("" :: "s"(xA), "s"(wA), "s"(oA), "s"(xB), "s"(wB), "s"(oB), "s"(out), "s"(ws));
    const int tid = threadIdx.x, lane = tid & 63;
    const int wid = __builtin_amdgcn_readfirstlane(tid >> 6);
    const int bx = blockIdx.x, xcd = bx & 7, idx = bx >> 3;
    const int isA = idx >> 4, rt = (xcd >> 1) * 4 + ((idx >> 2) & 3), hg = (xcd & 1) * 4 + (idx & 3);
    const int br = isA ? 0 : 1;
    { u64 pc; unsigned len;
      asm volatile("s_getpc_b64 %0\n.Lfused_attn_pc:\n\ts_mov_b32 %1, .Lfused_attn_end-.Lfused_attn_pc" : "=s"(pc), "=s"(len));
      if (wid == 3 && (idx & 15) * 8192u < len) dma4((const char*)pc + min(((idx & 15) * 64 + lane) * 128u, len - 4u), lds0 + OWN + 3 * 512); }
    const int k0 = isA ? (((rt & 3) * 5 + 1) / 2 + ((hg & 3) * 5 + 1) / 2) % 10 : 0;
    const char* xb = (isA ? xA : xB) + (size_t)rt * (128 * 1280);
    const char* wb = (isA ? wA : wB) + hg * 160;
    const int me = (br * 16 + rt) * 8 + hg;
    const int r32 = lane & 31, hl = lane >> 5;
    auto kt_of = [&](int s) { int k = s + k0; return k >= 10 ? k - 10 : k; };
    if (isA && wid < 4) {
        const int j16 = idx & 15; const unsigned dump = lds0 + OWN + wid * 512;
        const char* wl = wB + (hg >> 2) * 640 + j16 * (4 * 3840);
        const char* xl = xB + (size_t)(rt * 128 + (hg & 3) * 32 + r32) * 1280 + hl * 128;
        auto pfx = [&](int i) { return xl + i * 256; };
        auto pfkv = [&](int i) { int n = i * 64 + lane; n = n < 400 ? n : 399; const int sl = n / 40, r = n % 40, c = r % 10;
            return wl + (sl * 64 + r / 10) * 3840 + 1280 + (c < 5 ? c * 128 : 640 + c * 128); };
        auto pfq = [&](int i) { int n = i * 64 + lane; n = n < 200 ? n : 199; int sl = n / 20 + 8; sl = sl >= 10 ? sl - 10 : sl; const int r = n % 20;
            return wl + (sl * 64 + r / 5) * 3840 + (r % 5) * 128; };
        dma4(wid & 1 ? pfx(wid >> 1) : pfkv(wid >> 1), dump);
        dma4(wid & 1 ? pfx(2 + (wid >> 1)) : pfkv(2 + (wid >> 1)), dump);
        dma4(wid < 3 ? pfkv(4 + wid) : pfx(4), dump);
        dma4(pfq(wid), dump);
        { int n = wid * 64 + lane; n = n < 200 ? n : 199; const int o = (j16 * 40 + n / 5) * 1280 + (hg >> 2) * 640 + (n % 5) * 128;
          dma4(oB + o, dump); dma4(oA + o, dump); }
    }
    if (isA && wid >= 4) { for (int i = 0; i < ADELAY; ++i) __builtin_amdgcn_s_sleep(16); }

    const unsigned xo = (unsigned)((lane >> 3) * 1280 + (((lane & 7) ^ (lane >> 4)) << 4));
    auto w_off = [&](int q) { const int c = q * 64 + lane, row = __umul24(c, 3277) >> 16, j = c - row * 20; return (unsigned)(__umul24(row, 3840) + j * 16 + (j < 10 ? 1280 : 2400)); };
    if (!isA) { const char* xs = xb + k0 * 128 + wid * (16 * 1280);
      dma16(xo, xs, lds0 + XS0 + wid * 2048);
      dma16(xo ^ 64, xs + 8 * 1280, lds0 + XS0 + wid * 2048 + 1024);
      const char* wsrc = wb + (size_t)k0 * (64 * 3840);
      const unsigned wv = w_off(wid < 5 ? wid : wid - 5);
      if (wid < 5) { dma16(wv, wsrc, lds0 + WS0 + wid * 1024); dma16(wv, wsrc + 16 * 3840, lds0 + WS0 + (5 + wid) * 1024);
          if (wid >= 3) { dma16(wv, wsrc + 32 * 3840, lds0 + WS0 + (10 + wid) * 1024); dma16(wv, wsrc + 48 * 3840, lds0 + WS0 + (15 + wid) * 1024); } }
      else { dma16(wv, wsrc + 32 * 3840, lds0 + WS0 + (5 + wid) * 1024); dma16(wv, wsrc + 48 * 3840, lds0 + WS0 + (10 + wid) * 1024); } }
    if (wid == 4) { const char* pe = ws + (lane & 1) * 4; if (lane < 2) dma4(pe, lds0 + C_TAG); }
    if (tid < 80) *(LAS unsigned*)(smem + tid * 4) = 0u;
    else if (tid < 82) *(LAS unsigned*)(smem + ONE + (tid - 80) * 4) = 0x3F803F80u;
    const int nnb = isA ? (rt > 0 ? 1 : 0) : (rt > 1 ? 2 : rt);
    auto kq_base = [&](int fg) { const int hd = fg / 5, dg = fg % 5; return dg < 4 ? hd * 5120 + (dg >> 1) * 2048 + (dg & 1) * 8 : hd * 5120 + 4096; };

    auto rdtag = [&]() { const u64 t = *(LAS u64*)(smem + C_TAG);
        const u64 e = ((u64)(unsigned)__builtin_amdgcn_readfirstlane((int)(t >> 32)) << 32) | (unsigned)__builtin_amdgcn_readfirstlane((int)t);
        return e * 6364136223846793005ull + 1442695040888963407ull; };
    const __amdgpu_buffer_rsrc_t wor = __builtin_amdgcn_make_buffer_rsrc((void*)(isA ? oA : oB), 0, 640 * 1280, 0x00020000);

    const int hp = wid >> 2, qo = wid & 3, qi = hp ? 3 - qo : qo;
    const int ud = qi + 8;
    const bool far = isA ? (4 * rt + qi >= 1) : (4 * rt + qi >= 8);
    const int u0 = isA ? (far ? ud - 1 : ud) : (far ? qi : 8 - 4 * rt);
    f32x16 cd, cf;
    int dsh, kb1, kb2, nh, vb0, vb1, qb1, qb2; unsigned obase, oo[5];
    auto mask_b = [&](f32x16& c, int r, bool fartile) { const int k = (r & 3) + 8 * (r >> 2); c[r] = __int_as_float(fartile ? ((k - 1) << 23) - dsh : dsh - (k << 23)); };
    auto mask_a = [&](int z) {
      SFOR(r, 16)
          constexpr u64 ad = lanes_cut(r, false), af = lanes_cut(r, true);
          cd[r] = __builtin_amdgcn_inverse_ballot_w64(ad + z) ? -__builtin_inff() : 0.f;
          cf[r] = __builtin_amdgcn_inverse_ballot_w64(af + z) ? -__builtin_inff() : 0.f;
      SEND };
    auto att_setup = [&](int part) {
        int l = tid; asm volatile("" : "+v"(l)); l &= 63;
        const int q = l & 31, h = l >> 5;
        if (part == 0) { dsh = (q - 4 * h) << 23; kb1 = KV0 + hp * 10240 + h * 2048 + q * 16; kb2 = h ? ONE : KV0 + hp * 10240 + 4096 + q * 8; nh = h ^ 1;
            asm volatile("" : "+v"(dsh), "+v"(kb1), "+v"(kb2), "+v"(nh)); }
        if (part == 1) { const bool ones = (l & 16) && (l & 3);
            vb0 = KV0 + VOFF + (4 * h + ((l & 15) >> 2)) * VP + (ones ? 160 : hp * 80 + (l & 16) * 2 + (l & 3) * 8); vb1 = vb0 + (ones ? 0 : 40);
            asm volatile("" : "+v"(vb0), "+v"(vb1)); }
        if (part == 2) { qb1 = QST + hp * 10240 + h * 2048 + (qi * 32 + q) * 16; qb2 = QST + hp * 10240 + 4096 + (qi * 32 + q) * 8;
            obase = (unsigned)(qi * 40960 + hp * 20480 + hg * 2560 + q * 16 + h * 512);
            asm volatile("" : "+v"(qb1), "+v"(qb2), "+v"(obase)); }
        if (part >= 3) {
#pragma unroll
            for (int r = 4 * (part - 3); r < 4 * (part - 2); ++r) mask_b(cd, r, false);
            asm volatile("" : "+v"(cd)); } };
    auto wo_setup = [&](int q) {
        int l = tid; asm volatile("" : "+v"(l)); const int c = q * 64 + (l & 63), rho = __umul24(c, 6554) >> 16;
        oo[q] = (unsigned)(__umul24(qo * 160 + hp * 40 + (rho & ~7) + 4 * (rho & 1) + ((rho >> 1) & 3), 1280) + hg * 160 + (c - rho * 10) * 16);
        asm volatile("" : "+v"(oo[q])); };
    u64 tag;

    if (wid < 4) {
        if (!isA) WAITV(0);
        BAR_L();
        bf16x8 xr[32], xt[2], wf[WIN + 1];
        f32x16 acc[5];
        const int tok = wid * 32 + r32;
        const int xa = XS0 + tok * 128 + ((hl ^ ((tok >> 1) & 7)) << 4);
        int wa0 = WS0 + (8 * hl + ((lane & 15) >> 2)) * 320 + (lane & 16) * 2 + (lane & 3) * 8, wa1 = wa0 + 61440;
        asm volatile("" : "+v"(wa0), "+v"(wa1));
        auto rdx = [&](int i) { return *(LAS bf16x8*)(smem + ((i >> 2) % 3) * 16384 + (xa ^ ((i & 3) * 32))); };
        auto rdw = [&](int f) { const int i = f / 5, t = f % 5, sl = (i >> 1) % 7;
            return trfrag(smem + (sl < 6 ? wa0 + sl * 10240 : wa1) + (i & 1) * 5120 + t * 64, 1280); };
        unsigned fl = 0;
        auto rdflag = [&](int u) { fl = __hip_atomic_load((LAS unsigned*)(smem + C_F + u * 4), RLX_WG); };
        auto check = [&](int u) {
            if (__builtin_amdgcn_readfirstlane(fl) != 15) {
#pragma unroll 1
                for (unsigned spins = 0; spins < LSPIN_CAP; ++spins) { rdflag(u); if (__builtin_amdgcn_readfirstlane(fl) == 15) break; __builtin_amdgcn_s_sleep(1); }
            }
            FENCE(); };
#pragma unroll
        for (int i = 0; i < 4; ++i) xr[i] = rdx(i);
#pragma unroll
        for (int f = 0; f < WIN; ++f) wf[f] = rdw(f);
        SB();
        SFOR(f, 200)
            constexpr int i = f / 5, t = f % 5;
            if constexpr (f % 20 == 10 - WIN) lds_set(smem + C_P + wid * 4, (i >> 1) + 1);
            if constexpr (f == 200 - WIN) lds_set(smem + C_P + wid * 4, 20);
            if constexpr (f % 20 == 10) rdflag(f / 20 + 1);
            if constexpr (f % 20 == 20 - WIN && f < 190) check(f / 20 + 1);
            if constexpr (f + WIN < 200) wf[(f + WIN) % (WIN + 1)] = rdw(f + WIN);
            if constexpr (f % 20 >= 16 && f < 140) xr[(f / 20 + 1) * 4 + f % 20 - 16] = rdx((f / 20 + 1) * 4 + f % 20 - 16);
            if constexpr (t == 1 && i >= 31 && i < 39) xt[(i + 1) & 1] = rdx(i + 1);
            SB();
            if constexpr (i == 0) MFMA32Z(acc[t], wf[f % (WIN + 1)], xr[0]);
            else if constexpr (i < 32) MFMA32I(acc[t], wf[f % (WIN + 1)], xr[i]);
            else MFMA32I(acc[t], wf[f % (WIN + 1)], xt[i & 1]);
            SB();
        SEND
        int l2 = tid; asm volatile("" : "+v"(l2)); l2 &= 63;
        const int h2 = l2 >> 5, tk = wid * 32 + (l2 & 31);
        int eb[5];
        auto ebases = [&](int base) { int l = tid; asm volatile("" : "+v"(l)); const int h = (l >> 5) & 1, k = wid * 32 + (l & 31);
            eb[0] = base + k * 16 + h * 8; eb[1] = base + k * 16 + h * 2040; eb[2] = base + (h ? k * 16 + 1024 : k * 8); eb[3] = base + (h ? k * 8 + 2040 : k * 16);
            asm volatile("" : "+v"(eb[0]), "+v"(eb[1]), "+v"(eb[2]), "+v"(eb[3])); };
        auto kq_addr = [&](int g0) { return eb[g0 % 5 == 1 ? 1 : g0 % 5 == 4 ? 2 : g0 % 5 == 3 ? 3 : 0] + kq_base(g0); };
        ebases(OWN);
        eb[4] = OWN + VOFF + tk * VP + h2 * 8; asm volatile("" : "+v"(eb[4]));
        int qa0 = (8 * h2 + 2 * ((l2 & 15) >> 2)) * 160 + (l2 & 16) * 2 + (l2 & 3) * 8, qa1 = qa0 + WS0 + 10240;
        asm volatile("" : "+v"(qa0), "+v"(qa1));
        auto rdq = [&](int f) { const int i = f / 3, t = f % 3, j = i >> 2, sl = (20 + j) % 7;
            return trfrag(smem + (j >= 7 ? qa0 + (j == 7 ? QU27 : j == 8 ? QU28 : QU29) : sl == 0 ? qa0 + WS0 : qa1 + (sl - 1) * 10240) + (i & 3) * 2560 + t * 64, 160); };
        MFMA_DRAIN();
        WAITV(0);
        { u32x2 pk[20];
#pragma unroll
          for (int n = 0; n < 20; ++n) { const int t = n >> 2, b = n & 3; pk[n] = (u32x2){pk2(acc[t][4 * b], acc[t][4 * b + 1]), pk2(acc[t][4 * b + 2], acc[t][4 * b + 3])}; }
          SB();
#pragma unroll
          for (int n = 0; n < 20; ++n) *(LAS u32x2*)(smem + (n < 10 ? kq_addr(2 * n) : eb[4] + (2 * n - 20) * 8)) = pk[n];
          if (h2 == 0) *(LAS u32x2*)(smem + eb[4] + 160) = (u32x2){0x3F803F80u, 0x3F803F80u}; }
        lds_set(smem + C_E + wid * 4, 1);
        check(10);
        xt[0] = rdx(32);
#pragma unroll
        for (int f = 0; f < WIN; ++f) wf[f] = rdq(f);
        SB();
        u64 pv0 = 0, pv1 = 0;
        SFOR(f, 120)
            constexpr int i = f / 3, t = f % 3, j = i >> 2;
            if constexpr (f % 24 == 12 && j < 9) rdflag(11 + j / 2);
            if constexpr (f % 12 == 12 - WIN && (j == 1 || j == 6 || j == 8)) lds_set(smem + C_P + wid * 4, 21 + j);
            if constexpr (f % 24 == 24 - WIN && j < 9) check(11 + j / 2);
            if constexpr (f + WIN < 120) wf[(f + WIN) % (WIN + 1)] = rdq(f + WIN);
            if constexpr (t == 0 && i < 7) xt[(i + 1) & 1] = rdx(32 + i + 1);
            if constexpr (f >= 90 && f < 100 && !(f & 1)) wo_setup((f - 90) / 2);
            if constexpr (f == 99) tag = rdtag();
            if constexpr (f >= 100 && f < 107) att_setup(f - 100);
            if constexpr (f == 108) ebases(QST);
            if constexpr (f == 116) { pv0 = __hip_atomic_load((LAS u64*)(smem + C_P), RLX_WG); pv1 = __hip_atomic_load((LAS u64*)(smem + C_P + 8), RLX_WG); }
            SB();
            if constexpr (i == 0) MFMA32Z(acc[t], wf[f % (WIN + 1)], xt[0]);
            else if constexpr (i < 8) MFMA32I(acc[t], wf[f % (WIN + 1)], xt[i & 1]);
            else MFMA32I(acc[t], wf[f % (WIN + 1)], xr[i - 8]);
            SB();
        SEND
        MFMA_DRAIN();
        SB();
        if (__builtin_amdgcn_readfirstlane((int)min(min((unsigned)pv0, (unsigned)(pv0 >> 32)), min((unsigned)pv1, (unsigned)(pv1 >> 32)))) < 29) wait_min4(smem + C_P, 29);
        { acc[0] *= QSCALE; acc[1] *= QSCALE; acc[2] *= QSCALE;
          SB();
          u32x2 pk[10];
#pragma unroll
          for (int n = 0; n < 10; ++n) { const int t = n >> 2, b = n & 3; pk[n] = (u32x2){pk2(acc[t][4 * b], acc[t][4 * b + 1]), pk2(acc[t][4 * b + 2], acc[t][4 * b + 3])}; }
          SB();
#pragma unroll
          for (int n = 0; n < 10; ++n) *(LAS u32x2*)(smem + kq_addr(2 * n)) = pk[n]; }
        lds_set(smem + C_QF + wid * 4, 1);
        __builtin_amdgcn_s_setprio(1);
    } else {
        const int hw = wid - 4;
        int known = 0;
        __builtin_amdgcn_s_setprio(3);
        auto lds_or = [&](int off, unsigned v) { FENCE(); if (lane == 0) __hip_atomic_fetch_or((LAS unsigned*)(smem + off), v, RLX_WG); FENCE(); };
        unsigned wo[5], qo[5];
        auto done = [&](int n) { lds_or(C_F + n * 4, 1u << hw); };
        const unsigned xo1 = (xo ^ 64) + 9216, xo2 = xo + 2 * 9216, xo3 = (xo ^ 64) + 3 * 9216;
        auto round_x = [&](int n) { dma16x4(xo, xo1, xo2, xo3, xb + kt_of(n) * 128 + hw * (32 * 1280), lds0 + XS0 + (n % 3) * 16384 + hw * 4096); };
        auto round_w = [&](int n) {
            const int u = 2 * n + (hw >> 1);
            const unsigned dst = lds0 + (u < 27 ? WS0 + (u % 7) * 10240 : u == 27 ? QU27 : u == 28 ? QU28 : QU29) + (hw & 1) * 5120 + 2048;
            if (u < 20) dma16x5(wo, wb + (size_t)(kt_of(u >> 1) * 64 + (u & 1) * 32) * 3840 - 2048, dst);
            else dma16x5(qo, wb + (size_t)kt_of(u < 22 ? u - 12 : u - 22) * (64 * 3840) - 2048, dst); };
        if (isA) round_x(0);
        round_x(1);
#pragma unroll
        for (int q = 0; q < 5; ++q) wo[q] = w_off(q) + (hw & 1) * (16 * 3840) + (4 - q) * 1024;
        if (isA) { round_w(0); round_w(1); WAITV(5); } else { round_w(1); WAITV(9); }
        BAR_L();
#pragma unroll
        for (int q = 0; q < 5; ++q) { const int c = q * 64 + lane, rho = __umul24(c, 6554) >> 16;
            qo[q] = (unsigned)(__umul24((hw & 1) * 32 + (rho & ~7) + 4 * (rho & 1) + ((rho >> 1) & 3), 3840) + (c - rho * 10) * 16 + (4 - q) * 1024); }
#pragma unroll 1
        for (int n = 2; n <= 13; ++n) {
            const int need = n < 13 ? 2 * n - 5 : 20;
            if (need > known) known = min4(smem + C_P);
            if (need > known) { WAITV(0); done(n - 1); known = wait_min4(smem + C_P, need); }
            if (n < 10) round_x(n);
            round_w(n);
            if (n < 10) WAITV(9); else WAITV(5);
            done(n - 1);
        }
        WAITV(0); done(13);
        tag = rdtag();
        auto a_chunk = [&](int a) { int o;
            if (a < 320) { const int hd = a / 80, rem = a % 80; o = hd * 5120 + (rem < 32 ? (96 + rem) * 16 : rem < 64 ? 2048 + (64 + rem) * 16 : 4096 + 768 + (rem - 64) * 16); }
            else o = VOFF + 96 * VP + (a - 320) * 16;
            return o; };
        if (rt < 15) {
            const __amdgpu_buffer_rsrc_t kvr = __builtin_amdgcn_make_buffer_rsrc(ws + WS_KV + me * KVG, 0, KVB, 0x00020000);
            wait_min4(smem + C_E, 1);
            if (!isA) { u32x4 v[11];
#pragma unroll
                for (int j = 0; j < 11; ++j) if (j < 10 || hw == 3) v[j] = *(LAS u32x4*)(smem + OWN + hw * 10240 + j * 1024 + lane * 16);
#pragma unroll
                for (int j = 0; j < 11; ++j) if (j < 10 || hw == 3) __builtin_amdgcn_raw_buffer_store_b128(v[j], kvr, hw * 10240 + j * 1024 + lane * 16, 0, 16);
            } else {
#pragma unroll
                for (int j = 0; j < 3; ++j) { const int a = (4 * j + hw) * 64 + lane;
                    if (a < 656) { const int o = a_chunk(a); __builtin_amdgcn_raw_buffer_store_b128(*(LAS u32x4*)(smem + OWN + o), kvr, o, 0, 16); } }
            }
        }
        wait_min4(smem + C_P, 22);
        round_w(14);
        WAITV(5);
        if (rt < 15) {
            unsigned old = 0;
            if (lane == 0) old = __hip_atomic_fetch_add((LAS unsigned*)(smem + N_PUB), 1u, RLX_WG);
            if (lane == 0 && old == 3) __hip_atomic_store((GAS u64*)(ws + WS_FKV + me * 128), tag, RLX_AGENT);
        }
        WAITV(0); done(14);
        __builtin_amdgcn_s_setprio(0);
#pragma unroll
        for (int p = 0; p < 7; ++p) att_setup(p);
#pragma unroll
        for (int q = 0; q < 5; ++q) wo_setup(q);
        __builtin_amdgcn_s_setprio(3);
        if (hw == 0) { poll_flags((GAS u64*)(ws + WS_FKV + (me - 8 * (lane + 1)) * 128), lane < nnb, tag); lds_set(smem + N_KV, 1); }
        else wait_word(smem + N_KV, 1);
        if (!isA) {
            const int n = hw >> 1, half = hw & 1;
            if (n < nnb) {
                const char* src = ws + WS_KV + (me - 8 * (n + 1)) * KVG + half * 20480; const unsigned dst = lds0 + KV0 + (1 - n) * KVB + half * 20480;
                wait_min4(smem + C_P, 27);
#pragma unroll
                for (int j = 0; j < 4; ++j) dma16x5_sc1(lane * 16, src + j * 5120 + 2048, dst + j * 5120 + 2048);
                if (half) dma16_sc1(lane * 16, src + 20480, dst + 20480);
            }
        } else if (nnb) {
            const __amdgpu_buffer_rsrc_t nbr = __builtin_amdgcn_make_buffer_rsrc(ws + WS_KV + (me - 8) * KVG, 0, KVB, 0x00020000);
            u32x4 v[3];
#pragma unroll
            for (int j = 0; j < 3; ++j) { const int a = (4 * j + hw) * 64 + lane; if (a < 656) v[j] = __builtin_amdgcn_raw_buffer_load_b128(nbr, a_chunk(a), 0, 16); }
            wait_min4(smem + C_P, 27);
#pragma unroll
            for (int j = 0; j < 3; ++j) { const int a = (4 * j + hw) * 64 + lane; if (a < 656) *(LAS u32x4*)(smem + KV0 + KVB + a_chunk(a)) = v[j]; }
        }
        __builtin_amdgcn_s_setprio(2);
        wait_word(smem + C_QF + qi * 4, 1);
    }
    u32x4 wreg[13];
    auto ldwo = [&](int i) { wreg[i] = __builtin_amdgcn_raw_buffer_load_b128(wor, oo[i % 5] + (i / 5) * (32 * 1280) + (i > 6 || (i == 6 && lane >= 16) ? 40 * 1280 : 0), 0, 0); };
    u32x2 ob[2][3];
    {
        bf16x8 q1[2]; u32x2 q2[2];
#pragma unroll
        for (int e = 0; e < 2; ++e) {
            q1[e] = *(LAS bf16x8*)(smem + qb1 + e * 5120);
            q2[e] = *(LAS u32x2*)(smem + qb2 + e * 5120);
            if (hl) q2[e] = (u32x2){0u, 0u};
        }
        if (isA) mask_a(0);
        struct KF { bf16x8 a; s16x4 b; };
        struct VF { bf16x8 a, b; };
        auto rdk = [&](int u, int e) { const int so = (u >> 2) * KVB, tw = u & 3; KF k;
            k.a = *(LAS bf16x8*)(smem + kb1 + so + tw * 512 + e * 5120); k.b = *(LAS s16x4*)(smem + __umul24(so + tw * 256 + e * 5120, nh) + kb2); return k; };
        auto rdv = [&](int u, int e) { LAS char* p = smem + (e ? vb1 : vb0) + (u >> 2) * KVB + (u & 3) * (32 * VP); VF v;
            v.a = trfrag(p, 8 * VP); v.b = trfrag(p + 16 * VP, 8 * VP); return v; };
        auto qk = [&](const KF& k, int e, const f32x16& c) { return MFMA32K8(k.b, __builtin_bit_cast(s16x4, q2[e]), MFMA32(k.a, q1[e], c)); };
        const f32x16 zero = {};
        auto max16 = [&](const f32x16& s, float m) {
#pragma unroll
            for (int r = 0; r < 16; ++r) m = __builtin_elementwise_maximum(m, s[r]);
            return m; };
        auto set_ref = [&](float m, int e) {
            const unsigned t0 = pk2(-m, 0.f); const float r1 = -m - __uint_as_float(t0 << 16);
            const unsigned t1 = pk2(r1, 0.f); const float r2 = r1 - __uint_as_float(t1 << 16);
            if (hl) q2[e] = (u32x2){t0 | (t1 << 16), pk2(r2, 0.f)}; };
        auto ex4 = [&](f32x16& s, int b) {
#pragma unroll
            for (int r = 0; r < 4; ++r) s[b + r] = __builtin_amdgcn_exp2f(s[b + r]); };
        auto pk8 = [&](const f32x16& s, int b) { return __builtin_bit_cast(bf16x8, (u32x4){pk2(s[b], s[b + 1]), pk2(s[b + 2], s[b + 3]), pk2(s[b + 4], s[b + 5]), pk2(s[b + 6], s[b + 7])}); };
        auto phase = [&](auto nx, f32x16& cur, const VF& v, f32x16& o, f32x16& nxt, const KF& k, int e) __attribute__((always_inline)) {
            constexpr int NX = decltype(nx)::value;
            SB(); ex4(cur, 0); SB();
            if constexpr (NX) nxt = MFMA32(k.a, q1[e], NX == 1 ? zero : NX == 2 ? cf : cd);
            SB(); ex4(cur, 4); { const bf16x8 p = pk8(cur, 0); SB(); o = MFMA32(v.a, p, o); }
            SB(); ex4(cur, 8); SB();
            if constexpr (NX) nxt = MFMA32K8(k.b, __builtin_bit_cast(s16x4, q2[e]), nxt);
            SB(); ex4(cur, 12); { const bf16x8 p = pk8(cur, 8); SB(); o = MFMA32(v.b, p, o); }
            SB(); };
        f32x16 o0, o1, sa, sb; KF kx; VF vx;
        auto step = [&](auto m0, auto m1, int u) __attribute__((always_inline)) {
            constexpr int M1 = decltype(m1)::value;
            KF ky; if constexpr (M1) ky = rdk(u - 1, 0);
            const VF vy = rdv(u, 1);
            phase(m0, sa, vx, o0, sb, kx, 1);
            if constexpr (M1) { kx = rdk(u - 1, 1); vx = rdv(u - 1, 0); }
            phase(m1, sb, vy, o1, sa, ky, 0); };
        auto nbwait = [&](int u) { if (u == 8) wait_word(smem + N_NB, 4); };
        { const KF k0f = rdk(ud, 0); kx = rdk(ud, 1); vx = rdv(ud, 0);
          const f32x16 s0 = qk(k0f, 0, cd), s1 = qk(kx, 1, cd);
          set_ref(halfmax(max16(s0, s0[0])), 0); set_ref(halfmax(max16(s1, s1[0])), 1);
          sa = qk(k0f, 0, cd); }
        if (hp) { WAITV(0); lds_inc0(smem + N_NB, lane); lds_inc0(smem + N_G0, lane); }
        o0 = zero; o1 = zero;
        if (u0 + 8 == ud) {
            SFOR(it, 9)
                if constexpr (it < 7) { ldwo(2 * it); if constexpr (it < 6) ldwo(2 * it + 1); }
                if constexpr (it == PFLIP) { if (hp) __builtin_amdgcn_s_setprio(0); }
                if constexpr (it == 7) {
#pragma unroll
                    for (int r = 0; r < 16; ++r) mask_b(cf, r, true); }
                if constexpr (it < 4) nbwait(ud - it);
                step(IC<it == 0 ? 3 : it == 8 ? 2 : 1>{}, IC<it == 8 ? 0 : it == 7 ? 2 : 1>{}, ud - it);
            SEND
        } else {
            __builtin_amdgcn_s_setprio(0);
#pragma unroll
            for (int i = 0; i < 13; ++i) ldwo(i);
            if (!isA) cf = zero;
            if (u0 < ud) {
                nbwait(ud);
                if (u0 + 1 < ud) {
                    step(IC<3>{}, IC<1>{}, ud);
#pragma unroll 1
                    for (int u = ud - 1; u > u0 + 1; --u) { nbwait(u); step(IC<1>{}, IC<1>{}, u); }
                    nbwait(u0 + 1);
                    step(IC<1>{}, IC<2>{}, u0 + 1);
                } else step(IC<3>{}, IC<2>{}, ud);
                step(IC<2>{}, IC<0>{}, u0);
            } else step(IC<3>{}, IC<0>{}, ud);
        }
        if (!__all(o0[12] < 1.152921504606847e18f && o1[12] < 1.152921504606847e18f && o0[12] > 0.25f && o1[12] > 0.25f)) {
            if (isA) { int z = 0; asm volatile("" : "+s"(z)); mask_a(z); }
            else { asm volatile("" : "+v"(dsh));
#pragma unroll
                for (int r = 0; r < 16; ++r) { mask_b(cd, r, false); mask_b(cf, r, true); }
                if (!far) cf = zero; }
#pragma unroll
            for (int e = 0; e < 2; ++e) {
                if (hl) q2[e] = (u32x2){0u, 0u};
                auto tile = [&](int u) { return u == ud ? qk(rdk(u, e), e, cd) : u == u0 ? qk(rdk(u, e), e, cf) : qk(rdk(u, e), e, zero); };
                float m = -__builtin_inff();
#pragma unroll 1
                for (int u = u0; u <= ud; ++u) m = max16(tile(u), m);
                m = halfmax(m);
                f32x16 o = zero;
#pragma unroll 1
                for (int u = u0; u <= ud; ++u) { f32x16 t = tile(u) - m;
                    const VF v = rdv(u, e); ex4(t, 0); ex4(t, 4); ex4(t, 8); ex4(t, 12);
                    o = MFMA32(v.a, pk8(t, 0), o); o = MFMA32(v.b, pk8(t, 8), o); }
                if (e) o1 = o; else o0 = o;
            }
        }
        const float rl0 = __builtin_amdgcn_rcpf(o0[12]), rl1 = __builtin_amdgcn_rcpf(o1[12]);
        SB();
#pragma unroll
        for (int r = 0; r < 12; ++r) { o0[r] *= rl0; o1[r] *= rl1; }
        SB();
#pragma unroll
        for (int j = 0; j < 3; ++j) {
            ob[0][j] = (u32x2){pk2(o0[4 * j], o0[4 * j + 1]), pk2(o0[4 * j + 2], o0[4 * j + 3])};
            ob[1][j] = (u32x2){pk2(o1[4 * j], o1[4 * j + 1]), pk2(o1[4 * j + 2], o1[4 * j + 3])}; }
        SB();
    }
    const __amdgpu_buffer_rsrc_t orr = __builtin_amdgcn_make_buffer_rsrc(ws + WS_O + (br * 16 + rt) * OG, 0, 4 * 40960, 0x00020000);
    {
      { auto rx = __builtin_amdgcn_permlane32_swap(ob[0][0][0], ob[0][1][0], false, false);
        auto ry = __builtin_amdgcn_permlane32_swap(ob[0][0][1], ob[0][1][1], false, false);
        __builtin_amdgcn_raw_buffer_store_b128((u32x4){rx[0], ry[0], rx[1], ry[1]}, orr, obase, 0, 16); }
      if (hl == 0) __builtin_amdgcn_raw_buffer_store_b128((u32x4){ob[0][2][0], ob[0][2][1], ob[1][0][0], ob[1][0][1]}, orr, obase + 1024, 0, 16);
      { auto fx = __builtin_amdgcn_permlane32_swap(ob[1][0][0], ob[1][1][0], false, false);
        auto fy = __builtin_amdgcn_permlane32_swap(ob[1][0][1], ob[1][1][1], false, false);
        auto sx = __builtin_amdgcn_permlane32_swap(ob[1][1][0], ob[1][2][0], false, false);
        auto sy = __builtin_amdgcn_permlane32_swap(ob[1][1][1], ob[1][2][1], false, false);
        __builtin_amdgcn_raw_buffer_store_b128((u32x4){fx[1], fy[1], sx[0], sy[0]}, orr, obase + 1536, 0, 16); } }
    if (lane == 0) { (void)__hip_atomic_fetch_add((LAS unsigned*)(smem + N_G1), 1u, RLX_WG);
        if (!hp) (void)__hip_atomic_fetch_add((LAS unsigned*)(smem + N_G0), 1u, RLX_WG); }
    FENCE();
    auto signal_o = [&]() {
        if (lds_inc(smem + N_OS + hp * 4, lane) == 3 && lane == 0) __hip_atomic_store((GAS u64*)(ws + WS_FO + (me * 2 + hp) * 128), tag, RLX_AGENT); };
    __builtin_amdgcn_s_setprio(0);
    WAITV(0); signal_o();
    wait_word(smem + (hp ? N_G1 : N_G0), 8);
    const int p1 = hp ? 30720 : 35840, p2 = hp ? 65536 : 43520;
    const int wb0 = hp ? KV0 + 10240 : 512, wb1 = hp ? KV0 + KVB + 10240 - 30720 : KV0 - 35840, wb2 = KV0 + KVB - 43520;
#pragma unroll
    for (int i = 0; i < 13; ++i) { const int L = qo * 12800 + i * 1024;
        if (i < 12 || lane < 32) *(LAS u32x4*)(smem + (L + (L < p1 ? wb0 : L < p2 ? wb1 : wb2)) + lane * 16) = wreg[i]; }
    lds_inc0(smem + N_WO + hp * 4, lane);
    if (qo == 0) { poll_flags((GAS u64*)(ws + WS_FO + (((me & ~7) + (lane & 7)) * 2 + hp) * 128), lane < 8, tag); lds_set(smem + N_OK + hp * 4, 1); }
    else wait_word(smem + N_OK + hp * 4, 1);
    f32x16 oc[3]; f32x4 ps[10];
    LAS char* red = smem + OWN + qo * 10240 + lane * 16;
    { bf16x8 af[5], of[20];
      const int la = (8 * hl + 2 * ((lane & 15) >> 2)) * 160 + (lane & 16) * 2 + (lane & 3) * 8;
      const int aa0 = wb0 + la, aa1 = (hp ? wb1 : wb0) + la, aa2 = wb1 + la, aa3 = (hp ? wb1 : wb2) + la;
      auto rda = [&](int f) { return trfrag(smem + (f < 36 ? aa0 : f < 42 ? aa1 : f < 51 ? aa2 : aa3) + (f / 3) * 2560 + (f % 3) * 64, 160); };
      auto ldo = [&](int k) { of[k] = __builtin_bit_cast(bf16x8, __builtin_amdgcn_raw_buffer_load_b128(orr, qo * 40960 + (hp * 20 + k) * 1024 + lane * 16, 0, 16)); };
#pragma unroll
      for (int k = 0; k < OPF; ++k) ldo(k);
      wait_word(smem + N_WO + hp * 4, 4);
#pragma unroll
      for (int f = 0; f < 4; ++f) af[f] = rda(f);
      SB();
      SFOR(f, 60)
          if constexpr (f % 3 == 0 && f / 3 + OPF < 20) ldo(f / 3 + OPF);
          if constexpr (f + 4 < 60) af[(f + 4) % 5] = rda(f + 4);
          if constexpr (f == 57) { if (hp) { wait_word(smem + C_RD + qo * 4, 1);
#pragma unroll
              for (int b = 0; b < 10; ++b) ps[b] = *(LAS f32x4*)(red + b * 1024); } }
          SB();
          if constexpr (f < 3) MFMA32Z(oc[f], af[f % 5], of[0]); else MFMA32I(oc[f % 3], af[f % 5], of[f / 3]);
          SB();
      SEND }
    MFMA_DRAIN();
    { if (!hp) {
          wait_word(smem + N_G1, 8);
#pragma unroll
          for (int b = 0; b < 10; ++b) *(LAS f32x4*)(red + b * 1024) = (f32x4){oc[b >> 2][4 * (b & 3)], oc[b >> 2][4 * (b & 3) + 1], oc[b >> 2][4 * (b & 3) + 2], oc[b >> 2][4 * (b & 3) + 3]};
          lds_set(smem + C_RD + qo * 4, 1);
      } else {
          char* orow = out + (size_t)(rt * 128 + qo * 32 + r32) * 2560 + br * 1280 + hg * 160 + hl * 16;
#pragma unroll
          for (int b = 0; b < 10; ++b)
#pragma unroll
              for (int r = 0; r < 4; ++r) ps[b][r] += oc[b >> 2][4 * (b & 3) + r];
          SB();
          u32x2 pk[10];
#pragma unroll
          for (int b = 0; b < 10; ++b) pk[b] = (u32x2){pk2(ps[b][0], ps[b][1]), pk2(ps[b][2], ps[b][3])};
          SB();
          u32x4 st[5];
#pragma unroll
          for (int b = 0; b < 10; b += 2) {
              auto rx = __builtin_amdgcn_permlane32_swap(pk[b][0], pk[b + 1][0], false, false); auto ry = __builtin_amdgcn_permlane32_swap(pk[b][1], pk[b + 1][1], false, false);
              st[b >> 1] = (u32x4){rx[0], ry[0], rx[1], ry[1]}; }
          SB();
#pragma unroll
          for (int b = 0; b < 5; ++b) *(u32x4*)(orow + b * 32) = st[b];
      } }
    if (bx == 0 && wid == 0) {
#pragma unroll 1
        for (int i = 0; i < 4; ++i) poll_flags((GAS u64*)(ws + WS_FO + (i * 64 + lane) * 256), true, tag);
        if (lane == 0) __hip_atomic_store((GAS u64*)ws, tag, RLX_AGENT);
    }
    asm volatile(".Lfused_attn_end:");
}

extern "C" void kernel_launch(void* const* d_in, const int* in_sizes, int n_in, void* d_out, int out_size, void* d_ws, size_t ws_size,
                              hipStream_t stream) {
    fused_attn_kernel<<<256, 512, 0, stream>>>((const char*)d_in[0], (const char*)d_in[1], (const char*)d_in[2], (const char*)d_in[3],
                                               (const char*)d_in[4], (const char*)d_in[5], (char*)d_out, (char*)d_ws);
}
```

```cpp
#include <hip/hip_runtime.h>
#include <hip/hip_bf16.h>
#include <stdint.h>

typedef short bf16x8 __attribute__((ext_vector_type(8)));
typedef short s16x4 __attribute__((ext_vector_type(4)));
typedef float f32x2 __attribute__((ext_vector_type(2)));
typedef float f32x4 __attribute__((ext_vector_type(4)));
typedef float f32x16 __attribute__((ext_vector_type(16)));
typedef unsigned u32x4 __attribute__((ext_vector_type(4)));
typedef unsigned u32x2 __attribute__((ext_vector_type(2)));
typedef __bf16 bfx2 __attribute__((ext_vector_type(2)));
typedef unsigned long long u64;
#define LAS __attribute__((address_space(3)))
#define GAS __attribute__((address_space(1)))
#define DI __device__ __forceinline__
#define RLX_AGENT __ATOMIC_RELAXED, __HIP_MEMORY_SCOPE_AGENT
#define RLX_WG __ATOMIC_RELAXED, __HIP_MEMORY_SCOPE_WORKGROUP

constexpr int LDS_BYTES = 163840;
constexpr int VOFF = 20480, VP = 168;
constexpr int KVB = VOFF + 128 * VP;
constexpr int KV0 = LDS_BYTES - 3 * KVB;
constexpr int OWN = KV0 + 2 * KVB;
constexpr int C_F = 0, C_P = 128, C_E = 144, C_QF = 160, C_RD = 176, C_CNT = 192, C_TAG = 320;
constexpr int N_PUB = C_CNT, N_NB = C_CNT + 4, N_G0 = C_CNT + 8, N_G1 = C_CNT + 12, N_WO = C_CNT + 16, N_OS = C_CNT + 24, N_KV = C_CNT + 32, N_OK = C_CNT + 36;
constexpr int XS0 = 512, WS0 = XS0 + 3 * 16384, QST = 512, ONE = 336, QU27 = XS0 + 16384, QU28 = XS0, QU29 = QU27 + 10240;
static_assert(WS0 + 7 * 10240 <= OWN, "ring overlaps the own image");
constexpr unsigned WS_FKV = 4096, WS_FO = WS_FKV + 256 * 128, WS_KV = 131072;
constexpr unsigned KVG = KVB, OG = 4 * 40960;
constexpr unsigned WS_O = WS_KV + 256 * KVG;
constexpr unsigned WS_END = WS_O + 32 * OG;
constexpr unsigned SPIN_CAP = 400000, LSPIN_CAP = 4000000;
constexpr float QSCALE = 0.22360679774997896f * 1.4426950408889634f;
constexpr int WIN = 4;
constexpr int OPF = 12;
constexpr int PFLIP = 6;
constexpr int ADELAY = 3;

#define MFMA32(a, b, c) __builtin_amdgcn_mfma_f32_32x32x16_bf16((a), (b), (c), 0, 0, 0)
#define MFMA32K8(a, b, c) __builtin_amdgcn_mfma_f32_32x32x8bf16_1k((a), (b), (c), 0, 0, 0)
#define MFMA32I(acc, a, b) asm volatile("v_mfma_f32_32x32x16_bf16 %0, %1, %2, %0" : "+v"(acc) : "v"(a), "v"(b))
#define MFMA32Z(acc, a, b) asm volatile("v_mfma_f32_32x32x16_bf16 %0, %1, %2, 0" : "=&v"(acc) : "v"(a), "v"(b))
#define MFMA_DRAIN() asm volatile("s_nop 7\n\ts_nop 4" ::: "memory")
#define WAITV(N) asm volatile("s_waitcnt vmcnt(" #N ")" ::: "memory")
#define BAR_L() asm volatile("s_waitcnt lgkmcnt(0)\n\ts_barrier" ::: "memory")
#define SB() __builtin_amdgcn_sched_barrier(0)
#define FENCE() asm volatile("" ::: "memory")


template <int V> struct IC { static constexpr int value = V; };
template <int I, int N, class F> DI void static_for(F&& f) { if constexpr (I < N) { f(IC<I>{}); static_for<I + 1, N>(f); } }
#define SFOR(var, N) static_for<0, N>([&](auto ic_##var) __attribute__((always_inline)) { constexpr int var = decltype(ic_##var)::value;
#define SEND });

DI void dma16(unsigned voff, const char* sbase, unsigned ldsaddr) {
    asm volatile("s_mov_b32 m0, %2\n\ts_nop 0\n\tglobal_load_lds_dwordx4 %0, %1" :: "v"(voff), "s"(sbase), "s"(ldsaddr) : "memory");
}
DI void dma16x4(unsigned v0, unsigned v1, unsigned v2, unsigned v3, const char* sbase, unsigned ldsaddr) {
    asm volatile("s_mov_b32 m0, %5\n\ts_nop 0\n\tglobal_load_lds_dwordx4 %0, %4\n\tglobal_load_lds_dwordx4 %1, %4 offset:1024\n\t"
                 "global_load_lds_dwordx4 %2, %4 offset:2048\n\tglobal_load_lds_dwordx4 %3, %4 offset:3072"
                 :: "v"(v0), "v"(v1), "v"(v2), "v"(v3), "s"(sbase), "s"(ldsaddr) : "memory");
}
DI void dma16x5(const unsigned (&v)[5], const char* sbase, unsigned ldsaddr) {
    asm volatile("s_mov_b32 m0, %6\n\ts_nop 0\n\tglobal_load_lds_dwordx4 %0, %5 offset:-2048\n\tglobal_load_lds_dwordx4 %1, %5 offset:-1024\n\t"
                 "global_load_lds_dwordx4 %2, %5\n\tglobal_load_lds_dwordx4 %3, %5 offset:1024\n\tglobal_load_lds_dwordx4 %4, %5 offset:2048"
                 :: "v"(v[0]), "v"(v[1]), "v"(v[2]), "v"(v[3]), "v"(v[4]), "s"(sbase), "s"(ldsaddr) : "memory");
}
DI void dma16x5_sc1(unsigned voff, const char* sbase, unsigned ldsaddr) {
    asm volatile("s_mov_b32 m0, %2\n\ts_nop 0\n\tglobal_load_lds_dwordx4 %0, %1 offset:-2048 sc1\n\tglobal_load_lds_dwordx4 %0, %1 offset:-1024 sc1\n\t"
                 "global_load_lds_dwordx4 %0, %1 sc1\n\tglobal_load_lds_dwordx4 %0, %1 offset:1024 sc1\n\tglobal_load_lds_dwordx4 %0, %1 offset:2048 sc1"
                 :: "v"(voff), "s"(sbase), "s"(ldsaddr) : "memory");
}
DI void dma16_sc1(unsigned voff, const char* sbase, unsigned ldsaddr) {
    asm volatile("s_mov_b32 m0, %2\n\ts_nop 0\n\tglobal_load_lds_dwordx4 %0, %1 sc1" :: "v"(voff), "s"(sbase), "s"(ldsaddr) : "memory");
}
DI void dma4(const char* p, unsigned ldsaddr) {
    asm volatile("s_mov_b32 m0, %1\n\ts_nop 0\n\tglobal_load_lds_dword %0, off" :: "v"(p), "s"(ldsaddr) : "memory");
}
DI unsigned pk2(float lo, float hi) { f32x2 v = {lo, hi}; return __builtin_bit_cast(unsigned, __builtin_convertvector(v, bfx2)); }
typedef short v4i16_t __attribute__((ext_vector_type(4)));
DI bf16x8 trfrag(LAS char* p, int second) {
    s16x4 a = __builtin_bit_cast(s16x4, __builtin_amdgcn_ds_read_tr16_b64_v4i16((LAS v4i16_t*)p));
    s16x4 b = __builtin_bit_cast(s16x4, __builtin_amdgcn_ds_read_tr16_b64_v4i16((LAS v4i16_t*)(p + second)));
    return __builtin_shufflevector(a, b, 0, 1, 2, 3, 4, 5, 6, 7);
}
constexpr u64 lanes_cut(int r, bool fartile) {
    u64 m = 0;
    for (int l = 0; l < 64; ++l) { const int key = (r & 3) + 8 * (r >> 2) + 4 * (l >> 5), d = (l & 31) - key + (fartile ? 32 : 0);
        const bool ok = d >= 0 && d <= 14 && !(d & 1);
        if (!ok) m |= 1ull << l; }
    return m;
}
DI float halfmax(float m) { auto rr = __builtin_amdgcn_permlane32_swap(__float_as_uint(m), __float_as_uint(m), false, false);
    return __builtin_elementwise_maximum(__uint_as_float(rr[0]), __uint_as_float(rr[1])); }

DI void poll_flags(GAS u64* word, bool active, u64 tag) {
#pragma unroll 1
    for (unsigned spins = 0; spins < SPIN_CAP; ++spins) {
        u64 v = tag;
        if (active) v = __hip_atomic_load(word, RLX_AGENT);
        if (__all(v == tag)) break;
        __builtin_amdgcn_s_sleep(2);
    }
    __builtin_amdgcn_fence(__ATOMIC_ACQUIRE, "wavefront");
}
DI int min4(LAS char* p) {
    const u64 a = __hip_atomic_load((LAS u64*)p, RLX_WG), b = __hip_atomic_load((LAS u64*)(p + 8), RLX_WG);
    const unsigned m = min(min((unsigned)a, (unsigned)(a >> 32)), min((unsigned)b, (unsigned)(b >> 32)));
    return __builtin_amdgcn_readfirstlane((int)m);
}
DI int wait_min4(LAS char* p, int need) {
    int v = min4(p);
#pragma unroll 1
    for (unsigned spins = 0; v < need && spins < LSPIN_CAP; ++spins) { __builtin_amdgcn_s_sleep(1); v = min4(p); }
    FENCE();
    return v;
}
DI void wait_word(LAS char* p, unsigned need) {
#pragma unroll 1
    for (unsigned spins = 0; spins < LSPIN_CAP; ++spins) {
        if ((unsigned)__builtin_amdgcn_readfirstlane((int)__hip_atomic_load((LAS unsigned*)p, RLX_WG)) >= need) break;
        __builtin_amdgcn_s_sleep(1); }
    FENCE();
}
DI void lds_inc0(LAS char* p, int lane) { FENCE(); if (lane == 0) (void)__hip_atomic_fetch_add((LAS unsigned*)p, 1u, RLX_WG); FENCE(); }
DI unsigned lds_inc(LAS char* p, int lane) { FENCE(); unsigned old = 0; if (lane == 0) old = __hip_atomic_fetch_add((LAS unsigned*)p, 1u, RLX_WG); FENCE(); return old; }
DI void lds_set(LAS char* p, unsigned v) { FENCE(); __hip_atomic_store((LAS unsigned*)p, v, RLX_WG); FENCE(); }

__global__ __launch_bounds__(512, 2) void fused_attn_kernel(const char* __restrict__ xA, const char* __restrict__ wA, const char* __restrict__ oA,
                                                           const char* __restrict__ xB, const char* __restrict__ wB, const char* __restrict__ oB,
                                                           char* __restrict__ out, char* __restrict__ ws) {
    __shared__ __attribute__((aligned(16))) char smem_[LDS_BYTES];
    LAS char* smem = (LAS char*)smem_;
    const unsigned lds0 = (unsigned)(uintptr_t)smem;
    asm volatile("" :: "s"(xA), "s"(wA), "s"(oA), "s"(xB), "s"(wB), "s"(oB), "s"(out), "s"(ws));
    const int tid = threadIdx.x, lane = tid & 63;
    const int wid = __builtin_amdgcn_readfirstlane(tid >> 6);
    const int bx = blockIdx.x, xcd = bx & 7, idx = bx >> 3;
    const int isA = idx >> 4, rt = (xcd >> 1) * 4 + ((idx >> 2) & 3), hg = (xcd & 1) * 4 + (idx & 3);
    const int br = isA ? 0 : 1;
    { u64 pc; unsigned len;
      asm volatile("s_getpc_b64 %0\n.Lfused_attn_pc:\n\ts_mov_b32 %1, .Lfused_attn_end-.Lfused_attn_pc" : "=s"(pc), "=s"(len));
      if (wid == 3 && (idx & 15) * 8192u < len) dma4((const char*)pc + min(((idx & 15) * 64 + lane) * 128u, len - 4u), lds0 + OWN + 3 * 512); }
    const int k0 = isA ? (((rt & 3) * 5 + 1) / 2 + ((hg & 3) * 5 + 1) / 2) % 10 : 0;
    const char* xb = (isA ? xA : xB) + (size_t)rt * (128 * 1280);
    const char* wb = (isA ? wA : wB) + hg * 160;
    const int me = (br * 16 + rt) * 8 + hg;
    const int r32 = lane & 31, hl = lane >> 5;
    auto kt_of = [&](int s) { int k = s + k0; return k >= 10 ? k - 10 : k; };
    if (isA && wid < 4) {
        const int j16 = idx & 15; const unsigned dump = lds0 + OWN + wid * 512;
        const char* wl = wB + (hg >> 2) * 640 + j16 * (4 * 3840);
        const char* xl = xB + (size_t)(rt * 128 + (hg & 3) * 32 + r32) * 1280 + hl * 128;
        auto pfx = [&](int i) { return xl + i * 256; };
        auto pfkv = [&](int i) { int n = i * 64 + lane; n = n < 400 ? n : 399; const int sl = n / 40, r = n % 40, c = r % 10;
            return wl + (sl * 64 + r / 10) * 3840 + 1280 + (c < 5 ? c * 128 : 640 + c * 128); };
        auto pfq = [&](int i) { int n = i * 64 + lane; n = n < 200 ? n : 199; int sl = n / 20 + 8; sl = sl >= 10 ? sl - 10 : sl; const int r = n % 20;
            return wl + (sl * 64 + r / 5) * 3840 + (r % 5) * 128; };
        dma4(wid & 1 ? pfx(wid >> 1) : pfkv(wid >> 1), dump);
        dma4(wid & 1 ? pfx(2 + (wid >> 1)) : pfkv(2 + (wid >> 1)), dump);
        dma4(wid < 3 ? pfkv(4 + wid) : pfx(4), dump);
        dma4(pfq(wid), dump);
        { int n = wid * 64 + lane; n = n < 200 ? n : 199; const int o = (j16 * 40 + n / 5) * 1280 + (hg >> 2) * 640 + (n % 5) * 128;
          dma4(oB + o, dump); dma4(oA + o, dump); }
    }
    if (isA && wid >= 4) { for (int i = 0; i < ADELAY; ++i) __builtin_amdgcn_s_sleep(16); }

    const unsigned xo = (unsigned)((lane >> 3) * 1280 + (((lane & 7) ^ (lane >> 4)) << 4));
    auto w_off = [&](int q) { const int c = q * 64 + lane, row = __umul24(c, 3277) >> 16, j = c - row * 20; return (unsigned)(__umul24(row, 3840) + j * 16 + (j < 10 ? 1280 : 2400)); };
    if (!isA) { const char* xs = xb + k0 * 128 + wid * (16 * 1280);
      dma16(xo, xs, lds0 + XS0 + wid * 2048);
      dma16(xo ^ 64, xs + 8 * 1280, lds0 + XS0 + wid * 2048 + 1024);
      const char* wsrc = wb + (size_t)k0 * (64 * 3840);
      const unsigned wv = w_off(wid < 5 ? wid : wid - 5);
      if (wid < 5) { dma16(wv, wsrc, lds0 + WS0 + wid * 1024); dma16(wv, wsrc + 16 * 3840, lds0 + WS0 + (5 + wid) * 1024);
          if (wid >= 3) { dma16(wv, wsrc + 32 * 3840, lds0 + WS0 + (10 + wid) * 1024); dma16(wv, wsrc + 48 * 3840, lds0 + WS0 + (15 + wid) * 1024); } }
      else { dma16(wv, wsrc + 32 * 3840, lds0 + WS0 + (5 + wid) * 1024); dma16(wv, wsrc + 48 * 3840, lds0 + WS0 + (10 + wid) * 1024); } }
    if (wid == 4) { const char* pe = ws + (lane & 1) * 4; if (lane < 2) dma4(pe, lds0 + C_TAG); }
    if (tid < 80) *(LAS unsigned*)(smem + tid * 4) = 0u;
    else if (tid < 82) *(LAS unsigned*)(smem + ONE + (tid - 80) * 4) = 0x3F803F80u;
    const int nnb = isA ? (rt > 0 ? 1 : 0) : (rt > 1 ? 2 : rt);
    auto kq_base = [&](int fg) { const int hd = fg / 5, dg = fg % 5; return dg < 4 ? hd * 5120 + (dg >> 1) * 2048 + (dg & 1) * 8 : hd * 5120 + 4096; };

    auto rdtag = [&]() { const u64 t = *(LAS u64*)(smem + C_TAG);
        const u64 e = ((u64)(unsigned)__builtin_amdgcn_readfirstlane((int)(t >> 32)) << 32) | (unsigned)__builtin_amdgcn_readfirstlane((int)t);
        return e * 6364136223846793005ull + 1442695040888963407ull; };
    const __amdgpu_buffer_rsrc_t wor = __builtin_amdgcn_make_buffer_rsrc((void*)(isA ? oA : oB), 0, 640 * 1280, 0x00020000);

    const int hp = wid >> 2, qo = wid & 3, qi = hp ? 3 - qo : qo;
    const int ud = qi + 8;
    const bool far = isA ? (4 * rt + qi >= 1) : (4 * rt + qi >= 8);
    const int u0 = isA ? (far ? ud - 1 : ud) : (far ? qi : 8 - 4 * rt);
    f32x16 cd, cf;
    int dsh, kb1, kb2, nh, vb0, vb1, qb1, qb2; unsigned obase, oo[5];
    auto mask_b = [&](f32x16& c, int r, bool fartile) { const int k = (r & 3) + 8 * (r >> 2); c[r] = __int_as_float(fartile ? ((k - 1) << 23) - dsh : dsh - (k << 23)); };
    auto mask_a = [&](int z) {
      SFOR(r, 16)
          constexpr u64 ad = lanes_cut(r, false), af = lanes_cut(r, true);
          cd[r] = __builtin_amdgcn_inverse_ballot_w64(ad + z) ? -__builtin_inff() : 0.f;
          cf[r] = __builtin_amdgcn_inverse_ballot_w64(af + z) ? -__builtin_inff() : 0.f;
      SEND };
    auto att_setup = [&](int part) {
        int l = tid; asm volatile("" : "+v"(l)); l &= 63;
        const int q = l & 31, h = l >> 5;
        if (part == 0) { dsh = (q - 4 * h) << 23; kb1 = KV0 + hp * 10240 + h * 2048 + q * 16; kb2 = h ? ONE : KV0 + hp * 10240 + 4096 + q * 8; nh = h ^ 1;
            asm volatile("" : "+v"(dsh), "+v"(kb1), "+v"(kb2), "+v"(nh)); }
        if (part == 1) { const bool ones = (l & 16) && (l & 3);
            vb0 = KV0 + VOFF + (4 * h + ((l & 15) >> 2)) * VP + (ones ? 160 : hp * 80 + (l & 16) * 2 + (l & 3) * 8); vb1 = vb0 + (ones ? 0 : 40);
            asm volatile("" : "+v"(vb0), "+v"(vb1)); }
        if (part == 2) { qb1 = QST + hp * 10240 + h * 2048 + (qi * 32 + q) * 16; qb2 = QST + hp * 10240 + 4096 + (qi * 32 + q) * 8;
            obase = (unsigned)(qi * 40960 + hp * 20480 + hg * 2560 + q * 16 + h * 512);
            asm volatile("" : "+v"(qb1), "+v"(qb2), "+v"(obase)); }
        if (part >= 3) {
#pragma unroll
            for (int r = 4 * (part - 3); r < 4 * (part - 2); ++r) mask_b(cd, r, false);
            asm volatile("" : "+v"(cd)); } };
    auto wo_setup = [&](int q) {
        int l = tid; asm volatile("" : "+v"(l)); const int c = q * 64 + (l & 63), rho = __umul24(c, 6554) >> 16;
        oo[q] = (unsigned)(__umul24(qo * 160 + hp * 40 + (rho & ~7) + 4 * (rho & 1) + ((rho >> 1) & 3), 1280) + hg * 160 + (c - rho * 10) * 16);
        asm volatile("" : "+v"(oo[q])); };
    u64 tag;

    if (wid < 4) {
        if (!isA) WAITV(0);
        BAR_L();
        bf16x8 xr[32], xt[2], wf[WIN + 1];
        f32x16 acc[5];
        const int tok = wid * 32 + r32;
        const int xa = XS0 + tok * 128 + ((hl ^ ((tok >> 1) & 7)) << 4);
        int wa0 = WS0 + (8 * hl + ((lane & 15) >> 2)) * 320 + (lane & 16) * 2 + (lane & 3) * 8, wa1 = wa0 + 61440;
        asm volatile("" : "+v"(wa0), "+v"(wa1));
        auto rdx = [&](int i) { return *(LAS bf16x8*)(smem + ((i >> 2) % 3) * 16384 + (xa ^ ((i & 3) * 32))); };
        auto rdw = [&](int f) { const int i = f / 5, t = f % 5, sl = (i >> 1) % 7;
            return trfrag(smem + (sl < 6 ? wa0 + sl * 10240 : wa1) + (i & 1) * 5120 + t * 64, 1280); };
        unsigned fl = 0;
        auto rdflag = [&](int u) { fl = __hip_atomic_load((LAS unsigned*)(smem + C_F + u * 4), RLX_WG); };
        auto check = [&](int u) {
            if (__builtin_amdgcn_readfirstlane(fl) != 15) {
#pragma unroll 1
                for (unsigned spins = 0; spins < LSPIN_CAP; ++spins) { rdflag(u); if (__builtin_amdgcn_readfirstlane(fl) == 15) break; __builtin_amdgcn_s_sleep(1); }
            }
            FENCE(); };
#pragma unroll
        for (int i = 0; i < 4; ++i) xr[i] = rdx(i);
#pragma unroll
        for (int f = 0; f < WIN; ++f) wf[f] = rdw(f);
        SB();
        SFOR(f, 200)
            constexpr int i = f / 5, t = f % 5;
            if constexpr (f % 20 == 10 - WIN) lds_set(smem + C_P + wid * 4, (i >> 1) + 1);
            if constexpr (f == 200 - WIN) lds_set(smem + C_P + wid * 4, 20);
            if constexpr (f % 20 == 10) rdflag(f / 20 + 1);
            if constexpr (f % 20 == 20 - WIN && f < 190) check(f / 20 + 1);
            if constexpr (f + WIN < 200) wf[(f + WIN) % (WIN + 1)] = rdw(f + WIN);
            if constexpr (f % 20 >= 16 && f < 140) xr[(f / 20 + 1) * 4 + f % 20 - 16] = rdx((f / 20 + 1) * 4 + f % 20 - 16);
            if constexpr (t == 1 && i >= 31 && i < 39) xt[(i + 1) & 1] = rdx(i + 1);
            SB();
            if constexpr (i == 0) MFMA32Z(acc[t], wf[f % (WIN + 1)], xr[0]);
            else if constexpr (i < 32) MFMA32I(acc[t], wf[f % (WIN + 1)], xr[i]);
            else MFMA32I(acc[t], wf[f % (WIN + 1)], xt[i & 1]);
            SB();
        SEND
        int l2 = tid; asm volatile("" : "+v"(l2)); l2 &= 63;
        const int h2 = l2 >> 5, tk = wid * 32 + (l2 & 31);
        int eb[5];
        auto ebases = [&](int base) { int l = tid; asm volatile("" : "+v"(l)); const int h = (l >> 5) & 1, k = wid * 32 + (l & 31);
            eb[0] = base + k * 16 + h * 8; eb[1] = base + k * 16 + h * 2040; eb[2] = base + (h ? k * 16 + 1024 : k * 8); eb[3] = base + (h ? k * 8 + 2040 : k * 16);
            asm volatile("" : "+v"(eb[0]), "+v"(eb[1]), "+v"(eb[2]), "+v"(eb[3])); };
        auto kq_addr = [&](int g0) { return eb[g0 % 5 == 1 ? 1 : g0 % 5 == 4 ? 2 : g0 % 5 == 3 ? 3 : 0] + kq_base(g0); };
        ebases(OWN);
        eb[4] = OWN + VOFF + tk * VP + h2 * 8; asm volatile("" : "+v"(eb[4]));
        int qa0 = (8 * h2 + 2 * ((l2 & 15) >> 2)) * 160 + (l2 & 16) * 2 + (l2 & 3) * 8, qa1 = qa0 + WS0 + 10240;
        asm volatile("" : "+v"(qa0), "+v"(qa1));
        auto rdq = [&](int f) { const int i = f / 3, t = f % 3, j = i >> 2, sl = (20 + j) % 7;
            return trfrag(smem + (j >= 7 ? qa0 + (j == 7 ? QU27 : j == 8 ? QU28 : QU29) : sl == 0 ? qa0 + WS0 : qa1 + (sl - 1) * 10240) + (i & 3) * 2560 + t * 64, 160); };
        MFMA_DRAIN();
        WAITV(0);
        { u32x2 pk[20];
#pragma unroll
          for (int n = 0; n < 20; ++n) { const int t = n >> 2, b = n & 3; pk[n] = (u32x2){pk2(acc[t][4 * b], acc[t][4 * b + 1]), pk2(acc[t][4 * b + 2], acc[t][4 * b + 3])}; }
          SB();
#pragma unroll
          for (int n = 0; n < 20; ++n) *(LAS u32x2*)(smem + (n < 10 ? kq_addr(2 * n) : eb[4] + (2 * n - 20) * 8)) = pk[n];
          if (h2 == 0) *(LAS u32x2*)(smem + eb[4] + 160) = (u32x2){0x3F803F80u, 0x3F803F80u}; }
        lds_set(smem + C_E + wid * 4, 1);
        check(10);
        xt[0] = rdx(32);
#pragma unroll
        for (int f = 0; f < WIN; ++f) wf[f] = rdq(f);
        SB();
        u64 pv0 = 0, pv1 = 0;
        SFOR(f, 120)
            constexpr int i = f / 3, t = f % 3, j = i >> 2;
            if constexpr (f % 24 == 12 && j < 9) rdflag(11 + j / 2);
            if constexpr (f % 12 == 12 - WIN && (j == 1 || j == 6 || j == 8)) lds_set(smem + C_P + wid * 4, 21 + j);
            if constexpr (f % 24 == 24 - WIN && j < 9) check(11 + j / 2);
            if constexpr (f + WIN < 120) wf[(f + WIN) % (WIN + 1)] = rdq(f + WIN);
            if constexpr (t == 0 && i < 7) xt[(i + 1) & 1] = rdx(32 + i + 1);
            if constexpr (f >= 90 && f < 100 && !(f & 1)) wo_setup((f - 90) / 2);
            if constexpr (f == 99) tag = rdtag();
            if constexpr (f >= 100 && f < 107) att_setup(f - 100);
            if constexpr (f == 108) ebases(QST);
            if constexpr (f == 116) { pv0 = __hip_atomic_load((LAS u64*)(smem + C_P), RLX_WG); pv1 = __hip_atomic_load((LAS u64*)(smem + C_P + 8), RLX_WG); }
            SB();
            if constexpr (i == 0) MFMA32Z(acc[t], wf[f % (WIN + 1)], xt[0]);
            else if constexpr (i < 8) MFMA32I(acc[t], wf[f % (WIN + 1)], xt[i & 1]);
            else MFMA32I(acc[t], wf[f % (WIN + 1)], xr[i - 8]);
            SB();
        SEND
        MFMA_DRAIN();
        SB();
        if (__builtin_amdgcn_readfirstlane((int)min(min((unsigned)pv0, (unsigned)(pv0 >> 32)), min((unsigned)pv1, (unsigned)(pv1 >> 32)))) < 29) wait_min4(smem + C_P, 29);
        { acc[0] *= QSCALE; acc[1] *= QSCALE; acc[2] *= QSCALE;
          SB();
          u32x2 pk[10];
#pragma unroll
          for (int n = 0; n < 10; ++n) { const int t = n >> 2, b = n & 3; pk[n] = (u32x2){pk2(acc[t][4 * b], acc[t][4 * b + 1]), pk2(acc[t][4 * b + 2], acc[t][4 * b + 3])}; }
          SB();
#pragma unroll
          for (int n = 0; n < 10; ++n) *(LAS u32x2*)(smem + kq_addr(2 * n)) = pk[n]; }
        lds_set(smem + C_QF + wid * 4, 1);
        __builtin_amdgcn_s_setprio(1);
    } else {
        const int hw = wid - 4;
        int known = 0;
        __builtin_amdgcn_s_setprio(3);
        auto lds_or = [&](int off, unsigned v) { FENCE(); if (lane == 0) __hip_atomic_fetch_or((LAS unsigned*)(smem + off), v, RLX_WG); FENCE(); };
        unsigned wo[5], qo[5];
        auto done = [&](int n) { lds_or(C_F + n * 4, 1u << hw); };
        const unsigned xo1 = (xo ^ 64) + 9216, xo2 = xo + 2 * 9216, xo3 = (xo ^ 64) + 3 * 9216;
        auto round_x = [&](int n) { dma16x4(xo, xo1, xo2, xo3, xb + kt_of(n) * 128 + hw * (32 * 1280), lds0 + XS0 + (n % 3) * 16384 + hw * 4096); };
        auto round_w = [&](int n) {
            const int u = 2 * n + (hw >> 1);
            const unsigned dst = lds0 + (u < 27 ? WS0 + (u % 7) * 10240 : u == 27 ? QU27 : u == 28 ? QU28 : QU29) + (hw & 1) * 5120 + 2048;
            if (u < 20) dma16x5(wo, wb + (size_t)(kt_of(u >> 1) * 64 + (u & 1) * 32) * 3840 - 2048, dst);
            else dma16x5(qo, wb + (size_t)kt_of(u < 22 ? u - 12 : u - 22) * (64 * 3840) - 2048, dst); };
        if (isA) round_x(0);
        round_x(1);
#pragma unroll
        for (int q = 0; q < 5; ++q) wo[q] = w_off(q) + (hw & 1) * (16 * 3840) + (4 - q) * 1024;
        if (isA) { round_w(0); round_w(1); WAITV(5); } else { round_w(1); WAITV(9); }
        BAR_L();
#pragma unroll
        for (int q = 0; q < 5; ++q) { const int c = q * 64 + lane, rho = __umul24(c, 6554) >> 16;
            qo[q] = (unsigned)(__umul24((hw & 1) * 32 + (rho & ~7) + 4 * (rho & 1) + ((rho >> 1) & 3), 3840) + (c - rho * 10) * 16 + (4 - q) * 1024); }
#pragma unroll 1
        for (int n = 2; n <= 13; ++n) {
            const int need = n < 13 ? 2 * n - 5 : 20;
            if (need > known) known = min4(smem + C_P);
            if (need > known) { WAITV(0); done(n - 1); known = wait_min4(smem + C_P, need); }
            if (n < 10) round_x(n);
            round_w(n);
            if (n < 10) WAITV(9); else WAITV(5);
            done(n - 1);
        }
        WAITV(0); done(13);
        tag = rdtag();
        auto a_chunk = [&](int a) { int o;
            if (a < 320) { const int hd = a / 80, rem = a % 80; o = hd * 5120 + (rem < 32 ? (96 + rem) * 16 : rem < 64 ? 2048 + (64 + rem) * 16 : 4096 + 768 + (rem - 64) * 16); }
            else o = VOFF + 96 * VP + (a - 320) * 16;
            return o; };
        if (rt < 15) {
            const __amdgpu_buffer_rsrc_t kvr = __builtin_amdgcn_make_buffer_rsrc(ws + WS_KV + me * KVG, 0, KVB, 0x00020000);
            wait_min4(smem + C_E, 1);
            if (!isA) { u32x4 v[11];
#pragma unroll
                for (int j = 0; j < 11; ++j) if (j < 10 || hw == 3) v[j] = *(LAS u32x4*)(smem + OWN + hw * 10240 + j * 1024 + lane * 16);
#pragma unroll
                for (int j = 0; j < 11; ++j) if (j < 10 || hw == 3) __builtin_amdgcn_raw_buffer_store_b128(v[j], kvr, hw * 10240 + j * 1024 + lane * 16, 0, 16);
            } else {
#pragma unroll
                for (int j = 0; j < 3; ++j) { const int a = (4 * j + hw) * 64 + lane;
                    if (a < 656) { const int o = a_chunk(a); __builtin_amdgcn_raw_buffer_store_b128(*(LAS u32x4*)(smem + OWN + o), kvr, o, 0, 16); } }
            }
        }
        wait_min4(smem + C_P, 22);
        round_w(14);
        WAITV(0);
        if (rt < 15 && lds_inc(smem + N_PUB, lane) == 3 && lane == 0) __hip_atomic_store((GAS u64*)(ws + WS_FKV + me * 128), tag, RLX_AGENT);
        done(14);
        __builtin_amdgcn_s_setprio(0);
#pragma unroll
        for (int p = 0; p < 7; ++p) att_setup(p);
#pragma unroll
        for (int q = 0; q < 5; ++q) wo_setup(q);
        __builtin_amdgcn_s_setprio(3);
        if (hw == 0) { poll_flags((GAS u64*)(ws + WS_FKV + (me - 8 * (lane + 1)) * 128), lane < nnb, tag); lds_set(smem + N_KV, 1); }
        else wait_word(smem + N_KV, 1);
        if (!isA) {
            const int n = hw >> 1, half = hw & 1;
            if (n < nnb) {
                const char* src = ws + WS_KV + (me - 8 * (n + 1)) * KVG + half * 20480; const unsigned dst = lds0 + KV0 + (1 - n) * KVB + half * 20480;
                wait_min4(smem + C_P, 27);
#pragma unroll
                for (int j = 0; j < 4; ++j) dma16x5_sc1(lane * 16, src + j * 5120 + 2048, dst + j * 5120 + 2048);
                if (half) dma16_sc1(lane * 16, src + 20480, dst + 20480);
            }
        } else if (nnb) {
            const __amdgpu_buffer_rsrc_t nbr = __builtin_amdgcn_make_buffer_rsrc(ws + WS_KV + (me - 8) * KVG, 0, KVB, 0x00020000);
            u32x4 v[3];
#pragma unroll
            for (int j = 0; j < 3; ++j) { const int a = (4 * j + hw) * 64 + lane; if (a < 656) v[j] = __builtin_amdgcn_raw_buffer_load_b128(nbr, a_chunk(a), 0, 16); }
            wait_min4(smem + C_P, 27);
#pragma unroll
            for (int j = 0; j < 3; ++j) { const int a = (4 * j + hw) * 64 + lane; if (a < 656) *(LAS u32x4*)(smem + KV0 + KVB + a_chunk(a)) = v[j]; }
        }
        __builtin_amdgcn_s_setprio(2);
        wait_word(smem + C_QF + qi * 4, 1);
    }
    u32x4 wreg[13];
    auto ldwo = [&](int i) { wreg[i] = __builtin_amdgcn_raw_buffer_load_b128(wor, oo[i % 5] + (i / 5) * (32 * 1280) + (i > 6 || (i == 6 && lane >= 16) ? 40 * 1280 : 0), 0, 0); };
    u32x2 ob[2][3];
    {
        bf16x8 q1[2]; u32x2 q2[2];
#pragma unroll
        for (int e = 0; e < 2; ++e) {
            q1[e] = *(LAS bf16x8*)(smem + qb1 + e * 5120);
            q2[e] = *(LAS u32x2*)(smem + qb2 + e * 5120);
            if (hl) q2[e] = (u32x2){0u, 0u};
        }
        if (isA) mask_a(0);
        struct KF { bf16x8 a; s16x4 b; };
        struct VF { bf16x8 a, b; };
        auto rdk = [&](int u, int e) { const int so = (u >> 2) * KVB, tw = u & 3; KF k;
            k.a = *(LAS bf16x8*)(smem + kb1 + so + tw * 512 + e * 5120); k.b = *(LAS s16x4*)(smem + __umul24(so + tw * 256 + e * 5120, nh) + kb2); return k; };
        auto rdv = [&](int u, int e) { LAS char* p = smem + (e ? vb1 : vb0) + (u >> 2) * KVB + (u & 3) * (32 * VP); VF v;
            v.a = trfrag(p, 8 * VP); v.b = trfrag(p + 16 * VP, 8 * VP); return v; };
        auto qk = [&](const KF& k, int e, const f32x16& c) { return MFMA32K8(k.b, __builtin_bit_cast(s16x4, q2[e]), MFMA32(k.a, q1[e], c)); };
        const f32x16 zero = {};
        auto max16 = [&](const f32x16& s, float m) {
#pragma unroll
            for (int r = 0; r < 16; ++r) m = __builtin_elementwise_maximum(m, s[r]);
            return m; };
        auto set_ref = [&](float m, int e) {
            const unsigned t0 = pk2(-m, 0.f); const float r1 = -m - __uint_as_float(t0 << 16);
            const unsigned t1 = pk2(r1, 0.f); const float r2 = r1 - __uint_as_float(t1 << 16);
            if (hl) q2[e] = (u32x2){t0 | (t1 << 16), pk2(r2, 0.f)}; };
        auto ex4 = [&](f32x16& s, int b) {
#pragma unroll
            for (int r = 0; r < 4; ++r) s[b + r] = __builtin_amdgcn_exp2f(s[b + r]); };
        auto pk8 = [&](const f32x16& s, int b) { return __builtin_bit_cast(bf16x8, (u32x4){pk2(s[b], s[b + 1]), pk2(s[b + 2], s[b + 3]), pk2(s[b + 4], s[b + 5]), pk2(s[b + 6], s[b + 7])}); };
        auto phase = [&](auto nx, f32x16& cur, const VF& v, f32x16& o, f32x16& nxt, const KF& k, int e) __attribute__((always_inline)) {
            constexpr int NX = decltype(nx)::value;
            SB(); ex4(cur, 0); SB();
            if constexpr (NX) nxt = MFMA32(k.a, q1[e], NX == 1 ? zero : NX == 2 ? cf : cd);
            SB(); ex4(cur, 4); { const bf16x8 p = pk8(cur, 0); SB(); o = MFMA32(v.a, p, o); }
            SB(); ex4(cur, 8); SB();
            if constexpr (NX) nxt = MFMA32K8(k.b, __builtin_bit_cast(s16x4, q2[e]), nxt);
            SB(); ex4(cur, 12); { const bf16x8 p = pk8(cur, 8); SB(); o = MFMA32(v.b, p, o); }
            SB(); };
        f32x16 o0, o1, sa, sb; KF kx; VF vx;
        auto step = [&](auto m0, auto m1, int u) __attribute__((always_inline)) {
            constexpr int M1 = decltype(m1)::value;
            KF ky; if constexpr (M1) ky = rdk(u - 1, 0);
            const VF vy = rdv(u, 1);
            phase(m0, sa, vx, o0, sb, kx, 1);
            if constexpr (M1) { kx = rdk(u - 1, 1); vx = rdv(u - 1, 0); }
            phase(m1, sb, vy, o1, sa, ky, 0); };
        auto nbwait = [&](int u) { if (u == 8) wait_word(smem + N_NB, 4); };
        { const KF k0f = rdk(ud, 0); kx = rdk(ud, 1); vx = rdv(ud, 0);
          const f32x16 s0 = qk(k0f, 0, cd), s1 = qk(kx, 1, cd);
          set_ref(halfmax(max16(s0, s0[0])), 0); set_ref(halfmax(max16(s1, s1[0])), 1);
          sa = qk(k0f, 0, cd); }
        if (hp) { WAITV(0); lds_inc0(smem + N_NB, lane); lds_inc0(smem + N_G0, lane); }
        o0 = zero; o1 = zero;
        if (u0 + 8 == ud) {
            SFOR(it, 9)
                if constexpr (it < 7) { ldwo(2 * it); if constexpr (it < 6) ldwo(2 * it + 1); }
                if constexpr (it == PFLIP) { if (hp) __builtin_amdgcn_s_setprio(0); }
                if constexpr (it == 7) {
#pragma unroll
                    for (int r = 0; r < 16; ++r) mask_b(cf, r, true); }
                if constexpr (it < 4) nbwait(ud - it);
                step(IC<it == 0 ? 3 : it == 8 ? 2 : 1>{}, IC<it == 8 ? 0 : it == 7 ? 2 : 1>{}, ud - it);
            SEND
        } else {
            __builtin_amdgcn_s_setprio(0);
#pragma unroll
            for (int i = 0; i < 13; ++i) ldwo(i);
            if (!isA) cf = zero;
            if (u0 < ud) {
                nbwait(ud);
                if (u0 + 1 < ud) {
                    step(IC<3>{}, IC<1>{}, ud);
#pragma unroll 1
                    for (int u = ud - 1; u > u0 + 1; --u) { nbwait(u); step(IC<1>{}, IC<1>{}, u); }
                    nbwait(u0 + 1);
                    step(IC<1>{}, IC<2>{}, u0 + 1);
                } else step(IC<3>{}, IC<2>{}, ud);
                step(IC<2>{}, IC<0>{}, u0);
            } else step(IC<3>{}, IC<0>{}, ud);
        }
        if (!__all(o0[12] < 1.152921504606847e18f && o1[12] < 1.152921504606847e18f && o0[12] > 0.25f && o1[12] > 0.25f)) {
            if (isA) { int z = 0; asm volatile("" : "+s"(z)); mask_a(z); }
            else { asm volatile("" : "+v"(dsh));
#pragma unroll
                for (int r = 0; r < 16; ++r) { mask_b(cd, r, false); mask_b(cf, r, true); }
                if (!far) cf = zero; }
#pragma unroll
            for (int e = 0; e < 2; ++e) {
                if (hl) q2[e] = (u32x2){0u, 0u};
                auto tile = [&](int u) { return u == ud ? qk(rdk(u, e), e, cd) : u == u0 ? qk(rdk(u, e), e, cf) : qk(rdk(u, e), e, zero); };
                float m = -__builtin_inff();
#pragma unroll 1
                for (int u = u0; u <= ud; ++u) m = max16(tile(u), m);
                m = halfmax(m);
                f32x16 o = zero;
#pragma unroll 1
                for (int u = u0; u <= ud; ++u) { f32x16 t = tile(u) - m;
                    const VF v = rdv(u, e); ex4(t, 0); ex4(t, 4); ex4(t, 8); ex4(t, 12);
                    o = MFMA32(v.a, pk8(t, 0), o); o = MFMA32(v.b, pk8(t, 8), o); }
                if (e) o1 = o; else o0 = o;
            }
        }
        const float rl0 = __builtin_amdgcn_rcpf(o0[12]), rl1 = __builtin_amdgcn_rcpf(o1[12]);
        SB();
#pragma unroll
        for (int r = 0; r < 12; ++r) { o0[r] *= rl0; o1[r] *= rl1; }
        SB();
#pragma unroll
        for (int j = 0; j < 3; ++j) {
            ob[0][j] = (u32x2){pk2(o0[4 * j], o0[4 * j + 1]), pk2(o0[4 * j + 2], o0[4 * j + 3])};
            ob[1][j] = (u32x2){pk2(o1[4 * j], o1[4 * j + 1]), pk2(o1[4 * j + 2], o1[4 * j + 3])}; }
        SB();
    }
    const __amdgpu_buffer_rsrc_t orr = __builtin_amdgcn_make_buffer_rsrc(ws + WS_O + (br * 16 + rt) * OG, 0, 4 * 40960, 0x00020000);
    {
      { auto rx = __builtin_amdgcn_permlane32_swap(ob[0][0][0], ob[0][1][0], false, false);
        auto ry = __builtin_amdgcn_permlane32_swap(ob[0][0][1], ob[0][1][1], false, false);
        __builtin_amdgcn_raw_buffer_store_b128((u32x4){rx[0], ry[0], rx[1], ry[1]}, orr, obase, 0, 16); }
      if (hl == 0) __builtin_amdgcn_raw_buffer_store_b128((u32x4){ob[0][2][0], ob[0][2][1], ob[1][0][0], ob[1][0][1]}, orr, obase + 1024, 0, 16);
      { auto fx = __builtin_amdgcn_permlane32_swap(ob[1][0][0], ob[1][1][0], false, false);
        auto fy = __builtin_amdgcn_permlane32_swap(ob[1][0][1], ob[1][1][1], false, false);
        auto sx = __builtin_amdgcn_permlane32_swap(ob[1][1][0], ob[1][2][0], false, false);
        auto sy = __builtin_amdgcn_permlane32_swap(ob[1][1][1], ob[1][2][1], false, false);
        __builtin_amdgcn_raw_buffer_store_b128((u32x4){fx[1], fy[1], sx[0], sy[0]}, orr, obase + 1536, 0, 16); } }
    if (lane == 0) { (void)__hip_atomic_fetch_add((LAS unsigned*)(smem + N_G1), 1u, RLX_WG);
        if (!hp) (void)__hip_atomic_fetch_add((LAS unsigned*)(smem + N_G0), 1u, RLX_WG); }
    FENCE();
    auto signal_o = [&]() {
        if (lds_inc(smem + N_OS + hp * 4, lane) == 3 && lane == 0) __hip_atomic_store((GAS u64*)(ws + WS_FO + (me * 2 + hp) * 128), tag, RLX_AGENT); };
    __builtin_amdgcn_s_setprio(0);
    WAITV(0); signal_o();
    wait_word(smem + (hp ? N_G1 : N_G0), 8);
    const int p1 = hp ? 30720 : 35840, p2 = hp ? 65536 : 43520;
    const int wb0 = hp ? KV0 + 10240 : 512, wb1 = hp ? KV0 + KVB + 10240 - 30720 : KV0 - 35840, wb2 = KV0 + KVB - 43520;
#pragma unroll
    for (int i = 0; i < 13; ++i) { const int L = qo * 12800 + i * 1024;
        if (i < 12 || lane < 32) *(LAS u32x4*)(smem + (L + (L < p1 ? wb0 : L < p2 ? wb1 : wb2)) + lane * 16) = wreg[i]; }
    lds_inc0(smem + N_WO + hp * 4, lane);
    if (qo == 0) { poll_flags((GAS u64*)(ws + WS_FO + (((me & ~7) + (lane & 7)) * 2 + hp) * 128), lane < 8, tag); lds_set(smem + N_OK + hp * 4, 1); }
    else wait_word(smem + N_OK + hp * 4, 1);
    f32x16 oc[3]; f32x4 ps[10];
    LAS char* red = smem + OWN + qo * 10240 + lane * 16;
    { bf16x8 af[5], of[20];
      const int la = (8 * hl + 2 * ((lane & 15) >> 2)) * 160 + (lane & 16) * 2 + (lane & 3) * 8;
      const int aa0 = wb0 + la, aa1 = (hp ? wb1 : wb0) + la, aa2 = wb1 + la, aa3 = (hp ? wb1 : wb2) + la;
      auto rda = [&](int f) { return trfrag(smem + (f < 36 ? aa0 : f < 42 ? aa1 : f < 51 ? aa2 : aa3) + (f / 3) * 2560 + (f % 3) * 64, 160); };
      auto ldo = [&](int k) { of[k] = __builtin_bit_cast(bf16x8, __builtin_amdgcn_raw_buffer_load_b128(orr, qo * 40960 + (hp * 20 + k) * 1024 + lane * 16, 0, 16)); };
#pragma unroll
      for (int k = 0; k < OPF; ++k) ldo(k);
      wait_word(smem + N_WO + hp * 4, 4);
#pragma unroll
      for (int f = 0; f < 4; ++f) af[f] = rda(f);
      SB();
      SFOR(f, 60)
          if constexpr (f % 3 == 0 && f / 3 + OPF < 20) ldo(f / 3 + OPF);
          if constexpr (f + 4 < 60) af[(f + 4) % 5] = rda(f + 4);
          if constexpr (f == 57) { if (hp) { wait_word(smem + C_RD + qo * 4, 1);
#pragma unroll
              for (int b = 0; b < 10; ++b) ps[b] = *(LAS f32x4*)(red + b * 1024); } }
          SB();
          if constexpr (f < 3) MFMA32Z(oc[f], af[f % 5], of[0]); else MFMA32I(oc[f % 3], af[f % 5], of[f / 3]);
          SB();
      SEND }
    MFMA_DRAIN();
    { if (!hp) {
          wait_word(smem + N_G1, 8);
#pragma unroll
          for (int b = 0; b < 10; ++b) *(LAS f32x4*)(red + b * 1024) = (f32x4){oc[b >> 2][4 * (b & 3)], oc[b >> 2][4 * (b & 3) + 1], oc[b >> 2][4 * (b & 3) + 2], oc[b >> 2][4 * (b & 3) + 3]};
          lds_set(smem + C_RD + qo * 4, 1);
      } else {
          char* orow = out + (size_t)(rt * 128 + qo * 32 + r32) * 2560 + br * 1280 + hg * 160 + hl * 16;
#pragma unroll
          for (int b = 0; b < 10; ++b)
#pragma unroll
              for (int r = 0; r < 4; ++r) ps[b][r] += oc[b >> 2][4 * (b & 3) + r];
          SB();
          u32x2 pk[10];
#pragma unroll
          for (int b = 0; b < 10; ++b) pk[b] = (u32x2){pk2(ps[b][0], ps[b][1]), pk2(ps[b][2], ps[b][3])};
          SB();
          u32x4 st[5];
#pragma unroll
          for (int b = 0; b < 10; b += 2) {
              auto rx = __builtin_amdgcn_permlane32_swap(pk[b][0], pk[b + 1][0], false, false); auto ry = __builtin_amdgcn_permlane32_swap(pk[b][1], pk[b + 1][1], false, false);
              st[b >> 1] = (u32x4){rx[0], ry[0], rx[1], ry[1]}; }
          SB();
#pragma unroll
          for (int b = 0; b < 5; ++b) *(u32x4*)(orow + b * 32) = st[b];
      } }
    if (bx == 0 && wid == 0) {
#pragma unroll 1
        for (int i = 0; i < 4; ++i) poll_flags((GAS u64*)(ws + WS_FO + (i * 64 + lane) * 256), true, tag);
        if (lane == 0) __hip_atomic_store((GAS u64*)ws, tag, RLX_AGENT);
    }
    asm volatile(".Lfused_attn_end:");
}

extern "C" void kernel_launch(void* const* d_in, const int* in_sizes, int n_in, void* d_out, int out_size, void* d_ws, size_t ws_size,
                              hipStream_t stream) {
    fused_attn_kernel<<<256, 512, 0, stream>>>((const char*)d_in[0], (const char*)d_in[1], (const char*)d_in[2], (const char*)d_in[3],
                                               (const char*)d_in[4], (const char*)d_in[5], (char*)d_out, (char*)d_ws + ((ws_size / 4 * 3) & ~(size_t)2097151));
}
```

```cpp
#include <hip/hip_runtime.h>
#include <hip/hip_bf16.h>
#include <stdint.h>

typedef short bf16x8 __attribute__((ext_vector_type(8)));
typedef short s16x4 __attribute__((ext_vector_type(4)));
typedef float f32x2 __attribute__((ext_vector_type(2)));
typedef float f32x4 __attribute__((ext_vector_type(4)));
typedef float f32x16 __attribute__((ext_vector_type(16)));
typedef unsigned u32x4 __attribute__((ext_vector_type(4)));
typedef unsigned u32x2 __attribute__((ext_vector_type(2)));
typedef __bf16 bfx2 __attribute__((ext_vector_type(2)));
typedef unsigned long long u64;
#define LAS __attribute__((address_space(3)))
#define GAS __attribute__((address_space(1)))
#define DI __device__ __forceinline__
#define RLX_AGENT __ATOMIC_RELAXED, __HIP_MEMORY_SCOPE_AGENT
#define RLX_WG __ATOMIC_RELAXED, __HIP_MEMORY_SCOPE_WORKGROUP

constexpr int LDS_BYTES = 163840;
constexpr int VOFF = 20480, VP = 168;
constexpr int KVB = VOFF + 128 * VP;
constexpr int KV0 = LDS_BYTES - 3 * KVB;
constexpr int OWN = KV0 + 2 * KVB;
constexpr int C_F = 0, C_P = 128, C_E = 144, C_QF = 160, C_RD = 176, C_CNT = 192, C_TAG = 320;
constexpr int N_PUB = C_CNT, N_NB = C_CNT + 4, N_G0 = C_CNT + 8, N_G1 = C_CNT + 12, N_WO = C_CNT + 16, N_OS = C_CNT + 24, N_KV = C_CNT + 32, N_OK = C_CNT + 36;
constexpr int XS0 = 512, WS0 = XS0 + 3 * 16384, QST = 512, ONE = 336, QU27 = XS0 + 16384, QU28 = XS0, QU29 = QU27 + 10240;
static_assert(WS0 + 7 * 10240 <= OWN, "ring overlaps the own image");
constexpr unsigned WS_FKV = 4096, WS_FO = WS_FKV + 256 * 128, WS_KV = 131072;
constexpr unsigned KVG = KVB, OG = 4 * 40960;
constexpr unsigned WS_O = WS_KV + 256 * KVG;
constexpr unsigned WS_END = WS_O + 32 * OG;
constexpr unsigned SPIN_CAP = 400000, LSPIN_CAP = 4000000;
constexpr float QSCALE = 0.22360679774997896f * 1.4426950408889634f;
constexpr int WIN = 4;
constexpr int OPF = 12;
constexpr int OP2AT = 27;
constexpr int PFLIP = 0;
constexpr int ADELAY = 3;

#define MFMA32(a, b, c) __builtin_amdgcn_mfma_f32_32x32x16_bf16((a), (b), (c), 0, 0, 0)
#define MFMA32K8(a, b, c) __builtin_amdgcn_mfma_f32_32x32x8bf16_1k((a), (b), (c), 0, 0, 0)
#define MFMA32I(acc, a, b) asm volatile("v_mfma_f32_32x32x16_bf16 %0, %1, %2, %0" : "+v"(acc) : "v"(a), "v"(b))
#define MFMA32Z(acc, a, b) asm volatile("v_mfma_f32_32x32x16_bf16 %0, %1, %2, 0" : "=&v"(acc) : "v"(a), "v"(b))
#define MFMA_DRAIN() asm volatile("s_nop 7\n\ts_nop 4" ::: "memory")
#define WAITV(N) asm volatile("s_waitcnt vmcnt(" #N ")" ::: "memory")
#define BAR_L() asm volatile("s_waitcnt lgkmcnt(0)\n\ts_barrier" ::: "memory")
#define SB() __builtin_amdgcn_sched_barrier(0)
#define FENCE() asm volatile("" ::: "memory")


template <int V> struct IC { static constexpr int value = V; };
template <int I, int N, class F> DI void static_for(F&& f) { if constexpr (I < N) { f(IC<I>{}); static_for<I + 1, N>(f); } }
#define SFOR(var, N) static_for<0, N>([&](auto ic_##var) __attribute__((always_inline)) { constexpr int var = decltype(ic_##var)::value;
#define SEND });

DI void dma16(unsigned voff, const char* sbase, unsigned ldsaddr) {
    asm volatile("s_mov_b32 m0, %2\n\ts_nop 0\n\tglobal_load_lds_dwordx4 %0, %1" :: "v"(voff), "s"(sbase), "s"(ldsaddr) : "memory");
}
DI void dma16x4(unsigned v0, unsigned v1, unsigned v2, unsigned v3, const char* sbase, unsigned ldsaddr) {
    asm volatile("s_mov_b32 m0, %5\n\ts_nop 0\n\tglobal_load_lds_dwordx4 %0, %4\n\tglobal_load_lds_dwordx4 %1, %4 offset:1024\n\t"
                 "global_load_lds_dwordx4 %2, %4 offset:2048\n\tglobal_load_lds_dwordx4 %3, %4 offset:3072"
                 :: "v"(v0), "v"(v1), "v"(v2), "v"(v3), "s"(sbase), "s"(ldsaddr) : "memory");
}
DI void dma16x5(const unsigned (&v)[5], const char* sbase, unsigned ldsaddr) {
    asm volatile("s_mov_b32 m0, %6\n\ts_nop 0\n\tglobal_load_lds_dwordx4 %0, %5 offset:-2048\n\tglobal_load_lds_dwordx4 %1, %5 offset:-1024\n\t"
                 "global_load_lds_dwordx4 %2, %5\n\tglobal_load_lds_dwordx4 %3, %5 offset:1024\n\tglobal_load_lds_dwordx4 %4, %5 offset:2048"
                 :: "v"(v[0]), "v"(v[1]), "v"(v[2]), "v"(v[3]), "v"(v[4]), "s"(sbase), "s"(ldsaddr) : "memory");
}
DI void dma16x5_sc1(unsigned voff, const char* sbase, unsigned ldsaddr) {
    asm volatile("s_mov_b32 m0, %2\n\ts_nop 0\n\tglobal_load_lds_dwordx4 %0, %1 offset:-2048 sc1\n\tglobal_load_lds_dwordx4 %0, %1 offset:-1024 sc1\n\t"
                 "global_load_lds_dwordx4 %0, %1 sc1\n\tglobal_load_lds_dwordx4 %0, %1 offset:1024 sc1\n\tglobal_load_lds_dwordx4 %0, %1 offset:2048 sc1"
                 :: "v"(voff), "s"(sbase), "s"(ldsaddr) : "memory");
}
DI void dma16_sc1(unsigned voff, const char* sbase, unsigned ldsaddr) {
    asm volatile("s_mov_b32 m0, %2\n\ts_nop 0\n\tglobal_load_lds_dwordx4 %0, %1 sc1" :: "v"(voff), "s"(sbase), "s"(ldsaddr) : "memory");
}
DI void dma4(const char* p, unsigned ldsaddr) {
    asm volatile("s_mov_b32 m0, %1\n\ts_nop 0\n\tglobal_load_lds_dword %0, off" :: "v"(p), "s"(ldsaddr) : "memory");
}
DI unsigned pk2(float lo, float hi) { f32x2 v = {lo, hi}; return __builtin_bit_cast(unsigned, __builtin_convertvector(v, bfx2)); }
typedef short v4i16_t __attribute__((ext_vector_type(4)));
DI bf16x8 trfrag(LAS char* p, int second) {
    s16x4 a = __builtin_bit_cast(s16x4, __builtin_amdgcn_ds_read_tr16_b64_v4i16((LAS v4i16_t*)p));
    s16x4 b = __builtin_bit_cast(s16x4, __builtin_amdgcn_ds_read_tr16_b64_v4i16((LAS v4i16_t*)(p + second)));
    return __builtin_shufflevector(a, b, 0, 1, 2, 3, 4, 5, 6, 7);
}
constexpr u64 lanes_cut(int r, bool fartile) {
    u64 m = 0;
    for (int l = 0; l < 64; ++l) { const int key = (r & 3) + 8 * (r >> 2) + 4 * (l >> 5), d = (l & 31) - key + (fartile ? 32 : 0);
        const bool ok = d >= 0 && d <= 14 && !(d & 1);
        if (!ok) m |= 1ull << l; }
    return m;
}
DI float halfmax(float m) { auto rr = __builtin_amdgcn_permlane32_swap(__float_as_uint(m), __float_as_uint(m), false, false);
    return __builtin_elementwise_maximum(__uint_as_float(rr[0]), __uint_as_float(rr[1])); }

DI void poll_flags(GAS u64* word, bool active, u64 tag) {
#pragma unroll 1
    for (unsigned spins = 0; spins < SPIN_CAP; ++spins) {
        u64 v = tag;
        if (active) v = __hip_atomic_load(word, RLX_AGENT);
        if (__all(v == tag)) break;
        __builtin_amdgcn_s_sleep(2);
    }
    __builtin_amdgcn_fence(__ATOMIC_ACQUIRE, "wavefront");
}
DI int min4(LAS char* p) {
    const u64 a = __hip_atomic_load((LAS u64*)p, RLX_WG), b = __hip_atomic_load((LAS u64*)(p + 8), RLX_WG);
    const unsigned m = min(min((unsigned)a, (unsigned)(a >> 32)), min((unsigned)b, (unsigned)(b >> 32)));
    return __builtin_amdgcn_readfirstlane((int)m);
}
DI int wait_min4(LAS char* p, int need) {
    int v = min4(p);
#pragma unroll 1
    for (unsigned spins = 0; v < need && spins < LSPIN_CAP; ++spins) { __builtin_amdgcn_s_sleep(1); v = min4(p); }
    FENCE();
    return v;
}
DI void wait_word(LAS char* p, unsigned need) {
#pragma unroll 1
    for (unsigned spins = 0; spins < LSPIN_CAP; ++spins) {
        if ((unsigned)__builtin_amdgcn_readfirstlane((int)__hip_atomic_load((LAS unsigned*)p, RLX_WG)) >= need) break;
        __builtin_amdgcn_s_sleep(1); }
    FENCE();
}
DI void lds_inc0(LAS char* p, int lane) { FENCE(); if (lane == 0) (void)__hip_atomic_fetch_add((LAS unsigned*)p, 1u, RLX_WG); FENCE(); }
DI unsigned lds_inc(LAS char* p, int lane) { FENCE(); unsigned old = 0; if (lane == 0) old = __hip_atomic_fetch_add((LAS unsigned*)p, 1u, RLX_WG); FENCE(); return old; }
DI void lds_set(LAS char* p, unsigned v) { FENCE(); __hip_atomic_store((LAS unsigned*)p, v, RLX_WG); FENCE(); }

__global__ __launch_bounds__(512, 2) void fused_attn_kernel(const char* __restrict__ xA, const char* __restrict__ wA, const char* __restrict__ oA,
                                                           const char* __restrict__ xB, const char* __restrict__ wB, const char* __restrict__ oB,
                                                           char* __restrict__ out, char* __restrict__ ws) {
    __shared__ __attribute__((aligned(16))) char smem_[LDS_BYTES];
    LAS char* smem = (LAS char*)smem_;
    const unsigned lds0 = (unsigned)(uintptr_t)smem;
    asm volatile("" :: "s"(xA), "s"(wA), "s"(oA), "s"(xB), "s"(wB), "s"(oB), "s"(out), "s"(ws));
    const int tid = threadIdx.x, lane = tid & 63;
    const int wid = __builtin_amdgcn_readfirstlane(tid >> 6);
    const int bx = blockIdx.x, xcd = bx & 7, idx = bx >> 3;
    const int isA = idx >> 4, rt = (xcd >> 1) * 4 + ((idx >> 2) & 3), hg = (xcd & 1) * 4 + (idx & 3);
    const int br = isA ? 0 : 1;
    { u64 pc; unsigned len;
      asm volatile("s_getpc_b64 %0\n.Lfused_attn_pc:\n\ts_mov_b32 %1, .Lfused_attn_end-.Lfused_attn_pc" : "=s"(pc), "=s"(len));
      if (wid == 3 && (idx & 15) * 8192u < len) dma4((const char*)pc + min(((idx & 15) * 64 + lane) * 128u, len - 4u), lds0 + OWN + 3 * 512); }
    const int k0 = isA ? (((rt & 3) * 5 + 1) / 2 + ((hg & 3) * 5 + 1) / 2) % 10 : 0;
    const char* xb = (isA ? xA : xB) + (size_t)rt * (128 * 1280);
    const char* wb = (isA ? wA : wB) + hg * 160;
    const int me = (br * 16 + rt) * 8 + hg;
    const int r32 = lane & 31, hl = lane >> 5;
    auto kt_of = [&](int s) { int k = s + k0; return k >= 10 ? k - 10 : k; };
    if (isA && wid < 4) {
        const int j16 = idx & 15; const unsigned dump = lds0 + OWN + wid * 512;
        const char* wl = wB + (hg >> 2) * 640 + j16 * (4 * 3840);
        const char* xl = xB + (size_t)(rt * 128 + (hg & 3) * 32 + r32) * 1280 + hl * 128;
        auto pfx = [&](int i) { return xl + i * 256; };
        auto pfkv = [&](int i) { int n = i * 64 + lane; n = n < 400 ? n : 399; const int sl = n / 40, r = n % 40, c = r % 10;
            return wl + (sl * 64 + r / 10) * 3840 + 1280 + (c < 5 ? c * 128 : 640 + c * 128); };
        auto pfq = [&](int i) { int n = i * 64 + lane; n = n < 200 ? n : 199; int sl = n / 20 + 8; sl = sl >= 10 ? sl - 10 : sl; const int r = n % 20;
            return wl + (sl * 64 + r / 5) * 3840 + (r % 5) * 128; };
        dma4(wid & 1 ? pfx(wid >> 1) : pfkv(wid >> 1), dump);
        dma4(wid & 1 ? pfx(2 + (wid >> 1)) : pfkv(2 + (wid >> 1)), dump);
        dma4(wid < 3 ? pfkv(4 + wid) : pfx(4), dump);
        dma4(pfq(wid), dump);
        { int n = wid * 64 + lane; n = n < 200 ? n : 199; const int o = (j16 * 40 + n / 5) * 1280 + (hg >> 2) * 640 + (n % 5) * 128;
          dma4(oB + o, dump); dma4(oA + o, dump); }
    }
    if (isA && wid >= 4) { for (int i = 0; i < ADELAY; ++i) __builtin_amdgcn_s_sleep(16); }

    const unsigned xo = (unsigned)((lane >> 3) * 1280 + (((lane & 7) ^ (lane >> 4)) << 4));
    auto w_off = [&](int q) { const int c = q * 64 + lane, row = __umul24(c, 3277) >> 16, j = c - row * 20; return (unsigned)(__umul24(row, 3840) + j * 16 + (j < 10 ? 1280 : 2400)); };
    if (!isA) { const char* xs = xb + k0 * 128 + wid * (16 * 1280);
      dma16(xo, xs, lds0 + XS0 + wid * 2048);
      dma16(xo ^ 64, xs + 8 * 1280, lds0 + XS0 + wid * 2048 + 1024);
      const char* wsrc = wb + (size_t)k0 * (64 * 3840);
      const unsigned wv = w_off(wid < 5 ? wid : wid - 5);
      if (wid < 5) { dma16(wv, wsrc, lds0 + WS0 + wid * 1024); dma16(wv, wsrc + 16 * 3840, lds0 + WS0 + (5 + wid) * 1024);
          if (wid >= 3) { dma16(wv, wsrc + 32 * 3840, lds0 + WS0 + (10 + wid) * 1024); dma16(wv, wsrc + 48 * 3840, lds0 + WS0 + (15 + wid) * 1024); } }
      else { dma16(wv, wsrc + 32 * 3840, lds0 + WS0 + (5 + wid) * 1024); dma16(wv, wsrc + 48 * 3840, lds0 + WS0 + (10 + wid) * 1024); } }
    if (wid == 4) { const char* pe = ws + (lane & 1) * 4; if (lane < 2) dma4(pe, lds0 + C_TAG); }
    if (tid < 80) *(LAS unsigned*)(smem + tid * 4) = 0u;
    else if (tid < 82) *(LAS unsigned*)(smem + ONE + (tid - 80) * 4) = 0x3F803F80u;
    const int nnb = isA ? (rt > 0 ? 1 : 0) : (rt > 1 ? 2 : rt);
    auto kq_base = [&](int fg) { const int hd = fg / 5, dg = fg % 5; return dg < 4 ? hd * 5120 + (dg >> 1) * 2048 + (dg & 1) * 8 : hd * 5120 + 4096; };

    auto rdtag = [&]() { const u64 t = *(LAS u64*)(smem + C_TAG);
        const u64 e = ((u64)(unsigned)__builtin_amdgcn_readfirstlane((int)(t >> 32)) << 32) | (unsigned)__builtin_amdgcn_readfirstlane((int)t);
        return e * 6364136223846793005ull + 1442695040888963407ull; };
    const __amdgpu_buffer_rsrc_t wor = __builtin_amdgcn_make_buffer_rsrc((void*)(isA ? oA : oB), 0, 640 * 1280, 0x00020000);

    const int hp = wid >> 2, qo = wid & 3, qi = hp ? 3 - qo : qo;
    const int ud = qi + 8;
    const bool far = isA ? (4 * rt + qi >= 1) : (4 * rt + qi >= 8);
    const int u0 = isA ? (far ? ud - 1 : ud) : (far ? qi : 8 - 4 * rt);
    f32x16 cd, cf;
    int dsh, kb1, kb2, nh, vb0, vb1, qb1, qb2; unsigned obase, oo[5];
    auto mask_b = [&](f32x16& c, int r, bool fartile) { const int k = (r & 3) + 8 * (r >> 2); c[r] = __int_as_float(fartile ? ((k - 1) << 23) - dsh : dsh - (k << 23)); };
    auto mask_a = [&](int z) {
      SFOR(r, 16)
          constexpr u64 ad = lanes_cut(r, false), af = lanes_cut(r, true);
          cd[r] = __builtin_amdgcn_inverse_ballot_w64(ad + z) ? -__builtin_inff() : 0.f;
          cf[r] = __builtin_amdgcn_inverse_ballot_w64(af + z) ? -__builtin_inff() : 0.f;
      SEND };
    auto att_setup = [&](int part) {
        int l = tid; asm volatile("" : "+v"(l)); l &= 63;
        const int q = l & 31, h = l >> 5;
        if (part == 0) { dsh = (q - 4 * h) << 23; kb1 = KV0 + hp * 10240 + h * 2048 + q * 16; kb2 = h ? ONE : KV0 + hp * 10240 + 4096 + q * 8; nh = h ^ 1;
            asm volatile("" : "+v"(dsh), "+v"(kb1), "+v"(kb2), "+v"(nh)); }
        if (part == 1) { const bool ones = (l & 16) && (l & 3);
            vb0 = KV0 + VOFF + (4 * h + ((l & 15) >> 2)) * VP + (ones ? 160 : hp * 80 + (l & 16) * 2 + (l & 3) * 8); vb1 = vb0 + (ones ? 0 : 40);
            asm volatile("" : "+v"(vb0), "+v"(vb1)); }
        if (part == 2) { qb1 = QST + hp * 10240 + h * 2048 + (qi * 32 + q) * 16; qb2 = QST + hp * 10240 + 4096 + (qi * 32 + q) * 8;
            obase = (unsigned)(qi * 40960 + hp * 20480 + hg * 2560 + q * 16 + h * 512);
            asm volatile("" : "+v"(qb1), "+v"(qb2), "+v"(obase)); }
        if (part >= 3) {
#pragma unroll
            for (int r = 4 * (part - 3); r < 4 * (part - 2); ++r) mask_b(cd, r, false);
            asm volatile("" : "+v"(cd)); } };
    auto wo_setup = [&](int q) {
        int l = tid; asm volatile("" : "+v"(l)); const int c = q * 64 + (l & 63), rho = __umul24(c, 6554) >> 16;
        oo[q] = (unsigned)(__umul24(qo * 160 + hp * 40 + (rho & ~7) + 4 * (rho & 1) + ((rho >> 1) & 3), 1280) + hg * 160 + (c - rho * 10) * 16);
        asm volatile("" : "+v"(oo[q])); };
    u64 tag;

    if (wid < 4) {
        if (!isA) WAITV(0);
        BAR_L();
        bf16x8 xr[32], xt[2], wf[WIN + 1];
        f32x16 acc[5];
        const int tok = wid * 32 + r32;
        const int xa = XS0 + tok * 128 + ((hl ^ ((tok >> 1) & 7)) << 4);
        int wa0 = WS0 + (8 * hl + ((lane & 15) >> 2)) * 320 + (lane & 16) * 2 + (lane & 3) * 8, wa1 = wa0 + 61440;
        asm volatile("" : "+v"(wa0), "+v"(wa1));
        auto rdx = [&](int i) { return *(LAS bf16x8*)(smem + ((i >> 2) % 3) * 16384 + (xa ^ ((i & 3) * 32))); };
        auto rdw = [&](int f) { const int i = f / 5, t = f % 5, sl = (i >> 1) % 7;
            return trfrag(smem + (sl < 6 ? wa0 + sl * 10240 : wa1) + (i & 1) * 5120 + t * 64, 1280); };
        unsigned fl = 0;
        auto rdflag = [&](int u) { fl = __hip_atomic_load((LAS unsigned*)(smem + C_F + u * 4), RLX_WG); };
        auto check = [&](int u) {
            if (__builtin_amdgcn_readfirstlane(fl) != 15) {
#pragma unroll 1
                for (unsigned spins = 0; spins < LSPIN_CAP; ++spins) { rdflag(u); if (__builtin_amdgcn_readfirstlane(fl) == 15) break; __builtin_amdgcn_s_sleep(1); }
            }
            FENCE(); };
#pragma unroll
        for (int i = 0; i < 4; ++i) xr[i] = rdx(i);
#pragma unroll
        for (int f = 0; f < WIN; ++f) wf[f] = rdw(f);
        SB();
        SFOR(f, 200)
            constexpr int i = f / 5, t = f % 5;
            if constexpr (f % 20 == 10 - WIN) lds_set(smem + C_P + wid * 4, (i >> 1) + 1);
            if constexpr (f == 200 - WIN) lds_set(smem + C_P + wid * 4, 20);
            if constexpr (f % 20 == 10) rdflag(f / 20 + 1);
            if constexpr (f % 20 == 20 - WIN && f < 190) check(f / 20 + 1);
            if constexpr (f + WIN < 200) wf[(f + WIN) % (WIN + 1)] = rdw(f + WIN);
            if constexpr (f % 20 >= 16 && f < 140) xr[(f / 20 + 1) * 4 + f % 20 - 16] = rdx((f / 20 + 1) * 4 + f % 20 - 16);
            if constexpr (t == 1 && i >= 31 && i < 39) xt[(i + 1) & 1] = rdx(i + 1);
            SB();
            if constexpr (i == 0) MFMA32Z(acc[t], wf[f % (WIN + 1)], xr[0]);
            else if constexpr (i < 32) MFMA32I(acc[t], wf[f % (WIN + 1)], xr[i]);
            else MFMA32I(acc[t], wf[f % (WIN + 1)], xt[i & 1]);
            SB();
        SEND
        int l2 = tid; asm volatile("" : "+v"(l2)); l2 &= 63;
        const int h2 = l2 >> 5, tk = wid * 32 + (l2 & 31);
        int eb[5];
        auto ebases = [&](int base) { int l = tid; asm volatile("" : "+v"(l)); const int h = (l >> 5) & 1, k = wid * 32 + (l & 31);
            eb[0] = base + k * 16 + h * 8; eb[1] = base + k * 16 + h * 2040; eb[2] = base + (h ? k * 16 + 1024 : k * 8); eb[3] = base + (h ? k * 8 + 2040 : k * 16);
            asm volatile("" : "+v"(eb[0]), "+v"(eb[1]), "+v"(eb[2]), "+v"(eb[3])); };
        auto kq_addr = [&](int g0) { return eb[g0 % 5 == 1 ? 1 : g0 % 5 == 4 ? 2 : g0 % 5 == 3 ? 3 : 0] + kq_base(g0); };
        ebases(OWN);
        eb[4] = OWN + VOFF + tk * VP + h2 * 8; asm volatile("" : "+v"(eb[4]));
        int qa0 = (8 * h2 + 2 * ((l2 & 15) >> 2)) * 160 + (l2 & 16) * 2 + (l2 & 3) * 8, qa1 = qa0 + WS0 + 10240;
        asm volatile("" : "+v"(qa0), "+v"(qa1));
        auto rdq = [&](int f) { const int i = f / 3, t = f % 3, j = i >> 2, sl = (20 + j) % 7;
            return trfrag(smem + (j >= 7 ? qa0 + (j == 7 ? QU27 : j == 8 ? QU28 : QU29) : sl == 0 ? qa0 + WS0 : qa1 + (sl - 1) * 10240) + (i & 3) * 2560 + t * 64, 160); };
        MFMA_DRAIN();
        WAITV(0);
        { u32x2 pk[20];
#pragma unroll
          for (int n = 0; n < 20; ++n) { const int t = n >> 2, b = n & 3; pk[n] = (u32x2){pk2(acc[t][4 * b], acc[t][4 * b + 1]), pk2(acc[t][4 * b + 2], acc[t][4 * b + 3])}; }
          SB();
#pragma unroll
          for (int n = 0; n < 20; ++n) *(LAS u32x2*)(smem + (n < 10 ? kq_addr(2 * n) : eb[4] + (2 * n - 20) * 8)) = pk[n];
          if (h2 == 0) *(LAS u32x2*)(smem + eb[4] + 160) = (u32x2){0x3F803F80u, 0x3F803F80u}; }
        lds_set(smem + C_E + wid * 4, 1);
        check(10);
        xt[0] = rdx(32);
#pragma unroll
        for (int f = 0; f < WIN; ++f) wf[f] = rdq(f);
        SB();
        u64 pv0 = 0, pv1 = 0;
        SFOR(f, 120)
            constexpr int i = f / 3, t = f % 3, j = i >> 2;
            if constexpr (f % 24 == 12 && j < 9) rdflag(11 + j / 2);
            if constexpr (f % 12 == 12 - WIN && (j == 1 || j == 6 || j == 8)) lds_set(smem + C_P + wid * 4, 21 + j);
            if constexpr (f % 24 == 24 - WIN && j < 9) check(11 + j / 2);
            if constexpr (f + WIN < 120) wf[(f + WIN) % (WIN + 1)] = rdq(f + WIN);
            if constexpr (t == 0 && i < 7) xt[(i + 1) & 1] = rdx(32 + i + 1);
            if constexpr (f >= 90 && f < 100 && !(f & 1)) wo_setup((f - 90) / 2);
            if constexpr (f == 99) tag = rdtag();
            if constexpr (f >= 100 && f < 107) att_setup(f - 100);
            if constexpr (f == 108) ebases(QST);
            if constexpr (f == 116) { pv0 = __hip_atomic_load((LAS u64*)(smem + C_P), RLX_WG); pv1 = __hip_atomic_load((LAS u64*)(smem + C_P + 8), RLX_WG); }
            SB();
            if constexpr (i == 0) MFMA32Z(acc[t], wf[f % (WIN + 1)], xt[0]);
            else if constexpr (i < 8) MFMA32I(acc[t], wf[f % (WIN + 1)], xt[i & 1]);
            else MFMA32I(acc[t], wf[f % (WIN + 1)], xr[i - 8]);
            SB();
        SEND
        MFMA_DRAIN();
        SB();
        if (__builtin_amdgcn_readfirstlane((int)min(min((unsigned)pv0, (unsigned)(pv0 >> 32)), min((unsigned)pv1, (unsigned)(pv1 >> 32)))) < 29) wait_min4(smem + C_P, 29);
        { acc[0] *= QSCALE; acc[1] *= QSCALE; acc[2] *= QSCALE;
          SB();
          u32x2 pk[10];
#pragma unroll
          for (int n = 0; n < 10; ++n) { const int t = n >> 2, b = n & 3; pk[n] = (u32x2){pk2(acc[t][4 * b], acc[t][4 * b + 1]), pk2(acc[t][4 * b + 2], acc[t][4 * b + 3])}; }
          SB();
#pragma unroll
          for (int n = 0; n < 10; ++n) *(LAS u32x2*)(smem + kq_addr(2 * n)) = pk[n]; }
        lds_set(smem + C_QF + wid * 4, 1);
        __builtin_amdgcn_s_setprio(1);
    } else {
        const int hw = wid - 4;
        int known = 0;
        __builtin_amdgcn_s_setprio(3);
        auto lds_or = [&](int off, unsigned v) { FENCE(); if (lane == 0) __hip_atomic_fetch_or((LAS unsigned*)(smem + off), v, RLX_WG); FENCE(); };
        unsigned wo[5], qo[5];
        auto done = [&](int n) { lds_or(C_F + n * 4, 1u << hw); };
        const unsigned xo1 = (xo ^ 64) + 9216, xo2 = xo + 2 * 9216, xo3 = (xo ^ 64) + 3 * 9216;
        auto round_x = [&](int n) { dma16x4(xo, xo1, xo2, xo3, xb + kt_of(n) * 128 + hw * (32 * 1280), lds0 + XS0 + (n % 3) * 16384 + hw * 4096); };
        auto round_w = [&](int n) {
            const int u = 2 * n + (hw >> 1);
            const unsigned dst = lds0 + (u < 27 ? WS0 + (u % 7) * 10240 : u == 27 ? QU27 : u == 28 ? QU28 : QU29) + (hw & 1) * 5120 + 2048;
            if (u < 20) dma16x5(wo, wb + (size_t)(kt_of(u >> 1) * 64 + (u & 1) * 32) * 3840 - 2048, dst);
            else dma16x5(qo, wb + (size_t)kt_of(u < 22 ? u - 12 : u - 22) * (64 * 3840) - 2048, dst); };
        if (isA) round_x(0);
        round_x(1);
#pragma unroll
        for (int q = 0; q < 5; ++q) wo[q] = w_off(q) + (hw & 1) * (16 * 3840) + (4 - q) * 1024;
        if (isA) { round_w(0); round_w(1); WAITV(5); } else { round_w(1); WAITV(9); }
        BAR_L();
#pragma unroll
        for (int q = 0; q < 5; ++q) { const int c = q * 64 + lane, rho = __umul24(c, 6554) >> 16;
            qo[q] = (unsigned)(__umul24((hw & 1) * 32 + (rho & ~7) + 4 * (rho & 1) + ((rho >> 1) & 3), 3840) + (c - rho * 10) * 16 + (4 - q) * 1024); }
#pragma unroll 1
        for (int n = 2; n <= 13; ++n) {
            const int need = n < 13 ? 2 * n - 5 : 20;
            if (need > known) known = min4(smem + C_P);
            if (need > known) { WAITV(0); done(n - 1); known = wait_min4(smem + C_P, need); }
            if (n < 10) round_x(n);
            round_w(n);
            if (n < 10) WAITV(9); else WAITV(5);
            done(n - 1);
        }
        WAITV(0); done(13);
        tag = rdtag();
        auto a_chunk = [&](int a) { int o;
            if (a < 320) { const int hd = a / 80, rem = a % 80; o = hd * 5120 + (rem < 32 ? (96 + rem) * 16 : rem < 64 ? 2048 + (64 + rem) * 16 : 4096 + 768 + (rem - 64) * 16); }
            else o = VOFF + 96 * VP + (a - 320) * 16;
            return o; };
        if (rt < 15) {
            const __amdgpu_buffer_rsrc_t kvr = __builtin_amdgcn_make_buffer_rsrc(ws + WS_KV + me * KVG, 0, KVB, 0x00020000);
            wait_min4(smem + C_E, 1);
            if (!isA) { u32x4 v[11];
#pragma unroll
                for (int j = 0; j < 11; ++j) if (j < 10 || hw == 3) v[j] = *(LAS u32x4*)(smem + OWN + hw * 10240 + j * 1024 + lane * 16);
#pragma unroll
                for (int j = 0; j < 11; ++j) if (j < 10 || hw == 3) __builtin_amdgcn_raw_buffer_store_b128(v[j], kvr, hw * 10240 + j * 1024 + lane * 16, 0, 16);
            } else {
#pragma unroll
                for (int j = 0; j < 3; ++j) { const int a = (4 * j + hw) * 64 + lane;
                    if (a < 656) { const int o = a_chunk(a); __builtin_amdgcn_raw_buffer_store_b128(*(LAS u32x4*)(smem + OWN + o), kvr, o, 0, 16); } }
            }
        }
        wait_min4(smem + C_P, 22);
        round_w(14);
        WAITV(0);
        if (rt < 15 && lds_inc(smem + N_PUB, lane) == 3 && lane == 0) __hip_atomic_store((GAS u64*)(ws + WS_FKV + me * 128), tag, RLX_AGENT);
        done(14);
        __builtin_amdgcn_s_setprio(0);
#pragma unroll
        for (int p = 0; p < 7; ++p) att_setup(p);
#pragma unroll
        for (int q = 0; q < 5; ++q) wo_setup(q);
        __builtin_amdgcn_s_setprio(3);
        if (hw == 0) { poll_flags((GAS u64*)(ws + WS_FKV + (me - 8 * (lane + 1)) * 128), lane < nnb, tag); lds_set(smem + N_KV, 1); }
        else wait_word(smem + N_KV, 1);
        if (!isA) {
            const int n = hw >> 1, half = hw & 1;
            if (n < nnb) {
                const char* src = ws + WS_KV + (me - 8 * (n + 1)) * KVG + half * 20480; const unsigned dst = lds0 + KV0 + (1 - n) * KVB + half * 20480;
                wait_min4(smem + C_P, 27);
#pragma unroll
                for (int j = 0; j < 4; ++j) dma16x5_sc1(lane * 16, src + j * 5120 + 2048, dst + j * 5120 + 2048);
                if (half) dma16_sc1(lane * 16, src + 20480, dst + 20480);
            }
        } else if (nnb) {
            const __amdgpu_buffer_rsrc_t nbr = __builtin_amdgcn_make_buffer_rsrc(ws + WS_KV + (me - 8) * KVG, 0, KVB, 0x00020000);
            u32x4 v[3];
#pragma unroll
            for (int j = 0; j < 3; ++j) { const int a = (4 * j + hw) * 64 + lane; if (a < 656) v[j] = __builtin_amdgcn_raw_buffer_load_b128(nbr, a_chunk(a), 0, 16); }
            wait_min4(smem + C_P, 27);
#pragma unroll
            for (int j = 0; j < 3; ++j) { const int a = (4 * j + hw) * 64 + lane; if (a < 656) *(LAS u32x4*)(smem + KV0 + KVB + a_chunk(a)) = v[j]; }
        }
        __builtin_amdgcn_s_setprio(2);
        wait_word(smem + C_QF + qi * 4, 1);
    }
    u32x4 wreg[13];
    auto ldwo = [&](int i) { wreg[i] = __builtin_amdgcn_raw_buffer_load_b128(wor, oo[i % 5] + (i / 5) * (32 * 1280) + (i > 6 || (i == 6 && lane >= 16) ? 40 * 1280 : 0), 0, 0); };
    u32x2 ob[2][3];
    {
        bf16x8 q1[2]; u32x2 q2[2];
#pragma unroll
        for (int e = 0; e < 2; ++e) {
            q1[e] = *(LAS bf16x8*)(smem + qb1 + e * 5120);
            q2[e] = *(LAS u32x2*)(smem + qb2 + e * 5120);
            if (hl) q2[e] = (u32x2){0u, 0u};
        }
        if (isA) mask_a(0);
        struct KF { bf16x8 a; s16x4 b; };
        struct VF { bf16x8 a, b; };
        auto rdk = [&](int u, int e) { const int so = (u >> 2) * KVB, tw = u & 3; KF k;
            k.a = *(LAS bf16x8*)(smem + kb1 + so + tw * 512 + e * 5120); k.b = *(LAS s16x4*)(smem + __umul24(so + tw * 256 + e * 5120, nh) + kb2); return k; };
        auto rdv = [&](int u, int e) { LAS char* p = smem + (e ? vb1 : vb0) + (u >> 2) * KVB + (u & 3) * (32 * VP); VF v;
            v.a = trfrag(p, 8 * VP); v.b = trfrag(p + 16 * VP, 8 * VP); return v; };
        auto qk = [&](const KF& k, int e, const f32x16& c) { return MFMA32K8(k.b, __builtin_bit_cast(s16x4, q2[e]), MFMA32(k.a, q1[e], c)); };
        const f32x16 zero = {};
        auto max16 = [&](const f32x16& s, float m) {
#pragma unroll
            for (int r = 0; r < 16; ++r) m = __builtin_elementwise_maximum(m, s[r]);
            return m; };
        auto set_ref = [&](float m, int e) {
            const unsigned t0 = pk2(-m, 0.f); const float r1 = -m - __uint_as_float(t0 << 16);
            const unsigned t1 = pk2(r1, 0.f); const float r2 = r1 - __uint_as_float(t1 << 16);
            if (hl) q2[e] = (u32x2){t0 | (t1 << 16), pk2(r2, 0.f)}; };
        auto ex4 = [&](f32x16& s, int b) {
#pragma unroll
            for (int r = 0; r < 4; ++r) s[b + r] = __builtin_amdgcn_exp2f(s[b + r]); };
        auto pk8 = [&](const f32x16& s, int b) { return __builtin_bit_cast(bf16x8, (u32x4){pk2(s[b], s[b + 1]), pk2(s[b + 2], s[b + 3]), pk2(s[b + 4], s[b + 5]), pk2(s[b + 6], s[b + 7])}); };
        auto phase = [&](auto nx, f32x16& cur, const VF& v, f32x16& o, f32x16& nxt, const KF& k, int e) __attribute__((always_inline)) {
            constexpr int NX = decltype(nx)::value;
            SB(); ex4(cur, 0); SB();
            if constexpr (NX) nxt = MFMA32(k.a, q1[e], NX == 1 ? zero : NX == 2 ? cf : cd);
            SB(); ex4(cur, 4); { const bf16x8 p = pk8(cur, 0); SB(); o = MFMA32(v.a, p, o); }
            SB(); ex4(cur, 8); SB();
            if constexpr (NX) nxt = MFMA32K8(k.b, __builtin_bit_cast(s16x4, q2[e]), nxt);
            SB(); ex4(cur, 12); { const bf16x8 p = pk8(cur, 8); SB(); o = MFMA32(v.b, p, o); }
            SB(); };
        f32x16 o0, o1, sa, sb; KF kx; VF vx;
        auto step = [&](auto m0, auto m1, int u) __attribute__((always_inline)) {
            constexpr int M1 = decltype(m1)::value;
            KF ky; if constexpr (M1) ky = rdk(u - 1, 0);
            const VF vy = rdv(u, 1);
            phase(m0, sa, vx, o0, sb, kx, 1);
            if constexpr (M1) { kx = rdk(u - 1, 1); vx = rdv(u - 1, 0); }
            phase(m1, sb, vy, o1, sa, ky, 0); };
        auto nbwait = [&](int u) { if (u == 8) wait_word(smem + N_NB, 4); };
        { const KF k0f = rdk(ud, 0); kx = rdk(ud, 1); vx = rdv(ud, 0);
          const f32x16 s0 = qk(k0f, 0, cd), s1 = qk(kx, 1, cd);
          set_ref(halfmax(max16(s0, s0[0])), 0); set_ref(halfmax(max16(s1, s1[0])), 1);
          sa = qk(k0f, 0, cd); }
        if (hp) { WAITV(0); lds_inc0(smem + N_NB, lane); lds_inc0(smem + N_G0, lane); }
        o0 = zero; o1 = zero;
        if (u0 + 8 == ud) {
            SFOR(it, 9)
                if constexpr (it < 7) { ldwo(2 * it); if constexpr (it < 6) ldwo(2 * it + 1); }
                if constexpr (it == PFLIP) { if (hp) __builtin_amdgcn_s_setprio(0); }
                if constexpr (it == 7) {
#pragma unroll
                    for (int r = 0; r < 16; ++r) mask_b(cf, r, true); }
                if constexpr (it < 4) nbwait(ud - it);
                step(IC<it == 0 ? 3 : it == 8 ? 2 : 1>{}, IC<it == 8 ? 0 : it == 7 ? 2 : 1>{}, ud - it);
            SEND
        } else {
            __builtin_amdgcn_s_setprio(0);
#pragma unroll
            for (int i = 0; i < 13; ++i) ldwo(i);
            if (!isA) cf = zero;
            if (u0 < ud) {
                nbwait(ud);
                if (u0 + 1 < ud) {
                    step(IC<3>{}, IC<1>{}, ud);
#pragma unroll 1
                    for (int u = ud - 1; u > u0 + 1; --u) { nbwait(u); step(IC<1>{}, IC<1>{}, u); }
                    nbwait(u0 + 1);
                    step(IC<1>{}, IC<2>{}, u0 + 1);
                } else step(IC<3>{}, IC<2>{}, ud);
                step(IC<2>{}, IC<0>{}, u0);
            } else step(IC<3>{}, IC<0>{}, ud);
        }
        if (!__all(o0[12] < 1.152921504606847e18f && o1[12] < 1.152921504606847e18f && o0[12] > 0.25f && o1[12] > 0.25f)) {
            if (isA) { int z = 0; asm volatile("" : "+s"(z)); mask_a(z); }
            else { asm volatile("" : "+v"(dsh));
#pragma unroll
                for (int r = 0; r < 16; ++r) { mask_b(cd, r, false); mask_b(cf, r, true); }
                if (!far) cf = zero; }
#pragma unroll
            for (int e = 0; e < 2; ++e) {
                if (hl) q2[e] = (u32x2){0u, 0u};
                auto tile = [&](int u) { return u == ud ? qk(rdk(u, e), e, cd) : u == u0 ? qk(rdk(u, e), e, cf) : qk(rdk(u, e), e, zero); };
                float m = -__builtin_inff();
#pragma unroll 1
                for (int u = u0; u <= ud; ++u) m = max16(tile(u), m);
                m = halfmax(m);
                f32x16 o = zero;
#pragma unroll 1
                for (int u = u0; u <= ud; ++u) { f32x16 t = tile(u) - m;
                    const VF v = rdv(u, e); ex4(t, 0); ex4(t, 4); ex4(t, 8); ex4(t, 12);
                    o = MFMA32(v.a, pk8(t, 0), o); o = MFMA32(v.b, pk8(t, 8), o); }
                if (e) o1 = o; else o0 = o;
            }
        }
        const float rl0 = __builtin_amdgcn_rcpf(o0[12]), rl1 = __builtin_amdgcn_rcpf(o1[12]);
        SB();
#pragma unroll
        for (int r = 0; r < 12; ++r) { o0[r] *= rl0; o1[r] *= rl1; }
        SB();
#pragma unroll
        for (int j = 0; j < 3; ++j) {
            ob[0][j] = (u32x2){pk2(o0[4 * j], o0[4 * j + 1]), pk2(o0[4 * j + 2], o0[4 * j + 3])};
            ob[1][j] = (u32x2){pk2(o1[4 * j], o1[4 * j + 1]), pk2(o1[4 * j + 2], o1[4 * j + 3])}; }
        SB();
    }
    const __amdgpu_buffer_rsrc_t orr = __builtin_amdgcn_make_buffer_rsrc(ws + WS_O + (br * 16 + rt) * OG, 0, 4 * 40960, 0x00020000);
    {
      { auto rx = __builtin_amdgcn_permlane32_swap(ob[0][0][0], ob[0][1][0], false, false);
        auto ry = __builtin_amdgcn_permlane32_swap(ob[0][0][1], ob[0][1][1], false, false);
        __builtin_amdgcn_raw_buffer_store_b128((u32x4){rx[0], ry[0], rx[1], ry[1]}, orr, obase, 0, 16); }
      if (hl == 0) __builtin_amdgcn_raw_buffer_store_b128((u32x4){ob[0][2][0], ob[0][2][1], ob[1][0][0], ob[1][0][1]}, orr, obase + 1024, 0, 16);
      { auto fx = __builtin_amdgcn_permlane32_swap(ob[1][0][0], ob[1][1][0], false, false);
        auto fy = __builtin_amdgcn_permlane32_swap(ob[1][0][1], ob[1][1][1], false, false);
        auto sx = __builtin_amdgcn_permlane32_swap(ob[1][1][0], ob[1][2][0], false, false);
        auto sy = __builtin_amdgcn_permlane32_swap(ob[1][1][1], ob[1][2][1], false, false);
        __builtin_amdgcn_raw_buffer_store_b128((u32x4){fx[1], fy[1], sx[0], sy[0]}, orr, obase + 1536, 0, 16); } }
    if (lane == 0) { (void)__hip_atomic_fetch_add((LAS unsigned*)(smem + N_G1), 1u, RLX_WG);
        if (!hp) (void)__hip_atomic_fetch_add((LAS unsigned*)(smem + N_G0), 1u, RLX_WG); }
    FENCE();
    auto signal_o = [&]() {
        if (lds_inc(smem + N_OS + hp * 4, lane) == 3 && lane == 0) __hip_atomic_store((GAS u64*)(ws + WS_FO + (me * 2 + hp) * 128), tag, RLX_AGENT); };
    __builtin_amdgcn_s_setprio(0);
    WAITV(0); signal_o();
    wait_word(smem + N_G1, 8);
#pragma unroll
    for (int i = 0; i < 13; ++i)
        if (i < 12 || lane < 32) *(LAS u32x4*)(smem + 512 + hp * 51200 + qo * 12800 + i * 1024 + lane * 16) = wreg[i];
    lds_inc0(smem + N_WO, lane);
    auto opoll = [&](int g) { if (wid == 0) { poll_flags((GAS u64*)(ws + WS_FO + (((me & ~7) + (lane & 7)) * 2 + g) * 128), lane < 8, tag); lds_set(smem + N_OK + g * 4, 1); }
        else wait_word(smem + N_OK + g * 4, 1); };
    opoll(0);
    f32x16 oc[3]; f32x4 ps[10];
    LAS char* red = smem + OWN + qo * 10240 + lane * 16;
    { bf16x8 af[5], of[20];
      const int aa0 = 512 + hp * 25600 + (8 * hl + 2 * ((lane & 15) >> 2)) * 160 + (lane & 16) * 2 + (lane & 3) * 8, aa1 = aa0 + 51200;
      auto rda = [&](int f) { return trfrag(smem + (f < 30 ? aa0 : aa1) + ((f / 3) % 10) * 2560 + (f % 3) * 64, 160); };
      auto ldo = [&](int k) { of[k] = __builtin_bit_cast(bf16x8, __builtin_amdgcn_raw_buffer_load_b128(orr, qo * 40960 + ((k / 10) * 20 + hp * 10 + k % 10) * 1024 + lane * 16, 0, 16)); };
#pragma unroll
      for (int k = 0; k < 10; ++k) ldo(k);
      wait_word(smem + N_WO, 8);
#pragma unroll
      for (int f = 0; f < 4; ++f) af[f] = rda(f);
      SB();
      SFOR(f, 60)
          if constexpr (f == OP2AT) { opoll(1);
#pragma unroll
              for (int k = 10; k < 20; ++k) ldo(k); }
          if constexpr (f + 4 < 60) af[(f + 4) % 5] = rda(f + 4);
          if constexpr (f == 57) { if (hp) { wait_word(smem + C_RD + qo * 4, 1);
#pragma unroll
              for (int b = 0; b < 10; ++b) ps[b] = *(LAS f32x4*)(red + b * 1024); } }
          SB();
          if constexpr (f < 3) MFMA32Z(oc[f], af[f % 5], of[0]); else MFMA32I(oc[f % 3], af[f % 5], of[f / 3]);
          SB();
      SEND }
    MFMA_DRAIN();
    { if (!hp) {
          wait_word(smem + N_G1, 8);
#pragma unroll
          for (int b = 0; b < 10; ++b) *(LAS f32x4*)(red + b * 1024) = (f32x4){oc[b >> 2][4 * (b & 3)], oc[b >> 2][4 * (b & 3) + 1], oc[b >> 2][4 * (b & 3) + 2], oc[b >> 2][4 * (b & 3) + 3]};
          lds_set(smem + C_RD + qo * 4, 1);
      } else {
          char* orow = out + (size_t)(rt * 128 + qo * 32 + r32) * 2560 + br * 1280 + hg * 160 + hl * 16;
#pragma unroll
          for (int b = 0; b < 10; ++b)
#pragma unroll
              for (int r = 0; r < 4; ++r) ps[b][r] += oc[b >> 2][4 * (b & 3) + r];
          SB();
          u32x2 pk[10];
#pragma unroll
          for (int b = 0; b < 10; ++b) pk[b] = (u32x2){pk2(ps[b][0], ps[b][1]), pk2(ps[b][2], ps[b][3])};
          SB();
          u32x4 st[5];
#pragma unroll
          for (int b = 0; b < 10; b += 2) {
              auto rx = __builtin_amdgcn_permlane32_swap(pk[b][0], pk[b + 1][0], false, false); auto ry = __builtin_amdgcn_permlane32_swap(pk[b][1], pk[b + 1][1], false, false);
              st[b >> 1] = (u32x4){rx[0], ry[0], rx[1], ry[1]}; }
          SB();
#pragma unroll
          for (int b = 0; b < 5; ++b) *(u32x4*)(orow + b * 32) = st[b];
      } }
    if (bx == 0 && wid == 0) {
#pragma unroll 1
        for (int i = 0; i < 4; ++i) poll_flags((GAS u64*)(ws + WS_FO + (i * 64 + lane) * 256), true, tag);
        if (lane == 0) __hip_atomic_store((GAS u64*)ws, tag, RLX_AGENT);
    }
    asm volatile(".Lfused_attn_end:");
}

extern "C" void kernel_launch(void* const* d_in, const int* in_sizes, int n_in, void* d_out, int out_size, void* d_ws, size_t ws_size,
                              hipStream_t stream) {
    fused_attn_kernel<<<256, 512, 0, stream>>>((const char*)d_in[0], (const char*)d_in[1], (const char*)d_in[2], (const char*)d_in[3],
                                               (const char*)d_in[4], (const char*)d_in[5], (char*)d_out, (char*)d_ws);
}
```

```cpp
#include <hip/hip_runtime.h>
#include <hip/hip_bf16.h>
#include <stdint.h>

typedef short bf16x8 __attribute__((ext_vector_type(8)));
typedef short s16x4 __attribute__((ext_vector_type(4)));
typedef float f32x2 __attribute__((ext_vector_type(2)));
typedef float f32x4 __attribute__((ext_vector_type(4)));
typedef float f32x16 __attribute__((ext_vector_type(16)));
typedef unsigned u32x4 __attribute__((ext_vector_type(4)));
typedef unsigned u32x2 __attribute__((ext_vector_type(2)));
typedef __bf16 bfx2 __attribute__((ext_vector_type(2)));
typedef unsigned long long u64;
#define LAS __attribute__((address_space(3)))
#define GAS __attribute__((address_space(1)))
#define DI __device__ __forceinline__
#define RLX_AGENT __ATOMIC_RELAXED, __HIP_MEMORY_SCOPE_AGENT
#define RLX_WG __ATOMIC_RELAXED, __HIP_MEMORY_SCOPE_WORKGROUP

constexpr int LDS_BYTES = 163840;
constexpr int VOFF = 20480, VP = 168;
constexpr int KVB = VOFF + 128 * VP;
constexpr int KV0 = LDS_BYTES - 3 * KVB;
constexpr int OWN = KV0 + 2 * KVB;
constexpr int C_F = 0, C_P = 128, C_E = 144, C_QF = 160, C_RD = 176, C_CNT = 192, C_TAG = 320;
constexpr int N_PUB = C_CNT, N_NB = C_CNT + 4, N_G0 = C_CNT + 8, N_G1 = C_CNT + 12, N_WO = C_CNT + 16, N_OS = C_CNT + 24, N_KV = C_CNT + 32, N_OK = C_CNT + 36;
constexpr int XS0 = 512, WS0 = XS0 + 3 * 16384, QST = 512, ONE = 336, QU27 = XS0 + 16384, QU28 = XS0, QU29 = QU27 + 10240;
static_assert(WS0 + 7 * 10240 <= OWN, "ring overlaps the own image");
constexpr unsigned WS_FKV = 4096, WS_FO = WS_FKV + 256 * 128, WS_KV = 131072;
constexpr unsigned KVG = KVB, OG = 4 * 40960;
constexpr unsigned WS_O = WS_KV + 256 * KVG;
constexpr unsigned WS_END = WS_O + 32 * OG;
constexpr unsigned WS_DBG = WS_END;
constexpr unsigned SPIN_CAP = 400000, LSPIN_CAP = 4000000;
constexpr float QSCALE = 0.22360679774997896f * 1.4426950408889634f;
constexpr int WIN = 4;
constexpr int OPF = 12;
constexpr int PFLIP = 6;
constexpr int ADELAY = 3;

#define MFMA32(a, b, c) __builtin_amdgcn_mfma_f32_32x32x16_bf16((a), (b), (c), 0, 0, 0)
#define MFMA32K8(a, b, c) __builtin_amdgcn_mfma_f32_32x32x8bf16_1k((a), (b), (c), 0, 0, 0)
#define MFMA32I(acc, a, b) asm volatile("v_mfma_f32_32x32x16_bf16 %0, %1, %2, %0" : "+v"(acc) : "v"(a), "v"(b))
#define MFMA32Z(acc, a, b) asm volatile("v_mfma_f32_32x32x16_bf16 %0, %1, %2, 0" : "=&v"(acc) : "v"(a), "v"(b))
#define MFMA_DRAIN() asm volatile("s_nop 7\n\ts_nop 4" ::: "memory")
#define WAITV(N) asm volatile("s_waitcnt vmcnt(" #N ")" ::: "memory")
#define BAR_L() asm volatile("s_waitcnt lgkmcnt(0)\n\ts_barrier" ::: "memory")
#define SB() __builtin_amdgcn_sched_barrier(0)
#define FENCE() asm volatile("" ::: "memory")


template <int V> struct IC { static constexpr int value = V; };
template <int I, int N, class F> DI void static_for(F&& f) { if constexpr (I < N) { f(IC<I>{}); static_for<I + 1, N>(f); } }
#define SFOR(var, N) static_for<0, N>([&](auto ic_##var) __attribute__((always_inline)) { constexpr int var = decltype(ic_##var)::value;
#define SEND });

DI void dma16(unsigned voff, const char* sbase, unsigned ldsaddr) {
    asm volatile("s_mov_b32 m0, %2\n\ts_nop 0\n\tglobal_load_lds_dwordx4 %0, %1" :: "v"(voff), "s"(sbase), "s"(ldsaddr) : "memory");
}
DI void dma16x4(unsigned v0, unsigned v1, unsigned v2, unsigned v3, const char* sbase, unsigned ldsaddr) {
    asm volatile("s_mov_b32 m0, %5\n\ts_nop 0\n\tglobal_load_lds_dwordx4 %0, %4\n\tglobal_load_lds_dwordx4 %1, %4 offset:1024\n\t"
                 "global_load_lds_dwordx4 %2, %4 offset:2048\n\tglobal_load_lds_dwordx4 %3, %4 offset:3072"
                 :: "v"(v0), "v"(v1), "v"(v2), "v"(v3), "s"(sbase), "s"(ldsaddr) : "memory");
}
DI void dma16x5(const unsigned (&v)[5], const char* sbase, unsigned ldsaddr) {
    asm volatile("s_mov_b32 m0, %6\n\ts_nop 0\n\tglobal_load_lds_dwordx4 %0, %5 offset:-2048\n\tglobal_load_lds_dwordx4 %1, %5 offset:-1024\n\t"
                 "global_load_lds_dwordx4 %2, %5\n\tglobal_load_lds_dwordx4 %3, %5 offset:1024\n\tglobal_load_lds_dwordx4 %4, %5 offset:2048"
                 :: "v"(v[0]), "v"(v[1]), "v"(v[2]), "v"(v[3]), "v"(v[4]), "s"(sbase), "s"(ldsaddr) : "memory");
}
DI void dma16x5_sc1(unsigned voff, const char* sbase, unsigned ldsaddr) {
    asm volatile("s_mov_b32 m0, %2\n\ts_nop 0\n\tglobal_load_lds_dwordx4 %0, %1 offset:-2048 sc1\n\tglobal_load_lds_dwordx4 %0, %1 offset:-1024 sc1\n\t"
                 "global_load_lds_dwordx4 %0, %1 sc1\n\tglobal_load_lds_dwordx4 %0, %1 offset:1024 sc1\n\tglobal_load_lds_dwordx4 %0, %1 offset:2048 sc1"
                 :: "v"(voff), "s"(sbase), "s"(ldsaddr) : "memory");
}
DI void dma16_sc1(unsigned voff, const char* sbase, unsigned ldsaddr) {
    asm volatile("s_mov_b32 m0, %2\n\ts_nop 0\n\tglobal_load_lds_dwordx4 %0, %1 sc1" :: "v"(voff), "s"(sbase), "s"(ldsaddr) : "memory");
}
DI void dma4(const char* p, unsigned ldsaddr) {
    asm volatile("s_mov_b32 m0, %1\n\ts_nop 0\n\tglobal_load_lds_dword %0, off" :: "v"(p), "s"(ldsaddr) : "memory");
}
DI unsigned pk2(float lo, float hi) { f32x2 v = {lo, hi}; return __builtin_bit_cast(unsigned, __builtin_convertvector(v, bfx2)); }
typedef short v4i16_t __attribute__((ext_vector_type(4)));
DI bf16x8 trfrag(LAS char* p, int second) {
    s16x4 a = __builtin_bit_cast(s16x4, __builtin_amdgcn_ds_read_tr16_b64_v4i16((LAS v4i16_t*)p));
    s16x4 b = __builtin_bit_cast(s16x4, __builtin_amdgcn_ds_read_tr16_b64_v4i16((LAS v4i16_t*)(p + second)));
    return __builtin_shufflevector(a, b, 0, 1, 2, 3, 4, 5, 6, 7);
}
constexpr u64 lanes_cut(int r, bool fartile) {
    u64 m = 0;
    for (int l = 0; l < 64; ++l) { const int key = (r & 3) + 8 * (r >> 2) + 4 * (l >> 5), d = (l & 31) - key + (fartile ? 32 : 0);
        const bool ok = d >= 0 && d <= 14 && !(d & 1);
        if (!ok) m |= 1ull << l; }
    return m;
}
DI float halfmax(float m) { auto rr = __builtin_amdgcn_permlane32_swap(__float_as_uint(m), __float_as_uint(m), false, false);
    return __builtin_elementwise_maximum(__uint_as_float(rr[0]), __uint_as_float(rr[1])); }

DI void poll_flags(GAS u64* word, bool active, u64 tag) {
#pragma unroll 1
    for (unsigned spins = 0; spins < SPIN_CAP; ++spins) {
        u64 v = tag;
        if (active) v = __hip_atomic_load(word, RLX_AGENT);
        if (__all(v == tag)) break;
        __builtin_amdgcn_s_sleep(2);
    }
    __builtin_amdgcn_fence(__ATOMIC_ACQUIRE, "wavefront");
}
DI int min4(LAS char* p) {
    const u64 a = __hip_atomic_load((LAS u64*)p, RLX_WG), b = __hip_atomic_load((LAS u64*)(p + 8), RLX_WG);
    const unsigned m = min(min((unsigned)a, (unsigned)(a >> 32)), min((unsigned)b, (unsigned)(b >> 32)));
    return __builtin_amdgcn_readfirstlane((int)m);
}
DI int wait_min4(LAS char* p, int need) {
    int v = min4(p);
#pragma unroll 1
    for (unsigned spins = 0; v < need && spins < LSPIN_CAP; ++spins) { __builtin_amdgcn_s_sleep(1); v = min4(p); }
    FENCE();
    return v;
}
DI void wait_word(LAS char* p, unsigned need) {
#pragma unroll 1
    for (unsigned spins = 0; spins < LSPIN_CAP; ++spins) {
        if ((unsigned)__builtin_amdgcn_readfirstlane((int)__hip_atomic_load((LAS unsigned*)p, RLX_WG)) >= need) break;
        __builtin_amdgcn_s_sleep(1); }
    FENCE();
}
DI void lds_inc0(LAS char* p, int lane) { FENCE(); if (lane == 0) (void)__hip_atomic_fetch_add((LAS unsigned*)p, 1u, RLX_WG); FENCE(); }
DI unsigned lds_inc(LAS char* p, int lane) { FENCE(); unsigned old = 0; if (lane == 0) old = __hip_atomic_fetch_add((LAS unsigned*)p, 1u, RLX_WG); FENCE(); return old; }
DI void lds_set(LAS char* p, unsigned v) { FENCE(); __hip_atomic_store((LAS unsigned*)p, v, RLX_WG); FENCE(); }

__global__ __launch_bounds__(512, 2) void fused_attn_kernel(const char* __restrict__ xA, const char* __restrict__ wA, const char* __restrict__ oA,
                                                           const char* __restrict__ xB, const char* __restrict__ wB, const char* __restrict__ oB,
                                                           char* __restrict__ out, char* __restrict__ ws) {
    __shared__ __attribute__((aligned(16))) char smem_[LDS_BYTES];
    LAS char* smem = (LAS char*)smem_;
    const unsigned lds0 = (unsigned)(uintptr_t)smem;
    const unsigned dt0 = (unsigned)__builtin_amdgcn_s_memrealtime(); SB();
    asm volatile("" :: "s"(xA), "s"(wA), "s"(oA), "s"(xB), "s"(wB), "s"(oB), "s"(out), "s"(ws));
    SB(); const unsigned dt1 = (unsigned)__builtin_amdgcn_s_memrealtime(); SB();
    const int tid = threadIdx.x, lane = tid & 63;
    const int wid = __builtin_amdgcn_readfirstlane(tid >> 6);
    const int bx = blockIdx.x, xcd = bx & 7, idx = bx >> 3;
    const int isA = idx >> 4, rt = (xcd >> 1) * 4 + ((idx >> 2) & 3), hg = (xcd & 1) * 4 + (idx & 3);
    const int br = isA ? 0 : 1;
    { u64 pc; unsigned len;
      asm volatile("s_getpc_b64 %0\n.Lfused_attn_pc:\n\ts_mov_b32 %1, .Lfused_attn_end-.Lfused_attn_pc" : "=s"(pc), "=s"(len));
      if (wid == 3 && (idx & 15) * 8192u < len) dma4((const char*)pc + min(((idx & 15) * 64 + lane) * 128u, len - 4u), lds0 + OWN + 3 * 512); }
    const int k0 = isA ? (((rt & 3) * 5 + 1) / 2 + ((hg & 3) * 5 + 1) / 2) % 10 : 0;
    const char* xb = (isA ? xA : xB) + (size_t)rt * (128 * 1280);
    const char* wb = (isA ? wA : wB) + hg * 160;
    const int me = (br * 16 + rt) * 8 + hg;
    const int r32 = lane & 31, hl = lane >> 5;
    auto kt_of = [&](int s) { int k = s + k0; return k >= 10 ? k - 10 : k; };
    if (isA && wid < 4) {
        const int j16 = idx & 15; const unsigned dump = lds0 + OWN + wid * 512;
        const char* wl = wB + (hg >> 2) * 640 + j16 * (4 * 3840);
        const char* xl = xB + (size_t)(rt * 128 + (hg & 3) * 32 + r32) * 1280 + hl * 128;
        auto pfx = [&](int i) { return xl + i * 256; };
        auto pfkv = [&](int i) { int n = i * 64 + lane; n = n < 400 ? n : 399; const int sl = n / 40, r = n % 40, c = r % 10;
            return wl + (sl * 64 + r / 10) * 3840 + 1280 + (c < 5 ? c * 128 : 640 + c * 128); };
        auto pfq = [&](int i) { int n = i * 64 + lane; n = n < 200 ? n : 199; int sl = n / 20 + 8; sl = sl >= 10 ? sl - 10 : sl; const int r = n % 20;
            return wl + (sl * 64 + r / 5) * 3840 + (r % 5) * 128; };
        dma4(wid & 1 ? pfx(wid >> 1) : pfkv(wid >> 1), dump);
        dma4(wid & 1 ? pfx(2 + (wid >> 1)) : pfkv(2 + (wid >> 1)), dump);
        dma4(wid < 3 ? pfkv(4 + wid) : pfx(4), dump);
        dma4(pfq(wid), dump);
        { int n = wid * 64 + lane; n = n < 200 ? n : 199; const int o = (j16 * 40 + n / 5) * 1280 + (hg >> 2) * 640 + (n % 5) * 128;
          dma4(oB + o, dump); dma4(oA + o, dump); }
    }
    if (isA && wid >= 4) { for (int i = 0; i < ADELAY; ++i) __builtin_amdgcn_s_sleep(16); }

    const unsigned xo = (unsigned)((lane >> 3) * 1280 + (((lane & 7) ^ (lane >> 4)) << 4));
    auto w_off = [&](int q) { const int c = q * 64 + lane, row = __umul24(c, 3277) >> 16, j = c - row * 20; return (unsigned)(__umul24(row, 3840) + j * 16 + (j < 10 ? 1280 : 2400)); };
    if (!isA) { const char* xs = xb + k0 * 128 + wid * (16 * 1280);
      dma16(xo, xs, lds0 + XS0 + wid * 2048);
      dma16(xo ^ 64, xs + 8 * 1280, lds0 + XS0 + wid * 2048 + 1024);
      const char* wsrc = wb + (size_t)k0 * (64 * 3840);
      const unsigned wv = w_off(wid < 5 ? wid : wid - 5);
      if (wid < 5) { dma16(wv, wsrc, lds0 + WS0 + wid * 1024); dma16(wv, wsrc + 16 * 3840, lds0 + WS0 + (5 + wid) * 1024);
          if (wid >= 3) { dma16(wv, wsrc + 32 * 3840, lds0 + WS0 + (10 + wid) * 1024); dma16(wv, wsrc + 48 * 3840, lds0 + WS0 + (15 + wid) * 1024); } }
      else { dma16(wv, wsrc + 32 * 3840, lds0 + WS0 + (5 + wid) * 1024); dma16(wv, wsrc + 48 * 3840, lds0 + WS0 + (10 + wid) * 1024); } }
    if (wid == 4) { const char* pe = ws + (lane & 1) * 4; if (lane < 2) dma4(pe, lds0 + C_TAG); }
    if (tid < 80) *(LAS unsigned*)(smem + tid * 4) = 0u;
    else if (tid < 82) *(LAS unsigned*)(smem + ONE + (tid - 80) * 4) = 0x3F803F80u;
    const int nnb = isA ? (rt > 0 ? 1 : 0) : (rt > 1 ? 2 : rt);
    auto kq_base = [&](int fg) { const int hd = fg / 5, dg = fg % 5; return dg < 4 ? hd * 5120 + (dg >> 1) * 2048 + (dg & 1) * 8 : hd * 5120 + 4096; };

    auto rdtag = [&]() { const u64 t = *(LAS u64*)(smem + C_TAG);
        const u64 e = ((u64)(unsigned)__builtin_amdgcn_readfirstlane((int)(t >> 32)) << 32) | (unsigned)__builtin_amdgcn_readfirstlane((int)t);
        return e * 6364136223846793005ull + 1442695040888963407ull; };
    const __amdgpu_buffer_rsrc_t wor = __builtin_amdgcn_make_buffer_rsrc((void*)(isA ? oA : oB), 0, 640 * 1280, 0x00020000);

    const int hp = wid >> 2, qo = wid & 3, qi = hp ? 3 - qo : qo;
    const int ud = qi + 8;
    const bool far = isA ? (4 * rt + qi >= 1) : (4 * rt + qi >= 8);
    const int u0 = isA ? (far ? ud - 1 : ud) : (far ? qi : 8 - 4 * rt);
    f32x16 cd, cf;
    int dsh, kb1, kb2, nh, vb0, vb1, qb1, qb2; unsigned obase, oo[5];
    auto mask_b = [&](f32x16& c, int r, bool fartile) { const int k = (r & 3) + 8 * (r >> 2); c[r] = __int_as_float(fartile ? ((k - 1) << 23) - dsh : dsh - (k << 23)); };
    auto mask_a = [&](int z) {
      SFOR(r, 16)
          constexpr u64 ad = lanes_cut(r, false), af = lanes_cut(r, true);
          cd[r] = __builtin_amdgcn_inverse_ballot_w64(ad + z) ? -__builtin_inff() : 0.f;
          cf[r] = __builtin_amdgcn_inverse_ballot_w64(af + z) ? -__builtin_inff() : 0.f;
      SEND };
    auto att_setup = [&](int part) {
        int l = tid; asm volatile("" : "+v"(l)); l &= 63;
        const int q = l & 31, h = l >> 5;
        if (part == 0) { dsh = (q - 4 * h) << 23; kb1 = KV0 + hp * 10240 + h * 2048 + q * 16; kb2 = h ? ONE : KV0 + hp * 10240 + 4096 + q * 8; nh = h ^ 1;
            asm volatile("" : "+v"(dsh), "+v"(kb1), "+v"(kb2), "+v"(nh)); }
        if (part == 1) { const bool ones = (l & 16) && (l & 3);
            vb0 = KV0 + VOFF + (4 * h + ((l & 15) >> 2)) * VP + (ones ? 160 : hp * 80 + (l & 16) * 2 + (l & 3) * 8); vb1 = vb0 + (ones ? 0 : 40);
            asm volatile("" : "+v"(vb0), "+v"(vb1)); }
        if (part == 2) { qb1 = QST + hp * 10240 + h * 2048 + (qi * 32 + q) * 16; qb2 = QST + hp * 10240 + 4096 + (qi * 32 + q) * 8;
            obase = (unsigned)(qi * 40960 + hp * 20480 + hg * 2560 + q * 16 + h * 512);
            asm volatile("" : "+v"(qb1), "+v"(qb2), "+v"(obase)); }
        if (part >= 3) {
#pragma unroll
            for (int r = 4 * (part - 3); r < 4 * (part - 2); ++r) mask_b(cd, r, false);
            asm volatile("" : "+v"(cd)); } };
    auto wo_setup = [&](int q) {
        int l = tid; asm volatile("" : "+v"(l)); const int c = q * 64 + (l & 63), rho = __umul24(c, 6554) >> 16;
        oo[q] = (unsigned)(__umul24(qo * 160 + hp * 40 + (rho & ~7) + 4 * (rho & 1) + ((rho >> 1) & 3), 1280) + hg * 160 + (c - rho * 10) * 16);
        asm volatile("" : "+v"(oo[q])); };
    u64 tag;

    if (wid < 4) {
        if (!isA) WAITV(0);
        BAR_L();
        bf16x8 xr[32], xt[2], wf[WIN + 1];
        f32x16 acc[5];
        const int tok = wid * 32 + r32;
        const int xa = XS0 + tok * 128 + ((hl ^ ((tok >> 1) & 7)) << 4);
        int wa0 = WS0 + (8 * hl + ((lane & 15) >> 2)) * 320 + (lane & 16) * 2 + (lane & 3) * 8, wa1 = wa0 + 61440;
        asm volatile("" : "+v"(wa0), "+v"(wa1));
        auto rdx = [&](int i) { return *(LAS bf16x8*)(smem + ((i >> 2) % 3) * 16384 + (xa ^ ((i & 3) * 32))); };
        auto rdw = [&](int f) { const int i = f / 5, t = f % 5, sl = (i >> 1) % 7;
            return trfrag(smem + (sl < 6 ? wa0 + sl * 10240 : wa1) + (i & 1) * 5120 + t * 64, 1280); };
        unsigned fl = 0;
        auto rdflag = [&](int u) { fl = __hip_atomic_load((LAS unsigned*)(smem + C_F + u * 4), RLX_WG); };
        auto check = [&](int u) {
            if (__builtin_amdgcn_readfirstlane(fl) != 15) {
#pragma unroll 1
                for (unsigned spins = 0; spins < LSPIN_CAP; ++spins) { rdflag(u); if (__builtin_amdgcn_readfirstlane(fl) == 15) break; __builtin_amdgcn_s_sleep(1); }
            }
            FENCE(); };
#pragma unroll
        for (int i = 0; i < 4; ++i) xr[i] = rdx(i);
#pragma unroll
        for (int f = 0; f < WIN; ++f) wf[f] = rdw(f);
        SB();
        SFOR(f, 200)
            constexpr int i = f / 5, t = f % 5;
            if constexpr (f % 20 == 10 - WIN) lds_set(smem + C_P + wid * 4, (i >> 1) + 1);
            if constexpr (f == 200 - WIN) lds_set(smem + C_P + wid * 4, 20);
            if constexpr (f % 20 == 10) rdflag(f / 20 + 1);
            if constexpr (f % 20 == 20 - WIN && f < 190) check(f / 20 + 1);
            if constexpr (f + WIN < 200) wf[(f + WIN) % (WIN + 1)] = rdw(f + WIN);
            if constexpr (f % 20 >= 16 && f < 140) xr[(f / 20 + 1) * 4 + f % 20 - 16] = rdx((f / 20 + 1) * 4 + f % 20 - 16);
            if constexpr (t == 1 && i >= 31 && i < 39) xt[(i + 1) & 1] = rdx(i + 1);
            SB();
            if constexpr (i == 0) MFMA32Z(acc[t], wf[f % (WIN + 1)], xr[0]);
            else if constexpr (i < 32) MFMA32I(acc[t], wf[f % (WIN + 1)], xr[i]);
            else MFMA32I(acc[t], wf[f % (WIN + 1)], xt[i & 1]);
            SB();
        SEND
        int l2 = tid; asm volatile("" : "+v"(l2)); l2 &= 63;
        const int h2 = l2 >> 5, tk = wid * 32 + (l2 & 31);
        int eb[5];
        auto ebases = [&](int base) { int l = tid; asm volatile("" : "+v"(l)); const int h = (l >> 5) & 1, k = wid * 32 + (l & 31);
            eb[0] = base + k * 16 + h * 8; eb[1] = base + k * 16 + h * 2040; eb[2] = base + (h ? k * 16 + 1024 : k * 8); eb[3] = base + (h ? k * 8 + 2040 : k * 16);
            asm volatile("" : "+v"(eb[0]), "+v"(eb[1]), "+v"(eb[2]), "+v"(eb[3])); };
        auto kq_addr = [&](int g0) { return eb[g0 % 5 == 1 ? 1 : g0 % 5 == 4 ? 2 : g0 % 5 == 3 ? 3 : 0] + kq_base(g0); };
        ebases(OWN);
        eb[4] = OWN + VOFF + tk * VP + h2 * 8; asm volatile("" : "+v"(eb[4]));
        int qa0 = (8 * h2 + 2 * ((l2 & 15) >> 2)) * 160 + (l2 & 16) * 2 + (l2 & 3) * 8, qa1 = qa0 + WS0 + 10240;
        asm volatile("" : "+v"(qa0), "+v"(qa1));
        auto rdq = [&](int f) { const int i = f / 3, t = f % 3, j = i >> 2, sl = (20 + j) % 7;
            return trfrag(smem + (j >= 7 ? qa0 + (j == 7 ? QU27 : j == 8 ? QU28 : QU29) : sl == 0 ? qa0 + WS0 : qa1 + (sl - 1) * 10240) + (i & 3) * 2560 + t * 64, 160); };
        MFMA_DRAIN();
        WAITV(0);
        { u32x2 pk[20];
#pragma unroll
          for (int n = 0; n < 20; ++n) { const int t = n >> 2, b = n & 3; pk[n] = (u32x2){pk2(acc[t][4 * b], acc[t][4 * b + 1]), pk2(acc[t][4 * b + 2], acc[t][4 * b + 3])}; }
          SB();
#pragma unroll
          for (int n = 0; n < 20; ++n) *(LAS u32x2*)(smem + (n < 10 ? kq_addr(2 * n) : eb[4] + (2 * n - 20) * 8)) = pk[n];
          if (h2 == 0) *(LAS u32x2*)(smem + eb[4] + 160) = (u32x2){0x3F803F80u, 0x3F803F80u}; }
        lds_set(smem + C_E + wid * 4, 1);
        check(10);
        xt[0] = rdx(32);
#pragma unroll
        for (int f = 0; f < WIN; ++f) wf[f] = rdq(f);
        SB();
        u64 pv0 = 0, pv1 = 0;
        SFOR(f, 120)
            constexpr int i = f / 3, t = f % 3, j = i >> 2;
            if constexpr (f % 24 == 12 && j < 9) rdflag(11 + j / 2);
            if constexpr (f % 12 == 12 - WIN && (j == 1 || j == 6 || j == 8)) lds_set(smem + C_P + wid * 4, 21 + j);
            if constexpr (f % 24 == 24 - WIN && j < 9) check(11 + j / 2);
            if constexpr (f + WIN < 120) wf[(f + WIN) % (WIN + 1)] = rdq(f + WIN);
            if constexpr (t == 0 && i < 7) xt[(i + 1) & 1] = rdx(32 + i + 1);
            if constexpr (f >= 90 && f < 100 && !(f & 1)) wo_setup((f - 90) / 2);
            if constexpr (f == 99) tag = rdtag();
            if constexpr (f >= 100 && f < 107) att_setup(f - 100);
            if constexpr (f == 108) ebases(QST);
            if constexpr (f == 116) { pv0 = __hip_atomic_load((LAS u64*)(smem + C_P), RLX_WG); pv1 = __hip_atomic_load((LAS u64*)(smem + C_P + 8), RLX_WG); }
            SB();
            if constexpr (i == 0) MFMA32Z(acc[t], wf[f % (WIN + 1)], xt[0]);
            else if constexpr (i < 8) MFMA32I(acc[t], wf[f % (WIN + 1)], xt[i & 1]);
            else MFMA32I(acc[t], wf[f % (WIN + 1)], xr[i - 8]);
            SB();
        SEND
        MFMA_DRAIN();
        SB();
        if (__builtin_amdgcn_readfirstlane((int)min(min((unsigned)pv0, (unsigned)(pv0 >> 32)), min((unsigned)pv1, (unsigned)(pv1 >> 32)))) < 29) wait_min4(smem + C_P, 29);
        { acc[0] *= QSCALE; acc[1] *= QSCALE; acc[2] *= QSCALE;
          SB();
          u32x2 pk[10];
#pragma unroll
          for (int n = 0; n < 10; ++n) { const int t = n >> 2, b = n & 3; pk[n] = (u32x2){pk2(acc[t][4 * b], acc[t][4 * b + 1]), pk2(acc[t][4 * b + 2], acc[t][4 * b + 3])}; }
          SB();
#pragma unroll
          for (int n = 0; n < 10; ++n) *(LAS u32x2*)(smem + kq_addr(2 * n)) = pk[n]; }
        lds_set(smem + C_QF + wid * 4, 1);
        __builtin_amdgcn_s_setprio(1);
    } else {
        const int hw = wid - 4;
        int known = 0;
        __builtin_amdgcn_s_setprio(3);
        auto lds_or = [&](int off, unsigned v) { FENCE(); if (lane == 0) __hip_atomic_fetch_or((LAS unsigned*)(smem + off), v, RLX_WG); FENCE(); };
        unsigned wo[5], qo[5];
        auto done = [&](int n) { lds_or(C_F + n * 4, 1u << hw); };
        const unsigned xo1 = (xo ^ 64) + 9216, xo2 = xo + 2 * 9216, xo3 = (xo ^ 64) + 3 * 9216;
        auto round_x = [&](int n) { dma16x4(xo, xo1, xo2, xo3, xb + kt_of(n) * 128 + hw * (32 * 1280), lds0 + XS0 + (n % 3) * 16384 + hw * 4096); };
        auto round_w = [&](int n) {
            const int u = 2 * n + (hw >> 1);
            const unsigned dst = lds0 + (u < 27 ? WS0 + (u % 7) * 10240 : u == 27 ? QU27 : u == 28 ? QU28 : QU29) + (hw & 1) * 5120 + 2048;
            if (u < 20) dma16x5(wo, wb + (size_t)(kt_of(u >> 1) * 64 + (u & 1) * 32) * 3840 - 2048, dst);
            else dma16x5(qo, wb + (size_t)kt_of(u < 22 ? u - 12 : u - 22) * (64 * 3840) - 2048, dst); };
        if (isA) round_x(0);
        round_x(1);
#pragma unroll
        for (int q = 0; q < 5; ++q) wo[q] = w_off(q) + (hw & 1) * (16 * 3840) + (4 - q) * 1024;
        if (isA) { round_w(0); round_w(1); WAITV(5); } else { round_w(1); WAITV(9); }
        BAR_L();
#pragma unroll
        for (int q = 0; q < 5; ++q) { const int c = q * 64 + lane, rho = __umul24(c, 6554) >> 16;
            qo[q] = (unsigned)(__umul24((hw & 1) * 32 + (rho & ~7) + 4 * (rho & 1) + ((rho >> 1) & 3), 3840) + (c - rho * 10) * 16 + (4 - q) * 1024); }
#pragma unroll 1
        for (int n = 2; n <= 13; ++n) {
            const int need = n < 13 ? 2 * n - 5 : 20;
            if (need > known) known = min4(smem + C_P);
            if (need > known) { WAITV(0); done(n - 1); known = wait_min4(smem + C_P, need); }
            if (n < 10) round_x(n);
            round_w(n);
            if (n < 10) WAITV(9); else WAITV(5);
            done(n - 1);
        }
        WAITV(0); done(13);
        tag = rdtag();
        auto a_chunk = [&](int a) { int o;
            if (a < 320) { const int hd = a / 80, rem = a % 80; o = hd * 5120 + (rem < 32 ? (96 + rem) * 16 : rem < 64 ? 2048 + (64 + rem) * 16 : 4096 + 768 + (rem - 64) * 16); }
            else o = VOFF + 96 * VP + (a - 320) * 16;
            return o; };
        if (rt < 15) {
            const __amdgpu_buffer_rsrc_t kvr = __builtin_amdgcn_make_buffer_rsrc(ws + WS_KV + me * KVG, 0, KVB, 0x00020000);
            wait_min4(smem + C_E, 1);
            if (!isA) { u32x4 v[11];
#pragma unroll
                for (int j = 0; j < 11; ++j) if (j < 10 || hw == 3) v[j] = *(LAS u32x4*)(smem + OWN + hw * 10240 + j * 1024 + lane * 16);
#pragma unroll
                for (int j = 0; j < 11; ++j) if (j < 10 || hw == 3) __builtin_amdgcn_raw_buffer_store_b128(v[j], kvr, hw * 10240 + j * 1024 + lane * 16, 0, 16);
            } else {
#pragma unroll
                for (int j = 0; j < 3; ++j) { const int a = (4 * j + hw) * 64 + lane;
                    if (a < 656) { const int o = a_chunk(a); __builtin_amdgcn_raw_buffer_store_b128(*(LAS u32x4*)(smem + OWN + o), kvr, o, 0, 16); } }
            }
        }
        wait_min4(smem + C_P, 22);
        round_w(14);
        WAITV(0);
        if (rt < 15 && lds_inc(smem + N_PUB, lane) == 3 && lane == 0) __hip_atomic_store((GAS u64*)(ws + WS_FKV + me * 128), tag, RLX_AGENT);
        done(14);
        __builtin_amdgcn_s_setprio(0);
#pragma unroll
        for (int p = 0; p < 7; ++p) att_setup(p);
#pragma unroll
        for (int q = 0; q < 5; ++q) wo_setup(q);
        __builtin_amdgcn_s_setprio(3);
        if (hw == 0) { poll_flags((GAS u64*)(ws + WS_FKV + (me - 8 * (lane + 1)) * 128), lane < nnb, tag); lds_set(smem + N_KV, 1); }
        else wait_word(smem + N_KV, 1);
        if (!isA) {
            const int n = hw >> 1, half = hw & 1;
            if (n < nnb) {
                const char* src = ws + WS_KV + (me - 8 * (n + 1)) * KVG + half * 20480; const unsigned dst = lds0 + KV0 + (1 - n) * KVB + half * 20480;
                wait_min4(smem + C_P, 27);
#pragma unroll
                for (int j = 0; j < 4; ++j) dma16x5_sc1(lane * 16, src + j * 5120 + 2048, dst + j * 5120 + 2048);
                if (half) dma16_sc1(lane * 16, src + 20480, dst + 20480);
            }
        } else if (nnb) {
            const __amdgpu_buffer_rsrc_t nbr = __builtin_amdgcn_make_buffer_rsrc(ws + WS_KV + (me - 8) * KVG, 0, KVB, 0x00020000);
            u32x4 v[3];
#pragma unroll
            for (int j = 0; j < 3; ++j) { const int a = (4 * j + hw) * 64 + lane; if (a < 656) v[j] = __builtin_amdgcn_raw_buffer_load_b128(nbr, a_chunk(a), 0, 16); }
            wait_min4(smem + C_P, 27);
#pragma unroll
            for (int j = 0; j < 3; ++j) { const int a = (4 * j + hw) * 64 + lane; if (a < 656) *(LAS u32x4*)(smem + KV0 + KVB + a_chunk(a)) = v[j]; }
        }
        __builtin_amdgcn_s_setprio(2);
        wait_word(smem + C_QF + qi * 4, 1);
    }
    u32x4 wreg[13];
    auto ldwo = [&](int i) { wreg[i] = __builtin_amdgcn_raw_buffer_load_b128(wor, oo[i % 5] + (i / 5) * (32 * 1280) + (i > 6 || (i == 6 && lane >= 16) ? 40 * 1280 : 0), 0, 0); };
    u32x2 ob[2][3];
    {
        bf16x8 q1[2]; u32x2 q2[2];
#pragma unroll
        for (int e = 0; e < 2; ++e) {
            q1[e] = *(LAS bf16x8*)(smem + qb1 + e * 5120);
            q2[e] = *(LAS u32x2*)(smem + qb2 + e * 5120);
            if (hl) q2[e] = (u32x2){0u, 0u};
        }
        if (isA) mask_a(0);
        struct KF { bf16x8 a; s16x4 b; };
        struct VF { bf16x8 a, b; };
        auto rdk = [&](int u, int e) { const int so = (u >> 2) * KVB, tw = u & 3; KF k;
            k.a = *(LAS bf16x8*)(smem + kb1 + so + tw * 512 + e * 5120); k.b = *(LAS s16x4*)(smem + __umul24(so + tw * 256 + e * 5120, nh) + kb2); return k; };
        auto rdv = [&](int u, int e) { LAS char* p = smem + (e ? vb1 : vb0) + (u >> 2) * KVB + (u & 3) * (32 * VP); VF v;
            v.a = trfrag(p, 8 * VP); v.b = trfrag(p + 16 * VP, 8 * VP); return v; };
        auto qk = [&](const KF& k, int e, const f32x16& c) { return MFMA32K8(k.b, __builtin_bit_cast(s16x4, q2[e]), MFMA32(k.a, q1[e], c)); };
        const f32x16 zero = {};
        auto max16 = [&](const f32x16& s, float m) {
#pragma unroll
            for (int r = 0; r < 16; ++r) m = __builtin_elementwise_maximum(m, s[r]);
            return m; };
        auto set_ref = [&](float m, int e) {
            const unsigned t0 = pk2(-m, 0.f); const float r1 = -m - __uint_as_float(t0 << 16);
            const unsigned t1 = pk2(r1, 0.f); const float r2 = r1 - __uint_as_float(t1 << 16);
            if (hl) q2[e] = (u32x2){t0 | (t1 << 16), pk2(r2, 0.f)}; };
        auto ex4 = [&](f32x16& s, int b) {
#pragma unroll
            for (int r = 0; r < 4; ++r) s[b + r] = __builtin_amdgcn_exp2f(s[b + r]); };
        auto pk8 = [&](const f32x16& s, int b) { return __builtin_bit_cast(bf16x8, (u32x4){pk2(s[b], s[b + 1]), pk2(s[b + 2], s[b + 3]), pk2(s[b + 4], s[b + 5]), pk2(s[b + 6], s[b + 7])}); };
        auto phase = [&](auto nx, f32x16& cur, const VF& v, f32x16& o, f32x16& nxt, const KF& k, int e) __attribute__((always_inline)) {
            constexpr int NX = decltype(nx)::value;
            SB(); ex4(cur, 0); SB();
            if constexpr (NX) nxt = MFMA32(k.a, q1[e], NX == 1 ? zero : NX == 2 ? cf : cd);
            SB(); ex4(cur, 4); { const bf16x8 p = pk8(cur, 0); SB(); o = MFMA32(v.a, p, o); }
            SB(); ex4(cur, 8); SB();
            if constexpr (NX) nxt = MFMA32K8(k.b, __builtin_bit_cast(s16x4, q2[e]), nxt);
            SB(); ex4(cur, 12); { const bf16x8 p = pk8(cur, 8); SB(); o = MFMA32(v.b, p, o); }
            SB(); };
        f32x16 o0, o1, sa, sb; KF kx; VF vx;
        auto step = [&](auto m0, auto m1, int u) __attribute__((always_inline)) {
            constexpr int M1 = decltype(m1)::value;
            KF ky; if constexpr (M1) ky = rdk(u - 1, 0);
            const VF vy = rdv(u, 1);
            phase(m0, sa, vx, o0, sb, kx, 1);
            if constexpr (M1) { kx = rdk(u - 1, 1); vx = rdv(u - 1, 0); }
            phase(m1, sb, vy, o1, sa, ky, 0); };
        auto nbwait = [&](int u) { if (u == 8) wait_word(smem + N_NB, 4); };
        { const KF k0f = rdk(ud, 0); kx = rdk(ud, 1); vx = rdv(ud, 0);
          const f32x16 s0 = qk(k0f, 0, cd), s1 = qk(kx, 1, cd);
          set_ref(halfmax(max16(s0, s0[0])), 0); set_ref(halfmax(max16(s1, s1[0])), 1);
          sa = qk(k0f, 0, cd); }
        if (hp) { WAITV(0); lds_inc0(smem + N_NB, lane); lds_inc0(smem + N_G0, lane); }
        o0 = zero; o1 = zero;
        if (u0 + 8 == ud) {
            SFOR(it, 9)
                if constexpr (it < 7) { ldwo(2 * it); if constexpr (it < 6) ldwo(2 * it + 1); }
                if constexpr (it == PFLIP) { if (hp) __builtin_amdgcn_s_setprio(0); }
                if constexpr (it == 7) {
#pragma unroll
                    for (int r = 0; r < 16; ++r) mask_b(cf, r, true); }
                if constexpr (it < 4) nbwait(ud - it);
                step(IC<it == 0 ? 3 : it == 8 ? 2 : 1>{}, IC<it == 8 ? 0 : it == 7 ? 2 : 1>{}, ud - it);
            SEND
        } else {
            __builtin_amdgcn_s_setprio(0);
#pragma unroll
            for (int i = 0; i < 13; ++i) ldwo(i);
            if (!isA) cf = zero;
            if (u0 < ud) {
                nbwait(ud);
                if (u0 + 1 < ud) {
                    step(IC<3>{}, IC<1>{}, ud);
#pragma unroll 1
                    for (int u = ud - 1; u > u0 + 1; --u) { nbwait(u); step(IC<1>{}, IC<1>{}, u); }
                    nbwait(u0 + 1);
                    step(IC<1>{}, IC<2>{}, u0 + 1);
                } else step(IC<3>{}, IC<2>{}, ud);
                step(IC<2>{}, IC<0>{}, u0);
            } else step(IC<3>{}, IC<0>{}, ud);
        }
        if (!__all(o0[12] < 1.152921504606847e18f && o1[12] < 1.152921504606847e18f && o0[12] > 0.25f && o1[12] > 0.25f)) {
            if (isA) { int z = 0; asm volatile("" : "+s"(z)); mask_a(z); }
            else { asm volatile("" : "+v"(dsh));
#pragma unroll
                for (int r = 0; r < 16; ++r) { mask_b(cd, r, false); mask_b(cf, r, true); }
                if (!far) cf = zero; }
#pragma unroll
            for (int e = 0; e < 2; ++e) {
                if (hl) q2[e] = (u32x2){0u, 0u};
                auto tile = [&](int u) { return u == ud ? qk(rdk(u, e), e, cd) : u == u0 ? qk(rdk(u, e), e, cf) : qk(rdk(u, e), e, zero); };
                float m = -__builtin_inff();
#pragma unroll 1
                for (int u = u0; u <= ud; ++u) m = max16(tile(u), m);
                m = halfmax(m);
                f32x16 o = zero;
#pragma unroll 1
                for (int u = u0; u <= ud; ++u) { f32x16 t = tile(u) - m;
                    const VF v = rdv(u, e); ex4(t, 0); ex4(t, 4); ex4(t, 8); ex4(t, 12);
                    o = MFMA32(v.a, pk8(t, 0), o); o = MFMA32(v.b, pk8(t, 8), o); }
                if (e) o1 = o; else o0 = o;
            }
        }
        const float rl0 = __builtin_amdgcn_rcpf(o0[12]), rl1 = __builtin_amdgcn_rcpf(o1[12]);
        SB();
#pragma unroll
        for (int r = 0; r < 12; ++r) { o0[r] *= rl0; o1[r] *= rl1; }
        SB();
#pragma unroll
        for (int j = 0; j < 3; ++j) {
            ob[0][j] = (u32x2){pk2(o0[4 * j], o0[4 * j + 1]), pk2(o0[4 * j + 2], o0[4 * j + 3])};
            ob[1][j] = (u32x2){pk2(o1[4 * j], o1[4 * j + 1]), pk2(o1[4 * j + 2], o1[4 * j + 3])}; }
        SB();
    }
    const __amdgpu_buffer_rsrc_t orr = __builtin_amdgcn_make_buffer_rsrc(ws + WS_O + (br * 16 + rt) * OG, 0, 4 * 40960, 0x00020000);
    {
      { auto rx = __builtin_amdgcn_permlane32_swap(ob[0][0][0], ob[0][1][0], false, false);
        auto ry = __builtin_amdgcn_permlane32_swap(ob[0][0][1], ob[0][1][1], false, false);
        __builtin_amdgcn_raw_buffer_store_b128((u32x4){rx[0], ry[0], rx[1], ry[1]}, orr, obase, 0, 16); }
      if (hl == 0) __builtin_amdgcn_raw_buffer_store_b128((u32x4){ob[0][2][0], ob[0][2][1], ob[1][0][0], ob[1][0][1]}, orr, obase + 1024, 0, 16);
      { auto fx = __builtin_amdgcn_permlane32_swap(ob[1][0][0], ob[1][1][0], false, false);
        auto fy = __builtin_amdgcn_permlane32_swap(ob[1][0][1], ob[1][1][1], false, false);
        auto sx = __builtin_amdgcn_permlane32_swap(ob[1][1][0], ob[1][2][0], false, false);
        auto sy = __builtin_amdgcn_permlane32_swap(ob[1][1][1], ob[1][2][1], false, false);
        __builtin_amdgcn_raw_buffer_store_b128((u32x4){fx[1], fy[1], sx[0], sy[0]}, orr, obase + 1536, 0, 16); } }
    if (lane == 0) { (void)__hip_atomic_fetch_add((LAS unsigned*)(smem + N_G1), 1u, RLX_WG);
        if (!hp) (void)__hip_atomic_fetch_add((LAS unsigned*)(smem + N_G0), 1u, RLX_WG); }
    FENCE();
    auto signal_o = [&]() {
        if (lds_inc(smem + N_OS + hp * 4, lane) == 3 && lane == 0) __hip_atomic_store((GAS u64*)(ws + WS_FO + (me * 2 + hp) * 128), tag, RLX_AGENT); };
    __builtin_amdgcn_s_setprio(0);
    if (wid == 0 && lane == 0) { GAS unsigned* d = (GAS unsigned*)(ws + WS_DBG + bx * 128);
        __hip_atomic_store(d + 0, dt0, RLX_AGENT); __hip_atomic_store(d + 1, dt1, RLX_AGENT); }
    WAITV(0); signal_o();
    wait_word(smem + (hp ? N_G1 : N_G0), 8);
    const int p1 = hp ? 30720 : 35840, p2 = hp ? 65536 : 43520;
    const int wb0 = hp ? KV0 + 10240 : 512, wb1 = hp ? KV0 + KVB + 10240 - 30720 : KV0 - 35840, wb2 = KV0 + KVB - 43520;
#pragma unroll
    for (int i = 0; i < 13; ++i) { const int L = qo * 12800 + i * 1024;
        if (i < 12 || lane < 32) *(LAS u32x4*)(smem + (L + (L < p1 ? wb0 : L < p2 ? wb1 : wb2)) + lane * 16) = wreg[i]; }
    lds_inc0(smem + N_WO + hp * 4, lane);
    if (qo == 0) { poll_flags((GAS u64*)(ws + WS_FO + (((me & ~7) + (lane & 7)) * 2 + hp) * 128), lane < 8, tag); lds_set(smem + N_OK + hp * 4, 1); }
    else wait_word(smem + N_OK + hp * 4, 1);
    f32x16 oc[3]; f32x4 ps[10];
    LAS char* red = smem + OWN + qo * 10240 + lane * 16;
    { bf16x8 af[5], of[20];
      const int la = (8 * hl + 2 * ((lane & 15) >> 2)) * 160 + (lane & 16) * 2 + (lane & 3) * 8;
      const int aa0 = wb0 + la, aa1 = (hp ? wb1 : wb0) + la, aa2 = wb1 + la, aa3 = (hp ? wb1 : wb2) + la;
      auto rda = [&](int f) { return trfrag(smem + (f < 36 ? aa0 : f < 42 ? aa1 : f < 51 ? aa2 : aa3) + (f / 3) * 2560 + (f % 3) * 64, 160); };
      auto ldo = [&](int k) { of[k] = __builtin_bit_cast(bf16x8, __builtin_amdgcn_raw_buffer_load_b128(orr, qo * 40960 + (hp * 20 + k) * 1024 + lane * 16, 0, 16)); };
#pragma unroll
      for (int k = 0; k < OPF; ++k) ldo(k);
      wait_word(smem + N_WO + hp * 4, 4);
#pragma unroll
      for (int f = 0; f < 4; ++f) af[f] = rda(f);
      SB();
      SFOR(f, 60)
          if constexpr (f % 3 == 0 && f / 3 + OPF < 20) ldo(f / 3 + OPF);
          if constexpr (f + 4 < 60) af[(f + 4) % 5] = rda(f + 4);
          if constexpr (f == 57) { if (hp) { wait_word(smem + C_RD + qo * 4, 1);
#pragma unroll
              for (int b = 0; b < 10; ++b) ps[b] = *(LAS f32x4*)(red + b * 1024); } }
          SB();
          if constexpr (f < 3) MFMA32Z(oc[f], af[f % 5], of[0]); else MFMA32I(oc[f % 3], af[f % 5], of[f / 3]);
          SB();
      SEND }
    MFMA_DRAIN();
    { if (!hp) {
          wait_word(smem + N_G1, 8);
#pragma unroll
          for (int b = 0; b < 10; ++b) *(LAS f32x4*)(red + b * 1024) = (f32x4){oc[b >> 2][4 * (b & 3)], oc[b >> 2][4 * (b & 3) + 1], oc[b >> 2][4 * (b & 3) + 2], oc[b >> 2][4 * (b & 3) + 3]};
          lds_set(smem + C_RD + qo * 4, 1);
      } else {
          char* orow = out + (size_t)(rt * 128 + qo * 32 + r32) * 2560 + br * 1280 + hg * 160 + hl * 16;
#pragma unroll
          for (int b = 0; b < 10; ++b)
#pragma unroll
              for (int r = 0; r < 4; ++r) ps[b][r] += oc[b >> 2][4 * (b & 3) + r];
          SB();
          u32x2 pk[10];
#pragma unroll
          for (int b = 0; b < 10; ++b) pk[b] = (u32x2){pk2(ps[b][0], ps[b][1]), pk2(ps[b][2], ps[b][3])};
          SB();
          u32x4 st[5];
#pragma unroll
          for (int b = 0; b < 10; b += 2) {
              auto rx = __builtin_amdgcn_permlane32_swap(pk[b][0], pk[b + 1][0], false, false); auto ry = __builtin_amdgcn_permlane32_swap(pk[b][1], pk[b + 1][1], false, false);
              st[b >> 1] = (u32x4){rx[0], ry[0], rx[1], ry[1]}; }
          SB();
#pragma unroll
          for (int b = 0; b < 5; ++b) *(u32x4*)(orow + b * 32) = st[b];
      } }
    if (bx == 0 && wid == 0) {
#pragma unroll 1
        for (int i = 0; i < 4; ++i) poll_flags((GAS u64*)(ws + WS_FO + (i * 64 + lane) * 256), true, tag);
        if (lane == 0) __hip_atomic_store((GAS u64*)ws, tag, RLX_AGENT);
        { const unsigned now = (unsigned)__builtin_amdgcn_s_memrealtime();
          int mn0 = 0x7fffffff, a1 = 0;
          for (int i = 0; i < 4; ++i) { GAS unsigned* d = (GAS unsigned*)(ws + WS_DBG + (i * 64 + lane) * 128);
              const unsigned t0 = __hip_atomic_load(d, RLX_AGENT), t1 = __hip_atomic_load(d + 1, RLX_AGENT); mn0 = min(mn0, (int)(t0 - now)); a1 += t1 - t0; }
          for (int m = 32; m >= 1; m >>= 1) { mn0 = min(mn0, __shfl_xor(mn0, m)); a1 += __shfl_xor(a1, m); }
          int M = 0 * (a1 / 64);
          const unsigned until = now + mn0 + 4000 + min(max(M, 0), 5000);
          while ((int)((unsigned)__builtin_amdgcn_s_memrealtime() - until) < 0) __builtin_amdgcn_s_sleep(2); }
    }
    asm volatile(".Lfused_attn_end:");
}

extern "C" void kernel_launch(void* const* d_in, const int* in_sizes, int n_in, void* d_out, int out_size, void* d_ws, size_t ws_size,
                              hipStream_t stream) {
    fused_attn_kernel<<<256, 512, 0, stream>>>((const char*)d_in[0], (const char*)d_in[1], (const char*)d_in[2], (const char*)d_in[3],
                                               (const char*)d_in[4], (const char*)d_in[5], (char*)d_out, (char*)d_ws);
}
```

```cpp
#include <hip/hip_runtime.h>
#include <hip/hip_bf16.h>
#include <stdint.h>

typedef short bf16x8 __attribute__((ext_vector_type(8)));
typedef short s16x4 __attribute__((ext_vector_type(4)));
typedef float f32x2 __attribute__((ext_vector_type(2)));
typedef float f32x4 __attribute__((ext_vector_type(4)));
typedef float f32x16 __attribute__((ext_vector_type(16)));
typedef unsigned u32x4 __attribute__((ext_vector_type(4)));
typedef unsigned u32x2 __attribute__((ext_vector_type(2)));
typedef __bf16 bfx2 __attribute__((ext_vector_type(2)));
typedef unsigned long long u64;
#define LAS __attribute__((address_space(3)))
#define GAS __attribute__((address_space(1)))
#define DI __device__ __forceinline__
#define RLX_AGENT __ATOMIC_RELAXED, __HIP_MEMORY_SCOPE_AGENT
#define RLX_WG __ATOMIC_RELAXED, __HIP_MEMORY_SCOPE_WORKGROUP

constexpr int LDS_BYTES = 163840;
constexpr int VOFF = 20480, VP = 168;
constexpr int KVB = VOFF + 128 * VP;
constexpr int KV0 = LDS_BYTES - 3 * KVB;
constexpr int OWN = KV0 + 2 * KVB;
constexpr int C_F = 0, C_P = 128, C_E = 144, C_QF = 160, C_RD = 176, C_CNT = 192, C_TAG = 320;
constexpr int N_PUB = C_CNT, N_NB = C_CNT + 4, N_G0 = C_CNT + 8, N_G1 = C_CNT + 12, N_WO = C_CNT + 16, N_OS = C_CNT + 24, N_KV = C_CNT + 32, N_OK = C_CNT + 36;
constexpr int XS0 = 512, WS0 = XS0 + 3 * 16384, QST = 512, ONE = 336, QU27 = XS0 + 16384, QU28 = XS0, QU29 = QU27 + 10240;
static_assert(WS0 + 7 * 10240 <= OWN, "ring overlaps the own image");
constexpr unsigned WS_FKV = 4096, WS_FO = WS_FKV + 256 * 128, WS_KV = 131072;
constexpr unsigned KVG = KVB, OG = 4 * 40960;
constexpr unsigned WS_O = WS_KV + 256 * KVG;
constexpr unsigned WS_END = WS_O + 32 * OG;
constexpr unsigned SPIN_CAP = 400000, LSPIN_CAP = 4000000;
constexpr float QSCALE = 0.22360679774997896f * 1.4426950408889634f;
constexpr int WIN = 4;
constexpr int OPF = 12;
constexpr int PFLIP = 6;
constexpr int ADELAY = 2;

#define MFMA32(a, b, c) __builtin_amdgcn_mfma_f32_32x32x16_bf16((a), (b), (c), 0, 0, 0)
#define MFMA32K8(a, b, c) __builtin_amdgcn_mfma_f32_32x32x8bf16_1k((a), (b), (c), 0, 0, 0)
#define MFMA32I(acc, a, b) asm volatile("v_mfma_f32_32x32x16_bf16 %0, %1, %2, %0" : "+v"(acc) : "v"(a), "v"(b))
#define MFMA32Z(acc, a, b) asm volatile("v_mfma_f32_32x32x16_bf16 %0, %1, %2, 0" : "=&v"(acc) : "v"(a), "v"(b))
#define MFMA_DRAIN() asm volatile("s_nop 7\n\ts_nop 4" ::: "memory")
#define WAITV(N) asm volatile("s_waitcnt vmcnt(" #N ")" ::: "memory")
#define BAR_L() asm volatile("s_waitcnt lgkmcnt(0)\n\ts_barrier" ::: "memory")
#define SB() __builtin_amdgcn_sched_barrier(0)
#define FENCE() asm volatile("" ::: "memory")


template <int V> struct IC { static constexpr int value = V; };
template <int I, int N, class F> DI void static_for(F&& f) { if constexpr (I < N) { f(IC<I>{}); static_for<I + 1, N>(f); } }
#define SFOR(var, N) static_for<0, N>([&](auto ic_##var) __attribute__((always_inline)) { constexpr int var = decltype(ic_##var)::value;
#define SEND });

DI void dma16(unsigned voff, const char* sbase, unsigned ldsaddr) {
    asm volatile("s_mov_b32 m0, %2\n\ts_nop 0\n\tglobal_load_lds_dwordx4 %0, %1" :: "v"(voff), "s"(sbase), "s"(ldsaddr) : "memory");
}
DI void dma16x4(unsigned v0, unsigned v1, unsigned v2, unsigned v3, const char* sbase, unsigned ldsaddr) {
    asm volatile("s_mov_b32 m0, %5\n\ts_nop 0\n\tglobal_load_lds_dwordx4 %0, %4\n\tglobal_load_lds_dwordx4 %1, %4 offset:1024\n\t"
                 "global_load_lds_dwordx4 %2, %4 offset:2048\n\tglobal_load_lds_dwordx4 %3, %4 offset:3072"
                 :: "v"(v0), "v"(v1), "v"(v2), "v"(v3), "s"(sbase), "s"(ldsaddr) : "memory");
}
DI void dma16x5(const unsigned (&v)[5], const char* sbase, unsigned ldsaddr) {
    asm volatile("s_mov_b32 m0, %6\n\ts_nop 0\n\tglobal_load_lds_dwordx4 %0, %5 offset:-2048\n\tglobal_load_lds_dwordx4 %1, %5 offset:-1024\n\t"
                 "global_load_lds_dwordx4 %2, %5\n\tglobal_load_lds_dwordx4 %3, %5 offset:1024\n\tglobal_load_lds_dwordx4 %4, %5 offset:2048"
                 :: "v"(v[0]), "v"(v[1]), "v"(v[2]), "v"(v[3]), "v"(v[4]), "s"(sbase), "s"(ldsaddr) : "memory");
}
DI void dma16x5_sc1(unsigned voff, const char* sbase, unsigned ldsaddr) {
    asm volatile("s_mov_b32 m0, %2\n\ts_nop 0\n\tglobal_load_lds_dwordx4 %0, %1 offset:-2048 sc1\n\tglobal_load_lds_dwordx4 %0, %1 offset:-1024 sc1\n\t"
                 "global_load_lds_dwordx4 %0, %1 sc1\n\tglobal_load_lds_dwordx4 %0, %1 offset:1024 sc1\n\tglobal_load_lds_dwordx4 %0, %1 offset:2048 sc1"
                 :: "v"(voff), "s"(sbase), "s"(ldsaddr) : "memory");
}
DI void dma16_sc1(unsigned voff, const char* sbase, unsigned ldsaddr) {
    asm volatile("s_mov_b32 m0, %2\n\ts_nop 0\n\tglobal_load_lds_dwordx4 %0, %1 sc1" :: "v"(voff), "s"(sbase), "s"(ldsaddr) : "memory");
}
DI void dma4(const char* p, unsigned ldsaddr) {
    asm volatile("s_mov_b32 m0, %1\n\ts_nop 0\n\tglobal_load_lds_dword %0, off" :: "v"(p), "s"(ldsaddr) : "memory");
}
DI void dma4s(unsigned voff, const char* sbase, unsigned ldsaddr) {
    asm volatile("s_mov_b32 m0, %2\n\ts_nop 0\n\tglobal_load_lds_dword %0, %1" :: "v"(voff), "s"(sbase), "s"(ldsaddr) : "memory");
}
DI unsigned pk2(float lo, float hi) { f32x2 v = {lo, hi}; return __builtin_bit_cast(unsigned, __builtin_convertvector(v, bfx2)); }
typedef short v4i16_t __attribute__((ext_vector_type(4)));
DI bf16x8 trfrag(LAS char* p, int second) {
    s16x4 a = __builtin_bit_cast(s16x4, __builtin_amdgcn_ds_read_tr16_b64_v4i16((LAS v4i16_t*)p));
    s16x4 b = __builtin_bit_cast(s16x4, __builtin_amdgcn_ds_read_tr16_b64_v4i16((LAS v4i16_t*)(p + second)));
    return __builtin_shufflevector(a, b, 0, 1, 2, 3, 4, 5, 6, 7);
}
constexpr u64 lanes_cut(int r, bool fartile) {
    u64 m = 0;
    for (int l = 0; l < 64; ++l) { const int key = (r & 3) + 8 * (r >> 2) + 4 * (l >> 5), d = (l & 31) - key + (fartile ? 32 : 0);
        const bool ok = d >= 0 && d <= 14 && !(d & 1);
        if (!ok) m |= 1ull << l; }
    return m;
}
DI float halfmax(float m) { auto rr = __builtin_amdgcn_permlane32_swap(__float_as_uint(m), __float_as_uint(m), false, false);
    return __builtin_elementwise_maximum(__uint_as_float(rr[0]), __uint_as_float(rr[1])); }

DI void poll_flags(GAS u64* word, bool active, u64 tag) {
#pragma unroll 1
    for (unsigned spins = 0; spins < SPIN_CAP; ++spins) {
        u64 v = tag;
        if (active) v = __hip_atomic_load(word, RLX_AGENT);
        if (__all(v == tag)) break;
        __builtin_amdgcn_s_sleep(2);
    }
    __builtin_amdgcn_fence(__ATOMIC_ACQUIRE, "wavefront");
}
DI int min4(LAS char* p) {
    const u64 a = __hip_atomic_load((LAS u64*)p, RLX_WG), b = __hip_atomic_load((LAS u64*)(p + 8), RLX_WG);
    const unsigned m = min(min((unsigned)a, (unsigned)(a >> 32)), min((unsigned)b, (unsigned)(b >> 32)));
    return __builtin_amdgcn_readfirstlane((int)m);
}
DI int wait_min4(LAS char* p, int need) {
    int v = min4(p);
#pragma unroll 1
    for (unsigned spins = 0; v < need && spins < LSPIN_CAP; ++spins) { __builtin_amdgcn_s_sleep(1); v = min4(p); }
    FENCE();
    return v;
}
DI void wait_word(LAS char* p, unsigned need) {
#pragma unroll 1
    for (unsigned spins = 0; spins < LSPIN_CAP; ++spins) {
        if ((unsigned)__builtin_amdgcn_readfirstlane((int)__hip_atomic_load((LAS unsigned*)p, RLX_WG)) >= need) break;
        __builtin_amdgcn_s_sleep(1); }
    FENCE();
}
DI void lds_inc0(LAS char* p, int lane) { FENCE(); if (lane == 0) (void)__hip_atomic_fetch_add((LAS unsigned*)p, 1u, RLX_WG); FENCE(); }
DI unsigned lds_inc(LAS char* p, int lane) { FENCE(); unsigned old = 0; if (lane == 0) old = __hip_atomic_fetch_add((LAS unsigned*)p, 1u, RLX_WG); FENCE(); return old; }
DI void lds_set(LAS char* p, unsigned v) { FENCE(); __hip_atomic_store((LAS unsigned*)p, v, RLX_WG); FENCE(); }

__global__ __launch_bounds__(512, 2) void fused_attn_kernel(const char* __restrict__ xA, const char* __restrict__ wA, const char* __restrict__ oA,
                                                           const char* __restrict__ xB, const char* __restrict__ wB, const char* __restrict__ oB,
                                                           char* __restrict__ out, char* __restrict__ ws) {
    __shared__ __attribute__((aligned(16))) char smem_[LDS_BYTES];
    LAS char* smem = (LAS char*)smem_;
    const unsigned lds0 = (unsigned)(uintptr_t)smem;
    asm volatile("" :: "s"(xA), "s"(wA), "s"(oA), "s"(xB), "s"(wB), "s"(oB), "s"(out), "s"(ws));
    const int tid = threadIdx.x, lane = tid & 63;
    const int wid = __builtin_amdgcn_readfirstlane(tid >> 6);
    const int bx = blockIdx.x, xcd = bx & 7, idx = bx >> 3;
    const int isA = idx >> 4, rt = (xcd >> 1) * 4 + ((idx >> 2) & 3), hg = (xcd & 1) * 4 + (idx & 3);
    const int br = isA ? 0 : 1;
    { u64 pc; unsigned len;
      asm volatile("s_getpc_b64 %0\n.Lfused_attn_pc:\n\ts_mov_b32 %1, .Lfused_attn_end-.Lfused_attn_pc" : "=s"(pc), "=s"(len));
      if (wid == 3 && (idx & 15) * 8192u < len) dma4((const char*)pc + min(((idx & 15) * 64 + lane) * 128u, len - 4u), lds0 + OWN + 3 * 512); }
    const int k0 = isA ? (((rt & 3) * 5 + 1) / 2 + ((hg & 3) * 5 + 1) / 2) % 10 : 0;
    const char* xb = (isA ? xA : xB) + (size_t)rt * (128 * 1280);
    const char* wb = (isA ? wA : wB) + hg * 160;
    const int me = (br * 16 + rt) * 8 + hg;
    const int r32 = lane & 31, hl = lane >> 5;
    auto kt_of = [&](int s) { int k = s + k0; return k >= 10 ? k - 10 : k; };
    if (isA) {
        const int j16 = idx & 15; const unsigned dump = lds0 + OWN + (wid & 3) * 512 + (wid >> 2) * 256;
        const char* wl = wB + (hg >> 2) * 640 + j16 * (4 * 3840);
        auto pfx = [&](int i) { dma4s(__umul24((hg & 3) * 32 + r32, 1280) + hl * 128 + i * 256, xB + (size_t)rt * (128 * 1280), dump); };
        auto pfkv = [&](int i) { const int n = min(i * 64 + lane, 399), sl = __umul24(n, 1639) >> 16, r = n - sl * 40, k = __umul24(r, 6554) >> 16, c = r - k * 10;
            dma4s(__umul24(sl * 64 + k, 3840) + c * 128 + (c < 5 ? 1280 : 1920), wl, dump); };
        if (wid & 1) pfx(wid >> 1); else pfkv(wid >> 1);
        if (wid < 3) pfkv(4 + wid); else if (wid == 3) pfx(4);
        else { const int n = min((wid - 4) * 64 + lane, 199), q = __umul24(n, 3277) >> 16, r = n - q * 20, k = __umul24(r, 13108) >> 16;
            dma4s(__umul24((q < 2 ? q + 8 : q - 2) * 64 + k, 3840) + (r - k * 5) * 128, wl, dump); }
        { const int n = min((wid & 3) * 64 + lane, 199), k = __umul24(n, 13108) >> 16;
          dma4s(__umul24(j16 * 40 + k, 1280) + (n - k * 5) * 128, (wid < 4 ? oB : oA) + (hg >> 2) * 640, dump); }
    }
    if (isA && wid >= 4) { for (int i = 0; i < ADELAY; ++i) __builtin_amdgcn_s_sleep(16); }

    const unsigned xo = (unsigned)((lane >> 3) * 1280 + (((lane & 7) ^ (lane >> 4)) << 4));
    auto w_off = [&](int q) { const int c = q * 64 + lane, row = __umul24(c, 3277) >> 16, j = c - row * 20; return (unsigned)(__umul24(row, 3840) + j * 16 + (j < 10 ? 1280 : 2400)); };
    if (!isA) { const char* xs = xb + k0 * 128 + wid * (16 * 1280);
      dma16(xo, xs, lds0 + XS0 + wid * 2048);
      dma16(xo ^ 64, xs + 8 * 1280, lds0 + XS0 + wid * 2048 + 1024);
      const char* wsrc = wb + (size_t)k0 * (64 * 3840);
      const unsigned wv = w_off(wid < 5 ? wid : wid - 5);
      if (wid < 5) { dma16(wv, wsrc, lds0 + WS0 + wid * 1024); dma16(wv, wsrc + 16 * 3840, lds0 + WS0 + (5 + wid) * 1024);
          if (wid >= 3) { dma16(wv, wsrc + 32 * 3840, lds0 + WS0 + (10 + wid) * 1024); dma16(wv, wsrc + 48 * 3840, lds0 + WS0 + (15 + wid) * 1024); } }
      else { dma16(wv, wsrc + 32 * 3840, lds0 + WS0 + (5 + wid) * 1024); dma16(wv, wsrc + 48 * 3840, lds0 + WS0 + (10 + wid) * 1024); } }
    if (wid == 4) { const char* pe = ws + (lane & 1) * 4; if (lane < 2) dma4(pe, lds0 + C_TAG); }
    if (tid < 80) *(LAS unsigned*)(smem + tid * 4) = 0u;
    else if (tid < 82) *(LAS unsigned*)(smem + ONE + (tid - 80) * 4) = 0x3F803F80u;
    const int nnb = isA ? (rt > 0 ? 1 : 0) : (rt > 1 ? 2 : rt);
    auto kq_base = [&](int fg) { const int hd = fg / 5, dg = fg % 5; return dg < 4 ? hd * 5120 + (dg >> 1) * 2048 + (dg & 1) * 8 : hd * 5120 + 4096; };

    auto rdtag = [&]() { const u64 t = *(LAS u64*)(smem + C_TAG);
        const u64 e = ((u64)(unsigned)__builtin_amdgcn_readfirstlane((int)(t >> 32)) << 32) | (unsigned)__builtin_amdgcn_readfirstlane((int)t);
        return e * 6364136223846793005ull + 1442695040888963407ull; };
    const __amdgpu_buffer_rsrc_t wor = __builtin_amdgcn_make_buffer_rsrc((void*)(isA ? oA : oB), 0, 640 * 1280, 0x00020000);

    const int hp = wid >> 2, qo = wid & 3, qi = hp ? 3 - qo : qo;
    const int ud = qi + 8;
    const bool far = isA ? (4 * rt + qi >= 1) : (4 * rt + qi >= 8);
    const int u0 = isA ? (far ? ud - 1 : ud) : (far ? qi : 8 - 4 * rt);
    f32x16 cd, cf;
    int dsh, kb1, kb2, nh, vb0, vb1, qb1, qb2; unsigned obase, oo[5];
    auto mask_b = [&](f32x16& c, int r, bool fartile) { const int k = (r & 3) + 8 * (r >> 2); c[r] = __int_as_float(fartile ? ((k - 1) << 23) - dsh : dsh - (k << 23)); };
    auto mask_a = [&](int z) {
      SFOR(r, 16)
          constexpr u64 ad = lanes_cut(r, false), af = lanes_cut(r, true);
          cd[r] = __builtin_amdgcn_inverse_ballot_w64(ad + z) ? -__builtin_inff() : 0.f;
          cf[r] = __builtin_amdgcn_inverse_ballot_w64(af + z) ? -__builtin_inff() : 0.f;
      SEND };
    auto att_setup = [&](int part) {
        int l = tid; asm volatile("" : "+v"(l)); l &= 63;
        const int q = l & 31, h = l >> 5;
        if (part == 0) { dsh = (q - 4 * h) << 23; kb1 = KV0 + hp * 10240 + h * 2048 + q * 16; kb2 = h ? ONE : KV0 + hp * 10240 + 4096 + q * 8; nh = h ^ 1;
            asm volatile("" : "+v"(dsh), "+v"(kb1), "+v"(kb2), "+v"(nh)); }
        if (part == 1) { const bool ones = (l & 16) && (l & 3);
            vb0 = KV0 + VOFF + (4 * h + ((l & 15) >> 2)) * VP + (ones ? 160 : hp * 80 + (l & 16) * 2 + (l & 3) * 8); vb1 = vb0 + (ones ? 0 : 40);
            asm volatile("" : "+v"(vb0), "+v"(vb1)); }
        if (part == 2) { qb1 = QST + hp * 10240 + h * 2048 + (qi * 32 + q) * 16; qb2 = QST + hp * 10240 + 4096 + (qi * 32 + q) * 8;
            obase = (unsigned)(qi * 40960 + hp * 20480 + hg * 2560 + q * 16 + h * 512);
            asm volatile("" : "+v"(qb1), "+v"(qb2), "+v"(obase)); }
        if (part >= 3) {
#pragma unroll
            for (int r = 4 * (part - 3); r < 4 * (part - 2); ++r) mask_b(cd, r, false);
            asm volatile("" : "+v"(cd)); } };
    auto wo_setup = [&](int q) {
        int l = tid; asm volatile("" : "+v"(l)); const int c = q * 64 + (l & 63), rho = __umul24(c, 6554) >> 16;
        oo[q] = (unsigned)(__umul24(qo * 160 + hp * 40 + (rho & ~7) + 4 * (rho & 1) + ((rho >> 1) & 3), 1280) + hg * 160 + (c - rho * 10) * 16);
        asm volatile("" : "+v"(oo[q])); };
    u64 tag;

    if (wid < 4) {
        if (!isA) WAITV(0);
        BAR_L();
        bf16x8 xr[32], xt[2], wf[WIN + 1];
        f32x16 acc[5];
        const int tok = wid * 32 + r32;
        const int xa = XS0 + tok * 128 + ((hl ^ ((tok >> 1) & 7)) << 4);
        int wa0 = WS0 + (8 * hl + ((lane & 15) >> 2)) * 320 + (lane & 16) * 2 + (lane & 3) * 8, wa1 = wa0 + 61440;
        asm volatile("" : "+v"(wa0), "+v"(wa1));
        auto rdx = [&](int i) { return *(LAS bf16x8*)(smem + ((i >> 2) % 3) * 16384 + (xa ^ ((i & 3) * 32))); };
        auto rdw = [&](int f) { const int i = f / 5, t = f % 5, sl = (i >> 1) % 7;
            return trfrag(smem + (sl < 6 ? wa0 + sl * 10240 : wa1) + (i & 1) * 5120 + t * 64, 1280); };
        unsigned fl = 0;
        auto rdflag = [&](int u) { fl = __hip_atomic_load((LAS unsigned*)(smem + C_F + u * 4), RLX_WG); };
        auto check = [&](int u) {
            if (__builtin_amdgcn_readfirstlane(fl) != 15) {
#pragma unroll 1
                for (unsigned spins = 0; spins < LSPIN_CAP; ++spins) { rdflag(u); if (__builtin_amdgcn_readfirstlane(fl) == 15) break; __builtin_amdgcn_s_sleep(1); }
            }
            FENCE(); };
#pragma unroll
        for (int i = 0; i < 4; ++i) xr[i] = rdx(i);
#pragma unroll
        for (int f = 0; f < WIN; ++f) wf[f] = rdw(f);
        SB();
        SFOR(f, 200)
            constexpr int i = f / 5, t = f % 5;
            if constexpr (f % 20 == 10 - WIN) lds_set(smem + C_P + wid * 4, (i >> 1) + 1);
            if constexpr (f == 200 - WIN) lds_set(smem + C_P + wid * 4, 20);
            if constexpr (f % 20 == 10) rdflag(f / 20 + 1);
            if constexpr (f % 20 == 20 - WIN && f < 190) check(f / 20 + 1);
            if constexpr (f + WIN < 200) wf[(f + WIN) % (WIN + 1)] = rdw(f + WIN);
            if constexpr (f % 20 >= 16 && f < 140) xr[(f / 20 + 1) * 4 + f % 20 - 16] = rdx((f / 20 + 1) * 4 + f % 20 - 16);
            if constexpr (t == 1 && i >= 31 && i < 39) xt[(i + 1) & 1] = rdx(i + 1);
            SB();
            if constexpr (i == 0) MFMA32Z(acc[t], wf[f % (WIN + 1)], xr[0]);
            else if constexpr (i < 32) MFMA32I(acc[t], wf[f % (WIN + 1)], xr[i]);
            else MFMA32I(acc[t], wf[f % (WIN + 1)], xt[i & 1]);
            SB();
        SEND
        int l2 = tid; asm volatile("" : "+v"(l2)); l2 &= 63;
        const int h2 = l2 >> 5, tk = wid * 32 + (l2 & 31);
        int eb[5];
        auto ebases = [&](int base) { int l = tid; asm volatile("" : "+v"(l)); const int h = (l >> 5) & 1, k = wid * 32 + (l & 31);
            eb[0] = base + k * 16 + h * 8; eb[1] = base + k * 16 + h * 2040; eb[2] = base + (h ? k * 16 + 1024 : k * 8); eb[3] = base + (h ? k * 8 + 2040 : k * 16);
            asm volatile("" : "+v"(eb[0]), "+v"(eb[1]), "+v"(eb[2]), "+v"(eb[3])); };
        auto kq_addr = [&](int g0) { return eb[g0 % 5 == 1 ? 1 : g0 % 5 == 4 ? 2 : g0 % 5 == 3 ? 3 : 0] + kq_base(g0); };
        ebases(OWN);
        eb[4] = OWN + VOFF + tk * VP + h2 * 8; asm volatile("" : "+v"(eb[4]));
        int qa0 = (8 * h2 + 2 * ((l2 & 15) >> 2)) * 160 + (l2 & 16) * 2 + (l2 & 3) * 8, qa1 = qa0 + WS0 + 10240;
        asm volatile("" : "+v"(qa0), "+v"(qa1));
        auto rdq = [&](int f) { const int i = f / 3, t = f % 3, j = i >> 2, sl = (20 + j) % 7;
            return trfrag(smem + (j >= 7 ? qa0 + (j == 7 ? QU27 : j == 8 ? QU28 : QU29) : sl == 0 ? qa0 + WS0 : qa1 + (sl - 1) * 10240) + (i & 3) * 2560 + t * 64, 160); };
        MFMA_DRAIN();
        WAITV(0);
        { u32x2 pk[20];
#pragma unroll
          for (int n = 0; n < 20; ++n) { const int t = n >> 2, b = n & 3; pk[n] = (u32x2){pk2(acc[t][4 * b], acc[t][4 * b + 1]), pk2(acc[t][4 * b + 2], acc[t][4 * b + 3])}; }
          SB();
#pragma unroll
          for (int n = 0; n < 20; ++n) *(LAS u32x2*)(smem + (n < 10 ? kq_addr(2 * n) : eb[4] + (2 * n - 20) * 8)) = pk[n];
          if (h2 == 0) *(LAS u32x2*)(smem + eb[4] + 160) = (u32x2){0x3F803F80u, 0x3F803F80u}; }
        lds_set(smem + C_E + wid * 4, 1);
        check(10);
        xt[0] = rdx(32);
#pragma unroll
        for (int f = 0; f < WIN; ++f) wf[f] = rdq(f);
        SB();
        u64 pv0 = 0, pv1 = 0;
        SFOR(f, 120)
            constexpr int i = f / 3, t = f % 3, j = i >> 2;
            if constexpr (f % 24 == 12 && j < 9) rdflag(11 + j / 2);
            if constexpr (f % 12 == 12 - WIN && (j == 1 || j == 6 || j == 8)) lds_set(smem + C_P + wid * 4, 21 + j);
            if constexpr (f % 24 == 24 - WIN && j < 9) check(11 + j / 2);
            if constexpr (f + WIN < 120) wf[(f + WIN) % (WIN + 1)] = rdq(f + WIN);
            if constexpr (t == 0 && i < 7) xt[(i + 1) & 1] = rdx(32 + i + 1);
            if constexpr (f >= 90 && f < 100 && !(f & 1)) wo_setup((f - 90) / 2);
            if constexpr (f == 99) tag = rdtag();
            if constexpr (f >= 100 && f < 107) att_setup(f - 100);
            if constexpr (f == 108) ebases(QST);
            if constexpr (f == 116) { pv0 = __hip_atomic_load((LAS u64*)(smem + C_P), RLX_WG); pv1 = __hip_atomic_load((LAS u64*)(smem + C_P + 8), RLX_WG); }
            SB();
            if constexpr (i == 0) MFMA32Z(acc[t], wf[f % (WIN + 1)], xt[0]);
            else if constexpr (i < 8) MFMA32I(acc[t], wf[f % (WIN + 1)], xt[i & 1]);
            else MFMA32I(acc[t], wf[f % (WIN + 1)], xr[i - 8]);
            SB();
        SEND
        MFMA_DRAIN();
        SB();
        if (__builtin_amdgcn_readfirstlane((int)min(min((unsigned)pv0, (unsigned)(pv0 >> 32)), min((unsigned)pv1, (unsigned)(pv1 >> 32)))) < 29) wait_min4(smem + C_P, 29);
        { acc[0] *= QSCALE; acc[1] *= QSCALE; acc[2] *= QSCALE;
          SB();
          u32x2 pk[10];
#pragma unroll
          for (int n = 0; n < 10; ++n) { const int t = n >> 2, b = n & 3; pk[n] = (u32x2){pk2(acc[t][4 * b], acc[t][4 * b + 1]), pk2(acc[t][4 * b + 2], acc[t][4 * b + 3])}; }
          SB();
#pragma unroll
          for (int n = 0; n < 10; ++n) *(LAS u32x2*)(smem + kq_addr(2 * n)) = pk[n]; }
        lds_set(smem + C_QF + wid * 4, 1);
        __builtin_amdgcn_s_setprio(1);
    } else {
        const int hw = wid - 4;
        int known = 0;
        __builtin_amdgcn_s_setprio(3);
        auto lds_or = [&](int off, unsigned v) { FENCE(); if (lane == 0) __hip_atomic_fetch_or((LAS unsigned*)(smem + off), v, RLX_WG); FENCE(); };
        unsigned wo[5], qo[5];
        auto done = [&](int n) { lds_or(C_F + n * 4, 1u << hw); };
        const unsigned xo1 = (xo ^ 64) + 9216, xo2 = xo + 2 * 9216, xo3 = (xo ^ 64) + 3 * 9216;
        auto round_x = [&](int n) { dma16x4(xo, xo1, xo2, xo3, xb + kt_of(n) * 128 + hw * (32 * 1280), lds0 + XS0 + (n % 3) * 16384 + hw * 4096); };
        auto round_w = [&](int n) {
            const int u = 2 * n + (hw >> 1);
            const unsigned dst = lds0 + (u < 27 ? WS0 + (u % 7) * 10240 : u == 27 ? QU27 : u == 28 ? QU28 : QU29) + (hw & 1) * 5120 + 2048;
            if (u < 20) dma16x5(wo, wb + (size_t)(kt_of(u >> 1) * 64 + (u & 1) * 32) * 3840 - 2048, dst);
            else dma16x5(qo, wb + (size_t)kt_of(u < 22 ? u - 12 : u - 22) * (64 * 3840) - 2048, dst); };
        if (isA) round_x(0);
        round_x(1);
#pragma unroll
        for (int q = 0; q < 5; ++q) wo[q] = w_off(q) + (hw & 1) * (16 * 3840) + (4 - q) * 1024;
        if (isA) { round_w(0); round_w(1); WAITV(5); } else { round_w(1); WAITV(9); }
        BAR_L();
#pragma unroll
        for (int q = 0; q < 5; ++q) { const int c = q * 64 + lane, rho = __umul24(c, 6554) >> 16;
            qo[q] = (unsigned)(__umul24((hw & 1) * 32 + (rho & ~7) + 4 * (rho & 1) + ((rho >> 1) & 3), 3840) + (c - rho * 10) * 16 + (4 - q) * 1024); }
#pragma unroll 1
        for (int n = 2; n <= 13; ++n) {
            const int need = n < 13 ? 2 * n - 5 : 20;
            if (need > known) known = min4(smem + C_P);
            if (need > known) { WAITV(0); done(n - 1); known = wait_min4(smem + C_P, need); }
            if (n < 10) round_x(n);
            round_w(n);
            if (n < 10) WAITV(9); else WAITV(5);
            done(n - 1);
        }
        WAITV(0); done(13);
        tag = rdtag();
        auto a_chunk = [&](int a) { int o;
            if (a < 320) { const int hd = a / 80, rem = a % 80; o = hd * 5120 + (rem < 32 ? (96 + rem) * 16 : rem < 64 ? 2048 + (64 + rem) * 16 : 4096 + 768 + (rem - 64) * 16); }
            else o = VOFF + 96 * VP + (a - 320) * 16;
            return o; };
        if (rt < 15) {
            const __amdgpu_buffer_rsrc_t kvr = __builtin_amdgcn_make_buffer_rsrc(ws + WS_KV + me * KVG, 0, KVB, 0x00020000);
            wait_min4(smem + C_E, 1);
            if (!isA) { u32x4 v[11];
#pragma unroll
                for (int j = 0; j < 11; ++j) if (j < 10 || hw == 3) v[j] = *(LAS u32x4*)(smem + OWN + hw * 10240 + j * 1024 + lane * 16);
#pragma unroll
                for (int j = 0; j < 11; ++j) if (j < 10 || hw == 3) __builtin_amdgcn_raw_buffer_store_b128(v[j], kvr, hw * 10240 + j * 1024 + lane * 16, 0, 16);
            } else {
#pragma unroll
                for (int j = 0; j < 3; ++j) { const int a = (4 * j + hw) * 64 + lane;
                    if (a < 656) { const int o = a_chunk(a); __builtin_amdgcn_raw_buffer_store_b128(*(LAS u32x4*)(smem + OWN + o), kvr, o, 0, 16); } }
            }
        }
        wait_min4(smem + C_P, 22);
        round_w(14);
        WAITV(0);
        if (rt < 15 && lds_inc(smem + N_PUB, lane) == 3 && lane == 0) __hip_atomic_store((GAS u64*)(ws + WS_FKV + me * 128), tag, RLX_AGENT);
        done(14);
        __builtin_amdgcn_s_setprio(0);
#pragma unroll
        for (int p = 0; p < 7; ++p) att_setup(p);
#pragma unroll
        for (int q = 0; q < 5; ++q) wo_setup(q);
        __builtin_amdgcn_s_setprio(3);
        if (hw == 0) { poll_flags((GAS u64*)(ws + WS_FKV + (me - 8 * (lane + 1)) * 128), lane < nnb, tag); lds_set(smem + N_KV, 1); }
        else wait_word(smem + N_KV, 1);
        if (!isA) {
            const int n = hw >> 1, half = hw & 1;
            if (n < nnb) {
                const char* src = ws + WS_KV + (me - 8 * (n + 1)) * KVG + half * 20480; const unsigned dst = lds0 + KV0 + (1 - n) * KVB + half * 20480;
                wait_min4(smem + C_P, 27);
#pragma unroll
                for (int j = 0; j < 4; ++j) dma16x5_sc1(lane * 16, src + j * 5120 + 2048, dst + j * 5120 + 2048);
                if (half) dma16_sc1(lane * 16, src + 20480, dst + 20480);
            }
        } else if (nnb) {
            const __amdgpu_buffer_rsrc_t nbr = __builtin_amdgcn_make_buffer_rsrc(ws + WS_KV + (me - 8) * KVG, 0, KVB, 0x00020000);
            u32x4 v[3];
#pragma unroll
            for (int j = 0; j < 3; ++j) { const int a = (4 * j + hw) * 64 + lane; if (a < 656) v[j] = __builtin_amdgcn_raw_buffer_load_b128(nbr, a_chunk(a), 0, 16); }
            wait_min4(smem + C_P, 27);
#pragma unroll
            for (int j = 0; j < 3; ++j) { const int a = (4 * j + hw) * 64 + lane; if (a < 656) *(LAS u32x4*)(smem + KV0 + KVB + a_chunk(a)) = v[j]; }
        }
        __builtin_amdgcn_s_setprio(2);
        wait_word(smem + C_QF + qi * 4, 1);
    }
    u32x4 wreg[13];
    auto ldwo = [&](int i) { wreg[i] = __builtin_amdgcn_raw_buffer_load_b128(wor, oo[i % 5] + (i / 5) * (32 * 1280) + (i > 6 || (i == 6 && lane >= 16) ? 40 * 1280 : 0), 0, 0); };
    u32x2 ob[2][3];
    {
        bf16x8 q1[2]; u32x2 q2[2];
#pragma unroll
        for (int e = 0; e < 2; ++e) {
            q1[e] = *(LAS bf16x8*)(smem + qb1 + e * 5120);
            q2[e] = *(LAS u32x2*)(smem + qb2 + e * 5120);
            if (hl) q2[e] = (u32x2){0u, 0u};
        }
        if (isA) mask_a(0);
        struct KF { bf16x8 a; s16x4 b; };
        struct VF { bf16x8 a, b; };
        auto rdk = [&](int u, int e) { const int so = (u >> 2) * KVB, tw = u & 3; KF k;
            k.a = *(LAS bf16x8*)(smem + kb1 + so + tw * 512 + e * 5120); k.b = *(LAS s16x4*)(smem + __umul24(so + tw * 256 + e * 5120, nh) + kb2); return k; };
        auto rdv = [&](int u, int e) { LAS char* p = smem + (e ? vb1 : vb0) + (u >> 2) * KVB + (u & 3) * (32 * VP); VF v;
            v.a = trfrag(p, 8 * VP); v.b = trfrag(p + 16 * VP, 8 * VP); return v; };
        auto qk = [&](const KF& k, int e, const f32x16& c) { return MFMA32K8(k.b, __builtin_bit_cast(s16x4, q2[e]), MFMA32(k.a, q1[e], c)); };
        const f32x16 zero = {};
        auto max16 = [&](const f32x16& s, float m) {
#pragma unroll
            for (int r = 0; r < 16; ++r) m = __builtin_elementwise_maximum(m, s[r]);
            return m; };
        auto set_ref = [&](float m, int e) {
            const unsigned t0 = pk2(-m, 0.f); const float r1 = -m - __uint_as_float(t0 << 16);
            const unsigned t1 = pk2(r1, 0.f); const float r2 = r1 - __uint_as_float(t1 << 16);
            if (hl) q2[e] = (u32x2){t0 | (t1 << 16), pk2(r2, 0.f)}; };
        auto ex4 = [&](f32x16& s, int b) {
#pragma unroll
            for (int r = 0; r < 4; ++r) s[b + r] = __builtin_amdgcn_exp2f(s[b + r]); };
        auto pk8 = [&](const f32x16& s, int b) { return __builtin_bit_cast(bf16x8, (u32x4){pk2(s[b], s[b + 1]), pk2(s[b + 2], s[b + 3]), pk2(s[b + 4], s[b + 5]), pk2(s[b + 6], s[b + 7])}); };
        auto phase = [&](auto nx, f32x16& cur, const VF& v, f32x16& o, f32x16& nxt, const KF& k, int e) __attribute__((always_inline)) {
            constexpr int NX = decltype(nx)::value;
            SB(); ex4(cur, 0); SB();
            if constexpr (NX) nxt = MFMA32(k.a, q1[e], NX == 1 ? zero : NX == 2 ? cf : cd);
            SB(); ex4(cur, 4); { const bf16x8 p = pk8(cur, 0); SB(); o = MFMA32(v.a, p, o); }
            SB(); ex4(cur, 8); SB();
            if constexpr (NX) nxt = MFMA32K8(k.b, __builtin_bit_cast(s16x4, q2[e]), nxt);
            SB(); ex4(cur, 12); { const bf16x8 p = pk8(cur, 8); SB(); o = MFMA32(v.b, p, o); }
            SB(); };
        f32x16 o0, o1, sa, sb; KF kx; VF vx;
        auto step = [&](auto m0, auto m1, int u) __attribute__((always_inline)) {
            constexpr int M1 = decltype(m1)::value;
            KF ky; if constexpr (M1) ky = rdk(u - 1, 0);
            const VF vy = rdv(u, 1);
            phase(m0, sa, vx, o0, sb, kx, 1);
            if constexpr (M1) { kx = rdk(u - 1, 1); vx = rdv(u - 1, 0); }
            phase(m1, sb, vy, o1, sa, ky, 0); };
        auto nbwait = [&](int u) { if (u == 8) wait_word(smem + N_NB, 4); };
        { const KF k0f = rdk(ud, 0); kx = rdk(ud, 1); vx = rdv(ud, 0);
          const f32x16 s0 = qk(k0f, 0, cd), s1 = qk(kx, 1, cd);
          set_ref(halfmax(max16(s0, s0[0])), 0); set_ref(halfmax(max16(s1, s1[0])), 1);
          sa = qk(k0f, 0, cd); }
        if (hp) { WAITV(0); lds_inc0(smem + N_NB, lane); lds_inc0(smem + N_G0, lane); }
        o0 = zero; o1 = zero;
        if (u0 + 8 == ud) {
            SFOR(it, 9)
                if constexpr (it < 7) { ldwo(2 * it); if constexpr (it < 6) ldwo(2 * it + 1); }
                if constexpr (it == PFLIP) { if (hp) __builtin_amdgcn_s_setprio(0); }
                if constexpr (it == 7) {
#pragma unroll
                    for (int r = 0; r < 16; ++r) mask_b(cf, r, true); }
                if constexpr (it < 4) nbwait(ud - it);
                step(IC<it == 0 ? 3 : it == 8 ? 2 : 1>{}, IC<it == 8 ? 0 : it == 7 ? 2 : 1>{}, ud - it);
            SEND
        } else {
            __builtin_amdgcn_s_setprio(0);
#pragma unroll
            for (int i = 0; i < 13; ++i) ldwo(i);
            if (!isA) cf = zero;
            if (u0 < ud) {
                nbwait(ud);
                if (u0 + 1 < ud) {
                    step(IC<3>{}, IC<1>{}, ud);
#pragma unroll 1
                    for (int u = ud - 1; u > u0 + 1; --u) { nbwait(u); step(IC<1>{}, IC<1>{}, u); }
                    nbwait(u0 + 1);
                    step(IC<1>{}, IC<2>{}, u0 + 1);
                } else step(IC<3>{}, IC<2>{}, ud);
                step(IC<2>{}, IC<0>{}, u0);
            } else step(IC<3>{}, IC<0>{}, ud);
        }
        if (!__all(o0[12] < 1.152921504606847e18f && o1[12] < 1.152921504606847e18f && o0[12] > 0.25f && o1[12] > 0.25f)) {
            if (isA) { int z = 0; asm volatile("" : "+s"(z)); mask_a(z); }
            else { asm volatile("" : "+v"(dsh));
#pragma unroll
                for (int r = 0; r < 16; ++r) { mask_b(cd, r, false); mask_b(cf, r, true); }
                if (!far) cf = zero; }
#pragma unroll
            for (int e = 0; e < 2; ++e) {
                if (hl) q2[e] = (u32x2){0u, 0u};
                auto tile = [&](int u) { return u == ud ? qk(rdk(u, e), e, cd) : u == u0 ? qk(rdk(u, e), e, cf) : qk(rdk(u, e), e, zero); };
                float m = -__builtin_inff();
#pragma unroll 1
                for (int u = u0; u <= ud; ++u) m = max16(tile(u), m);
                m = halfmax(m);
                f32x16 o = zero;
#pragma unroll 1
                for (int u = u0; u <= ud; ++u) { f32x16 t = tile(u) - m;
                    const VF v = rdv(u, e); ex4(t, 0); ex4(t, 4); ex4(t, 8); ex4(t, 12);
                    o = MFMA32(v.a, pk8(t, 0), o); o = MFMA32(v.b, pk8(t, 8), o); }
                if (e) o1 = o; else o0 = o;
            }
        }
        const float rl0 = __builtin_amdgcn_rcpf(o0[12]), rl1 = __builtin_amdgcn_rcpf(o1[12]);
        SB();
#pragma unroll
        for (int r = 0; r < 12; ++r) { o0[r] *= rl0; o1[r] *= rl1; }
        SB();
#pragma unroll
        for (int j = 0; j < 3; ++j) {
            ob[0][j] = (u32x2){pk2(o0[4 * j], o0[4 * j + 1]), pk2(o0[4 * j + 2], o0[4 * j + 3])};
            ob[1][j] = (u32x2){pk2(o1[4 * j], o1[4 * j + 1]), pk2(o1[4 * j + 2], o1[4 * j + 3])}; }
        SB();
    }
    const __amdgpu_buffer_rsrc_t orr = __builtin_amdgcn_make_buffer_rsrc(ws + WS_O + (br * 16 + rt) * OG, 0, 4 * 40960, 0x00020000);
    {
      { auto rx = __builtin_amdgcn_permlane32_swap(ob[0][0][0], ob[0][1][0], false, false);
        auto ry = __builtin_amdgcn_permlane32_swap(ob[0][0][1], ob[0][1][1], false, false);
        __builtin_amdgcn_raw_buffer_store_b128((u32x4){rx[0], ry[0], rx[1], ry[1]}, orr, obase, 0, 16); }
      if (hl == 0) __builtin_amdgcn_raw_buffer_store_b128((u32x4){ob[0][2][0], ob[0][2][1], ob[1][0][0], ob[1][0][1]}, orr, obase + 1024, 0, 16);
      { auto fx = __builtin_amdgcn_permlane32_swap(ob[1][0][0], ob[1][1][0], false, false);
        auto fy = __builtin_amdgcn_permlane32_swap(ob[1][0][1], ob[1][1][1], false, false);
        auto sx = __builtin_amdgcn_permlane32_swap(ob[1][1][0], ob[1][2][0], false, false);
        auto sy = __builtin_amdgcn_permlane32_swap(ob[1][1][1], ob[1][2][1], false, false);
        __builtin_amdgcn_raw_buffer_store_b128((u32x4){fx[1], fy[1], sx[0], sy[0]}, orr, obase + 1536, 0, 16); } }
    if (lane == 0) { (void)__hip_atomic_fetch_add((LAS unsigned*)(smem + N_G1), 1u, RLX_WG);
        if (!hp) (void)__hip_atomic_fetch_add((LAS unsigned*)(smem + N_G0), 1u, RLX_WG); }
    FENCE();
    auto signal_o = [&]() {
        if (lds_inc(smem + N_OS + hp * 4, lane) == 3 && lane == 0) __hip_atomic_store((GAS u64*)(ws + WS_FO + (me * 2 + hp) * 128), tag, RLX_AGENT); };
    __builtin_amdgcn_s_setprio(0);
    WAITV(0); signal_o();
    wait_word(smem + (hp ? N_G1 : N_G0), 8);
    const int p1 = hp ? 30720 : 35840, p2 = hp ? 65536 : 43520;
    const int wb0 = hp ? KV0 + 10240 : 512, wb1 = hp ? KV0 + KVB + 10240 - 30720 : KV0 - 35840, wb2 = KV0 + KVB - 43520;
#pragma unroll
    for (int i = 0; i < 13; ++i) { const int L = qo * 12800 + i * 1024;
        if (i < 12 || lane < 32) *(LAS u32x4*)(smem + (L + (L < p1 ? wb0 : L < p2 ? wb1 : wb2)) + lane * 16) = wreg[i]; }
    lds_inc0(smem + N_WO + hp * 4, lane);
    if (qo == 0) { poll_flags((GAS u64*)(ws + WS_FO + (((me & ~7) + (lane & 7)) * 2 + hp) * 128), lane < 8, tag); lds_set(smem + N_OK + hp * 4, 1); }
    else wait_word(smem + N_OK + hp * 4, 1);
    f32x16 oc[3]; f32x4 ps[10];
    LAS char* red = smem + OWN + qo * 10240 + lane * 16;
    { bf16x8 af[5], of[20];
      const int la = (8 * hl + 2 * ((lane & 15) >> 2)) * 160 + (lane & 16) * 2 + (lane & 3) * 8;
      const int aa0 = wb0 + la, aa1 = (hp ? wb1 : wb0) + la, aa2 = wb1 + la, aa3 = (hp ? wb1 : wb2) + la;
      auto rda = [&](int f) { return trfrag(smem + (f < 36 ? aa0 : f < 42 ? aa1 : f < 51 ? aa2 : aa3) + (f / 3) * 2560 + (f % 3) * 64, 160); };
      auto ldo = [&](int k) { of[k] = __builtin_bit_cast(bf16x8, __builtin_amdgcn_raw_buffer_load_b128(orr, qo * 40960 + (hp * 20 + k) * 1024 + lane * 16, 0, 16)); };
#pragma unroll
      for (int k = 0; k < OPF; ++k) ldo(k);
      wait_word(smem + N_WO + hp * 4, 4);
#pragma unroll
      for (int f = 0; f < 4; ++f) af[f] = rda(f);
      SB();
      SFOR(f, 60)
          if constexpr (f % 3 == 0 && f / 3 + OPF < 20) ldo(f / 3 + OPF);
          if constexpr (f + 4 < 60) af[(f + 4) % 5] = rda(f + 4);
          if constexpr (f == 57) { if (hp) { wait_word(smem + C_RD + qo * 4, 1);
#pragma unroll
              for (int b = 0; b < 10; ++b) ps[b] = *(LAS f32x4*)(red + b * 1024); } }
          SB();
          if constexpr (f < 3) MFMA32Z(oc[f], af[f % 5], of[0]); else MFMA32I(oc[f % 3], af[f % 5], of[f / 3]);
          SB();
      SEND }
    MFMA_DRAIN();
    { if (!hp) {
          wait_word(smem + N_G1, 8);
#pragma unroll
          for (int b = 0; b < 10; ++b) *(LAS f32x4*)(red + b * 1024) = (f32x4){oc[b >> 2][4 * (b & 3)], oc[b >> 2][4 * (b & 3) + 1], oc[b >> 2][4 * (b & 3) + 2], oc[b >> 2][4 * (b & 3) + 3]};
          lds_set(smem + C_RD + qo * 4, 1);
      } else {
          char* orow = out + (size_t)(rt * 128 + qo * 32 + r32) * 2560 + br * 1280 + hg * 160 + hl * 16;
#pragma unroll
          for (int b = 0; b < 10; ++b)
#pragma unroll
              for (int r = 0; r < 4; ++r) ps[b][r] += oc[b >> 2][4 * (b & 3) + r];
          SB();
          u32x2 pk[10];
#pragma unroll
          for (int b = 0; b < 10; ++b) pk[b] = (u32x2){pk2(ps[b][0], ps[b][1]), pk2(ps[b][2], ps[b][3])};
          SB();
          u32x4 st[5];
#pragma unroll
          for (int b = 0; b < 10; b += 2) {
              auto rx = __builtin_amdgcn_permlane32_swap(pk[b][0], pk[b + 1][0], false, false); auto ry = __builtin_amdgcn_permlane32_swap(pk[b][1], pk[b + 1][1], false, false);
              st[b >> 1] = (u32x4){rx[0], ry[0], rx[1], ry[1]}; }
          SB();
#pragma unroll
          for (int b = 0; b < 5; ++b) *(u32x4*)(orow + b * 32) = st[b];
      } }
    if (bx == 0 && wid == 0) {
#pragma unroll 1
        for (int i = 0; i < 4; ++i) poll_flags((GAS u64*)(ws + WS_FO + (i * 64 + lane) * 256), true, tag);
        if (lane == 0) __hip_atomic_store((GAS u64*)ws, tag, RLX_AGENT);
    }
    asm volatile(".Lfused_attn_end:");
}

extern "C" void kernel_launch(void* const* d_in, const int* in_sizes, int n_in, void* d_out, int out_size, void* d_ws, size_t ws_size,
                              hipStream_t stream) {
    fused_attn_kernel<<<256, 512, 0, stream>>>((const char*)d_in[0], (const char*)d_in[1], (const char*)d_in[2], (const char*)d_in[3],
                                               (const char*)d_in[4], (const char*)d_in[5], (char*)d_out, (char*)d_ws);
}
```

```cpp
#include <hip/hip_runtime.h>
#include <hip/hip_bf16.h>
#include <stdint.h>

typedef short bf16x8 __attribute__((ext_vector_type(8)));
typedef short s16x4 __attribute__((ext_vector_type(4)));
typedef float f32x2 __attribute__((ext_vector_type(2)));
typedef float f32x4 __attribute__((ext_vector_type(4)));
typedef float f32x16 __attribute__((ext_vector_type(16)));
typedef unsigned u32x4 __attribute__((ext_vector_type(4)));
typedef unsigned u32x2 __attribute__((ext_vector_type(2)));
typedef __bf16 bfx2 __attribute__((ext_vector_type(2)));
typedef unsigned long long u64;
#define LAS __attribute__((address_space(3)))
#define GAS __attribute__((address_space(1)))
#define DI __device__ __forceinline__
#define RLX_AGENT __ATOMIC_RELAXED, __HIP_MEMORY_SCOPE_AGENT
#define RLX_WG __ATOMIC_RELAXED, __HIP_MEMORY_SCOPE_WORKGROUP

constexpr int LDS_BYTES = 163840;
constexpr int VOFF = 20480, VP = 168;
constexpr int KVB = VOFF + 128 * VP;
constexpr int KV0 = LDS_BYTES - 3 * KVB;
constexpr int OWN = KV0 + 2 * KVB;
constexpr int C_F = 0, C_P = 128, C_E = 144, C_QF = 160, C_RD = 176, C_CNT = 192, C_TAG = 320;
constexpr int N_PUB = C_CNT, N_NB = C_CNT + 4, N_G0 = C_CNT + 8, N_G1 = C_CNT + 12, N_WO = C_CNT + 16, N_OS = C_CNT + 24, N_KV = C_CNT + 32, N_OK = C_CNT + 36;
constexpr int XS0 = 512, WS0 = XS0 + 3 * 16384, QST = 512, ONE = 336, QU27 = XS0 + 16384, QU28 = XS0, QU29 = QU27 + 10240;
static_assert(WS0 + 7 * 10240 <= OWN, "ring overlaps the own image");
constexpr unsigned WS_FKV = 4096, WS_FO = WS_FKV + 256 * 128, WS_KV = 131072;
constexpr unsigned KVG = KVB, OG = 4 * 40960;
constexpr unsigned WS_O = WS_KV + 256 * KVG;
constexpr unsigned WS_END = WS_O + 32 * OG;
constexpr unsigned SPIN_CAP = 400000, LSPIN_CAP = 4000000;
constexpr float QSCALE = 0.22360679774997896f * 1.4426950408889634f;
constexpr int WIN = 4;
constexpr int OPF = 12;
constexpr int PFLIP = 6;
constexpr int ADELAY = 4;

#define MFMA32(a, b, c) __builtin_amdgcn_mfma_f32_32x32x16_bf16((a), (b), (c), 0, 0, 0)
#define MFMA32K8(a, b, c) __builtin_amdgcn_mfma_f32_32x32x8bf16_1k((a), (b), (c), 0, 0, 0)
#define MFMA32I(acc, a, b) asm volatile("v_mfma_f32_32x32x16_bf16 %0, %1, %2, %0" : "+v"(acc) : "v"(a), "v"(b))
#define MFMA32Z(acc, a, b) asm volatile("v_mfma_f32_32x32x16_bf16 %0, %1, %2, 0" : "=&v"(acc) : "v"(a), "v"(b))
#define MFMA_DRAIN() asm volatile("s_nop 7\n\ts_nop 4" ::: "memory")
#define WAITV(N) asm volatile("s_waitcnt vmcnt(" #N ")" ::: "memory")
#define BAR_L() asm volatile("s_waitcnt lgkmcnt(0)\n\ts_barrier" ::: "memory")
#define SB() __builtin_amdgcn_sched_barrier(0)
#define FENCE() asm volatile("" ::: "memory")


template <int V> struct IC { static constexpr int value = V; };
template <int I, int N, class F> DI void static_for(F&& f) { if constexpr (I < N) { f(IC<I>{}); static_for<I + 1, N>(f); } }
#define SFOR(var, N) static_for<0, N>([&](auto ic_##var) __attribute__((always_inline)) { constexpr int var = decltype(ic_##var)::value;
#define SEND });

DI void dma16(unsigned voff, const char* sbase, unsigned ldsaddr) {
    asm volatile("s_mov_b32 m0, %2\n\ts_nop 0\n\tglobal_load_lds_dwordx4 %0, %1" :: "v"(voff), "s"(sbase), "s"(ldsaddr) : "memory");
}
DI void dma16x4(unsigned v0, unsigned v1, unsigned v2, unsigned v3, const char* sbase, unsigned ldsaddr) {
    asm volatile("s_mov_b32 m0, %5\n\ts_nop 0\n\tglobal_load_lds_dwordx4 %0, %4\n\tglobal_load_lds_dwordx4 %1, %4 offset:1024\n\t"
                 "global_load_lds_dwordx4 %2, %4 offset:2048\n\tglobal_load_lds_dwordx4 %3, %4 offset:3072"
                 :: "v"(v0), "v"(v1), "v"(v2), "v"(v3), "s"(sbase), "s"(ldsaddr) : "memory");
}
DI void dma16x5(const unsigned (&v)[5], const char* sbase, unsigned ldsaddr) {
    asm volatile("s_mov_b32 m0, %6\n\ts_nop 0\n\tglobal_load_lds_dwordx4 %0, %5 offset:-2048\n\tglobal_load_lds_dwordx4 %1, %5 offset:-1024\n\t"
                 "global_load_lds_dwordx4 %2, %5\n\tglobal_load_lds_dwordx4 %3, %5 offset:1024\n\tglobal_load_lds_dwordx4 %4, %5 offset:2048"
                 :: "v"(v[0]), "v"(v[1]), "v"(v[2]), "v"(v[3]), "v"(v[4]), "s"(sbase), "s"(ldsaddr) : "memory");
}
DI void dma16x5_sc1(unsigned voff, const char* sbase, unsigned ldsaddr) {
    asm volatile("s_mov_b32 m0, %2\n\ts_nop 0\n\tglobal_load_lds_dwordx4 %0, %1 offset:-2048 sc1\n\tglobal_load_lds_dwordx4 %0, %1 offset:-1024 sc1\n\t"
                 "global_load_lds_dwordx4 %0, %1 sc1\n\tglobal_load_lds_dwordx4 %0, %1 offset:1024 sc1\n\tglobal_load_lds_dwordx4 %0, %1 offset:2048 sc1"
                 :: "v"(voff), "s"(sbase), "s"(ldsaddr) : "memory");
}
DI void dma16_sc1(unsigned voff, const char* sbase, unsigned ldsaddr) {
    asm volatile("s_mov_b32 m0, %2\n\ts_nop 0\n\tglobal_load_lds_dwordx4 %0, %1 sc1" :: "v"(voff), "s"(sbase), "s"(ldsaddr) : "memory");
}
DI void dma4(const char* p, unsigned ldsaddr) {
    asm volatile("s_mov_b32 m0, %1\n\ts_nop 0\n\tglobal_load_lds_dword %0, off" :: "v"(p), "s"(ldsaddr) : "memory");
}
DI unsigned pk2(float lo, float hi) { f32x2 v = {lo, hi}; return __builtin_bit_cast(unsigned, __builtin_convertvector(v, bfx2)); }
typedef short v4i16_t __attribute__((ext_vector_type(4)));
DI bf16x8 trfrag(LAS char* p, int second) {
    s16x4 a = __builtin_bit_cast(s16x4, __builtin_amdgcn_ds_read_tr16_b64_v4i16((LAS v4i16_t*)p));
    s16x4 b = __builtin_bit_cast(s16x4, __builtin_amdgcn_ds_read_tr16_b64_v4i16((LAS v4i16_t*)(p + second)));
    return __builtin_shufflevector(a, b, 0, 1, 2, 3, 4, 5, 6, 7);
}
DI float halfmax(float m) { auto rr = __builtin_amdgcn_permlane32_swap(__float_as_uint(m), __float_as_uint(m), false, false);
    return __builtin_elementwise_maximum(__uint_as_float(rr[0]), __uint_as_float(rr[1])); }

DI void poll_flags(GAS u64* word, bool active, u64 tag) {
#pragma unroll 1
    for (unsigned spins = 0; spins < SPIN_CAP; ++spins) {
        u64 v = tag;
        if (active) v = __hip_atomic_load(word, RLX_AGENT);
        if (__all(v == tag)) break;
        __builtin_amdgcn_s_sleep(2);
    }
    __builtin_amdgcn_fence(__ATOMIC_ACQUIRE, "wavefront");
}
DI int min4(LAS char* p) {
    const u64 a = __hip_atomic_load((LAS u64*)p, RLX_WG), b = __hip_atomic_load((LAS u64*)(p + 8), RLX_WG);
    const unsigned m = min(min((unsigned)a, (unsigned)(a >> 32)), min((unsigned)b, (unsigned)(b >> 32)));
    return __builtin_amdgcn_readfirstlane((int)m);
}
DI int wait_min4(LAS char* p, int need) {
    int v = min4(p);
#pragma unroll 1
    for (unsigned spins = 0; v < need && spins < LSPIN_CAP; ++spins) { __builtin_amdgcn_s_sleep(1); v = min4(p); }
    FENCE();
    return v;
}
DI void wait_word(LAS char* p, unsigned need) {
#pragma unroll 1
    for (unsigned spins = 0; spins < LSPIN_CAP; ++spins) {
        if ((unsigned)__builtin_amdgcn_readfirstlane((int)__hip_atomic_load((LAS unsigned*)p, RLX_WG)) >= need) break;
        __builtin_amdgcn_s_sleep(1); }
    FENCE();
}
DI void lds_inc0(LAS char* p, int lane) { FENCE(); if (lane == 0) (void)__hip_atomic_fetch_add((LAS unsigned*)p, 1u, RLX_WG); FENCE(); }
DI unsigned lds_inc(LAS char* p, int lane) { FENCE(); unsigned old = 0; if (lane == 0) old = __hip_atomic_fetch_add((LAS unsigned*)p, 1u, RLX_WG); FENCE(); return old; }
DI void lds_set(LAS char* p, unsigned v) { FENCE(); __hip_atomic_store((LAS unsigned*)p, v, RLX_WG); FENCE(); }

__global__ __launch_bounds__(512, 2) void fused_attn_kernel(const char* __restrict__ xA, const char* __restrict__ wA, const char* __restrict__ oA,
                                                           const char* __restrict__ xB, const char* __restrict__ wB, const char* __restrict__ oB,
                                                           char* __restrict__ out, char* __restrict__ ws) {
    __shared__ __attribute__((aligned(16))) char smem_[LDS_BYTES];
    LAS char* smem = (LAS char*)smem_;
    const unsigned lds0 = (unsigned)(uintptr_t)smem;
    asm volatile("" :: "s"(xA), "s"(wA), "s"(oA), "s"(xB), "s"(wB), "s"(oB), "s"(out), "s"(ws));
    const int tid = threadIdx.x, lane = tid & 63;
    const int wid = __builtin_amdgcn_readfirstlane(tid >> 6);
    const int bx = blockIdx.x, xcd = bx & 7, idx = bx >> 3;
    const int isA = idx >> 4, rt = (xcd >> 1) * 4 + ((idx >> 2) & 3), hg = (xcd & 1) * 4 + (idx & 3);
    const int br = isA ? 0 : 1;
    { u64 pc; unsigned len;
      asm volatile("s_getpc_b64 %0\n.Lfused_attn_pc:\n\ts_mov_b32 %1, .Lfused_attn_end-.Lfused_attn_pc" : "=s"(pc), "=s"(len));
      if (wid == 3 && (idx & 15) * 8192u < len) dma4((const char*)pc + min(((idx & 15) * 64 + lane) * 128u, len - 4u), lds0 + OWN + 3 * 512); }
    const int k0 = isA ? (((rt & 3) * 5 + 1) / 2 + ((hg & 3) * 5 + 1) / 2) % 10 : 0;
    const char* xb = (isA ? xA : xB) + (size_t)rt * (128 * 1280);
    const char* wb = (isA ? wA : wB) + hg * 160;
    const int me = (br * 16 + rt) * 8 + hg;
    const int r32 = lane & 31, hl = lane >> 5;
    auto kt_of = [&](int s) { int k = s + k0; return k >= 10 ? k - 10 : k; };
    if (isA && wid < 4) {
        const int j16 = idx & 15; const unsigned dump = lds0 + OWN + wid * 512;
        const char* wl = wB + (hg >> 2) * 640 + j16 * (4 * 3840);
        const char* xl = xB + (size_t)(rt * 128 + (hg & 3) * 32 + r32) * 1280 + hl * 128;
        auto pfx = [&](int i) { return xl + i * 256; };
        auto pfkv = [&](int i) { int n = i * 64 + lane; n = n < 400 ? n : 399; const int sl = n / 40, r = n % 40, c = r % 10;
            return wl + (sl * 64 + r / 10) * 3840 + 1280 + (c < 5 ? c * 128 : 640 + c * 128); };
        auto pfq = [&](int i) { int n = i * 64 + lane; n = n < 200 ? n : 199; int sl = n / 20 + 8; sl = sl >= 10 ? sl - 10 : sl; const int r = n % 20;
            return wl + (sl * 64 + r / 5) * 3840 + (r % 5) * 128; };
        dma4(wid & 1 ? pfx(wid >> 1) : pfkv(wid >> 1), dump);
        dma4(wid & 1 ? pfx(2 + (wid >> 1)) : pfkv(2 + (wid >> 1)), dump);
        dma4(wid < 3 ? pfkv(4 + wid) : pfx(4), dump);
        dma4(pfq(wid), dump);
        { int n = wid * 64 + lane; n = n < 200 ? n : 199; const int o = (j16 * 40 + n / 5) * 1280 + (hg >> 2) * 640 + (n % 5) * 128;
          dma4(oB + o, dump); dma4(oA + o, dump); }
    }
    if (isA && wid >= 4) { for (int i = 0; i < ADELAY; ++i) __builtin_amdgcn_s_sleep(16); }

    const unsigned xo = (unsigned)((lane >> 3) * 1280 + (((lane & 7) ^ (lane >> 4)) << 4));
    auto w_off = [&](int q) { const int c = q * 64 + lane, row = __umul24(c, 3277) >> 16, j = c - row * 20; return (unsigned)(__umul24(row, 3840) + j * 16 + (j < 10 ? 1280 : 2400)); };
    if (!isA) { const char* xs = xb + k0 * 128 + wid * (16 * 1280);
      dma16(xo, xs, lds0 + XS0 + wid * 2048);
      dma16(xo ^ 64, xs + 8 * 1280, lds0 + XS0 + wid * 2048 + 1024);
      const char* wsrc = wb + (size_t)k0 * (64 * 3840);
      const unsigned wv = w_off(wid < 5 ? wid : wid - 5);
      if (wid < 5) { dma16(wv, wsrc, lds0 + WS0 + wid * 1024); dma16(wv, wsrc + 16 * 3840, lds0 + WS0 + (5 + wid) * 1024);
          if (wid >= 3) { dma16(wv, wsrc + 32 * 3840, lds0 + WS0 + (10 + wid) * 1024); dma16(wv, wsrc + 48 * 3840, lds0 + WS0 + (15 + wid) * 1024); } }
      else { dma16(wv, wsrc + 32 * 3840, lds0 + WS0 + (5 + wid) * 1024); dma16(wv, wsrc + 48 * 3840, lds0 + WS0 + (10 + wid) * 1024); } }
    if (wid == 4) { const char* pe = ws + (lane & 1) * 4; if (lane < 2) dma4(pe, lds0 + C_TAG); }
    if (tid < 80) *(LAS unsigned*)(smem + tid * 4) = 0u;
    else if (tid < 82) *(LAS unsigned*)(smem + ONE + (tid - 80) * 4) = 0x3F803F80u;
    const int nnb = isA ? (rt > 0 ? 1 : 0) : (rt > 1 ? 2 : rt);
    auto kq_base = [&](int fg) { const int hd = fg / 5, dg = fg % 5; return dg < 4 ? hd * 5120 + (dg >> 1) * 2048 + (dg & 1) * 8 : hd * 5120 + 4096; };

    auto rdtag = [&]() { const u64 t = *(LAS u64*)(smem + C_TAG);
        const u64 e = ((u64)(unsigned)__builtin_amdgcn_readfirstlane((int)(t >> 32)) << 32) | (unsigned)__builtin_amdgcn_readfirstlane((int)t);
        return e * 6364136223846793005ull + 1442695040888963407ull; };
    const __amdgpu_buffer_rsrc_t wor = __builtin_amdgcn_make_buffer_rsrc((void*)(isA ? oA : oB), 0, 640 * 1280, 0x00020000);

    const int hp = wid >> 2, qo = wid & 3, qi = hp ? 3 - qo : qo;
    const int ud = qi + 8;
    const bool far = isA ? (4 * rt + qi >= 1) : (4 * rt + qi >= 8);
    const int u0 = isA ? (far ? ud - 1 : ud) : (far ? qi : 8 - 4 * rt);
    f32x16 cd, cf;
    int dsh, kb1, kb2, nh, vb0, vb1, qb1, qb2; unsigned obase, oo[5];
    auto mask_b = [&](f32x16& c, int r, bool fartile) { const int k = (r & 3) + 8 * (r >> 2); c[r] = __int_as_float(fartile ? ((k - 1) << 23) - dsh : dsh - (k << 23)); };
    auto mask_a = [&](int l) {
        const u64 w = ~(0x5555ull << ((l & 31) + 18)); const unsigned id = (unsigned)(w >> 32) >> ((l >> 3) & 4), jf = (unsigned)w >> ((l >> 3) & 4);
#pragma unroll
        for (int r = 0; r < 16; ++r) { const int k = (r & 3) + 8 * (r >> 2);
            cd[r] = __uint_as_float((unsigned)__builtin_amdgcn_sbfe((int)id, k, 1) & 0xFF800000u);
            cf[r] = __uint_as_float((unsigned)__builtin_amdgcn_sbfe((int)jf, k, 1) & 0xFF800000u); } };
    auto att_setup = [&](int part) {
        int l = tid; asm volatile("" : "+v"(l)); l &= 63;
        const int q = l & 31, h = l >> 5;
        if (part == 0) { dsh = (q - 4 * h) << 23; kb1 = KV0 + hp * 10240 + h * 2048 + q * 16; kb2 = h ? ONE : KV0 + hp * 10240 + 4096 + q * 8; nh = h ^ 1;
            asm volatile("" : "+v"(dsh), "+v"(kb1), "+v"(kb2), "+v"(nh)); }
        if (part == 1) { const bool ones = (l & 16) && (l & 3);
            vb0 = KV0 + VOFF + (4 * h + ((l & 15) >> 2)) * VP + (ones ? 160 : hp * 80 + (l & 16) * 2 + (l & 3) * 8); vb1 = vb0 + (ones ? 0 : 40);
            asm volatile("" : "+v"(vb0), "+v"(vb1)); }
        if (part == 2) { qb1 = QST + hp * 10240 + h * 2048 + (qi * 32 + q) * 16; qb2 = QST + hp * 10240 + 4096 + (qi * 32 + q) * 8;
            obase = (unsigned)(qi * 40960 + hp * 20480 + hg * 2560 + q * 16 + h * 512);
            asm volatile("" : "+v"(qb1), "+v"(qb2), "+v"(obase)); }
        if (part >= 3) {
#pragma unroll
            for (int r = 4 * (part - 3); r < 4 * (part - 2); ++r) mask_b(cd, r, false);
            asm volatile("" : "+v"(cd)); } };
    auto wo_setup = [&](int q) {
        int l = tid; asm volatile("" : "+v"(l)); const int c = q * 64 + (l & 63), rho = __umul24(c, 6554) >> 16;
        oo[q] = (unsigned)(__umul24(qo * 160 + hp * 40 + (rho & ~7) + 4 * (rho & 1) + ((rho >> 1) & 3), 1280) + hg * 160 + (c - rho * 10) * 16);
        asm volatile("" : "+v"(oo[q])); };
    u64 tag;

    if (wid < 4) {
        if (!isA) WAITV(0);
        BAR_L();
        bf16x8 xr[32], xt[2], wf[WIN + 1];
        f32x16 acc[5];
        const int tok = wid * 32 + r32;
        const int xa = XS0 + tok * 128 + ((hl ^ ((tok >> 1) & 7)) << 4);
        int wa0 = WS0 + (8 * hl + ((lane & 15) >> 2)) * 320 + (lane & 16) * 2 + (lane & 3) * 8, wa1 = wa0 + 61440;
        asm volatile("" : "+v"(wa0), "+v"(wa1));
        auto rdx = [&](int i) { return *(LAS bf16x8*)(smem + ((i >> 2) % 3) * 16384 + (xa ^ ((i & 3) * 32))); };
        auto rdw = [&](int f) { const int i = f / 5, t = f % 5, sl = (i >> 1) % 7;
            return trfrag(smem + (sl < 6 ? wa0 + sl * 10240 : wa1) + (i & 1) * 5120 + t * 64, 1280); };
        unsigned fl = 0;
        auto rdflag = [&](int u) { fl = __hip_atomic_load((LAS unsigned*)(smem + C_F + u * 4), RLX_WG); };
        auto check = [&](int u) {
            if (__builtin_amdgcn_readfirstlane(fl) != 15) {
#pragma unroll 1
                for (unsigned spins = 0; spins < LSPIN_CAP; ++spins) { rdflag(u); if (__builtin_amdgcn_readfirstlane(fl) == 15) break; __builtin_amdgcn_s_sleep(1); }
            }
            FENCE(); };
#pragma unroll
        for (int i = 0; i < 4; ++i) xr[i] = rdx(i);
#pragma unroll
        for (int f = 0; f < WIN; ++f) wf[f] = rdw(f);
        SB();
        SFOR(f, 200)
            constexpr int i = f / 5, t = f % 5;
            if constexpr (f % 20 == 10 - WIN) lds_set(smem + C_P + wid * 4, (i >> 1) + 1);
            if constexpr (f == 200 - WIN) lds_set(smem + C_P + wid * 4, 20);
            if constexpr (f % 20 == 10) rdflag(f / 20 + 1);
            if constexpr (f % 20 == 20 - WIN && f < 190) check(f / 20 + 1);
            if constexpr (f + WIN < 200) wf[(f + WIN) % (WIN + 1)] = rdw(f + WIN);
            if constexpr (f % 20 >= 16 && f < 140) xr[(f / 20 + 1) * 4 + f % 20 - 16] = rdx((f / 20 + 1) * 4 + f % 20 - 16);
            if constexpr (t == 1 && i >= 31 && i < 39) xt[(i + 1) & 1] = rdx(i + 1);
            SB();
            if constexpr (i == 0) MFMA32Z(acc[t], wf[f % (WIN + 1)], xr[0]);
            else if constexpr (i < 32) MFMA32I(acc[t], wf[f % (WIN + 1)], xr[i]);
            else MFMA32I(acc[t], wf[f % (WIN + 1)], xt[i & 1]);
            SB();
        SEND
        int l2 = tid; asm volatile("" : "+v"(l2)); l2 &= 63;
        const int h2 = l2 >> 5, tk = wid * 32 + (l2 & 31);
        int eb[5];
        auto ebases = [&](int base) { int l = tid; asm volatile("" : "+v"(l)); const int h = (l >> 5) & 1, k = wid * 32 + (l & 31);
            eb[0] = base + k * 16 + h * 8; eb[1] = base + k * 16 + h * 2040; eb[2] = base + (h ? k * 16 + 1024 : k * 8); eb[3] = base + (h ? k * 8 + 2040 : k * 16);
            asm volatile("" : "+v"(eb[0]), "+v"(eb[1]), "+v"(eb[2]), "+v"(eb[3])); };
        auto kq_addr = [&](int g0) { return eb[g0 % 5 == 1 ? 1 : g0 % 5 == 4 ? 2 : g0 % 5 == 3 ? 3 : 0] + kq_base(g0); };
        ebases(OWN);
        eb[4] = OWN + VOFF + tk * VP + h2 * 8; asm volatile("" : "+v"(eb[4]));
        int qa0 = (8 * h2 + 2 * ((l2 & 15) >> 2)) * 160 + (l2 & 16) * 2 + (l2 & 3) * 8, qa1 = qa0 + WS0 + 10240;
        asm volatile("" : "+v"(qa0), "+v"(qa1));
        auto rdq = [&](int f) { const int i = f / 3, t = f % 3, j = i >> 2, sl = (20 + j) % 7;
            return trfrag(smem + (j >= 7 ? qa0 + (j == 7 ? QU27 : j == 8 ? QU28 : QU29) : sl == 0 ? qa0 + WS0 : qa1 + (sl - 1) * 10240) + (i & 3) * 2560 + t * 64, 160); };
        MFMA_DRAIN();
        WAITV(0);
        { u32x2 pk[20];
#pragma unroll
          for (int n = 0; n < 20; ++n) { const int t = n >> 2, b = n & 3; pk[n] = (u32x2){pk2(acc[t][4 * b], acc[t][4 * b + 1]), pk2(acc[t][4 * b + 2], acc[t][4 * b + 3])}; }
          SB();
#pragma unroll
          for (int n = 0; n < 20; ++n) *(LAS u32x2*)(smem + (n < 10 ? kq_addr(2 * n) : eb[4] + (2 * n - 20) * 8)) = pk[n];
          if (h2 == 0) *(LAS u32x2*)(smem + eb[4] + 160) = (u32x2){0x3F803F80u, 0x3F803F80u}; }
        lds_set(smem + C_E + wid * 4, 1);
        check(10);
        xt[0] = rdx(32);
#pragma unroll
        for (int f = 0; f < WIN; ++f) wf[f] = rdq(f);
        SB();
        u64 pv0 = 0, pv1 = 0;
        SFOR(f, 120)
            constexpr int i = f / 3, t = f % 3, j = i >> 2;
            if constexpr (f % 24 == 12 && j < 9) rdflag(11 + j / 2);
            if constexpr (f % 12 == 12 - WIN && (j == 1 || j == 6 || j == 8)) lds_set(smem + C_P + wid * 4, 21 + j);
            if constexpr (f % 24 == 24 - WIN && j < 9) check(11 + j / 2);
            if constexpr (f + WIN < 120) wf[(f + WIN) % (WIN + 1)] = rdq(f + WIN);
            if constexpr (t == 0 && i < 7) xt[(i + 1) & 1] = rdx(32 + i + 1);
            if constexpr (f >= 90 && f < 100 && !(f & 1)) wo_setup((f - 90) / 2);
            if constexpr (f == 99) tag = rdtag();
            if constexpr (f >= 100 && f < 107) att_setup(f - 100);
            if constexpr (f == 108) ebases(QST);
            if constexpr (f == 116) { pv0 = __hip_atomic_load((LAS u64*)(smem + C_P), RLX_WG); pv1 = __hip_atomic_load((LAS u64*)(smem + C_P + 8), RLX_WG); }
            SB();
            if constexpr (i == 0) MFMA32Z(acc[t], wf[f % (WIN + 1)], xt[0]);
            else if constexpr (i < 8) MFMA32I(acc[t], wf[f % (WIN + 1)], xt[i & 1]);
            else MFMA32I(acc[t], wf[f % (WIN + 1)], xr[i - 8]);
            SB();
        SEND
        MFMA_DRAIN();
        SB();
        if (__builtin_amdgcn_readfirstlane((int)min(min((unsigned)pv0, (unsigned)(pv0 >> 32)), min((unsigned)pv1, (unsigned)(pv1 >> 32)))) < 29) wait_min4(smem + C_P, 29);
        { acc[0] *= QSCALE; acc[1] *= QSCALE; acc[2] *= QSCALE;
          SB();
          u32x2 pk[10];
#pragma unroll
          for (int n = 0; n < 10; ++n) { const int t = n >> 2, b = n & 3; pk[n] = (u32x2){pk2(acc[t][4 * b], acc[t][4 * b + 1]), pk2(acc[t][4 * b + 2], acc[t][4 * b + 3])}; }
          SB();
#pragma unroll
          for (int n = 0; n < 10; ++n) *(LAS u32x2*)(smem + kq_addr(2 * n)) = pk[n]; }
        lds_set(smem + C_QF + wid * 4, 1);
        __builtin_amdgcn_s_setprio(1);
    } else {
        const int hw = wid - 4;
        int known = 0;
        __builtin_amdgcn_s_setprio(3);
        auto lds_or = [&](int off, unsigned v) { FENCE(); if (lane == 0) __hip_atomic_fetch_or((LAS unsigned*)(smem + off), v, RLX_WG); FENCE(); };
        unsigned wo[5], qo[5];
        auto done = [&](int n) { lds_or(C_F + n * 4, 1u << hw); };
        const unsigned xo1 = (xo ^ 64) + 9216, xo2 = xo + 2 * 9216, xo3 = (xo ^ 64) + 3 * 9216;
        auto round_x = [&](int n) { dma16x4(xo, xo1, xo2, xo3, xb + kt_of(n) * 128 + hw * (32 * 1280), lds0 + XS0 + (n % 3) * 16384 + hw * 4096); };
        auto round_w = [&](int n) {
            const int u = 2 * n + (hw >> 1);
            const unsigned dst = lds0 + (u < 27 ? WS0 + (u % 7) * 10240 : u == 27 ? QU27 : u == 28 ? QU28 : QU29) + (hw & 1) * 5120 + 2048;
            if (u < 20) dma16x5(wo, wb + (size_t)(kt_of(u >> 1) * 64 + (u & 1) * 32) * 3840 - 2048, dst);
            else dma16x5(qo, wb + (size_t)kt_of(u < 22 ? u - 12 : u - 22) * (64 * 3840) - 2048, dst); };
        if (isA) round_x(0);
        round_x(1);
#pragma unroll
        for (int q = 0; q < 5; ++q) wo[q] = w_off(q) + (hw & 1) * (16 * 3840) + (4 - q) * 1024;
        if (isA) { round_w(0); round_w(1); WAITV(5); } else { round_w(1); WAITV(9); }
        BAR_L();
#pragma unroll
        for (int q = 0; q < 5; ++q) { const int c = q * 64 + lane, rho = __umul24(c, 6554) >> 16;
            qo[q] = (unsigned)(__umul24((hw & 1) * 32 + (rho & ~7) + 4 * (rho & 1) + ((rho >> 1) & 3), 3840) + (c - rho * 10) * 16 + (4 - q) * 1024); }
#pragma unroll 1
        for (int n = 2; n <= 13; ++n) {
            const int need = n < 13 ? 2 * n - 5 : 20;
            if (need > known) known = min4(smem + C_P);
            if (need > known) { WAITV(0); done(n - 1); known = wait_min4(smem + C_P, need); }
            if (n < 10) round_x(n);
            round_w(n);
            if (n < 10) WAITV(9); else WAITV(5);
            done(n - 1);
        }
        WAITV(0); done(13);
        tag = rdtag();
        auto a_chunk = [&](int a) { int o;
            if (a < 320) { const int hd = a / 80, rem = a % 80; o = hd * 5120 + (rem < 32 ? (96 + rem) * 16 : rem < 64 ? 2048 + (64 + rem) * 16 : 4096 + 768 + (rem - 64) * 16); }
            else o = VOFF + 96 * VP + (a - 320) * 16;
            return o; };
        if (rt < 15) {
            const __amdgpu_buffer_rsrc_t kvr = __builtin_amdgcn_make_buffer_rsrc(ws + WS_KV + me * KVG, 0, KVB, 0x00020000);
            wait_min4(smem + C_E, 1);
            if (!isA) { u32x4 v[11];
#pragma unroll
                for (int j = 0; j < 11; ++j) if (j < 10 || hw == 3) v[j] = *(LAS u32x4*)(smem + OWN + hw * 10240 + j * 1024 + lane * 16);
#pragma unroll
                for (int j = 0; j < 11; ++j) if (j < 10 || hw == 3) __builtin_amdgcn_raw_buffer_store_b128(v[j], kvr, hw * 10240 + j * 1024 + lane * 16, 0, 16);
            } else {
#pragma unroll
                for (int j = 0; j < 3; ++j) { const int a = (4 * j + hw) * 64 + lane;
                    if (a < 656) { const int o = a_chunk(a); __builtin_amdgcn_raw_buffer_store_b128(*(LAS u32x4*)(smem + OWN + o), kvr, o, 0, 16); } }
            }
        }
        wait_min4(smem + C_P, 22);
        round_w(14);
        WAITV(0);
        if (rt < 15 && lds_inc(smem + N_PUB, lane) == 3 && lane == 0) __hip_atomic_store((GAS u64*)(ws + WS_FKV + me * 128), tag, RLX_AGENT);
        done(14);
        __builtin_amdgcn_s_setprio(0);
#pragma unroll
        for (int p = 0; p < 7; ++p) att_setup(p);
#pragma unroll
        for (int q = 0; q < 5; ++q) wo_setup(q);
        __builtin_amdgcn_s_setprio(3);
        if (hw == 0) { poll_flags((GAS u64*)(ws + WS_FKV + (me - 8 * (lane + 1)) * 128), lane < nnb, tag); lds_set(smem + N_KV, 1); }
        else wait_word(smem + N_KV, 1);
        if (!isA) {
            const int n = hw >> 1, half = hw & 1;
            if (n < nnb) {
                const char* src = ws + WS_KV + (me - 8 * (n + 1)) * KVG + half * 20480; const unsigned dst = lds0 + KV0 + (1 - n) * KVB + half * 20480;
                wait_min4(smem + C_P, 27);
#pragma unroll
                for (int j = 0; j < 4; ++j) dma16x5_sc1(lane * 16, src + j * 5120 + 2048, dst + j * 5120 + 2048);
                if (half) dma16_sc1(lane * 16, src + 20480, dst + 20480);
            }
        } else if (nnb) {
            const __amdgpu_buffer_rsrc_t nbr = __builtin_amdgcn_make_buffer_rsrc(ws + WS_KV + (me - 8) * KVG, 0, KVB, 0x00020000);
            u32x4 v[3];
#pragma unroll
            for (int j = 0; j < 3; ++j) { const int a = (4 * j + hw) * 64 + lane; if (a < 656) v[j] = __builtin_amdgcn_raw_buffer_load_b128(nbr, a_chunk(a), 0, 16); }
            wait_min4(smem + C_P, 27);
#pragma unroll
            for (int j = 0; j < 3; ++j) { const int a = (4 * j + hw) * 64 + lane; if (a < 656) *(LAS u32x4*)(smem + KV0 + KVB + a_chunk(a)) = v[j]; }
        }
        __builtin_amdgcn_s_setprio(2);
        wait_word(smem + C_QF + qi * 4, 1);
    }
    u32x4 wreg[13];
    auto ldwo = [&](int i) { wreg[i] = __builtin_amdgcn_raw_buffer_load_b128(wor, oo[i % 5] + (i / 5) * (32 * 1280) + (i > 6 || (i == 6 && lane >= 16) ? 40 * 1280 : 0), 0, 0); };
    u32x2 ob[2][3];
    {
        bf16x8 q1[2]; u32x2 q2[2];
#pragma unroll
        for (int e = 0; e < 2; ++e) {
            q1[e] = *(LAS bf16x8*)(smem + qb1 + e * 5120);
            q2[e] = *(LAS u32x2*)(smem + qb2 + e * 5120);
            if (hl) q2[e] = (u32x2){0u, 0u};
        }
        if (isA) mask_a(tid);
        struct KF { bf16x8 a; s16x4 b; };
        struct VF { bf16x8 a, b; };
        auto rdk = [&](int u, int e) { const int so = (u >> 2) * KVB, tw = u & 3; KF k;
            k.a = *(LAS bf16x8*)(smem + kb1 + so + tw * 512 + e * 5120); k.b = *(LAS s16x4*)(smem + __umul24(so + tw * 256 + e * 5120, nh) + kb2); return k; };
        auto rdv = [&](int u, int e) { LAS char* p = smem + (e ? vb1 : vb0) + (u >> 2) * KVB + (u & 3) * (32 * VP); VF v;
            v.a = trfrag(p, 8 * VP); v.b = trfrag(p + 16 * VP, 8 * VP); return v; };
        auto qk = [&](const KF& k, int e, const f32x16& c) { return MFMA32K8(k.b, __builtin_bit_cast(s16x4, q2[e]), MFMA32(k.a, q1[e], c)); };
        const f32x16 zero = {};
        auto max16 = [&](const f32x16& s, float m) {
#pragma unroll
            for (int r = 0; r < 16; ++r) m = __builtin_elementwise_maximum(m, s[r]);
            return m; };
        auto set_ref = [&](float m, int e) {
            const unsigned t0 = pk2(-m, 0.f); const float r1 = -m - __uint_as_float(t0 << 16);
            const unsigned t1 = pk2(r1, 0.f); const float r2 = r1 - __uint_as_float(t1 << 16);
            if (hl) q2[e] = (u32x2){t0 | (t1 << 16), pk2(r2, 0.f)}; };
        auto ex4 = [&](f32x16& s, int b) {
#pragma unroll
            for (int r = 0; r < 4; ++r) s[b + r] = __builtin_amdgcn_exp2f(s[b + r]); };
        auto pk8 = [&](const f32x16& s, int b) { return __builtin_bit_cast(bf16x8, (u32x4){pk2(s[b], s[b + 1]), pk2(s[b + 2], s[b + 3]), pk2(s[b + 4], s[b + 5]), pk2(s[b + 6], s[b + 7])}); };
        auto phase = [&](auto nx, f32x16& cur, const VF& v, f32x16& o, f32x16& nxt, const KF& k, int e) __attribute__((always_inline)) {
            constexpr int NX = decltype(nx)::value;
            SB(); ex4(cur, 0); SB();
            if constexpr (NX) nxt = MFMA32(k.a, q1[e], NX == 1 ? zero : NX == 2 ? cf : cd);
            SB(); ex4(cur, 4); { const bf16x8 p = pk8(cur, 0); SB(); o = MFMA32(v.a, p, o); }
            SB(); ex4(cur, 8); SB();
            if constexpr (NX) nxt = MFMA32K8(k.b, __builtin_bit_cast(s16x4, q2[e]), nxt);
            SB(); ex4(cur, 12); { const bf16x8 p = pk8(cur, 8); SB(); o = MFMA32(v.b, p, o); }
            SB(); };
        f32x16 o0, o1, sa, sb; KF kx; VF vx;
        auto step = [&](auto m0, auto m1, int u) __attribute__((always_inline)) {
            constexpr int M1 = decltype(m1)::value;
            KF ky; if constexpr (M1) ky = rdk(u - 1, 0);
            const VF vy = rdv(u, 1);
            phase(m0, sa, vx, o0, sb, kx, 1);
            if constexpr (M1) { kx = rdk(u - 1, 1); vx = rdv(u - 1, 0); }
            phase(m1, sb, vy, o1, sa, ky, 0); };
        auto nbwait = [&](int u) { if (u == 8) wait_word(smem + N_NB, 4); };
        { const KF k0f = rdk(ud, 0); kx = rdk(ud, 1); vx = rdv(ud, 0);
          const f32x16 s0 = qk(k0f, 0, cd), s1 = qk(kx, 1, cd);
          set_ref(halfmax(max16(s0, s0[0])), 0); set_ref(halfmax(max16(s1, s1[0])), 1);
          sa = qk(k0f, 0, cd); }
        if (hp) { WAITV(0); lds_inc0(smem + N_NB, lane); lds_inc0(smem + N_G0, lane); }
        o0 = zero; o1 = zero;
        if (u0 + 8 == ud) {
            SFOR(it, 9)
                if constexpr (it < 7) { ldwo(2 * it); if constexpr (it < 6) ldwo(2 * it + 1); }
                if constexpr (it == PFLIP) { if (hp) __builtin_amdgcn_s_setprio(0); }
                if constexpr (it == 7) {
#pragma unroll
                    for (int r = 0; r < 16; ++r) mask_b(cf, r, true); }
                if constexpr (it < 4) nbwait(ud - it);
                step(IC<it == 0 ? 3 : it == 8 ? 2 : 1>{}, IC<it == 8 ? 0 : it == 7 ? 2 : 1>{}, ud - it);
            SEND
        } else {
            __builtin_amdgcn_s_setprio(0);
#pragma unroll
            for (int i = 0; i < 6; ++i) ldwo(i);
            if (!isA) cf = zero;
            if (u0 < ud) {
                nbwait(ud);
                if (u0 + 1 < ud) {
                    step(IC<3>{}, IC<1>{}, ud);
#pragma unroll 1
                    for (int u = ud - 1; u > u0 + 1; --u) { nbwait(u); step(IC<1>{}, IC<1>{}, u); }
                    nbwait(u0 + 1);
                    step(IC<1>{}, IC<2>{}, u0 + 1);
                } else step(IC<3>{}, IC<2>{}, ud);
#pragma unroll
                for (int i = 6; i < 13; ++i) ldwo(i);
                step(IC<2>{}, IC<0>{}, u0);
            } else {
#pragma unroll
                for (int i = 6; i < 13; ++i) ldwo(i);
                step(IC<3>{}, IC<0>{}, ud); }
        }
        if (!__all(o0[12] < 1.152921504606847e18f && o1[12] < 1.152921504606847e18f && o0[12] > 0.25f && o1[12] > 0.25f)) {
            if (isA) { int z = tid; asm volatile("" : "+v"(z)); mask_a(z); }
            else { asm volatile("" : "+v"(dsh));
#pragma unroll
                for (int r = 0; r < 16; ++r) { mask_b(cd, r, false); mask_b(cf, r, true); }
                if (!far) cf = zero; }
#pragma unroll
            for (int e = 0; e < 2; ++e) {
                if (hl) q2[e] = (u32x2){0u, 0u};
                auto tile = [&](int u) { return u == ud ? qk(rdk(u, e), e, cd) : u == u0 ? qk(rdk(u, e), e, cf) : qk(rdk(u, e), e, zero); };
                float m = -__builtin_inff();
#pragma unroll 1
                for (int u = u0; u <= ud; ++u) m = max16(tile(u), m);
                m = halfmax(m);
                f32x16 o = zero;
#pragma unroll 1
                for (int u = u0; u <= ud; ++u) { f32x16 t = tile(u) - m;
                    const VF v = rdv(u, e); ex4(t, 0); ex4(t, 4); ex4(t, 8); ex4(t, 12);
                    o = MFMA32(v.a, pk8(t, 0), o); o = MFMA32(v.b, pk8(t, 8), o); }
                if (e) o1 = o; else o0 = o;
            }
        }
        const float rl0 = __builtin_amdgcn_rcpf(o0[12]), rl1 = __builtin_amdgcn_rcpf(o1[12]);
        SB();
#pragma unroll
        for (int r = 0; r < 12; ++r) { o0[r] *= rl0; o1[r] *= rl1; }
        SB();
#pragma unroll
        for (int j = 0; j < 3; ++j) {
            ob[0][j] = (u32x2){pk2(o0[4 * j], o0[4 * j + 1]), pk2(o0[4 * j + 2], o0[4 * j + 3])};
            ob[1][j] = (u32x2){pk2(o1[4 * j], o1[4 * j + 1]), pk2(o1[4 * j + 2], o1[4 * j + 3])}; }
        SB();
    }
    const __amdgpu_buffer_rsrc_t orr = __builtin_amdgcn_make_buffer_rsrc(ws + WS_O + (br * 16 + rt) * OG, 0, 4 * 40960, 0x00020000);
    {
      { auto rx = __builtin_amdgcn_permlane32_swap(ob[0][0][0], ob[0][1][0], false, false);
        auto ry = __builtin_amdgcn_permlane32_swap(ob[0][0][1], ob[0][1][1], false, false);
        __builtin_amdgcn_raw_buffer_store_b128((u32x4){rx[0], ry[0], rx[1], ry[1]}, orr, obase, 0, 16); }
      if (hl == 0) __builtin_amdgcn_raw_buffer_store_b128((u32x4){ob[0][2][0], ob[0][2][1], ob[1][0][0], ob[1][0][1]}, orr, obase + 1024, 0, 16);
      { auto fx = __builtin_amdgcn_permlane32_swap(ob[1][0][0], ob[1][1][0], false, false);
        auto fy = __builtin_amdgcn_permlane32_swap(ob[1][0][1], ob[1][1][1], false, false);
        auto sx = __builtin_amdgcn_permlane32_swap(ob[1][1][0], ob[1][2][0], false, false);
        auto sy = __builtin_amdgcn_permlane32_swap(ob[1][1][1], ob[1][2][1], false, false);
        __builtin_amdgcn_raw_buffer_store_b128((u32x4){fx[1], fy[1], sx[0], sy[0]}, orr, obase + 1536, 0, 16); } }
    if (lane == 0) { (void)__hip_atomic_fetch_add((LAS unsigned*)(smem + N_G1), 1u, RLX_WG);
        if (!hp) (void)__hip_atomic_fetch_add((LAS unsigned*)(smem + N_G0), 1u, RLX_WG); }
    FENCE();
    auto signal_o = [&]() {
        if (lds_inc(smem + N_OS + hp * 4, lane) == 3 && lane == 0) __hip_atomic_store((GAS u64*)(ws + WS_FO + (me * 2 + hp) * 128), tag, RLX_AGENT); };
    __builtin_amdgcn_s_setprio(0);
    WAITV(0); signal_o();
    wait_word(smem + (hp ? N_G1 : N_G0), 8);
    const int p1 = hp ? 30720 : 35840, p2 = hp ? 65536 : 43520;
    const int wb0 = hp ? KV0 + 10240 : 512, wb1 = hp ? KV0 + KVB + 10240 - 30720 : KV0 - 35840, wb2 = KV0 + KVB - 43520;
#pragma unroll
    for (int i = 0; i < 13; ++i) { const int L = qo * 12800 + i * 1024;
        if (i < 12 || lane < 32) *(LAS u32x4*)(smem + (L + (L < p1 ? wb0 : L < p2 ? wb1 : wb2)) + lane * 16) = wreg[i]; }
    lds_inc0(smem + N_WO + hp * 4, lane);
    if (qo == 0) { poll_flags((GAS u64*)(ws + WS_FO + (((me & ~7) + (lane & 7)) * 2 + hp) * 128), lane < 8, tag); lds_set(smem + N_OK + hp * 4, 1); }
    else wait_word(smem + N_OK + hp * 4, 1);
    f32x16 oc[3]; f32x4 ps[10];
    LAS char* red = smem + OWN + qo * 10240 + lane * 16;
    { bf16x8 af[5], of[20];
      const int la = (8 * hl + 2 * ((lane & 15) >> 2)) * 160 + (lane & 16) * 2 + (lane & 3) * 8;
      const int aa0 = wb0 + la, aa1 = (hp ? wb1 : wb0) + la, aa2 = wb1 + la, aa3 = (hp ? wb1 : wb2) + la;
      auto rda = [&](int f) { return trfrag(smem + (f < 36 ? aa0 : f < 42 ? aa1 : f < 51 ? aa2 : aa3) + (f / 3) * 2560 + (f % 3) * 64, 160); };
      auto ldo = [&](int k) { of[k] = __builtin_bit_cast(bf16x8, __builtin_amdgcn_raw_buffer_load_b128(orr, qo * 40960 + (hp * 20 + k) * 1024 + lane * 16, 0, 16)); };
#pragma unroll
      for (int k = 0; k < OPF; ++k) ldo(k);
      wait_word(smem + N_WO + hp * 4, 4);
#pragma unroll
      for (int f = 0; f < 4; ++f) af[f] = rda(f);
      SB();
      SFOR(f, 60)
          if constexpr (f % 3 == 0 && f / 3 + OPF < 20) ldo(f / 3 + OPF);
          if constexpr (f + 4 < 60) af[(f + 4) % 5] = rda(f + 4);
          if constexpr (f == 57) { if (hp) { wait_word(smem + C_RD + qo * 4, 1);
#pragma unroll
              for (int b = 0; b < 10; ++b) ps[b] = *(LAS f32x4*)(red + b * 1024); } }
          SB();
          if constexpr (f < 3) MFMA32Z(oc[f], af[f % 5], of[0]); else MFMA32I(oc[f % 3], af[f % 5], of[f / 3]);
          SB();
      SEND }
    MFMA_DRAIN();
    { if (!hp) {
          wait_word(smem + N_G1, 8);
#pragma unroll
          for (int b = 0; b < 10; ++b) *(LAS f32x4*)(red + b * 1024) = (f32x4){oc[b >> 2][4 * (b & 3)], oc[b >> 2][4 * (b & 3) + 1], oc[b >> 2][4 * (b & 3) + 2], oc[b >> 2][4 * (b & 3) + 3]};
          lds_set(smem + C_RD + qo * 4, 1);
      } else {
          char* orow = out + (size_t)(rt * 128 + qo * 32 + r32) * 2560 + br * 1280 + hg * 160 + hl * 16;
#pragma unroll
          for (int b = 0; b < 10; ++b)
#pragma unroll
              for (int r = 0; r < 4; ++r) ps[b][r] += oc[b >> 2][4 * (b & 3) + r];
          SB();
          u32x2 pk[10];
#pragma unroll
          for (int b = 0; b < 10; ++b) pk[b] = (u32x2){pk2(ps[b][0], ps[b][1]), pk2(ps[b][2], ps[b][3])};
          SB();
          u32x4 st[5];
#pragma unroll
          for (int b = 0; b < 10; b += 2) {
              auto rx = __builtin_amdgcn_permlane32_swap(pk[b][0], pk[b + 1][0], false, false); auto ry = __builtin_amdgcn_permlane32_swap(pk[b][1], pk[b + 1][1], false, false);
              st[b >> 1] = (u32x4){rx[0], ry[0], rx[1], ry[1]}; }
          SB();
#pragma unroll
          for (int b = 0; b < 5; ++b) *(u32x4*)(orow + b * 32) = st[b];
      } }
    if (bx == 0 && wid == 0) {
#pragma unroll 1
        for (int i = 0; i < 4; ++i) poll_flags((GAS u64*)(ws + WS_FO + (i * 64 + lane) * 256), true, tag);
        if (lane == 0) __hip_atomic_store((GAS u64*)ws, tag, RLX_AGENT);
    }
    asm volatile(".Lfused_attn_end:");
}

extern "C" void kernel_launch(void* const* d_in, const int* in_sizes, int n_in, void* d_out, int out_size, void* d_ws, size_t ws_size,
                              hipStream_t stream) {
    fused_attn_kernel<<<256, 512, 0, stream>>>((const char*)d_in[0], (const char*)d_in[1], (const char*)d_in[2], (const char*)d_in[3],
                                               (const char*)d_in[4], (const char*)d_in[5], (char*)d_out, (char*)d_ws);
}
```

```cpp
#include <hip/hip_runtime.h>
#include <hip/hip_bf16.h>
#include <stdint.h>

typedef short bf16x8 __attribute__((ext_vector_type(8)));
typedef short s16x4 __attribute__((ext_vector_type(4)));
typedef float f32x2 __attribute__((ext_vector_type(2)));
typedef float f32x4 __attribute__((ext_vector_type(4)));
typedef float f32x16 __attribute__((ext_vector_type(16)));
typedef unsigned u32x4 __attribute__((ext_vector_type(4)));
typedef unsigned u32x2 __attribute__((ext_vector_type(2)));
typedef __bf16 bfx2 __attribute__((ext_vector_type(2)));
typedef unsigned long long u64;
#define LAS __attribute__((address_space(3)))
#define GAS __attribute__((address_space(1)))
#define DI __device__ __forceinline__
#define RLX_AGENT __ATOMIC_RELAXED, __HIP_MEMORY_SCOPE_AGENT
#define RLX_WG __ATOMIC_RELAXED, __HIP_MEMORY_SCOPE_WORKGROUP

constexpr int LDS_BYTES = 163840;
constexpr int VOFF = 20480, VP = 168;
constexpr int KVB = VOFF + 128 * VP;
constexpr int KV0 = LDS_BYTES - 3 * KVB;
constexpr int OWN = KV0 + 2 * KVB;
constexpr int C_F = 0, C_P = 128, C_E = 144, C_QF = 160, C_RD = 176, C_CNT = 192, C_TAG = 320;
constexpr int N_PUB = C_CNT, N_NB = C_CNT + 4, N_G0 = C_CNT + 8, N_G1 = C_CNT + 12, N_WO = C_CNT + 16, N_OS = C_CNT + 24, N_KV = C_CNT + 32, N_OK = C_CNT + 36;
constexpr int XS0 = 512, WS0 = XS0 + 3 * 16384, QST = 512, ONE = 336, QU27 = XS0 + 16384, QU28 = XS0, QU29 = QU27 + 10240;
static_assert(WS0 + 7 * 10240 <= OWN, "ring overlaps the own image");
constexpr unsigned WS_FKV = 4096, WS_FO = WS_FKV + 256 * 128, WS_KV = 131072;
constexpr unsigned KVG = KVB, OG = 4 * 40960;
constexpr unsigned WS_O = WS_KV + 256 * KVG;
constexpr unsigned WS_END = WS_O + 32 * OG;
constexpr unsigned SPIN_CAP = 400000, LSPIN_CAP = 4000000;
constexpr float QSCALE = 0.22360679774997896f * 1.4426950408889634f;
constexpr int WIN = 4;
constexpr int OPF = 12;
constexpr int PFLIP = 6;
constexpr int ADELAY = 3;

#define MFMA32(a, b, c) __builtin_amdgcn_mfma_f32_32x32x16_bf16((a), (b), (c), 0, 0, 0)
#define MFMA32K8(a, b, c) __builtin_amdgcn_mfma_f32_32x32x8bf16_1k((a), (b), (c), 0, 0, 0)
#define MFMA32I(acc, a, b) asm volatile("v_mfma_f32_32x32x16_bf16 %0, %1, %2, %0" : "+v"(acc) : "v"(a), "v"(b))
#define MFMA32Z(acc, a, b) asm volatile("v_mfma_f32_32x32x16_bf16 %0, %1, %2, 0" : "=&v"(acc) : "v"(a), "v"(b))
#define MFMA_DRAIN() asm volatile("s_nop 7\n\ts_nop 4" ::: "memory")
#define WAITV(N) asm volatile("s_waitcnt vmcnt(" #N ")" ::: "memory")
#define BAR_L() asm volatile("s_waitcnt lgkmcnt(0)\n\ts_barrier" ::: "memory")
#define SB() __builtin_amdgcn_sched_barrier(0)
#define FENCE() asm volatile("" ::: "memory")


template <int V> struct IC { static constexpr int value = V; };
template <int I, int N, class F> DI void static_for(F&& f) { if constexpr (I < N) { f(IC<I>{}); static_for<I + 1, N>(f); } }
#define SFOR(var, N) static_for<0, N>([&](auto ic_##var) __attribute__((always_inline)) { constexpr int var = decltype(ic_##var)::value;
#define SEND });

DI void dma16(unsigned voff, const char* sbase, unsigned ldsaddr) {
    asm volatile("s_mov_b32 m0, %2\n\ts_nop 0\n\tglobal_load_lds_dwordx4 %0, %1" :: "v"(voff), "s"(sbase), "s"(ldsaddr) : "memory");
}
DI void dma16x4(unsigned v0, unsigned v1, unsigned v2, unsigned v3, const char* sbase, unsigned ldsaddr) {
    asm volatile("s_mov_b32 m0, %5\n\ts_nop 0\n\tglobal_load_lds_dwordx4 %0, %4\n\tglobal_load_lds_dwordx4 %1, %4 offset:1024\n\t"
                 "global_load_lds_dwordx4 %2, %4 offset:2048\n\tglobal_load_lds_dwordx4 %3, %4 offset:3072"
                 :: "v"(v0), "v"(v1), "v"(v2), "v"(v3), "s"(sbase), "s"(ldsaddr) : "memory");
}
DI void dma16x5(const unsigned (&v)[5], const char* sbase, unsigned ldsaddr) {
    asm volatile("s_mov_b32 m0, %6\n\ts_nop 0\n\tglobal_load_lds_dwordx4 %0, %5 offset:-2048\n\tglobal_load_lds_dwordx4 %1, %5 offset:-1024\n\t"
                 "global_load_lds_dwordx4 %2, %5\n\tglobal_load_lds_dwordx4 %3, %5 offset:1024\n\tglobal_load_lds_dwordx4 %4, %5 offset:2048"
                 :: "v"(v[0]), "v"(v[1]), "v"(v[2]), "v"(v[3]), "v"(v[4]), "s"(sbase), "s"(ldsaddr) : "memory");
}
DI void dma16x5_sc1(unsigned voff, const char* sbase, unsigned ldsaddr) {
    asm volatile("s_mov_b32 m0, %2\n\ts_nop 0\n\tglobal_load_lds_dwordx4 %0, %1 offset:-2048 sc1\n\tglobal_load_lds_dwordx4 %0, %1 offset:-1024 sc1\n\t"
                 "global_load_lds_dwordx4 %0, %1 sc1\n\tglobal_load_lds_dwordx4 %0, %1 offset:1024 sc1\n\tglobal_load_lds_dwordx4 %0, %1 offset:2048 sc1"
                 :: "v"(voff), "s"(sbase), "s"(ldsaddr) : "memory");
}
DI void dma16_sc1(unsigned voff, const char* sbase, unsigned ldsaddr) {
    asm volatile("s_mov_b32 m0, %2\n\ts_nop 0\n\tglobal_load_lds_dwordx4 %0, %1 sc1" :: "v"(voff), "s"(sbase), "s"(ldsaddr) : "memory");
}
DI void dma4(const char* p, unsigned ldsaddr) {
    asm volatile("s_mov_b32 m0, %1\n\ts_nop 0\n\tglobal_load_lds_dword %0, off" :: "v"(p), "s"(ldsaddr) : "memory");
}
DI unsigned pk2(float lo, float hi) { f32x2 v = {lo, hi}; return __builtin_bit_cast(unsigned, __builtin_convertvector(v, bfx2)); }
typedef short v4i16_t __attribute__((ext_vector_type(4)));
DI bf16x8 trfrag(LAS char* p, int second) {
    s16x4 a = __builtin_bit_cast(s16x4, __builtin_amdgcn_ds_read_tr16_b64_v4i16((LAS v4i16_t*)p));
    s16x4 b = __builtin_bit_cast(s16x4, __builtin_amdgcn_ds_read_tr16_b64_v4i16((LAS v4i16_t*)(p + second)));
    return __builtin_shufflevector(a, b, 0, 1, 2, 3, 4, 5, 6, 7);
}
constexpr u64 lanes_cut(int r, bool fartile) {
    u64 m = 0;
    for (int l = 0; l < 64; ++l) { const int key = (r & 3) + 8 * (r >> 2) + 4 * (l >> 5), d = (l & 31) - key + (fartile ? 32 : 0);
        const bool ok = d >= 0 && d <= 14 && !(d & 1);
        if (!ok) m |= 1ull << l; }
    return m;
}
DI float halfmax(float m) { auto rr = __builtin_amdgcn_permlane32_swap(__float_as_uint(m), __float_as_uint(m), false, false);
    return __builtin_elementwise_maximum(__uint_as_float(rr[0]), __uint_as_float(rr[1])); }

DI void poll_flags(GAS u64* word, bool active, u64 tag) {
#pragma unroll 1
    for (unsigned spins = 0; spins < SPIN_CAP; ++spins) {
        u64 v = tag;
        if (active) v = __hip_atomic_load(word, RLX_AGENT);
        if (__all(v == tag)) break;
        __builtin_amdgcn_s_sleep(2);
    }
    __builtin_amdgcn_fence(__ATOMIC_ACQUIRE, "wavefront");
}
DI int min4(LAS char* p) {
    const u64 a = __hip_atomic_load((LAS u64*)p, RLX_WG), b = __hip_atomic_load((LAS u64*)(p + 8), RLX_WG);
    const unsigned m = min(min((unsigned)a, (unsigned)(a >> 32)), min((unsigned)b, (unsigned)(b >> 32)));
    return __builtin_amdgcn_readfirstlane((int)m);
}
DI int wait_min4(LAS char* p, int need) {
    int v = min4(p);
#pragma unroll 1
    for (unsigned spins = 0; v < need && spins < LSPIN_CAP; ++spins) { __builtin_amdgcn_s_sleep(1); v = min4(p); }
    FENCE();
    return v;
}
DI void wait_word(LAS char* p, unsigned need) {
#pragma unroll 1
    for (unsigned spins = 0; spins < LSPIN_CAP; ++spins) {
        if ((unsigned)__builtin_amdgcn_readfirstlane((int)__hip_atomic_load((LAS unsigned*)p, RLX_WG)) >= need) break;
        __builtin_amdgcn_s_sleep(1); }
    FENCE();
}
DI void lds_inc0(LAS char* p, int lane) { FENCE(); if (lane == 0) (void)__hip_atomic_fetch_add((LAS unsigned*)p, 1u, RLX_WG); FENCE(); }
DI unsigned lds_inc(LAS char* p, int lane) { FENCE(); unsigned old = 0; if (lane == 0) old = __hip_atomic_fetch_add((LAS unsigned*)p, 1u, RLX_WG); FENCE(); return old; }
DI void lds_set(LAS char* p, unsigned v) { FENCE(); __hip_atomic_store((LAS unsigned*)p, v, RLX_WG); FENCE(); }

__global__ __launch_bounds__(512, 2) void fused_attn_kernel(const char* __restrict__ xA, const char* __restrict__ wA, const char* __restrict__ oA,
                                                           const char* __restrict__ xB, const char* __restrict__ wB, const char* __restrict__ oB,
                                                           char* __restrict__ out, char* __restrict__ ws) {
    __shared__ __attribute__((aligned(16))) char smem_[LDS_BYTES];
    LAS char* smem = (LAS char*)smem_;
    const unsigned lds0 = (unsigned)(uintptr_t)smem;
    asm volatile("" :: "s"(xA), "s"(wA), "s"(oA), "s"(xB), "s"(wB), "s"(oB), "s"(out), "s"(ws));
    const int tid = threadIdx.x, lane = tid & 63;
    const int wid = __builtin_amdgcn_readfirstlane(tid >> 6);
    const int bx = blockIdx.x, xcd = bx & 7, idx = bx >> 3;
    const int isA = idx >> 4, rt = (xcd >> 1) * 4 + ((idx >> 2) & 3), hg = (xcd & 1) * 4 + (idx & 3);
    const int br = isA ? 0 : 1;
    { u64 pc; unsigned len;
      asm volatile("s_getpc_b64 %0\n.Lfused_attn_pc:\n\ts_mov_b32 %1, .Lfused_attn_end-.Lfused_attn_pc" : "=s"(pc), "=s"(len));
      if (wid == 3 && (idx & 15) * 8192u < len) dma4((const char*)pc + min(((idx & 15) * 64 + lane) * 128u, len - 4u), lds0 + OWN + 3 * 512); }
    const int k0 = isA ? (((rt & 3) * 5 + 1) / 2 + ((hg & 3) * 5 + 1) / 2) % 10 : 0;
    const char* xb = (isA ? xA : xB) + (size_t)rt * (128 * 1280);
    const char* wb = (isA ? wA : wB) + hg * 160;
    const int me = (br * 16 + rt) * 8 + hg;
    const int r32 = lane & 31, hl = lane >> 5;
    auto kt_of = [&](int s) { int k = s + k0; return k >= 10 ? k - 10 : k; };
    if (isA && wid < 4) {
        const int j16 = idx & 15; const unsigned dump = lds0 + OWN + wid * 512;
        const char* wl = wB + (hg >> 2) * 640 + j16 * (4 * 3840);
        const char* xl = xB + (size_t)(rt * 128 + (hg & 3) * 32 + r32) * 1280 + hl * 128;
        auto pfx = [&](int i) { return xl + i * 256; };
        auto pfkv = [&](int i) { int n = i * 64 + lane; n = n < 400 ? n : 399; const int sl = n / 40, r = n % 40, c = r % 10;
            return wl + (sl * 64 + r / 10) * 3840 + 1280 + (c < 5 ? c * 128 : 640 + c * 128); };
        auto pfq = [&](int i) { int n = i * 64 + lane; n = n < 200 ? n : 199; int sl = n / 20 + 8; sl = sl >= 10 ? sl - 10 : sl; const int r = n % 20;
            return wl + (sl * 64 + r / 5) * 3840 + (r % 5) * 128; };
        dma4(wid & 1 ? pfx(wid >> 1) : pfkv(wid >> 1), dump);
        dma4(wid & 1 ? pfx(2 + (wid >> 1)) : pfkv(2 + (wid >> 1)), dump);
        dma4(wid < 3 ? pfkv(4 + wid) : pfx(4), dump);
        dma4(pfq(wid), dump);
        { int n = wid * 64 + lane; n = n < 200 ? n : 199; const int o = (j16 * 40 + n / 5) * 1280 + (hg >> 2) * 640 + (n % 5) * 128;
          dma4(oB + o, dump); dma4(oA + o, dump); }
    }
    if (isA && wid >= 4) { for (int i = 0; i < ADELAY; ++i) __builtin_amdgcn_s_sleep(16); }

    const unsigned xo = (unsigned)((lane >> 3) * 1280 + (((lane & 7) ^ (lane >> 4)) << 4));
    auto w_off = [&](int q) { const int c = q * 64 + lane, row = __umul24(c, 3277) >> 16, j = c - row * 20; return (unsigned)(__umul24(row, 3840) + j * 16 + (j < 10 ? 1280 : 2400)); };
    if (!isA) { const char* xs = xb + k0 * 128 + wid * (16 * 1280);
      dma16(xo, xs, lds0 + XS0 + wid * 2048);
      dma16(xo ^ 64, xs + 8 * 1280, lds0 + XS0 + wid * 2048 + 1024);
      const char* wsrc = wb + (size_t)k0 * (64 * 3840);
      const unsigned wv = w_off(wid < 5 ? wid : wid - 5);
      if (wid < 5) { dma16(wv, wsrc, lds0 + WS0 + wid * 1024); dma16(wv, wsrc + 16 * 3840, lds0 + WS0 + (5 + wid) * 1024);
          if (wid >= 3) { dma16(wv, wsrc + 32 * 3840, lds0 + WS0 + (10 + wid) * 1024); dma16(wv, wsrc + 48 * 3840, lds0 + WS0 + (15 + wid) * 1024); } }
      else { dma16(wv, wsrc + 32 * 3840, lds0 + WS0 + (5 + wid) * 1024); dma16(wv, wsrc + 48 * 3840, lds0 + WS0 + (10 + wid) * 1024); } }
    if (wid == 4) { const char* pe = ws + (lane & 1) * 4; if (lane < 2) dma4(pe, lds0 + C_TAG); }
    if (tid < 80) *(LAS unsigned*)(smem + tid * 4) = 0u;
    else if (tid < 82) *(LAS unsigned*)(smem + ONE + (tid - 80) * 4) = 0x3F803F80u;
    const int nnb = isA ? (rt > 0 ? 1 : 0) : (rt > 1 ? 2 : rt);
    auto kq_base = [&](int fg) { const int hd = fg / 5, dg = fg % 5; return dg < 4 ? hd * 5120 + (dg >> 1) * 2048 + (dg & 1) * 8 : hd * 5120 + 4096; };

    auto rdtag = [&]() { const u64 t = *(LAS u64*)(smem + C_TAG);
        const u64 e = ((u64)(unsigned)__builtin_amdgcn_readfirstlane((int)(t >> 32)) << 32) | (unsigned)__builtin_amdgcn_readfirstlane((int)t);
        return e * 6364136223846793005ull + 1442695040888963407ull; };
    const __amdgpu_buffer_rsrc_t wor = __builtin_amdgcn_make_buffer_rsrc((void*)(isA ? oA : oB), 0, 640 * 1280, 0x00020000);

    const int hp = wid >> 2, qo = wid & 3, qi = hp ? 3 - qo : qo;
    const int ud = qi + 8;
    const bool far = isA ? (4 * rt + qi >= 1) : (4 * rt + qi >= 8);
    const int u0 = isA ? (far ? ud - 1 : ud) : (far ? qi : 8 - 4 * rt);
    f32x16 cd, cf;
    int dsh, kb1, kb2, nh, vb0, vb1, qb1, qb2; unsigned obase, oo[5];
    auto mask_b = [&](f32x16& c, int r, bool fartile) { const int k = (r & 3) + 8 * (r >> 2); c[r] = __int_as_float(fartile ? ((k - 1) << 23) - dsh : dsh - (k << 23)); };
    auto mask_a = [&](int z) {
      SFOR(r, 16)
          constexpr u64 ad = lanes_cut(r, false), af = lanes_cut(r, true);
          cd[r] = __builtin_amdgcn_inverse_ballot_w64(ad + z) ? -__builtin_inff() : 0.f;
          cf[r] = __builtin_amdgcn_inverse_ballot_w64(af + z) ? -__builtin_inff() : 0.f;
      SEND };
    auto att_setup = [&](int part) {
        int l = tid; asm volatile("" : "+v"(l)); l &= 63;
        const int q = l & 31, h = l >> 5;
        if (part == 0) { dsh = (q - 4 * h) << 23; kb1 = KV0 + hp * 10240 + h * 2048 + q * 16; kb2 = h ? ONE : KV0 + hp * 10240 + 4096 + q * 8; nh = h ^ 1;
            asm volatile("" : "+v"(dsh), "+v"(kb1), "+v"(kb2), "+v"(nh)); }
        if (part == 1) { const bool ones = (l & 16) && (l & 3);
            vb0 = KV0 + VOFF + (4 * h + ((l & 15) >> 2)) * VP + (ones ? 160 : hp * 80 + (l & 16) * 2 + (l & 3) * 8); vb1 = vb0 + (ones ? 0 : 40);
            asm volatile("" : "+v"(vb0), "+v"(vb1)); }
        if (part == 2) { qb1 = QST + hp * 10240 + h * 2048 + (qi * 32 + q) * 16; qb2 = QST + hp * 10240 + 4096 + (qi * 32 + q) * 8;
            obase = (unsigned)(qi * 40960 + hp * 20480 + hg * 2560 + q * 16 + h * 512);
            asm volatile("" : "+v"(qb1), "+v"(qb2), "+v"(obase)); }
        if (part >= 3) {
#pragma unroll
            for (int r = 4 * (part - 3); r < 4 * (part - 2); ++r) mask_b(cd, r, false);
            asm volatile("" : "+v"(cd)); } };
    auto wo_setup = [&](int q) {
        int l = tid; asm volatile("" : "+v"(l)); const int c = q * 64 + (l & 63), rho = __umul24(c, 6554) >> 16;
        oo[q] = (unsigned)(__umul24(qo * 160 + hp * 40 + (rho & ~7) + 4 * (rho & 1) + ((rho >> 1) & 3), 1280) + hg * 160 + (c - rho * 10) * 16);
        asm volatile("" : "+v"(oo[q])); };
    u64 tag;

    if (wid < 4) {
        if (!isA) WAITV(0);
        BAR_L();
        bf16x8 xr[32], xt[2], wf[WIN + 1];
        f32x16 acc[5];
        const int tok = wid * 32 + r32;
        const int xa = XS0 + tok * 128 + ((hl ^ ((tok >> 1) & 7)) << 4);
        int wa0 = WS0 + (8 * hl + ((lane & 15) >> 2)) * 320 + (lane & 16) * 2 + (lane & 3) * 8, wa1 = wa0 + 61440;
        asm volatile("" : "+v"(wa0), "+v"(wa1));
        auto rdx = [&](int i) { return *(LAS bf16x8*)(smem + ((i >> 2) % 3) * 16384 + (xa ^ ((i & 3) * 32))); };
        auto rdw = [&](int f) { const int i = f / 5, t = f % 5, sl = (i >> 1) % 7;
            return trfrag(smem + (sl < 6 ? wa0 + sl * 10240 : wa1) + (i & 1) * 5120 + t * 64, 1280); };
        unsigned fl = 0;
        auto rdflag = [&](int u) { fl = __hip_atomic_load((LAS unsigned*)(smem + C_F + u * 4), RLX_WG); };
        auto check = [&](int u) {
            if (__builtin_amdgcn_readfirstlane(fl) != 15) {
#pragma unroll 1
                for (unsigned spins = 0; spins < LSPIN_CAP; ++spins) { rdflag(u); if (__builtin_amdgcn_readfirstlane(fl) == 15) break; __builtin_amdgcn_s_sleep(1); }
            }
            FENCE(); };
#pragma unroll
        for (int i = 0; i < 4; ++i) xr[i] = rdx(i);
#pragma unroll
        for (int f = 0; f < WIN; ++f) wf[f] = rdw(f);
        SB();
        SFOR(f, 200)
            constexpr int i = f / 5, t = f % 5;
            if constexpr (f % 20 == 10 - WIN) lds_set(smem + C_P + wid * 4, (i >> 1) + 1);
            if constexpr (f == 200 - WIN) lds_set(smem + C_P + wid * 4, 20);
            if constexpr (f % 20 == 10) rdflag(f / 20 + 1);
            if constexpr (f % 20 == 20 - WIN && f < 190) check(f / 20 + 1);
            if constexpr (f + WIN < 200) wf[(f + WIN) % (WIN + 1)] = rdw(f + WIN);
            if constexpr (f % 20 >= 16 && f < 140) xr[(f / 20 + 1) * 4 + f % 20 - 16] = rdx((f / 20 + 1) * 4 + f % 20 - 16);
            if constexpr (t == 1 && i >= 31 && i < 39) xt[(i + 1) & 1] = rdx(i + 1);
            SB();
            if constexpr (i == 0) MFMA32Z(acc[t], wf[f % (WIN + 1)], xr[0]);
            else if constexpr (i < 32) MFMA32I(acc[t], wf[f % (WIN + 1)], xr[i]);
            else MFMA32I(acc[t], wf[f % (WIN + 1)], xt[i & 1]);
            SB();
        SEND
        int l2 = tid; asm volatile("" : "+v"(l2)); l2 &= 63;
        const int h2 = l2 >> 5, tk = wid * 32 + (l2 & 31);
        int eb[5];
        auto ebases = [&](int base) { int l = tid; asm volatile("" : "+v"(l)); const int h = (l >> 5) & 1, k = wid * 32 + (l & 31);
            eb[0] = base + k * 16 + h * 8; eb[1] = base + k * 16 + h * 2040; eb[2] = base + (h ? k * 16 + 1024 : k * 8); eb[3] = base + (h ? k * 8 + 2040 : k * 16);
            asm volatile("" : "+v"(eb[0]), "+v"(eb[1]), "+v"(eb[2]), "+v"(eb[3])); };
        auto kq_addr = [&](int g0) { return eb[g0 % 5 == 1 ? 1 : g0 % 5 == 4 ? 2 : g0 % 5 == 3 ? 3 : 0] + kq_base(g0); };
        ebases(OWN);
        eb[4] = OWN + VOFF + tk * VP + h2 * 8; asm volatile("" : "+v"(eb[4]));
        int qa0 = (8 * h2 + 2 * ((l2 & 15) >> 2)) * 160 + (l2 & 16) * 2 + (l2 & 3) * 8, qa1 = qa0 + WS0 + 10240;
        asm volatile("" : "+v"(qa0), "+v"(qa1));
        auto rdq = [&](int f) { const int i = f / 3, t = f % 3, j = i >> 2, sl = (20 + j) % 7;
            return trfrag(smem + (j >= 7 ? qa0 + (j == 7 ? QU27 : j == 8 ? QU28 : QU29) : sl == 0 ? qa0 + WS0 : qa1 + (sl - 1) * 10240) + (i & 3) * 2560 + t * 64, 160); };
        MFMA_DRAIN();
        WAITV(0);
        { u32x2 pk[20];
#pragma unroll
          for (int n = 0; n < 20; ++n) { const int t = n >> 2, b = n & 3; pk[n] = (u32x2){pk2(acc[t][4 * b], acc[t][4 * b + 1]), pk2(acc[t][4 * b + 2], acc[t][4 * b + 3])}; }
          SB();
#pragma unroll
          for (int n = 0; n < 20; ++n) *(LAS u32x2*)(smem + (n < 10 ? kq_addr(2 * n) : eb[4] + (2 * n - 20) * 8)) = pk[n];
          if (h2 == 0) *(LAS u32x2*)(smem + eb[4] + 160) = (u32x2){0x3F803F80u, 0x3F803F80u}; }
        lds_set(smem + C_E + wid * 4, 1);
        check(10);
        xt[0] = rdx(32);
#pragma unroll
        for (int f = 0; f < WIN; ++f) wf[f] = rdq(f);
        SB();
        u64 pv0 = 0, pv1 = 0;
        SFOR(f, 120)
            constexpr int i = f / 3, t = f % 3, j = i >> 2;
            if constexpr (f % 24 == 12 && j < 9) rdflag(11 + j / 2);
            if constexpr (f % 12 == 12 - WIN && (j == 1 || j == 6 || j == 8)) lds_set(smem + C_P + wid * 4, 21 + j);
            if constexpr (f % 24 == 24 - WIN && j < 9) check(11 + j / 2);
            if constexpr (f + WIN < 120) wf[(f + WIN) % (WIN + 1)] = rdq(f + WIN);
            if constexpr (t == 0 && i < 7) xt[(i + 1) & 1] = rdx(32 + i + 1);
            if constexpr (f >= 90 && f < 100 && !(f & 1)) wo_setup((f - 90) / 2);
            if constexpr (f == 99) tag = rdtag();
            if constexpr (f >= 100 && f < 107) att_setup(f - 100);
            if constexpr (f == 108) ebases(QST);
            if constexpr (f == 116) { pv0 = __hip_atomic_load((LAS u64*)(smem + C_P), RLX_WG); pv1 = __hip_atomic_load((LAS u64*)(smem + C_P + 8), RLX_WG); }
            SB();
            if constexpr (i == 0) MFMA32Z(acc[t], wf[f % (WIN + 1)], xt[0]);
            else if constexpr (i < 8) MFMA32I(acc[t], wf[f % (WIN + 1)], xt[i & 1]);
            else MFMA32I(acc[t], wf[f % (WIN + 1)], xr[i - 8]);
            SB();
        SEND
        MFMA_DRAIN();
        SB();
        if (__builtin_amdgcn_readfirstlane((int)min(min((unsigned)pv0, (unsigned)(pv0 >> 32)), min((unsigned)pv1, (unsigned)(pv1 >> 32)))) < 29) wait_min4(smem + C_P, 29);
        { acc[0] *= QSCALE; acc[1] *= QSCALE; acc[2] *= QSCALE;
          SB();
          u32x2 pk[10];
#pragma unroll
          for (int n = 0; n < 10; ++n) { const int t = n >> 2, b = n & 3; pk[n] = (u32x2){pk2(acc[t][4 * b], acc[t][4 * b + 1]), pk2(acc[t][4 * b + 2], acc[t][4 * b + 3])}; }
          SB();
#pragma unroll
          for (int n = 0; n < 10; ++n) *(LAS u32x2*)(smem + kq_addr(2 * n)) = pk[n]; }
        lds_set(smem + C_QF + wid * 4, 1);
        __builtin_amdgcn_s_setprio(1);
    } else {
        const int hw = wid - 4;
        int known = 0;
        __builtin_amdgcn_s_setprio(3);
        auto lds_or = [&](int off, unsigned v) { FENCE(); if (lane == 0) __hip_atomic_fetch_or((LAS unsigned*)(smem + off), v, RLX_WG); FENCE(); };
        unsigned wo[5], qo[5];
        auto done = [&](int n) { lds_or(C_F + n * 4, 1u << hw); };
        const unsigned xo1 = (xo ^ 64) + 9216, xo2 = xo + 2 * 9216, xo3 = (xo ^ 64) + 3 * 9216;
        auto round_x = [&](int n) { dma16x4(xo, xo1, xo2, xo3, xb + kt_of(n) * 128 + hw * (32 * 1280), lds0 + XS0 + (n % 3) * 16384 + hw * 4096); };
        auto round_w = [&](int n) {
            const int u = 2 * n + (hw >> 1);
            const unsigned dst = lds0 + (u < 27 ? WS0 + (u % 7) * 10240 : u == 27 ? QU27 : u == 28 ? QU28 : QU29) + (hw & 1) * 5120 + 2048;
            if (u < 20) dma16x5(wo, wb + (size_t)(kt_of(u >> 1) * 64 + (u & 1) * 32) * 3840 - 2048, dst);
            else dma16x5(qo, wb + (size_t)kt_of(u < 22 ? u - 12 : u - 22) * (64 * 3840) - 2048, dst); };
        if (isA) round_x(0);
        round_x(1);
#pragma unroll
        for (int q = 0; q < 5; ++q) wo[q] = w_off(q) + (hw & 1) * (16 * 3840) + (4 - q) * 1024;
        if (isA) { round_w(0); round_w(1); WAITV(5); } else { round_w(1); WAITV(9); }
        BAR_L();
#pragma unroll
        for (int q = 0; q < 5; ++q) { const int c = q * 64 + lane, rho = __umul24(c, 6554) >> 16;
            qo[q] = (unsigned)(__umul24((hw & 1) * 32 + (rho & ~7) + 4 * (rho & 1) + ((rho >> 1) & 3), 3840) + (c - rho * 10) * 16 + (4 - q) * 1024); }
#pragma unroll 1
        for (int n = 2; n <= 13; ++n) {
            const int need = n < 13 ? 2 * n - 5 : 20;
            if (need > known) known = min4(smem + C_P);
            if (need > known) { WAITV(0); done(n - 1); known = wait_min4(smem + C_P, need); }
            if (n < 10) round_x(n);
            round_w(n);
            if (n < 10) WAITV(9); else WAITV(5);
            done(n - 1);
        }
        WAITV(0); done(13);
        tag = rdtag();
        auto a_chunk = [&](int a) { int o;
            if (a < 320) { const int hd = a / 80, rem = a % 80; o = hd * 5120 + (rem < 32 ? (96 + rem) * 16 : rem < 64 ? 2048 + (64 + rem) * 16 : 4096 + 768 + (rem - 64) * 16); }
            else o = VOFF + 96 * VP + (a - 320) * 16;
            return o; };
        if (rt < 15) {
            const __amdgpu_buffer_rsrc_t kvr = __builtin_amdgcn_make_buffer_rsrc(ws + WS_KV + me * KVG, 0, KVB, 0x00020000);
            wait_min4(smem + C_E, 1);
            if (!isA) { u32x4 v[11];
#pragma unroll
                for (int j = 0; j < 11; ++j) if (j < 10 || hw == 3) v[j] = *(LAS u32x4*)(smem + OWN + hw * 10240 + j * 1024 + lane * 16);
#pragma unroll
                for (int j = 0; j < 11; ++j) if (j < 10 || hw == 3) __builtin_amdgcn_raw_buffer_store_b128(v[j], kvr, hw * 10240 + j * 1024 + lane * 16, 0, 16);
            } else {
#pragma unroll
                for (int j = 0; j < 3; ++j) { const int a = (4 * j + hw) * 64 + lane;
                    if (a < 656) { const int o = a_chunk(a); __builtin_amdgcn_raw_buffer_store_b128(*(LAS u32x4*)(smem + OWN + o), kvr, o, 0, 16); } }
            }
        }
        wait_min4(smem + C_P, 22);
        round_w(14);
        WAITV(0);
        if (rt < 15 && lds_inc(smem + N_PUB, lane) == 3 && lane == 0) __hip_atomic_store((GAS u64*)(ws + WS_FKV + me * 128), tag, RLX_AGENT);
        done(14);
        __builtin_amdgcn_s_setprio(0);
#pragma unroll
        for (int p = 0; p < 7; ++p) att_setup(p);
#pragma unroll
        for (int q = 0; q < 5; ++q) wo_setup(q);
        __builtin_amdgcn_s_setprio(3);
        if (hw == 0) { poll_flags((GAS u64*)(ws + WS_FKV + (me - 8 * (lane + 1)) * 128), lane < nnb, tag); lds_set(smem + N_KV, 1); }
        else wait_word(smem + N_KV, 1);
        if (!isA) {
            const int n = hw >> 1, half = hw & 1;
            if (n < nnb) {
                const char* src = ws + WS_KV + (me - 8 * (n + 1)) * KVG + half * 20480; const unsigned dst = lds0 + KV0 + (1 - n) * KVB + half * 20480;
                wait_min4(smem + C_P, 27);
#pragma unroll
                for (int j = 0; j < 4; ++j) dma16x5_sc1(lane * 16, src + j * 5120 + 2048, dst + j * 5120 + 2048);
                if (half) dma16_sc1(lane * 16, src + 20480, dst + 20480);
            }
        } else if (nnb) {
            const __amdgpu_buffer_rsrc_t nbr = __builtin_amdgcn_make_buffer_rsrc(ws + WS_KV + (me - 8) * KVG, 0, KVB, 0x00020000);
            u32x4 v[3];
#pragma unroll
            for (int j = 0; j < 3; ++j) { const int a = (4 * j + hw) * 64 + lane; if (a < 656) v[j] = __builtin_amdgcn_raw_buffer_load_b128(nbr, a_chunk(a), 0, 16); }
            wait_min4(smem + C_P, 27);
#pragma unroll
            for (int j = 0; j < 3; ++j) { const int a = (4 * j + hw) * 64 + lane; if (a < 656) *(LAS u32x4*)(smem + KV0 + KVB + a_chunk(a)) = v[j]; }
        }
        __builtin_amdgcn_s_setprio(2);
        wait_word(smem + C_QF + qi * 4, 1);
    }
    u32x4 wreg[13];
    auto ldwo = [&](int i) { wreg[i] = __builtin_amdgcn_raw_buffer_load_b128(wor, oo[i % 5] + (i / 5) * (32 * 1280) + (i > 6 || (i == 6 && lane >= 16) ? 40 * 1280 : 0), 0, 0); };
    u32x2 ob[2][3];
    {
        bf16x8 q1[2]; u32x2 q2[2];
#pragma unroll
        for (int e = 0; e < 2; ++e) {
            q1[e] = *(LAS bf16x8*)(smem + qb1 + e * 5120);
            q2[e] = *(LAS u32x2*)(smem + qb2 + e * 5120);
            if (hl) q2[e] = (u32x2){0u, 0u};
        }
        if (isA) mask_a(0);
        struct KF { bf16x8 a; s16x4 b; };
        struct VF { bf16x8 a, b; };
        auto rdk = [&](int u, int e) { const int so = (u >> 2) * KVB, tw = u & 3; KF k;
            k.a = *(LAS bf16x8*)(smem + kb1 + so + tw * 512 + e * 5120); k.b = *(LAS s16x4*)(smem + __umul24(so + tw * 256 + e * 5120, nh) + kb2); return k; };
        auto rdv = [&](int u, int e) { LAS char* p = smem + (e ? vb1 : vb0) + (u >> 2) * KVB + (u & 3) * (32 * VP); VF v;
            v.a = trfrag(p, 8 * VP); v.b = trfrag(p + 16 * VP, 8 * VP); return v; };
        auto qk = [&](const KF& k, int e, const f32x16& c) { return MFMA32K8(k.b, __builtin_bit_cast(s16x4, q2[e]), MFMA32(k.a, q1[e], c)); };
        const f32x16 zero = {};
        auto max16 = [&](const f32x16& s, float m) {
#pragma unroll
            for (int r = 0; r < 16; ++r) m = __builtin_elementwise_maximum(m, s[r]);
            return m; };
        auto set_ref = [&](float m, int e) {
            const unsigned t0 = pk2(-m, 0.f); const float r1 = -m - __uint_as_float(t0 << 16);
            const unsigned t1 = pk2(r1, 0.f); const float r2 = r1 - __uint_as_float(t1 << 16);
            if (hl) q2[e] = (u32x2){t0 | (t1 << 16), pk2(r2, 0.f)}; };
        auto ex4 = [&](f32x16& s, int b) {
#pragma unroll
            for (int r = 0; r < 4; ++r) s[b + r] = __builtin_amdgcn_exp2f(s[b + r]); };
        auto pk8 = [&](const f32x16& s, int b) { return __builtin_bit_cast(bf16x8, (u32x4){pk2(s[b], s[b + 1]), pk2(s[b + 2], s[b + 3]), pk2(s[b + 4], s[b + 5]), pk2(s[b + 6], s[b + 7])}); };
        auto phase = [&](auto nx, f32x16& cur, const VF& v, f32x16& o, f32x16& nxt, const KF& k, int e) __attribute__((always_inline)) {
            constexpr int NX = decltype(nx)::value;
            SB(); ex4(cur, 0); SB();
            if constexpr (NX) nxt = MFMA32(k.a, q1[e], NX == 1 ? zero : NX == 2 ? cf : cd);
            SB(); ex4(cur, 4); { const bf16x8 p = pk8(cur, 0); SB(); o = MFMA32(v.a, p, o); }
            SB(); ex4(cur, 8); SB();
            if constexpr (NX) nxt = MFMA32K8(k.b, __builtin_bit_cast(s16x4, q2[e]), nxt);
            SB(); ex4(cur, 12); { const bf16x8 p = pk8(cur, 8); SB(); o = MFMA32(v.b, p, o); }
            SB(); };
        f32x16 o0, o1, sa, sb; KF kx; VF vx;
        auto step = [&](auto m0, auto m1, int u) __attribute__((always_inline)) {
            constexpr int M1 = decltype(m1)::value;
            KF ky; if constexpr (M1) ky = rdk(u - 1, 0);
            const VF vy = rdv(u, 1);
            phase(m0, sa, vx, o0, sb, kx, 1);
            if constexpr (M1) { kx = rdk(u - 1, 1); vx = rdv(u - 1, 0); }
            phase(m1, sb, vy, o1, sa, ky, 0); };
        auto nbwait = [&](int u) { if (u == 8) wait_word(smem + N_NB, 4); };
        { const KF k0f = rdk(ud, 0); kx = rdk(ud, 1); vx = rdv(ud, 0);
          const f32x16 s0 = qk(k0f, 0, cd), s1 = qk(kx, 1, cd);
          set_ref(halfmax(max16(s0, s0[0])), 0); set_ref(halfmax(max16(s1, s1[0])), 1);
          sa = qk(k0f, 0, cd); }
        if (hp) { WAITV(0); lds_inc0(smem + N_NB, lane); lds_inc0(smem + N_G0, lane); }
        o0 = zero; o1 = zero;
        if (u0 + 8 == ud) {
            SFOR(it, 9)
                if constexpr (it < 7) { ldwo(2 * it); if constexpr (it < 6) ldwo(2 * it + 1); }
                if constexpr (it == PFLIP) { if (hp) __builtin_amdgcn_s_setprio(0); }
                if constexpr (it == 7) {
#pragma unroll
                    for (int r = 0; r < 16; ++r) mask_b(cf, r, true); }
                if constexpr (it < 4) nbwait(ud - it);
                step(IC<it == 0 ? 3 : it == 8 ? 2 : 1>{}, IC<it == 8 ? 0 : it == 7 ? 2 : 1>{}, ud - it);
            SEND
        } else {
            __builtin_amdgcn_s_setprio(0);
#pragma unroll
            for (int i = 0; i < 13; ++i) ldwo(i);
            if (!isA) cf = zero;
            if (u0 < ud) {
                nbwait(ud);
                if (u0 + 1 < ud) {
                    step(IC<3>{}, IC<1>{}, ud);
#pragma unroll 1
                    for (int u = ud - 1; u > u0 + 1; --u) { nbwait(u); step(IC<1>{}, IC<1>{}, u); }
                    nbwait(u0 + 1);
                    step(IC<1>{}, IC<2>{}, u0 + 1);
                } else step(IC<3>{}, IC<2>{}, ud);
                step(IC<2>{}, IC<0>{}, u0);
            } else step(IC<3>{}, IC<0>{}, ud);
        }
        if (!__all(o0[12] < 1.152921504606847e18f && o1[12] < 1.152921504606847e18f && o0[12] > 0.25f && o1[12] > 0.25f)) {
            if (isA) { int z = 0; asm volatile("" : "+s"(z)); mask_a(z); }
            else { asm volatile("" : "+v"(dsh));
#pragma unroll
                for (int r = 0; r < 16; ++r) { mask_b(cd, r, false); mask_b(cf, r, true); }
                if (!far) cf = zero; }
#pragma unroll
            for (int e = 0; e < 2; ++e) {
                if (hl) q2[e] = (u32x2){0u, 0u};
                auto tile = [&](int u) { return u == ud ? qk(rdk(u, e), e, cd) : u == u0 ? qk(rdk(u, e), e, cf) : qk(rdk(u, e), e, zero); };
                float m = -__builtin_inff();
#pragma unroll 1
                for (int u = u0; u <= ud; ++u) m = max16(tile(u), m);
                m = halfmax(m);
                f32x16 o = zero;
#pragma unroll 1
                for (int u = u0; u <= ud; ++u) { f32x16 t = tile(u) - m;
                    const VF v = rdv(u, e); ex4(t, 0); ex4(t, 4); ex4(t, 8); ex4(t, 12);
                    o = MFMA32(v.a, pk8(t, 0), o); o = MFMA32(v.b, pk8(t, 8), o); }
                if (e) o1 = o; else o0 = o;
            }
        }
        const float rl0 = __builtin_amdgcn_rcpf(o0[12]), rl1 = __builtin_amdgcn_rcpf(o1[12]);
        SB();
#pragma unroll
        for (int r = 0; r < 12; ++r) { o0[r] *= rl0; o1[r] *= rl1; }
        SB();
#pragma unroll
        for (int j = 0; j < 3; ++j) {
            ob[0][j] = (u32x2){pk2(o0[4 * j], o0[4 * j + 1]), pk2(o0[4 * j + 2], o0[4 * j + 3])};
            ob[1][j] = (u32x2){pk2(o1[4 * j], o1[4 * j + 1]), pk2(o1[4 * j + 2], o1[4 * j + 3])}; }
        SB();
    }
    const __amdgpu_buffer_rsrc_t orr = __builtin_amdgcn_make_buffer_rsrc(ws + WS_O + (br * 16 + rt) * OG, 0, 4 * 40960, 0x00020000);
    {
      { auto rx = __builtin_amdgcn_permlane32_swap(ob[0][0][0], ob[0][1][0], false, false);
        auto ry = __builtin_amdgcn_permlane32_swap(ob[0][0][1], ob[0][1][1], false, false);
        __builtin_amdgcn_raw_buffer_store_b128((u32x4){rx[0], ry[0], rx[1], ry[1]}, orr, obase, 0, 16); }
      if (hl == 0) __builtin_amdgcn_raw_buffer_store_b128((u32x4){ob[0][2][0], ob[0][2][1], ob[1][0][0], ob[1][0][1]}, orr, obase + 1024, 0, 16);
      { auto fx = __builtin_amdgcn_permlane32_swap(ob[1][0][0], ob[1][1][0], false, false);
        auto fy = __builtin_amdgcn_permlane32_swap(ob[1][0][1], ob[1][1][1], false, false);
        auto sx = __builtin_amdgcn_permlane32_swap(ob[1][1][0], ob[1][2][0], false, false);
        auto sy = __builtin_amdgcn_permlane32_swap(ob[1][1][1], ob[1][2][1], false, false);
        __builtin_amdgcn_raw_buffer_store_b128((u32x4){fx[1], fy[1], sx[0], sy[0]}, orr, obase + 1536, 0, 16); } }
    if (lane == 0) { (void)__hip_atomic_fetch_add((LAS unsigned*)(smem + N_G1), 1u, RLX_WG);
        if (!hp) (void)__hip_atomic_fetch_add((LAS unsigned*)(smem + N_G0), 1u, RLX_WG); }
    FENCE();
    auto signal_o = [&]() {
        if (lds_inc(smem + N_OS + hp * 4, lane) == 3 && lane == 0) __hip_atomic_store((GAS u64*)(ws + WS_FO + (me * 2 + hp) * 128), tag, RLX_AGENT); };
    __builtin_amdgcn_s_setprio(0);
    WAITV(0); signal_o();
    wait_word(smem + (hp ? N_G1 : N_G0), 8);
    const int p1 = hp ? 30720 : 35840, p2 = hp ? 65536 : 43520;
    const int wb0 = hp ? KV0 + 10240 : 512, wb1 = hp ? KV0 + KVB + 10240 - 30720 : KV0 - 35840, wb2 = KV0 + KVB - 43520;
#pragma unroll
    for (int i = 0; i < 13; ++i) { const int L = qo * 12800 + i * 1024;
        if (i < 12 || lane < 32) *(LAS u32x4*)(smem + (L + (L < p1 ? wb0 : L < p2 ? wb1 : wb2)) + lane * 16) = wreg[i]; }
    lds_inc0(smem + N_WO + hp * 4, lane);
    if (qo == 0) { poll_flags((GAS u64*)(ws + WS_FO + (((me & ~7) + (lane & 7)) * 2 + hp) * 128), lane < 8, tag); lds_set(smem + N_OK + hp * 4, 1); }
    else wait_word(smem + N_OK + hp * 4, 1);
    f32x16 oc[3]; f32x4 ps[10];
    LAS char* red = smem + OWN + qo * 10240 + lane * 16;
    { bf16x8 af[5], of[20];
      const int la = (8 * hl + 2 * ((lane & 15) >> 2)) * 160 + (lane & 16) * 2 + (lane & 3) * 8;
      const int aa0 = wb0 + la, aa1 = (hp ? wb1 : wb0) + la, aa2 = wb1 + la, aa3 = (hp ? wb1 : wb2) + la;
      auto rda = [&](int f) { return trfrag(smem + (f < 36 ? aa0 : f < 42 ? aa1 : f < 51 ? aa2 : aa3) + (f / 3) * 2560 + (f % 3) * 64, 160); };
      auto ldo = [&](int k) { of[k] = __builtin_bit_cast(bf16x8, __builtin_amdgcn_raw_buffer_load_b128(orr, qo * 40960 + (hp * 20 + k) * 1024 + lane * 16, 0, 16)); };
#pragma unroll
      for (int k = 0; k < OPF; ++k) ldo(k);
      wait_word(smem + N_WO + hp * 4, 4);
#pragma unroll
      for (int f = 0; f < 4; ++f) af[f] = rda(f);
      SB();
      SFOR(f, 60)
          if constexpr (f % 3 == 0 && f / 3 + OPF < 20) ldo(f / 3 + OPF);
          if constexpr (f + 4 < 60) af[(f + 4) % 5] = rda(f + 4);
          if constexpr (f == 57) { if (hp) { wait_word(smem + C_RD + qo * 4, 1);
#pragma unroll
              for (int b = 0; b < 10; ++b) ps[b] = *(LAS f32x4*)(red + b * 1024); } }
          SB();
          if constexpr (f < 3) MFMA32Z(oc[f], af[f % 5], of[0]); else MFMA32I(oc[f % 3], af[f % 5], of[f / 3]);
          SB();
      SEND }
    MFMA_DRAIN();
    { if (!hp) {
          wait_word(smem + N_G1, 8);
#pragma unroll
          for (int b = 0; b < 10; ++b) *(LAS f32x4*)(red + b * 1024) = (f32x4){oc[b >> 2][4 * (b & 3)], oc[b >> 2][4 * (b & 3) + 1], oc[b >> 2][4 * (b & 3) + 2], oc[b >> 2][4 * (b & 3) + 3]};
          lds_set(smem + C_RD + qo * 4, 1);
      } else {
          char* orow = out + (size_t)(rt * 128 + qo * 32 + r32) * 2560 + br * 1280 + hg * 160 + hl * 16;
#pragma unroll
          for (int b = 0; b < 10; ++b)
#pragma unroll
              for (int r = 0; r < 4; ++r) ps[b][r] += oc[b >> 2][4 * (b & 3) + r];
          SB();
          u32x2 pk[10];
#pragma unroll
          for (int b = 0; b < 10; ++b) pk[b] = (u32x2){pk2(ps[b][0], ps[b][1]), pk2(ps[b][2], ps[b][3])};
          SB();
          u32x4 st[5];
#pragma unroll
          for (int b = 0; b < 10; b += 2) {
              auto rx = __builtin_amdgcn_permlane32_swap(pk[b][0], pk[b + 1][0], false, false); auto ry = __builtin_amdgcn_permlane32_swap(pk[b][1], pk[b + 1][1], false, false);
              st[b >> 1] = (u32x4){rx[0], ry[0], rx[1], ry[1]}; }
          SB();
#pragma unroll
          for (int b = 0; b < 5; ++b) *(u32x4*)(orow + b * 32) = st[b];
      } }
    if (bx == 0 && wid == 0) {
#pragma unroll 1
        for (int i = 0; i < 4; ++i) poll_flags((GAS u64*)(ws + WS_FO + (i * 64 + lane) * 256), true, tag);
        if (lane == 0) __hip_atomic_store((GAS u64*)ws, tag, RLX_AGENT);
    }
    asm volatile(".Lfused_attn_end:");
}

extern "C" void kernel_launch(void* const* d_in, const int* in_sizes, int n_in, void* d_out, int out_size, void* d_ws, size_t ws_size,
                              hipStream_t stream) {
    fused_attn_kernel<<<256, 512, 0, stream>>>((const char*)d_in[0], (const char*)d_in[1], (const char*)d_in[2], (const char*)d_in[3],
                                               (const char*)d_in[4], (const char*)d_in[5], (char*)d_out, (char*)d_ws);
}
```
